```python
import torch

def _softmax(x, axis=-1):
    return torch.softmax(x, dim=axis)

def _gelu(x):
    return torch.nn.functional.gelu(x, approximate='tanh')

def _silu(x):
    return torch.nn.functional.silu(x)

def _sigmoid(x):
    return torch.sigmoid(x)

def _layernorm(x, axis=-1, eps=1e-05):
    m = x.mean(dim=axis, keepdim=True)
    v = x.var(dim=axis, keepdim=True, unbiased=False)
    return (x - m) / torch.sqrt(v + eps)

def _scatter_add(tgt, idx, src, axis=0):
    return tgt.clone().index_add_(axis, idx, src)

def _segment_sum(src, seg, n):
    out = torch.zeros((n,) + src.shape[1:], dtype=src.dtype)
    return out.index_add_(0, seg, src)

def _pad(x, pw):
    flat = tuple((v for pair in reversed(pw) for v in pair))
    return torch.nn.functional.pad(x, flat)
kernel_name = 'adaLN_cond__spectral_fnet__geglu_ffn__prompt'

def setup_inputs():

    _w = lambda a: (lambda t: t.to(torch.bfloat16) if t.is_floating_point() else t)(torch.as_tensor(a))

    def _A_setup():
        B, S, D = (1, 2048, 1024)
        ns = torch.arange(S)[:, None] * torch.arange(S)[None, :]
        nd = torch.arange(D)[:, None] * torch.arange(D)[None, :]
        Ws_re = torch.cos(-2.0 * torch.pi * ns / S).to(float)
        Ws_im = torch.sin(-2.0 * torch.pi * ns / S).to(float)
        Wd_re = torch.cos(-2.0 * torch.pi * nd / D).to(float)
        Wd_im = torch.sin(-2.0 * torch.pi * nd / D).to(float)
        return {'x': _w((lambda *s: torch.randn(*s, dtype=torch.bfloat16) * 0.05)(B, S, D)), 'dft_seq_re': _w(Ws_re), 'dft_seq_im': _w(Ws_im), 'dft_hid_re': _w(Wd_re), 'dft_hid_im': _w(Wd_im)}
    _A_in = _A_setup()

    def _B_setup():
        B, S, D, F = (1, 2048, 1024, 3584)
        return {'x': _w((lambda *s: torch.randn(*s, dtype=torch.bfloat16) * 0.05)(B, S, D)), 'w_gate': _w((lambda *s: torch.randn(*s, dtype=torch.bfloat16) * 0.4)(D, F)), 'w_up': _w((lambda *s: torch.randn(*s, dtype=torch.bfloat16) * 0.4)(D, F)), 'w_down': _w((lambda *s: torch.randn(*s, dtype=torch.bfloat16) * 0.4)(F, D))}
    _B_in = _B_setup()
    return {'A_x': _A_in['x'], 'A_dft_seq_re': _A_in['dft_seq_re'], 'A_dft_seq_im': _A_in['dft_seq_im'], 'A_dft_hid_re': _A_in['dft_hid_re'], 'A_dft_hid_im': _A_in['dft_hid_im'], 'B_x': _B_in['x'], 'B_w_gate': _B_in['w_gate'], 'B_w_up': _B_in['w_up'], 'B_w_down': _B_in['w_down'], 'c': _w((lambda *s: torch.randn(*s, dtype=torch.bfloat16) * 0.1)(1, 1024) * 8.0), 'w_mod': _w((lambda *s: torch.randn(*s, dtype=torch.bfloat16) * 0.1)(1024, 6 * 1024) * 1024 ** (-0.5) * 8.0)}

def reference(A_x, A_dft_seq_re, A_dft_seq_im, A_dft_hid_re, A_dft_hid_im, B_x, B_w_gate, B_w_up, B_w_down, c, w_mod):

    def _A_ref(x, dft_seq_re, dft_seq_im, dft_hid_re, dft_hid_im):
        a = torch.einsum('bsd,ed->bse', x, dft_hid_re)
        b = torch.einsum('bsd,ed->bse', x, dft_hid_im)
        y = torch.einsum('ts,bse->bte', dft_seq_re, a) - torch.einsum('ts,bse->bte', dft_seq_im, b)
        return y

    def _B_ref(x, w_gate, w_up, w_down):
        return _gelu(x @ w_gate) * (x @ w_up) @ w_down
    x = A_x
    mod = (_silu(c) @ w_mod).reshape(c.shape[0], 6, 1, -1)
    sa, ca, ga, sb, cb, gb = (mod[:, 0].clone(), mod[:, 1].clone(), mod[:, 2].clone(), mod[:, 3].clone(), mod[:, 4].clone(), mod[:, 5].clone())
    h = _layernorm(x, axis=-1) * (1 + ca) + sa
    x = x + ga * _A_ref(h, A_dft_seq_re, A_dft_seq_im, A_dft_hid_re, A_dft_hid_im)
    h = _layernorm(x, axis=-1) * (1 + cb) + sb
    x = x + gb * _B_ref(h, B_w_gate, B_w_up, B_w_down)
    return x
```

```cpp
#include <hip/hip_runtime.h>

__global__ __launch_bounds__(256, 1) __attribute__((aligned(4096))) void fused_kernel(const void* x, const void* wsre, const void* wsim, const void* wdre, const void* wdim,
                                                       const void* wg, const void* wu, const void* wdn, const void* c, const void* wmod,
                                                       void* out, void* ws) {
  asm volatile(
R"(
.p2align 3
LB_%=:
s_load_dwordx16 s[8:23], s[4:5], 0
s_load_dwordx8 s[24:31], s[4:5], 64
v_lshrrev_b32 v236, 6, v0
v_and_b32 v231, 63, v0
v_readfirstlane_b32 s72, v236
v_lshlrev_b32 v230, 4, v231
s_and_b32 s75, s6, 1
s_lshr_b32 s76, s6, 5
s_lshl_b32 s74, s75, 3
s_add_u32 s74, s74, s76
s_bfe_u32 s73, s6, 0x20001
s_lshl_b32 s73, s73, 2
s_bfe_u32 s85, s6, 0x20003
s_add_u32 s73, s73, s85
s_lshl_b32 s77, s72, 12
s_mov_b32 s84, 30000
s_mov_b32 s93, 0xffff0000
s_lshl_b32 s91, s75, 31
s_lshl_b32 s85, s6, 2
s_add_u32 s85, s85, s72
s_lshl_b32 s85, s85, 12
s_mov_b32 s86, 48234496
s_getpc_b64 s[96:97]
LT0_%=:
s_sub_u32 s96, s96, LT0_%=-LB_%=
s_subb_u32 s97, s97, 0
s_mov_b32 s98, LE_%=-LB_%=
s_mov_b32 s99, 0x20000
s_lshr_b32 s87, s6, 3
s_lshl2_add_u32 s87, s87, s72
s_lshl_b32 s87, s87, 10
v_and_b32 v247, 7, v231
v_lshlrev_b32 v247, 7, v247
buffer_load_dword v247, v247, s[96:99], s87 offen
s_lshl_b32 s88, s73, 3
s_add_u32 s88, s88, s72
s_lshl_b32 s88, s88, 16
v_lshrrev_b32 v236, 4, v231
v_and_b32 v237, 7, v231
v_xor_b32 v237, v237, v236
v_lshrrev_b32 v236, 3, v231
v_lshlrev_b32 v236, 12, v236
v_lshl_add_u32 v236, v237, 4, v236
v_add_u32 v228, s88, v236
v_xor_b32 v236, 64, v228
v_add_u32 v229, 31744, v236
v_add_u32 v252, 260096, v228
v_add_u32 v253, 260096, v229
v_and_b32 v236, 15, v231
v_lshrrev_b32 v237, 4, v231
s_lshl2_add_u32 s87, s76, s72
s_lshl_b32 s88, s73, 5
s_add_u32 s87, s87, s88
s_lshl_b32 s87, s87, 14
v_lshlrev_b32 v127, 6, v236
v_lshl_add_u32 v127, v237, 12, v127
v_add_u32 v127, s87, v127
s_waitcnt lgkmcnt(0)
s_mov_b64 s[56:57], s[8:9]
s_mov_b32 s58, 4194304
s_mov_b32 s59, 0x20000
s_mov_b64 s[52:53], s[30:31]
s_mov_b32 s54, 58720256
s_mov_b32 s55, 0x20000
buffer_load_dwordx2 v[234:235], off, s[52:55], s86 sc1
buffer_load_dwordx4 v[32:35], v230, s[56:59], s85 offen offset:0
buffer_load_dwordx4 v[36:39], v230, s[56:59], s85 offen offset:1024
buffer_load_dwordx4 v[40:43], v230, s[56:59], s85 offen offset:2048
buffer_load_dwordx4 v[44:47], v230, s[56:59], s85 offen offset:3072
s_cmp_eq_u32 s75, 0
s_cselect_b64 s[32:33], s[10:11], s[12:13]
s_cselect_b64 s[36:37], s[14:15], s[16:17]
s_mov_b32 s34, 8388608
s_mov_b32 s35, 0x20000
s_mov_b32 s4, 0
s_bitcmp1_b32 s6, 1
s_cselect_b32 s89, 17472, 0
s_xor_b32 s4, s4, s89
s_bitcmp1_b32 s6, 3
s_cselect_b32 s89, 4368, 0
s_xor_b32 s4, s4, s89
s_bitcmp1_b32 s6, 4
s_cselect_b32 s89, 8736, 0
s_xor_b32 s4, s4, s89
s_bitcmp1_b32 s6, 5
s_cselect_b32 s89, 4369, 0
s_xor_b32 s4, s4, s89
s_bitcmp1_b32 s6, 6
s_cselect_b32 s89, 8738, 0
s_xor_b32 s4, s4, s89
s_bitcmp1_b32 s6, 7
s_cselect_b32 s89, 17476, 0
s_xor_b32 s4, s4, s89
s_bfe_u32 s83, s4, 262144
s_lshl_b32 s83, s83, 7
s_lshl_b32 s82, s83, 7
s_sub_u32 s36, s36, 4096
s_subb_u32 s37, s37, 0
s_mov_b32 s38, 2101248
s_mov_b32 s39, 0x20000
s_mov_b64 s[40:41], s[18:19]
s_mov_b32 s42, 7340032
s_mov_b32 s43, 0x20000
s_sub_u32 s44, s20, 2048
s_subb_u32 s45, s21, 0
s_mov_b32 s46, 7342080
s_mov_b32 s47, 0x20000
s_sub_u32 s48, s22, 4096
s_subb_u32 s49, s23, 0
s_mov_b32 s50, 7344128
s_mov_b32 s51, 0x20000
s_mov_b64 s[60:61], s[28:29]
s_mov_b32 s62, 4194304
s_mov_b32 s63, 0x20000
s_mov_b64 s[64:65], s[26:27]
s_mov_b32 s66, 12582912
s_mov_b32 s67, 0x20000
s_mov_b64 s[68:69], s[24:25]
s_mov_b32 s70, 2048
s_mov_b32 s71, 0x20000
v_mov_b32 v126, -1
s_lshl4_add_u32 s85, s75, s73
s_lshl3_add_u32 s85, s85, s72
s_lshl3_add_u32 s85, s85, s76
s_lshl_b32 s85, s85, 12
s_add_u32 s18, s85, 4194304
s_add_u32 s19, s85, 4325376
s_lshl1_add_u32 s86, s73, s75
s_lshl2_add_u32 s85, s6, s72
s_lshl_b32 s85, s85, 12
s_add_u32 s20, s85, 54525952
s_add_u32 s23, s85, 39845888
s_lshl2_add_u32 s85, s76, s72
s_mul_i32 s85, s85, 4096
s_add_u32 s21, s85, 48496640
s_mul_i32 s86, s86, 128
s_add_u32 s22, s86, s21
s_lshl2_add_u32 s85, s73, s72
s_mul_i32 s85, s85, 2048
s_add_u32 s24, s85, 48758784
s_mul_i32 s90, s74, 128
s_add_u32 s90, s90, s24
s_lshl_b32 s85, s76, 19
s_add_u32 s85, s85, s77
s_add_u32 s85, s85, 54524928
v_add_u32 v213, s85, v230
v_add_u32 v214, 1024, v213
v_add_u32 v215, 2048, v213
v_add_u32 v212, 65536, v230
v_lshrrev_b32 v238, 1, v236
v_xor_b32 v238, v238, v237
v_lshlrev_b32 v238, 4, v238
v_lshl_add_u32 v208, v236, 7, v238
v_xor_b32 v209, 64, v208
v_xor_b32 v210, 65536, v208
v_xor_b32 v211, 65600, v208
v_mov_b32 v248, 0x3f803f80
v_mov_b32 v249, 0x3f803f80
v_mov_b32 v250, 0x3f803f80
v_mov_b32 v251, 0x3f803f80
s_lshr_b32 s85, s6, 5
s_lshl_b32 s85, s85, 2
s_add_u32 s85, s85, s72
s_lshl_b32 s86, s85, 6
v_lshrrev_b32 v244, 3, v231
v_lshlrev_b32 v245, 3, v244
buffer_load_dwordx2 v[100:101], v245, s[68:71], s86 offen
s_mul_i32 s85, s85, 393216
s_and_b32 s87, s6, 31
s_lshl_b32 s87, s87, 7
s_add_u32 s85, s85, s87
v_mul_u32_u24 v244, 49152, v244
v_and_b32 v246, 0x70, v230
v_add3_u32 v124, v244, v246, s85
s_mov_b32 s87, 0
buffer_load_dwordx4 v[128:131], v124, s[64:67], s87 offen
s_mov_b32 s87, 12288
buffer_load_dwordx4 v[132:135], v124, s[64:67], s87 offen
s_mov_b32 s87, 24576
buffer_load_dwordx4 v[136:139], v124, s[64:67], s87 offen
s_mov_b32 s87, 36864
buffer_load_dwordx4 v[140:143], v124, s[64:67], s87 offen
s_lshr_b32 s85, s6, 6
s_lshl_b32 s85, s85, 2
s_add_u32 s85, s85, s72
s_lshl_b32 s86, s85, 7
v_lshrrev_b32 v244, 3, v231
v_lshlrev_b32 v245, 4, v244
buffer_load_dwordx4 v[96:99], v245, s[68:71], s86 offen
s_mul_i32 s85, s85, 786432
s_and_b32 s87, s6, 63
s_lshl_b32 s87, s87, 7
s_add_u32 s85, s85, s87
v_mul_u32_u24 v244, 98304, v244
v_and_b32 v246, 0x70, v230
v_add3_u32 v125, v244, v246, s85
s_bfe_u32 s86, s4, 262152
s_xor_b32 s87, s86, 0
s_mul_i32 s10, s87, 458752
s_xor_b32 s87, s86, 1
s_mul_i32 s11, s87, 458752
s_xor_b32 s87, s86, 2
s_mul_i32 s12, s87, 458752
s_xor_b32 s87, s86, 3
s_mul_i32 s13, s87, 458752
s_xor_b32 s87, s86, 4
s_mul_i32 s14, s87, 458752
s_xor_b32 s87, s86, 5
s_mul_i32 s15, s87, 458752
s_xor_b32 s87, s86, 6
s_mul_i32 s16, s87, 458752
s_xor_b32 s87, s86, 7
s_mul_i32 s17, s87, 458752
s_lshl_b32 s85, s75, 1
s_sub_u32 s85, s85, 1
s_lshl_b32 s30, s85, 17
s_lshl_b32 s85, s76, 1
s_add_u32 s85, s85, s75
s_xor_b32 s85, s85, 1
s_lshl2_add_u32 s85, s85, s72
s_lshl_b32 s85, s85, 15
s_add_u32 s85, s85, 4096
v_lshrrev_b32 v244, 4, v231
v_and_b32 v245, 7, v231
v_xor_b32 v245, v245, v244
v_lshrrev_b32 v244, 3, v231
v_lshlrev_b32 v244, 11, v244
v_lshl_add_u32 v244, v245, 4, v244
v_add_u32 v224, s85, v244
v_xor_b32 v244, 64, v224
v_add_u32 v225, 15360, v244
s_sub_u32 s85, s30, 2048
v_add_u32 v226, s85, v224
v_add_u32 v227, s85, v225
s_mul_i32 s85, s72, 114688
s_mul_i32 s86, s74, 448
s_add_u32 s85, s85, s86
s_and_b32 s86, s74, 1
s_lshl_b32 s86, s86, 6
s_add_u32 s85, s85, s86
v_lshrrev_b32 v244, 3, v231
v_and_b32 v245, 6, v244
v_xor_b32 v245, v245, v231
v_and_b32 v245, 7, v245
v_mul_u32_u24 v244, 7168, v244
v_lshl_add_u32 v244, v245, 4, v244
v_add_u32 v232, s85, v244
v_add_u32 v233, 56320, v232
v_add_u32 v245, s77, v210
v_add_u32 v246, s77, v211
v_add_u32 v156, 65536, v245
v_add_u32 v157, 65536, v246
s_mul_i32 s85, s72, 4224
s_add_u32 s85, s85, 0
v_add_u32 v239, s85, v230
v_lshrrev_b32 v240, 2, v236
v_mul_u32_u24 v240, 2112, v240
v_and_b32 v242, 0x30, v230
v_add_u32 v240, v240, v242
v_and_b32 v242, 1, v237
v_lshl_add_u32 v240, v242, 3, v240
v_lshrrev_b32 v243, 1, v237
v_lshl_add_u32 v240, v243, 6, v240
s_lshl_b32 s85, s72, 9
s_add_u32 s85, s85, 0
v_add_u32 v240, s85, v240
v_lshlrev_b32 v241, 4, v236
v_lshl_add_u32 v241, v242, 11, v241
v_lshl_add_u32 v241, v243, 12, v241
s_lshl_b32 s85, s72, 20
s_lshr_b32 s86, s6, 2
s_lshl_b32 s86, s86, 13
s_add_u32 s85, s85, s86
s_and_b32 s86, s6, 3
s_lshl_b32 s86, s86, 8
s_add_u32 s85, s85, s86
s_add_u32 s88, s85, 54525952
s_add_u32 s89, s85, 55050240
s_waitcnt vmcnt(10)
v_readfirstlane_b32 s78, v234
v_readfirstlane_b32 s79, v235
s_waitcnt vmcnt(9)
v_lshlrev_b32 v0, 16, v32
v_and_b32 v1, s93, v32
v_lshlrev_b32 v2, 16, v33
v_and_b32 v3, s93, v33
v_lshlrev_b32 v4, 16, v34
v_and_b32 v5, s93, v34
v_lshlrev_b32 v6, 16, v35
v_and_b32 v7, s93, v35
s_waitcnt vmcnt(8)
v_lshlrev_b32 v8, 16, v36
v_and_b32 v9, s93, v36
v_lshlrev_b32 v10, 16, v37
v_and_b32 v11, s93, v37
v_lshlrev_b32 v12, 16, v38
v_and_b32 v13, s93, v38
v_lshlrev_b32 v14, 16, v39
v_and_b32 v15, s93, v39
s_waitcnt vmcnt(7)
v_lshlrev_b32 v16, 16, v40
v_and_b32 v17, s93, v40
v_lshlrev_b32 v18, 16, v41
v_and_b32 v19, s93, v41
v_lshlrev_b32 v20, 16, v42
v_and_b32 v21, s93, v42
v_lshlrev_b32 v22, 16, v43
v_and_b32 v23, s93, v43
s_waitcnt vmcnt(6)
v_lshlrev_b32 v24, 16, v44
v_and_b32 v25, s93, v44
v_lshlrev_b32 v26, 16, v45
v_and_b32 v27, s93, v45
v_lshlrev_b32 v28, 16, v46
v_and_b32 v29, s93, v46
v_lshlrev_b32 v30, 16, v47
v_and_b32 v31, s93, v47
s_xor_b32 s81, s83, 0
s_add_u32 m0, s77, 65536
s_nop 0
buffer_load_dwordx4 v228, s[32:35], s81 offen offset:0 lds
s_add_u32 s78, s78, 1
s_add_u32 s79, s79, 0x9e3779b9
v_mov_b32 v234, s78
v_mov_b32 v235, s79
v_pk_add_f32 v[48:49], v[0:1], v[2:3]
v_pk_add_f32 v[52:53], v[16:17], v[18:19]
buffer_load_dwordx4 v252, s[32:35], s81 offen offset:2048 lds
v_pk_add_f32 v[50:51], v[4:5], v[6:7]
v_pk_add_f32 v[54:55], v[20:21], v[22:23]
v_pk_add_f32 v[48:49], v[48:49], v[8:9]
v_pk_add_f32 v[52:53], v[52:53], v[24:25]
v_pk_add_f32 v[50:51], v[50:51], v[10:11]
v_pk_add_f32 v[54:55], v[54:55], v[26:27]
buffer_load_dwordx4 v229, s[32:35], s81 offen offset:1024 lds
v_pk_add_f32 v[48:49], v[48:49], v[12:13]
v_pk_add_f32 v[52:53], v[52:53], v[28:29]
v_pk_add_f32 v[50:51], v[50:51], v[14:15]
v_pk_add_f32 v[54:55], v[54:55], v[30:31]
v_pk_add_f32 v[48:49], v[48:49], v[50:51]
v_pk_add_f32 v[52:53], v[52:53], v[54:55]
buffer_load_dwordx4 v253, s[32:35], s81 offen offset:3072 lds
v_add_f32 v56, v48, v49
v_add_f32 v57, v52, v53
s_nop 1
v_permlane16_swap_b32 v56, v57
v_add_f32 v56, v56, v57
v_mov_b32 v57, v56
s_nop 1
v_permlane32_swap_b32 v56, v57
v_add_f32 v56, v56, v57
s_nop 1
v_add_f32_dpp v56, v56, v56 row_ror:8 row_mask:0xf bank_mask:0xf
s_nop 1
v_add_f32_dpp v56, v56, v56 row_ror:4 row_mask:0xf bank_mask:0xf
s_nop 1
v_add_f32_dpp v56, v56, v56 row_ror:2 row_mask:0xf bank_mask:0xf
s_nop 1
v_add_f32_dpp v56, v56, v56 row_ror:1 row_mask:0xf bank_mask:0xf
v_mul_f32 v56, 0xba800000, v56
s_nop 0
v_readlane_b32 s86, v56, 0
v_readlane_b32 s87, v56, 16
s_nop 1
v_pk_add_f32 v[0:1], v[0:1], s[86:87] op_sel:[0,0] op_sel_hi:[1,0]
v_pk_add_f32 v[2:3], v[2:3], s[86:87] op_sel:[0,0] op_sel_hi:[1,0]
v_pk_add_f32 v[4:5], v[4:5], s[86:87] op_sel:[0,0] op_sel_hi:[1,0]
v_pk_add_f32 v[6:7], v[6:7], s[86:87] op_sel:[0,0] op_sel_hi:[1,0]
v_pk_add_f32 v[8:9], v[8:9], s[86:87] op_sel:[0,0] op_sel_hi:[1,0]
v_pk_add_f32 v[10:11], v[10:11], s[86:87] op_sel:[0,0] op_sel_hi:[1,0]
v_pk_add_f32 v[12:13], v[12:13], s[86:87] op_sel:[0,0] op_sel_hi:[1,0]
v_pk_add_f32 v[14:15], v[14:15], s[86:87] op_sel:[0,0] op_sel_hi:[1,0]
v_pk_add_f32 v[16:17], v[16:17], s[86:87] op_sel:[0,1] op_sel_hi:[1,1]
v_pk_add_f32 v[18:19], v[18:19], s[86:87] op_sel:[0,1] op_sel_hi:[1,1]
v_pk_add_f32 v[20:21], v[20:21], s[86:87] op_sel:[0,1] op_sel_hi:[1,1]
v_pk_add_f32 v[22:23], v[22:23], s[86:87] op_sel:[0,1] op_sel_hi:[1,1]
v_pk_add_f32 v[24:25], v[24:25], s[86:87] op_sel:[0,1] op_sel_hi:[1,1]
v_pk_add_f32 v[26:27], v[26:27], s[86:87] op_sel:[0,1] op_sel_hi:[1,1]
v_pk_add_f32 v[28:29], v[28:29], s[86:87] op_sel:[0,1] op_sel_hi:[1,1]
v_pk_add_f32 v[30:31], v[30:31], s[86:87] op_sel:[0,1] op_sel_hi:[1,1]
v_pk_mul_f32 v[48:49], v[0:1], v[0:1]
v_pk_mul_f32 v[52:53], v[16:17], v[16:17]
v_pk_mul_f32 v[50:51], v[2:3], v[2:3]
v_pk_mul_f32 v[54:55], v[18:19], v[18:19]
v_pk_fma_f32 v[48:49], v[4:5], v[4:5], v[48:49]
v_pk_fma_f32 v[52:53], v[20:21], v[20:21], v[52:53]
v_pk_fma_f32 v[50:51], v[6:7], v[6:7], v[50:51]
v_pk_fma_f32 v[54:55], v[22:23], v[22:23], v[54:55]
v_pk_fma_f32 v[48:49], v[8:9], v[8:9], v[48:49]
v_pk_fma_f32 v[52:53], v[24:25], v[24:25], v[52:53]
v_pk_fma_f32 v[50:51], v[10:11], v[10:11], v[50:51]
v_pk_fma_f32 v[54:55], v[26:27], v[26:27], v[54:55]
v_pk_fma_f32 v[48:49], v[12:13], v[12:13], v[48:49]
v_pk_fma_f32 v[52:53], v[28:29], v[28:29], v[52:53]
v_pk_fma_f32 v[50:51], v[14:15], v[14:15], v[50:51]
v_pk_fma_f32 v[54:55], v[30:31], v[30:31], v[54:55]
v_pk_add_f32 v[48:49], v[48:49], v[50:51]
v_pk_add_f32 v[52:53], v[52:53], v[54:55]
)"
R"(
v_add_f32 v56, v48, v49
v_add_f32 v57, v52, v53
s_nop 1
v_permlane16_swap_b32 v56, v57
v_add_f32 v56, v56, v57
v_mov_b32 v57, v56
s_nop 1
v_permlane32_swap_b32 v56, v57
v_add_f32 v56, v56, v57
s_nop 1
v_add_f32_dpp v56, v56, v56 row_ror:8 row_mask:0xf bank_mask:0xf
s_nop 1
v_add_f32_dpp v56, v56, v56 row_ror:4 row_mask:0xf bank_mask:0xf
s_nop 1
v_add_f32_dpp v56, v56, v56 row_ror:2 row_mask:0xf bank_mask:0xf
s_nop 1
v_add_f32_dpp v56, v56, v56 row_ror:1 row_mask:0xf bank_mask:0xf
v_mov_b32 v57, 0x3727c5ac
v_fmac_f32 v57, 0x3a800000, v56
v_rsq_f32 v56, v57
s_nop 0
v_readlane_b32 s86, v56, 0
v_readlane_b32 s87, v56, 16
s_nop 1
v_pk_mul_f32 v[0:1], v[0:1], s[86:87] op_sel:[0,0] op_sel_hi:[1,0]
v_pk_mul_f32 v[2:3], v[2:3], s[86:87] op_sel:[0,0] op_sel_hi:[1,0]
v_pk_mul_f32 v[4:5], v[4:5], s[86:87] op_sel:[0,0] op_sel_hi:[1,0]
v_pk_mul_f32 v[6:7], v[6:7], s[86:87] op_sel:[0,0] op_sel_hi:[1,0]
v_pk_mul_f32 v[8:9], v[8:9], s[86:87] op_sel:[0,0] op_sel_hi:[1,0]
v_pk_mul_f32 v[10:11], v[10:11], s[86:87] op_sel:[0,0] op_sel_hi:[1,0]
v_pk_mul_f32 v[12:13], v[12:13], s[86:87] op_sel:[0,0] op_sel_hi:[1,0]
v_pk_mul_f32 v[14:15], v[14:15], s[86:87] op_sel:[0,0] op_sel_hi:[1,0]
v_cvt_pk_bf16_f32 v32, v0, v1
v_cvt_pk_bf16_f32 v33, v2, v3
v_cvt_pk_bf16_f32 v34, v4, v5
v_cvt_pk_bf16_f32 v35, v6, v7
v_cvt_pk_bf16_f32 v36, v8, v9
v_cvt_pk_bf16_f32 v37, v10, v11
v_cvt_pk_bf16_f32 v38, v12, v13
v_cvt_pk_bf16_f32 v39, v14, v15
ds_write_b128 v239, v[32:35] offset:0
ds_write_b128 v239, v[36:39] offset:1024
v_pk_mul_f32 v[16:17], v[16:17], s[86:87] op_sel:[0,1] op_sel_hi:[1,1]
v_pk_mul_f32 v[18:19], v[18:19], s[86:87] op_sel:[0,1] op_sel_hi:[1,1]
v_pk_mul_f32 v[20:21], v[20:21], s[86:87] op_sel:[0,1] op_sel_hi:[1,1]
v_pk_mul_f32 v[22:23], v[22:23], s[86:87] op_sel:[0,1] op_sel_hi:[1,1]
v_pk_mul_f32 v[24:25], v[24:25], s[86:87] op_sel:[0,1] op_sel_hi:[1,1]
v_pk_mul_f32 v[26:27], v[26:27], s[86:87] op_sel:[0,1] op_sel_hi:[1,1]
v_pk_mul_f32 v[28:29], v[28:29], s[86:87] op_sel:[0,1] op_sel_hi:[1,1]
v_pk_mul_f32 v[30:31], v[30:31], s[86:87] op_sel:[0,1] op_sel_hi:[1,1]
v_cvt_pk_bf16_f32 v40, v16, v17
v_cvt_pk_bf16_f32 v41, v18, v19
v_cvt_pk_bf16_f32 v42, v20, v21
v_cvt_pk_bf16_f32 v43, v22, v23
v_cvt_pk_bf16_f32 v44, v24, v25
v_cvt_pk_bf16_f32 v45, v26, v27
v_cvt_pk_bf16_f32 v46, v28, v29
v_cvt_pk_bf16_f32 v47, v30, v31
ds_write_b128 v239, v[40:43] offset:2112
ds_write_b128 v239, v[44:47] offset:3136
s_waitcnt lgkmcnt(0)
s_barrier
ds_read_b64_tr_b16 v[0:1], v240 offset:0
ds_read_b64_tr_b16 v[2:3], v240 offset:8448
ds_read_b64_tr_b16 v[4:5], v240 offset:128
ds_read_b64_tr_b16 v[6:7], v240 offset:8576
ds_read_b64_tr_b16 v[8:9], v240 offset:256
ds_read_b64_tr_b16 v[10:11], v240 offset:8704
ds_read_b64_tr_b16 v[12:13], v240 offset:384
ds_read_b64_tr_b16 v[14:15], v240 offset:8832
s_waitcnt lgkmcnt(7)
v_and_b32 v0, -2, v0
s_waitcnt lgkmcnt(6)
buffer_store_dwordx4 v[0:3], v241, s[52:55], s88 offen offset:0 sc1
s_waitcnt lgkmcnt(5)
v_and_b32 v4, -2, v4
s_waitcnt lgkmcnt(4)
buffer_store_dwordx4 v[4:7], v241, s[52:55], s88 offen offset:1024 sc1
s_waitcnt lgkmcnt(3)
v_and_b32 v8, -2, v8
s_waitcnt lgkmcnt(2)
buffer_store_dwordx4 v[8:11], v241, s[52:55], s89 offen offset:0 sc1
s_waitcnt lgkmcnt(1)
v_and_b32 v12, -2, v12
s_waitcnt lgkmcnt(0)
buffer_store_dwordx4 v[12:15], v241, s[52:55], s89 offen offset:1024 sc1
s_lshl_b32 s85, s72, 8
s_add_u32 s85, s85, s6
s_lshl_b32 s85, s85, 3
s_add_u32 s88, s85, 51380224
v_lshlrev_b32 v237, 13, v231
s_and_b32 s85, s6, 7
s_lshl_b32 s85, s85, 2
s_lshr_b32 s86, s76, 1
s_add_u32 s85, s85, s86
s_lshl_b32 s85, s85, 2
s_add_u32 s85, s85, s72
s_lshl_b32 s85, s85, 9
s_add_u32 s89, s85, 51380224
v_lshlrev_b32 v236, 3, v231
s_xor_b32 s81, s83, 1024
s_add_u32 m0, s77, 81920
s_nop 0
buffer_load_dwordx4 v228, s[32:35], s81 offen offset:0 lds
buffer_load_dwordx4 v252, s[32:35], s81 offen offset:2048 lds
buffer_load_dwordx4 v229, s[32:35], s81 offen offset:1024 lds
buffer_load_dwordx4 v253, s[32:35], s81 offen offset:3072 lds
s_waitcnt vmcnt(12)
v_lshlrev_b32 v112, 16, v96
v_and_b32 v113, s93, v96
v_lshlrev_b32 v114, 16, v97
v_and_b32 v115, s93, v97
v_lshlrev_b32 v116, 16, v98
v_and_b32 v117, s93, v98
v_lshlrev_b32 v118, 16, v99
v_and_b32 v119, s93, v99
v_lshlrev_b32 v120, 16, v100
v_and_b32 v121, s93, v100
v_lshlrev_b32 v122, 16, v101
v_and_b32 v123, s93, v101
v_mul_f32 v32, 0xbfb8aa3b, v112
v_mul_f32 v33, 0xbfb8aa3b, v113
v_mul_f32 v34, 0xbfb8aa3b, v114
v_mul_f32 v35, 0xbfb8aa3b, v115
v_mul_f32 v36, 0xbfb8aa3b, v116
v_mul_f32 v37, 0xbfb8aa3b, v117
v_mul_f32 v38, 0xbfb8aa3b, v118
v_mul_f32 v39, 0xbfb8aa3b, v119
v_mul_f32 v40, 0xbfb8aa3b, v120
v_mul_f32 v41, 0xbfb8aa3b, v121
v_mul_f32 v42, 0xbfb8aa3b, v122
v_mul_f32 v43, 0xbfb8aa3b, v123
v_exp_f32 v32, v32
v_exp_f32 v33, v33
v_exp_f32 v34, v34
v_exp_f32 v35, v35
v_exp_f32 v36, v36
v_exp_f32 v37, v37
v_exp_f32 v38, v38
v_exp_f32 v39, v39
v_exp_f32 v40, v40
v_exp_f32 v41, v41
v_exp_f32 v42, v42
v_exp_f32 v43, v43
v_add_f32 v32, 1.0, v32
v_add_f32 v33, 1.0, v33
v_add_f32 v34, 1.0, v34
v_add_f32 v35, 1.0, v35
v_add_f32 v36, 1.0, v36
v_add_f32 v37, 1.0, v37
v_add_f32 v38, 1.0, v38
v_add_f32 v39, 1.0, v39
v_add_f32 v40, 1.0, v40
v_add_f32 v41, 1.0, v41
v_add_f32 v42, 1.0, v42
v_add_f32 v43, 1.0, v43
v_rcp_f32 v32, v32
v_rcp_f32 v33, v33
v_rcp_f32 v34, v34
v_rcp_f32 v35, v35
v_rcp_f32 v36, v36
v_rcp_f32 v37, v37
v_rcp_f32 v38, v38
v_rcp_f32 v39, v39
v_rcp_f32 v40, v40
v_rcp_f32 v41, v41
v_rcp_f32 v42, v42
v_rcp_f32 v43, v43
v_mul_f32 v32, v32, v112
v_mul_f32 v33, v33, v113
v_mul_f32 v34, v34, v114
v_mul_f32 v35, v35, v115
v_mul_f32 v36, v36, v116
v_mul_f32 v37, v37, v117
v_mul_f32 v38, v38, v118
v_mul_f32 v39, v39, v119
v_mul_f32 v40, v40, v120
v_mul_f32 v41, v41, v121
v_mul_f32 v42, v42, v122
v_mul_f32 v43, v43, v123
v_cvt_pk_bf16_f32 v32, v32, v33
v_cvt_pk_bf16_f32 v34, v34, v35
v_cvt_pk_bf16_f32 v36, v36, v37
v_cvt_pk_bf16_f32 v38, v38, v39
v_cvt_pk_bf16_f32 v40, v40, v41
v_cvt_pk_bf16_f32 v42, v42, v43
v_lshlrev_b32 v112, 16, v32
v_and_b32 v113, s93, v32
v_lshlrev_b32 v114, 16, v34
v_and_b32 v115, s93, v34
v_lshlrev_b32 v116, 16, v36
v_and_b32 v117, s93, v36
v_lshlrev_b32 v118, 16, v38
v_and_b32 v119, s93, v38
v_lshlrev_b32 v120, 16, v40
v_and_b32 v121, s93, v40
v_lshlrev_b32 v122, 16, v42
v_and_b32 v123, s93, v42
s_waitcnt vmcnt(4)
s_mov_b64 exec, 255
buffer_store_dwordx2 v[234:235], v237, s[52:55], s88 offen sc1
s_mov_b64 exec, -1
s_xor_b32 s81, s83, 128
s_add_u32 m0, s77, 98304
s_nop 0
buffer_load_dwordx4 v228, s[32:35], s81 offen offset:0 lds
buffer_load_dwordx4 v252, s[32:35], s81 offen offset:2048 lds
buffer_load_dwordx4 v229, s[32:35], s81 offen offset:1024 lds
buffer_load_dwordx4 v253, s[32:35], s81 offen offset:3072 lds
s_xor_b32 s81, s83, 256
s_add_u32 m0, s77, 114688
s_nop 0
buffer_load_dwordx4 v228, s[32:35], s81 offen offset:0 lds
buffer_load_dwordx4 v252, s[32:35], s81 offen offset:2048 lds
buffer_load_dwordx4 v229, s[32:35], s81 offen offset:1024 lds
buffer_load_dwordx4 v253, s[32:35], s81 offen offset:3072 lds
ds_read_b128 a[0:3], v245 offset:0
ds_read_b128 a[4:7], v245 offset:2048
ds_read_b128 a[8:11], v246 offset:0
ds_read_b128 a[12:15], v246 offset:2048
s_xor_b32 s81, s83, 384
s_add_u32 m0, s77, 131072
s_nop 0
buffer_load_dwordx4 v228, s[32:35], s81 offen offset:0 lds
buffer_load_dwordx4 v252, s[32:35], s81 offen offset:2048 lds
buffer_load_dwordx4 v229, s[32:35], s81 offen offset:1024 lds
buffer_load_dwordx4 v253, s[32:35], s81 offen offset:3072 lds
s_waitcnt vmcnt(13)
ds_read_b128 a[16:19], v245 offset:16384
ds_read_b128 a[20:23], v245 offset:18432
ds_read_b128 a[24:27], v246 offset:16384
ds_read_b128 a[28:31], v246 offset:18432
s_xor_b32 s81, s83, 512
s_add_u32 m0, s77, 65536
s_waitcnt lgkmcnt(4)
buffer_load_dwordx4 v228, s[32:35], s81 offen offset:0 lds
buffer_load_dwordx4 v252, s[32:35], s81 offen offset:2048 lds
buffer_load_dwordx4 v229, s[32:35], s81 offen offset:1024 lds
buffer_load_dwordx4 v253, s[32:35], s81 offen offset:3072 lds
s_waitcnt vmcnt(12)
ds_read_b128 a[32:35], v245 offset:32768
ds_read_b128 a[36:39], v245 offset:34816
ds_read_b128 a[40:43], v246 offset:32768
ds_read_b128 a[44:47], v246 offset:34816
s_xor_b32 s81, s83, 640
s_add_u32 m0, s77, 81920
s_waitcnt lgkmcnt(4)
buffer_load_dwordx4 v228, s[32:35], s81 offen offset:0 lds
buffer_load_dwordx4 v252, s[32:35], s81 offen offset:2048 lds
buffer_load_dwordx4 v229, s[32:35], s81 offen offset:1024 lds
buffer_load_dwordx4 v253, s[32:35], s81 offen offset:3072 lds
s_waitcnt vmcnt(12)
ds_read_b128 a[48:51], v245 offset:49152
ds_read_b128 a[52:55], v245 offset:51200
ds_read_b128 a[56:59], v246 offset:49152
ds_read_b128 a[60:63], v246 offset:51200
s_xor_b32 s81, s83, 768
s_add_u32 m0, s77, 98304
s_waitcnt lgkmcnt(4)
buffer_load_dwordx4 v228, s[32:35], s81 offen offset:0 lds
buffer_load_dwordx4 v252, s[32:35], s81 offen offset:2048 lds
buffer_load_dwordx4 v229, s[32:35], s81 offen offset:1024 lds
buffer_load_dwordx4 v253, s[32:35], s81 offen offset:3072 lds
s_waitcnt vmcnt(12)
ds_read_b128 a[64:67], v156 offset:0
ds_read_b128 a[68:71], v156 offset:2048
ds_read_b128 a[72:75], v157 offset:0
ds_read_b128 a[76:79], v157 offset:2048
s_xor_b32 s81, s83, 896
s_add_u32 m0, s77, 114688
s_waitcnt lgkmcnt(4)
buffer_load_dwordx4 v228, s[32:35], s81 offen offset:0 lds
buffer_load_dwordx4 v252, s[32:35], s81 offen offset:2048 lds
buffer_load_dwordx4 v229, s[32:35], s81 offen offset:1024 lds
buffer_load_dwordx4 v253, s[32:35], s81 offen offset:3072 lds
s_waitcnt vmcnt(12)
ds_read_b128 a[80:83], v245 offset:0
ds_read_b128 a[84:87], v245 offset:2048
ds_read_b128 a[88:91], v246 offset:0
ds_read_b128 a[92:95], v246 offset:2048
s_xor_b32 s81, s83, 1152
s_add_u32 m0, s77, 131072
s_waitcnt lgkmcnt(4)
buffer_load_dwordx4 v228, s[32:35], s81 offen offset:0 lds
buffer_load_dwordx4 v252, s[32:35], s81 offen offset:2048 lds
buffer_load_dwordx4 v229, s[32:35], s81 offen offset:1024 lds
buffer_load_dwordx4 v253, s[32:35], s81 offen offset:3072 lds
s_xor_b32 s81, s83, 1280
s_add_u32 m0, s77, 65536
s_waitcnt lgkmcnt(0)
buffer_load_dwordx4 v228, s[32:35], s81 offen offset:0 lds
buffer_load_dwordx4 v252, s[32:35], s81 offen offset:2048 lds
buffer_load_dwordx4 v229, s[32:35], s81 offen offset:1024 lds
buffer_load_dwordx4 v253, s[32:35], s81 offen offset:3072 lds
s_lshl_b32 s85, s76, 2
s_add_u32 s85, s85, s72
s_lshl_b32 s85, s85, 8
s_add_u32 s85, s85, 50331648
v_and_b32 v238, 0xf0, v230
v_and_b32 v239, 16, v231
v_lshl_add_u32 v238, v239, 9, v238
v_lshrrev_b32 v240, 5, v231
v_lshl_add_u32 v238, v240, 16, v238
v_add_u32 v238, s85, v238
s_mov_b64 exec, 0
s_cmp_eq_u32 s72, 0
s_cbranch_scc1 LC2_%=
s_cmp_eq_u32 s72, 1
s_cbranch_scc1 LC3_%=
s_cmp_eq_u32 s72, 2
s_cbranch_scc1 LC4_%=
s_branch LC5_%=
LRETA_%=:
s_mov_b64 exec, -1
buffer_load_dwordx2 v[242:243], v236, s[52:55], s89 offen sc1
s_waitcnt vmcnt(0)
v_cmp_ne_u64 vcc, v[242:243], v[234:235]
s_nop 0
s_cbranch_vccz LRp0_%=
LSp0_%=:
s_sub_u32 s84, s84, 1
s_cbranch_scc1 LZp0_%=
buffer_load_dwordx2 v[242:243], v236, s[52:55], s89 offen sc1
s_waitcnt vmcnt(0)
v_cmp_ne_u64 vcc, v[242:243], v[234:235]
s_nop 0
s_cbranch_vccnz LSp0_%=
LZp0_%=:
s_max_i32 s84, s84, 0
LRp0_%=:
s_barrier
s_xor_b32 s80, s82, 0
s_add_u32 m0, s77, 0
s_nop 0
buffer_load_dwordx4 v214, s[52:55], s80 offen offset:0 sc1 lds
buffer_load_dwordx4 v214, s[52:55], s80 offen offset:2048 sc1 lds
buffer_load_dwordx4 v214, s[52:55], s80 offen offset:1024 sc1 lds
buffer_load_dwordx4 v214, s[52:55], s80 offen offset:3072 sc1 lds
s_xor_b32 s80, s82, 131072
s_add_u32 m0, s77, 16384
s_nop 0
buffer_load_dwordx4 v214, s[52:55], s80 offen offset:0 sc1 lds
buffer_load_dwordx4 v214, s[52:55], s80 offen offset:2048 sc1 lds
buffer_load_dwordx4 v214, s[52:55], s80 offen offset:1024 sc1 lds
buffer_load_dwordx4 v214, s[52:55], s80 offen offset:3072 sc1 lds
ds_read_b128 a[96:99], v245 offset:16384
ds_read_b128 a[100:103], v245 offset:18432
ds_read_b128 a[104:107], v246 offset:16384
ds_read_b128 a[108:111], v246 offset:18432
ds_read_b128 a[112:115], v245 offset:32768
ds_read_b128 a[116:119], v245 offset:34816
ds_read_b128 a[120:123], v246 offset:32768
ds_read_b128 a[124:127], v246 offset:34816
ds_read_b128 a[128:131], v245 offset:49152
ds_read_b128 a[132:135], v245 offset:51200
ds_read_b128 a[136:139], v246 offset:49152
ds_read_b128 a[140:143], v246 offset:51200
ds_read_b128 a[144:147], v156 offset:0
ds_read_b128 a[148:151], v156 offset:2048
ds_read_b128 a[152:155], v157 offset:0
ds_read_b128 a[156:159], v157 offset:2048
ds_read_b128 a[160:163], v245 offset:0
ds_read_b128 a[164:167], v245 offset:2048
ds_read_b128 a[168:171], v246 offset:0
ds_read_b128 a[172:175], v246 offset:2048
s_xor_b32 s81, s83, 1408
s_add_u32 m0, s77, 131072
s_waitcnt lgkmcnt(4)
buffer_load_dwordx4 v228, s[32:35], s81 offen offset:0 lds
buffer_load_dwordx4 v252, s[32:35], s81 offen offset:2048 lds
buffer_load_dwordx4 v229, s[32:35], s81 offen offset:1024 lds
buffer_load_dwordx4 v253, s[32:35], s81 offen offset:3072 lds
s_xor_b32 s81, s83, 2048
s_add_u32 m0, s77, 65536
s_waitcnt lgkmcnt(0)
buffer_load_dwordx4 v228, s[32:35], s81 offen offset:0 lds
buffer_load_dwordx4 v252, s[32:35], s81 offen offset:2048 lds
buffer_load_dwordx4 v229, s[32:35], s81 offen offset:1024 lds
buffer_load_dwordx4 v253, s[32:35], s81 offen offset:3072 lds
s_or_b32 s85, s6, s72
s_mov_b32 s85, 48234496
s_cmov_b32 s85, 0x7ff00000
s_mov_b64 exec, 1
buffer_store_dwordx2 v[234:235], off, s[52:55], s85 sc1
s_mov_b64 exec, -1
s_waitcnt vmcnt(13)
s_barrier
ds_read_b128 v[160:163], v230 offset:0
ds_read_b128 v[164:167], v230 offset:2048
ds_read_b128 v[168:171], v230 offset:4096
ds_read_b128 v[172:175], v230 offset:6144
ds_read_b128 v[176:179], v230 offset:1024
ds_read_b128 v[180:183], v230 offset:3072
ds_read_b128 v[184:187], v230 offset:5120
ds_read_b128 v[188:191], v230 offset:7168
s_waitcnt lgkmcnt(4)
v_mfma_f32_16x16x32_bf16 v[0:3], v[160:163], a[0:3], 0
ds_read_b128 v[192:195], v230 offset:8192
v_and_b32 v243, 15, v231
s_xor_b32 s80, s82, 16384
)"
R"(
s_add_u32 m0, s77, 32768
v_mfma_f32_16x16x32_bf16 v[16:19], v[160:163], a[4:7], 0
buffer_load_dwordx4 v214, s[52:55], s80 offen offset:0 sc1 lds
v_mfma_f32_16x16x32_bf16 v[4:7], v[164:167], a[0:3], 0
ds_read_b128 v[196:199], v230 offset:10240
v_mul_u32_u24 v243, 128, v243
v_mfma_f32_16x16x32_bf16 v[20:23], v[164:167], a[4:7], 0
buffer_load_dwordx4 v214, s[52:55], s80 offen offset:2048 sc1 lds
v_mfma_f32_16x16x32_bf16 v[216:219], v[248:251], a[0:3], 0
ds_read_b128 v[200:203], v230 offset:12288
v_mfma_f32_16x16x32_bf16 v[8:11], v[168:171], a[0:3], 0
buffer_load_dwordx4 v214, s[52:55], s80 offen offset:1024 sc1 lds
v_mfma_f32_16x16x32_bf16 v[24:27], v[168:171], a[4:7], 0
ds_read_b128 v[204:207], v230 offset:14336
v_mfma_f32_16x16x32_bf16 v[12:15], v[172:175], a[0:3], 0
buffer_load_dwordx4 v214, s[52:55], s80 offen offset:3072 sc1 lds
v_mfma_f32_16x16x32_bf16 v[28:31], v[172:175], a[4:7], 0
v_mfma_f32_16x16x32_bf16 v[220:223], v[248:251], a[4:7], 0
s_xor_b32 s80, s82, 32768
s_waitcnt lgkmcnt(4)
v_mfma_f32_16x16x32_bf16 v[32:35], v[176:179], a[0:3], 0
ds_read_b128 v[160:163], v230 offset:9216
s_add_u32 m0, s77, 49152
v_mfma_f32_16x16x32_bf16 v[48:51], v[176:179], a[4:7], 0
buffer_load_dwordx4 v214, s[52:55], s80 offen offset:0 sc1 lds
v_mfma_f32_16x16x32_bf16 v[36:39], v[180:183], a[0:3], 0
ds_read_b128 v[164:167], v230 offset:11264
v_mfma_f32_16x16x32_bf16 v[52:55], v[180:183], a[4:7], 0
v_mfma_f32_16x16x32_bf16 v[40:43], v[184:187], a[0:3], 0
ds_read_b128 v[168:171], v230 offset:13312
v_mfma_f32_16x16x32_bf16 v[56:59], v[184:187], a[4:7], 0
buffer_load_dwordx4 v214, s[52:55], s80 offen offset:2048 sc1 lds
v_mfma_f32_16x16x32_bf16 v[44:47], v[188:191], a[0:3], 0
ds_read_b128 v[172:175], v230 offset:15360
v_mfma_f32_16x16x32_bf16 v[60:63], v[188:191], a[4:7], 0
s_waitcnt vmcnt(15) lgkmcnt(4)
s_barrier
v_mfma_f32_16x16x32_bf16 v[0:3], v[192:195], a[8:11], v[0:3]
ds_read_b128 v[176:179], v230 offset:16384
v_mfma_f32_16x16x32_bf16 v[16:19], v[192:195], a[12:15], v[16:19]
buffer_load_dwordx4 v214, s[52:55], s80 offen offset:1024 sc1 lds
v_mfma_f32_16x16x32_bf16 v[4:7], v[196:199], a[8:11], v[4:7]
ds_read_b128 v[180:183], v230 offset:18432
v_mfma_f32_16x16x32_bf16 v[20:23], v[196:199], a[12:15], v[20:23]
v_mfma_f32_16x16x32_bf16 v[216:219], v[248:251], a[8:11], v[216:219]
ds_read_b128 v[184:187], v230 offset:20480
v_mfma_f32_16x16x32_bf16 v[8:11], v[200:203], a[8:11], v[8:11]
buffer_load_dwordx4 v214, s[52:55], s80 offen offset:3072 sc1 lds
v_mfma_f32_16x16x32_bf16 v[24:27], v[200:203], a[12:15], v[24:27]
ds_read_b128 v[188:191], v230 offset:22528
v_mfma_f32_16x16x32_bf16 v[12:15], v[204:207], a[8:11], v[12:15]
v_mfma_f32_16x16x32_bf16 v[28:31], v[204:207], a[12:15], v[28:31]
v_mfma_f32_16x16x32_bf16 v[220:223], v[248:251], a[12:15], v[220:223]
s_waitcnt lgkmcnt(4)
v_mfma_f32_16x16x32_bf16 v[32:35], v[160:163], a[8:11], v[32:35]
ds_read_b128 v[192:195], v230 offset:17408
v_mfma_f32_16x16x32_bf16 v[48:51], v[160:163], a[12:15], v[48:51]
buffer_store_dwordx4 v[126:129], v230, s[52:55], s18 offen offset:0 sc1
v_mfma_f32_16x16x32_bf16 v[36:39], v[164:167], a[8:11], v[36:39]
ds_read_b128 v[196:199], v230 offset:19456
s_xor_b32 s81, s83, 1536
v_mfma_f32_16x16x32_bf16 v[52:55], v[164:167], a[12:15], v[52:55]
v_mfma_f32_16x16x32_bf16 v[40:43], v[168:171], a[8:11], v[40:43]
ds_read_b128 v[200:203], v230 offset:21504
s_add_u32 m0, s77, 81920
v_mfma_f32_16x16x32_bf16 v[56:59], v[168:171], a[12:15], v[56:59]
buffer_load_dwordx4 v228, s[32:35], s81 offen offset:0 lds
v_mfma_f32_16x16x32_bf16 v[44:47], v[172:175], a[8:11], v[44:47]
ds_read_b128 v[204:207], v230 offset:23552
v_mfma_f32_16x16x32_bf16 v[60:63], v[172:175], a[12:15], v[60:63]
s_waitcnt lgkmcnt(4)
v_mfma_f32_16x16x32_bf16 v[0:3], v[176:179], a[16:19], v[0:3]
ds_read_b128 v[160:163], v230 offset:24576
v_mfma_f32_16x16x32_bf16 v[16:19], v[176:179], a[20:23], v[16:19]
buffer_load_dwordx4 v252, s[32:35], s81 offen offset:2048 lds
v_mfma_f32_16x16x32_bf16 v[4:7], v[180:183], a[16:19], v[4:7]
ds_read_b128 v[164:167], v230 offset:26624
v_mfma_f32_16x16x32_bf16 v[20:23], v[180:183], a[20:23], v[20:23]
v_mfma_f32_16x16x32_bf16 v[216:219], v[248:251], a[16:19], v[216:219]
ds_read_b128 v[168:171], v230 offset:28672
v_mfma_f32_16x16x32_bf16 v[8:11], v[184:187], a[16:19], v[8:11]
buffer_load_dwordx4 v229, s[32:35], s81 offen offset:1024 lds
v_mfma_f32_16x16x32_bf16 v[24:27], v[184:187], a[20:23], v[24:27]
ds_read_b128 v[172:175], v230 offset:30720
v_mfma_f32_16x16x32_bf16 v[12:15], v[188:191], a[16:19], v[12:15]
v_mfma_f32_16x16x32_bf16 v[28:31], v[188:191], a[20:23], v[28:31]
v_mfma_f32_16x16x32_bf16 v[220:223], v[248:251], a[20:23], v[220:223]
s_waitcnt lgkmcnt(4)
v_mfma_f32_16x16x32_bf16 v[32:35], v[192:195], a[16:19], v[32:35]
ds_read_b128 v[176:179], v230 offset:25600
v_mfma_f32_16x16x32_bf16 v[48:51], v[192:195], a[20:23], v[48:51]
buffer_load_dwordx4 v253, s[32:35], s81 offen offset:3072 lds
v_mfma_f32_16x16x32_bf16 v[36:39], v[196:199], a[16:19], v[36:39]
ds_read_b128 v[180:183], v230 offset:27648
s_xor_b32 s81, s83, 1664
v_mfma_f32_16x16x32_bf16 v[52:55], v[196:199], a[20:23], v[52:55]
v_mfma_f32_16x16x32_bf16 v[40:43], v[200:203], a[16:19], v[40:43]
ds_read_b128 v[184:187], v230 offset:29696
s_add_u32 m0, s77, 98304
v_mfma_f32_16x16x32_bf16 v[56:59], v[200:203], a[20:23], v[56:59]
buffer_load_dwordx4 v228, s[32:35], s81 offen offset:0 lds
v_mfma_f32_16x16x32_bf16 v[44:47], v[204:207], a[16:19], v[44:47]
ds_read_b128 v[188:191], v230 offset:31744
v_mfma_f32_16x16x32_bf16 v[60:63], v[204:207], a[20:23], v[60:63]
s_waitcnt vmcnt(10) lgkmcnt(4)
s_barrier
v_mfma_f32_16x16x32_bf16 v[0:3], v[160:163], a[24:27], v[0:3]
ds_read_b128 v[192:195], v230 offset:32768
s_mov_b32 s87, 4096
v_mfma_f32_16x16x32_bf16 v[16:19], v[160:163], a[28:31], v[16:19]
buffer_load_dwordx4 v[64:67], v125, s[64:67], s87 offen
v_mfma_f32_16x16x32_bf16 v[4:7], v[164:167], a[24:27], v[4:7]
ds_read_b128 v[196:199], v230 offset:34816
s_xor_b32 s80, s82, 49152
v_mfma_f32_16x16x32_bf16 v[20:23], v[164:167], a[28:31], v[20:23]
v_lshlrev_b32 v152, 16, v128
v_and_b32 v153, s93, v128
v_mfma_f32_16x16x32_bf16 v[216:219], v[248:251], a[24:27], v[216:219]
ds_read_b128 v[200:203], v230 offset:36864
s_add_u32 m0, s77, 0
v_mfma_f32_16x16x32_bf16 v[8:11], v[168:171], a[24:27], v[8:11]
buffer_load_dwordx4 v214, s[52:55], s80 offen offset:0 sc1 lds
v_mfma_f32_16x16x32_bf16 v[24:27], v[168:171], a[28:31], v[24:27]
ds_read_b128 v[204:207], v230 offset:38912
v_mul_f32 v144, v152, v120
v_mfma_f32_16x16x32_bf16 v[12:15], v[172:175], a[24:27], v[12:15]
v_mul_f32 v145, v153, v120
v_lshlrev_b32 v154, 16, v129
v_mfma_f32_16x16x32_bf16 v[28:31], v[172:175], a[28:31], v[28:31]
v_and_b32 v155, s93, v129
v_mul_f32 v146, v154, v120
v_mfma_f32_16x16x32_bf16 v[220:223], v[248:251], a[28:31], v[220:223]
v_mul_f32 v147, v155, v120
s_waitcnt lgkmcnt(4)
v_mfma_f32_16x16x32_bf16 v[32:35], v[176:179], a[24:27], v[32:35]
ds_read_b128 v[160:163], v230 offset:33792
v_lshlrev_b32 v152, 16, v130
v_mfma_f32_16x16x32_bf16 v[48:51], v[176:179], a[28:31], v[48:51]
buffer_load_dwordx4 v214, s[52:55], s80 offen offset:2048 sc1 lds
v_mfma_f32_16x16x32_bf16 v[36:39], v[180:183], a[24:27], v[36:39]
ds_read_b128 v[164:167], v230 offset:35840
v_and_b32 v153, s93, v130
v_mfma_f32_16x16x32_bf16 v[52:55], v[180:183], a[28:31], v[52:55]
v_mul_f32 v148, v152, v120
v_mul_f32 v149, v153, v120
v_mfma_f32_16x16x32_bf16 v[40:43], v[184:187], a[24:27], v[40:43]
ds_read_b128 v[168:171], v230 offset:37888
v_lshlrev_b32 v154, 16, v131
v_mfma_f32_16x16x32_bf16 v[56:59], v[184:187], a[28:31], v[56:59]
buffer_load_dwordx4 v214, s[52:55], s80 offen offset:1024 sc1 lds
v_mfma_f32_16x16x32_bf16 v[44:47], v[188:191], a[24:27], v[44:47]
ds_read_b128 v[172:175], v230 offset:39936
v_and_b32 v155, s93, v131
v_mfma_f32_16x16x32_bf16 v[60:63], v[188:191], a[28:31], v[60:63]
v_mul_f32 v150, v154, v120
s_waitcnt lgkmcnt(4)
v_mfma_f32_16x16x32_bf16 v[0:3], v[192:195], a[32:35], v[0:3]
ds_read_b128 v[176:179], v230 offset:40960
v_mul_f32 v151, v155, v120
v_mfma_f32_16x16x32_bf16 v[16:19], v[192:195], a[36:39], v[16:19]
buffer_load_dwordx4 v214, s[52:55], s80 offen offset:3072 sc1 lds
v_mfma_f32_16x16x32_bf16 v[4:7], v[196:199], a[32:35], v[4:7]
ds_read_b128 v[180:183], v230 offset:43008
v_lshlrev_b32 v152, 16, v132
v_mfma_f32_16x16x32_bf16 v[20:23], v[196:199], a[36:39], v[20:23]
v_and_b32 v153, s93, v132
v_fmac_f32 v144, v152, v121
v_mfma_f32_16x16x32_bf16 v[216:219], v[248:251], a[32:35], v[216:219]
ds_read_b128 v[184:187], v230 offset:45056
s_add_u32 m0, s77, 98304
v_mfma_f32_16x16x32_bf16 v[8:11], v[200:203], a[32:35], v[8:11]
buffer_load_dwordx4 v252, s[32:35], s81 offen offset:2048 lds
v_mfma_f32_16x16x32_bf16 v[24:27], v[200:203], a[36:39], v[24:27]
ds_read_b128 v[188:191], v230 offset:47104
v_fmac_f32 v145, v153, v121
v_mfma_f32_16x16x32_bf16 v[12:15], v[204:207], a[32:35], v[12:15]
v_lshlrev_b32 v154, 16, v133
v_and_b32 v155, s93, v133
v_mfma_f32_16x16x32_bf16 v[28:31], v[204:207], a[36:39], v[28:31]
v_fmac_f32 v146, v154, v121
v_fmac_f32 v147, v155, v121
v_mfma_f32_16x16x32_bf16 v[220:223], v[248:251], a[36:39], v[220:223]
v_lshlrev_b32 v152, 16, v134
s_waitcnt lgkmcnt(4)
v_mfma_f32_16x16x32_bf16 v[32:35], v[160:163], a[32:35], v[32:35]
ds_read_b128 v[192:195], v230 offset:41984
v_and_b32 v153, s93, v134
v_mfma_f32_16x16x32_bf16 v[48:51], v[160:163], a[36:39], v[48:51]
buffer_load_dwordx4 v229, s[32:35], s81 offen offset:1024 lds
v_mfma_f32_16x16x32_bf16 v[36:39], v[164:167], a[32:35], v[36:39]
ds_read_b128 v[196:199], v230 offset:44032
v_fmac_f32 v148, v152, v121
v_mfma_f32_16x16x32_bf16 v[52:55], v[164:167], a[36:39], v[52:55]
v_fmac_f32 v149, v153, v121
v_lshlrev_b32 v154, 16, v135
v_mfma_f32_16x16x32_bf16 v[40:43], v[168:171], a[32:35], v[40:43]
ds_read_b128 v[200:203], v230 offset:46080
s_xor_b32 s80, s82, 65536
v_mfma_f32_16x16x32_bf16 v[56:59], v[168:171], a[36:39], v[56:59]
buffer_load_dwordx4 v253, s[32:35], s81 offen offset:3072 lds
v_mfma_f32_16x16x32_bf16 v[44:47], v[172:175], a[32:35], v[44:47]
ds_read_b128 v[204:207], v230 offset:48128
s_add_u32 m0, s77, 16384
v_mfma_f32_16x16x32_bf16 v[60:63], v[172:175], a[36:39], v[60:63]
s_waitcnt vmcnt(14) lgkmcnt(4)
s_barrier
v_mfma_f32_16x16x32_bf16 v[0:3], v[176:179], a[40:43], v[0:3]
ds_read_b128 v[160:163], v230 offset:49152
ds_read_b128 a[176:179], v156 offset:0
v_mfma_f32_16x16x32_bf16 v[16:19], v[176:179], a[44:47], v[16:19]
buffer_load_dwordx4 v214, s[52:55], s80 offen offset:0 sc1 lds
v_mfma_f32_16x16x32_bf16 v[4:7], v[180:183], a[40:43], v[4:7]
ds_read_b128 v[164:167], v230 offset:51200
ds_read_b128 a[180:183], v156 offset:2048
v_mfma_f32_16x16x32_bf16 v[20:23], v[180:183], a[44:47], v[20:23]
v_and_b32 v155, s93, v135
v_fmac_f32 v150, v154, v121
v_mfma_f32_16x16x32_bf16 v[216:219], v[248:251], a[40:43], v[216:219]
ds_read_b128 v[168:171], v230 offset:53248
ds_read_b128 a[184:187], v157 offset:0
v_mfma_f32_16x16x32_bf16 v[8:11], v[184:187], a[40:43], v[8:11]
buffer_load_dwordx4 v214, s[52:55], s80 offen offset:2048 sc1 lds
v_mfma_f32_16x16x32_bf16 v[24:27], v[184:187], a[44:47], v[24:27]
ds_read_b128 v[172:175], v230 offset:55296
ds_read_b128 a[188:191], v157 offset:2048
v_mfma_f32_16x16x32_bf16 v[12:15], v[188:191], a[40:43], v[12:15]
v_fmac_f32 v151, v155, v121
v_lshlrev_b32 v152, 16, v136
v_mfma_f32_16x16x32_bf16 v[28:31], v[188:191], a[44:47], v[28:31]
v_and_b32 v153, s93, v136
v_fmac_f32 v144, v152, v122
v_mfma_f32_16x16x32_bf16 v[220:223], v[248:251], a[44:47], v[220:223]
v_fmac_f32 v145, v153, v122
s_waitcnt lgkmcnt(8)
v_mfma_f32_16x16x32_bf16 v[32:35], v[192:195], a[40:43], v[32:35]
ds_read_b128 v[176:179], v230 offset:50176
v_lshlrev_b32 v154, 16, v137
v_mfma_f32_16x16x32_bf16 v[48:51], v[192:195], a[44:47], v[48:51]
buffer_load_dwordx4 v214, s[52:55], s80 offen offset:1024 sc1 lds
v_mfma_f32_16x16x32_bf16 v[36:39], v[196:199], a[40:43], v[36:39]
ds_read_b128 v[180:183], v230 offset:52224
v_and_b32 v155, s93, v137
v_mfma_f32_16x16x32_bf16 v[52:55], v[196:199], a[44:47], v[52:55]
v_fmac_f32 v146, v154, v122
v_fmac_f32 v147, v155, v122
v_mfma_f32_16x16x32_bf16 v[40:43], v[200:203], a[40:43], v[40:43]
ds_read_b128 v[184:187], v230 offset:54272
v_lshlrev_b32 v152, 16, v138
v_mfma_f32_16x16x32_bf16 v[56:59], v[200:203], a[44:47], v[56:59]
buffer_load_dwordx4 v214, s[52:55], s80 offen offset:3072 sc1 lds
v_mfma_f32_16x16x32_bf16 v[44:47], v[204:207], a[40:43], v[44:47]
ds_read_b128 v[188:191], v230 offset:56320
v_and_b32 v153, s93, v138
v_mfma_f32_16x16x32_bf16 v[60:63], v[204:207], a[44:47], v[60:63]
s_xor_b32 s81, s83, 1792
s_waitcnt lgkmcnt(5)
v_mfma_f32_16x16x32_bf16 v[0:3], v[160:163], a[48:51], v[0:3]
ds_read_b128 v[192:195], v230 offset:57344
s_add_u32 m0, s77, 114688
v_mfma_f32_16x16x32_bf16 v[16:19], v[160:163], a[52:55], v[16:19]
buffer_load_dwordx4 v228, s[32:35], s81 offen offset:0 lds
v_mfma_f32_16x16x32_bf16 v[4:7], v[164:167], a[48:51], v[4:7]
ds_read_b128 v[196:199], v230 offset:59392
v_fmac_f32 v148, v152, v122
v_mfma_f32_16x16x32_bf16 v[20:23], v[164:167], a[52:55], v[20:23]
v_fmac_f32 v149, v153, v122
v_lshlrev_b32 v154, 16, v139
v_mfma_f32_16x16x32_bf16 v[216:219], v[248:251], a[48:51], v[216:219]
ds_read_b128 v[200:203], v230 offset:61440
v_and_b32 v155, s93, v139
v_mfma_f32_16x16x32_bf16 v[8:11], v[168:171], a[48:51], v[8:11]
buffer_load_dwordx4 v252, s[32:35], s81 offen offset:2048 lds
v_mfma_f32_16x16x32_bf16 v[24:27], v[168:171], a[52:55], v[24:27]
ds_read_b128 v[204:207], v230 offset:63488
v_fmac_f32 v150, v154, v122
v_mfma_f32_16x16x32_bf16 v[12:15], v[172:175], a[48:51], v[12:15]
v_fmac_f32 v151, v155, v122
v_lshlrev_b32 v152, 16, v140
v_mfma_f32_16x16x32_bf16 v[28:31], v[172:175], a[52:55], v[28:31]
v_and_b32 v153, s93, v140
v_fmac_f32 v144, v152, v123
v_mfma_f32_16x16x32_bf16 v[220:223], v[248:251], a[52:55], v[220:223]
v_fmac_f32 v145, v153, v123
s_waitcnt lgkmcnt(4)
v_mfma_f32_16x16x32_bf16 v[32:35], v[176:179], a[48:51], v[32:35]
ds_read_b128 v[160:163], v230 offset:58368
v_lshlrev_b32 v154, 16, v141
v_mfma_f32_16x16x32_bf16 v[48:51], v[176:179], a[52:55], v[48:51]
buffer_store_dwordx4 v[126:129], v230, s[52:55], s18 offen offset:1024 sc1
v_mfma_f32_16x16x32_bf16 v[36:39], v[180:183], a[48:51], v[36:39]
ds_read_b128 v[164:167], v230 offset:60416
s_xor_b32 s80, s82, 81920
v_mfma_f32_16x16x32_bf16 v[52:55], v[180:183], a[52:55], v[52:55]
v_and_b32 v155, s93, v141
v_fmac_f32 v146, v154, v123
v_mfma_f32_16x16x32_bf16 v[40:43], v[184:187], a[48:51], v[40:43]
ds_read_b128 v[168:171], v230 offset:62464
v_fmac_f32 v147, v155, v123
v_mfma_f32_16x16x32_bf16 v[56:59], v[184:187], a[52:55], v[56:59]
buffer_load_dwordx4 v229, s[32:35], s81 offen offset:1024 lds
v_mfma_f32_16x16x32_bf16 v[44:47], v[188:191], a[48:51], v[44:47]
ds_read_b128 v[172:175], v230 offset:64512
s_mov_b32 s87, 16384
v_mfma_f32_16x16x32_bf16 v[60:63], v[188:191], a[52:55], v[60:63]
s_waitcnt vmcnt(11) lgkmcnt(4)
s_barrier
v_mfma_f32_16x16x32_bf16 v[0:3], v[192:195], a[56:59], v[0:3]
ds_read_b128 v[176:179], v230 offset:0
ds_read_b128 a[192:195], v245 offset:0
v_mfma_f32_16x16x32_bf16 v[16:19], v[192:195], a[60:63], v[16:19]
buffer_load_dwordx4 v[68:71], v125, s[64:67], s87 offen
v_mfma_f32_16x16x32_bf16 v[4:7], v[196:199], a[56:59], v[4:7]
ds_read_b128 v[180:183], v230 offset:2048
ds_read_b128 a[196:199], v245 offset:2048
v_mfma_f32_16x16x32_bf16 v[20:23], v[196:199], a[60:63], v[20:23]
s_add_u32 m0, s77, 32768
v_lshlrev_b32 v152, 16, v142
v_mfma_f32_16x16x32_bf16 v[216:219], v[248:251], a[56:59], v[216:219]
ds_read_b128 v[184:187], v230 offset:4096
ds_read_b128 a[200:203], v246 offset:0
v_mfma_f32_16x16x32_bf16 v[8:11], v[200:203], a[56:59], v[8:11]
buffer_load_dwordx4 v214, s[52:55], s80 offen offset:0 sc1 lds
v_mfma_f32_16x16x32_bf16 v[24:27], v[200:203], a[60:63], v[24:27]
ds_read_b128 v[188:191], v230 offset:6144
ds_read_b128 a[204:207], v246 offset:2048
v_mfma_f32_16x16x32_bf16 v[12:15], v[204:207], a[56:59], v[12:15]
v_and_b32 v153, s93, v142
v_fmac_f32 v148, v152, v123
v_mfma_f32_16x16x32_bf16 v[28:31], v[204:207], a[60:63], v[28:31]
v_fmac_f32 v149, v153, v123
v_lshlrev_b32 v154, 16, v143
v_mfma_f32_16x16x32_bf16 v[220:223], v[248:251], a[60:63], v[220:223]
v_and_b32 v155, s93, v143
s_waitcnt lgkmcnt(8)
v_mfma_f32_16x16x32_bf16 v[32:35], v[160:163], a[56:59], v[32:35]
ds_read_b128 v[192:195], v230 offset:1024
v_fmac_f32 v150, v154, v123
v_mfma_f32_16x16x32_bf16 v[48:51], v[160:163], a[60:63], v[48:51]
buffer_load_dwordx4 v214, s[52:55], s80 offen offset:2048 sc1 lds
v_mfma_f32_16x16x32_bf16 v[36:39], v[164:167], a[56:59], v[36:39]
ds_read_b128 v[196:199], v230 offset:3072
v_fmac_f32 v151, v155, v123
v_mfma_f32_16x16x32_bf16 v[52:55], v[164:167], a[60:63], v[52:55]
v_permlane32_swap_b32 v144, v148
v_permlane32_swap_b32 v145, v149
v_mfma_f32_16x16x32_bf16 v[40:43], v[168:171], a[56:59], v[40:43]
ds_read_b128 v[200:203], v230 offset:5120
v_permlane32_swap_b32 v146, v150
v_mfma_f32_16x16x32_bf16 v[56:59], v[168:171], a[60:63], v[56:59]
buffer_load_dwordx4 v214, s[52:55], s80 offen offset:1024 sc1 lds
v_mfma_f32_16x16x32_bf16 v[44:47], v[172:175], a[56:59], v[44:47]
ds_read_b128 v[204:207], v230 offset:7168
v_permlane32_swap_b32 v147, v151
v_mfma_f32_16x16x32_bf16 v[60:63], v[172:175], a[60:63], v[60:63]
v_add_f32 v144, v144, v148
s_waitcnt lgkmcnt(5)
v_mfma_f32_16x16x32_bf16 v[0:3], v[176:179], a[64:67], v[0:3]
ds_read_b128 v[160:163], v230 offset:8192
v_add_f32 v145, v145, v149
v_mfma_f32_16x16x32_bf16 v[16:19], v[176:179], a[68:71], v[16:19]
buffer_load_dwordx4 v214, s[52:55], s80 offen offset:3072 sc1 lds
v_mfma_f32_16x16x32_bf16 v[4:7], v[180:183], a[64:67], v[4:7]
ds_read_b128 v[164:167], v230 offset:10240
v_add_f32 v146, v146, v150
v_mfma_f32_16x16x32_bf16 v[20:23], v[180:183], a[68:71], v[20:23]
v_add_f32 v147, v147, v151
v_and_b32 v152, 7, v231
v_mfma_f32_16x16x32_bf16 v[216:219], v[248:251], a[64:67], v[216:219]
ds_read_b128 v[168:171], v230 offset:12288
s_add_u32 m0, s77, 114688
v_mfma_f32_16x16x32_bf16 v[8:11], v[184:187], a[64:67], v[8:11]
buffer_load_dwordx4 v253, s[32:35], s81 offen offset:3072 lds
v_mfma_f32_16x16x32_bf16 v[24:27], v[184:187], a[68:71], v[24:27]
ds_read_b128 v[172:175], v230 offset:14336
v_permlane16_swap_b32 v144, v146
v_mfma_f32_16x16x32_bf16 v[12:15], v[188:191], a[64:67], v[12:15]
v_permlane16_swap_b32 v145, v147
v_add_f32 v144, v144, v146
v_mfma_f32_16x16x32_bf16 v[28:31], v[188:191], a[68:71], v[28:31]
v_add_f32 v145, v145, v147
v_lshrrev_b32 v153, 4, v231
v_mfma_f32_16x16x32_bf16 v[220:223], v[248:251], a[68:71], v[220:223]
s_xor_b32 s81, s83, 1920
s_waitcnt lgkmcnt(4)
v_mfma_f32_16x16x32_bf16 v[32:35], v[192:195], a[64:67], v[32:35]
ds_read_b128 v[176:179], v230 offset:9216
s_add_u32 m0, s77, 131072
v_mfma_f32_16x16x32_bf16 v[48:51], v[192:195], a[68:71], v[48:51]
buffer_load_dwordx4 v228, s[32:35], s81 offen offset:0 lds
v_mfma_f32_16x16x32_bf16 v[36:39], v[196:199], a[64:67], v[36:39]
ds_read_b128 v[180:183], v230 offset:11264
v_lshl_add_u32 v152, v152, 2, v153
v_mfma_f32_16x16x32_bf16 v[52:55], v[196:199], a[68:71], v[52:55]
s_lshl_b32 s85, s72, 8
s_add_u32 s85, s85, 147456
v_mfma_f32_16x16x32_bf16 v[40:43], v[200:203], a[64:67], v[40:43]
ds_read_b128 v[184:187], v230 offset:13312
s_xor_b32 s80, s82, 98304
v_mfma_f32_16x16x32_bf16 v[56:59], v[200:203], a[68:71], v[56:59]
buffer_load_dwordx4 v252, s[32:35], s81 offen offset:2048 lds
)"
R"(
v_mfma_f32_16x16x32_bf16 v[44:47], v[204:207], a[64:67], v[44:47]
ds_read_b128 v[188:191], v230 offset:15360
s_add_u32 m0, s77, 49152
v_mfma_f32_16x16x32_bf16 v[60:63], v[204:207], a[68:71], v[60:63]
s_waitcnt vmcnt(12) lgkmcnt(4)
s_barrier
v_mfma_f32_16x16x32_bf16 v[0:3], v[160:163], a[72:75], v[0:3]
ds_read_b128 v[192:195], v230 offset:16384
ds_read_b128 a[208:211], v245 offset:16384
v_mfma_f32_16x16x32_bf16 v[16:19], v[160:163], a[76:79], v[16:19]
buffer_load_dwordx4 v214, s[52:55], s80 offen offset:0 sc1 lds
v_mfma_f32_16x16x32_bf16 v[4:7], v[164:167], a[72:75], v[4:7]
ds_read_b128 v[196:199], v230 offset:18432
ds_read_b128 a[212:215], v245 offset:18432
v_mfma_f32_16x16x32_bf16 v[20:23], v[164:167], a[76:79], v[20:23]
v_lshl_add_u32 v152, v152, 3, s85
v_add_f32_dpp v144, v144, v144 row_ror:8 row_mask:0xf bank_mask:0xf
v_mfma_f32_16x16x32_bf16 v[216:219], v[248:251], a[72:75], v[216:219]
ds_read_b128 v[200:203], v230 offset:20480
ds_read_b128 a[216:219], v246 offset:16384
v_mfma_f32_16x16x32_bf16 v[8:11], v[168:171], a[72:75], v[8:11]
buffer_load_dwordx4 v214, s[52:55], s80 offen offset:2048 sc1 lds
v_mfma_f32_16x16x32_bf16 v[24:27], v[168:171], a[76:79], v[24:27]
ds_read_b128 v[204:207], v230 offset:22528
ds_read_b128 a[220:223], v246 offset:18432
v_mfma_f32_16x16x32_bf16 v[12:15], v[172:175], a[72:75], v[12:15]
v_add_f32_dpp v145, v145, v145 row_ror:8 row_mask:0xf bank_mask:0xf
v_lshlrev_b32 v108, 16, v64
v_mfma_f32_16x16x32_bf16 v[28:31], v[172:175], a[76:79], v[28:31]
ds_write_b64 v152, v[144:145]
v_and_b32 v109, s93, v64
v_mfma_f32_16x16x32_bf16 v[220:223], v[248:251], a[76:79], v[220:223]
v_mul_f32 v100, v108, v112
s_waitcnt lgkmcnt(9)
v_mfma_f32_16x16x32_bf16 v[32:35], v[176:179], a[72:75], v[32:35]
ds_read_b128 v[160:163], v230 offset:17408
v_mul_f32 v101, v109, v112
v_mfma_f32_16x16x32_bf16 v[48:51], v[176:179], a[76:79], v[48:51]
buffer_load_dwordx4 v214, s[52:55], s80 offen offset:1024 sc1 lds
v_mfma_f32_16x16x32_bf16 v[36:39], v[180:183], a[72:75], v[36:39]
ds_read_b128 v[164:167], v230 offset:19456
v_lshlrev_b32 v110, 16, v65
v_mfma_f32_16x16x32_bf16 v[52:55], v[180:183], a[76:79], v[52:55]
v_and_b32 v111, s93, v65
v_mul_f32 v102, v110, v112
v_mfma_f32_16x16x32_bf16 v[40:43], v[184:187], a[72:75], v[40:43]
ds_read_b128 v[168:171], v230 offset:21504
v_mul_f32 v103, v111, v112
v_mfma_f32_16x16x32_bf16 v[56:59], v[184:187], a[76:79], v[56:59]
buffer_load_dwordx4 v214, s[52:55], s80 offen offset:3072 sc1 lds
v_mfma_f32_16x16x32_bf16 v[44:47], v[188:191], a[72:75], v[44:47]
ds_read_b128 v[172:175], v230 offset:23552
v_lshlrev_b32 v108, 16, v66
v_mfma_f32_16x16x32_bf16 v[60:63], v[188:191], a[76:79], v[60:63]
v_and_b32 v109, s93, v66
s_waitcnt lgkmcnt(6)
v_mfma_f32_16x16x32_bf16 v[0:3], v[192:195], a[80:83], v[0:3]
ds_read_b128 v[176:179], v230 offset:24576
s_add_u32 m0, s77, 131072
v_mfma_f32_16x16x32_bf16 v[16:19], v[192:195], a[84:87], v[16:19]
buffer_load_dwordx4 v229, s[32:35], s81 offen offset:1024 lds
v_mfma_f32_16x16x32_bf16 v[4:7], v[196:199], a[80:83], v[4:7]
ds_read_b128 v[180:183], v230 offset:26624
v_mul_f32 v104, v108, v112
v_mfma_f32_16x16x32_bf16 v[20:23], v[196:199], a[84:87], v[20:23]
v_mul_f32 v105, v109, v112
v_lshlrev_b32 v110, 16, v67
v_mfma_f32_16x16x32_bf16 v[216:219], v[248:251], a[80:83], v[216:219]
ds_read_b128 v[184:187], v230 offset:28672
v_and_b32 v111, s93, v67
v_mfma_f32_16x16x32_bf16 v[8:11], v[200:203], a[80:83], v[8:11]
buffer_load_dwordx4 v253, s[32:35], s81 offen offset:3072 lds
v_mfma_f32_16x16x32_bf16 v[24:27], v[200:203], a[84:87], v[24:27]
ds_read_b128 v[188:191], v230 offset:30720
v_mul_f32 v106, v110, v112
v_mfma_f32_16x16x32_bf16 v[12:15], v[204:207], a[80:83], v[12:15]
v_mul_f32 v107, v111, v112
v_mfma_f32_16x16x32_bf16 v[28:31], v[204:207], a[84:87], v[28:31]
s_waitcnt lgkmcnt(8)
v_mfma_f32_16x16x32_bf16 v[220:223], v[248:251], a[84:87], v[220:223]
s_xor_b32 s81, s83, 2176
s_waitcnt lgkmcnt(4)
v_mfma_f32_16x16x32_bf16 v[32:35], v[160:163], a[80:83], v[32:35]
ds_read_b128 v[192:195], v230 offset:25600
s_add_u32 m0, s77, 65536
v_mfma_f32_16x16x32_bf16 v[48:51], v[160:163], a[84:87], v[48:51]
buffer_load_dwordx4 v228, s[32:35], s81 offen offset:0 lds
LC2_%=:
s_cbranch_execz LC6_%=
v_mfma_f32_16x16x32_bf16 v[36:39], v[164:167], a[80:83], v[36:39]
ds_read_b128 v[196:199], v230 offset:27648
s_xor_b32 s80, s82, 114688
v_mfma_f32_16x16x32_bf16 v[52:55], v[164:167], a[84:87], v[52:55]
v_mfma_f32_16x16x32_bf16 v[40:43], v[168:171], a[80:83], v[40:43]
ds_read_b128 v[200:203], v230 offset:29696
v_mfma_f32_16x16x32_bf16 v[56:59], v[168:171], a[84:87], v[56:59]
buffer_load_dwordx4 v252, s[32:35], s81 offen offset:2048 lds
v_mfma_f32_16x16x32_bf16 v[44:47], v[172:175], a[80:83], v[44:47]
ds_read_b128 v[204:207], v230 offset:31744
s_mov_b32 s87, 28672
v_mfma_f32_16x16x32_bf16 v[60:63], v[172:175], a[84:87], v[60:63]
s_waitcnt vmcnt(11) lgkmcnt(4)
s_barrier
v_mfma_f32_16x16x32_bf16 v[0:3], v[176:179], a[88:91], v[0:3]
ds_read_b128 v[160:163], v230 offset:32768
ds_read_b128 a[224:227], v245 offset:32768
v_mfma_f32_16x16x32_bf16 v[16:19], v[176:179], a[92:95], v[16:19]
buffer_load_dwordx4 v[72:75], v125, s[64:67], s87 offen
v_mfma_f32_16x16x32_bf16 v[4:7], v[180:183], a[88:91], v[4:7]
ds_read_b128 v[164:167], v230 offset:34816
ds_read_b128 a[228:231], v245 offset:34816
v_mfma_f32_16x16x32_bf16 v[20:23], v[180:183], a[92:95], v[20:23]
s_add_u32 m0, s77, 0
v_lshlrev_b32 v152, 3, v231
v_mfma_f32_16x16x32_bf16 v[216:219], v[248:251], a[88:91], v[216:219]
ds_read_b128 v[168:171], v230 offset:36864
ds_read_b128 a[232:235], v246 offset:32768
v_mfma_f32_16x16x32_bf16 v[8:11], v[184:187], a[88:91], v[8:11]
buffer_load_dwordx4 v214, s[52:55], s80 offen offset:0 sc1 lds
v_mfma_f32_16x16x32_bf16 v[24:27], v[184:187], a[92:95], v[24:27]
ds_read_b128 v[172:175], v230 offset:38912
ds_read_b128 a[236:239], v246 offset:34816
v_mfma_f32_16x16x32_bf16 v[12:15], v[188:191], a[88:91], v[12:15]
v_and_b32 v152, 255, v152
v_add_u32 v152, 147456, v152
v_mfma_f32_16x16x32_bf16 v[28:31], v[188:191], a[92:95], v[28:31]
ds_read_b64 v[144:145], v152 offset:0
ds_read_b64 v[146:147], v152 offset:256
v_mfma_f32_16x16x32_bf16 v[220:223], v[248:251], a[92:95], v[220:223]
ds_read_b64 v[148:149], v152 offset:512
s_waitcnt lgkmcnt(11)
v_mfma_f32_16x16x32_bf16 v[32:35], v[192:195], a[88:91], v[32:35]
ds_read_b128 v[176:179], v230 offset:33792
ds_read_b64 v[150:151], v152 offset:768
v_mfma_f32_16x16x32_bf16 v[48:51], v[192:195], a[92:95], v[48:51]
buffer_load_dwordx4 v214, s[52:55], s80 offen offset:2048 sc1 lds
v_mfma_f32_16x16x32_bf16 v[36:39], v[196:199], a[88:91], v[36:39]
ds_read_b128 v[180:183], v230 offset:35840
s_lshl_b32 s85, s72, 3
v_mfma_f32_16x16x32_bf16 v[52:55], v[196:199], a[92:95], v[52:55]
s_lshl_b32 s88, s6, 9
s_add_u32 s88, s88, 50331648
v_mfma_f32_16x16x32_bf16 v[40:43], v[200:203], a[88:91], v[40:43]
ds_read_b128 v[184:187], v230 offset:37888
v_lshlrev_b32 v108, 16, v68
v_mfma_f32_16x16x32_bf16 v[56:59], v[200:203], a[92:95], v[56:59]
buffer_load_dwordx4 v214, s[52:55], s80 offen offset:1024 sc1 lds
v_mfma_f32_16x16x32_bf16 v[44:47], v[204:207], a[88:91], v[44:47]
ds_read_b128 v[188:191], v230 offset:39936
v_and_b32 v109, s93, v68
v_mfma_f32_16x16x32_bf16 v[60:63], v[204:207], a[92:95], v[60:63]
v_fmac_f32 v100, v108, v113
s_waitcnt lgkmcnt(9)
v_mfma_f32_16x16x32_bf16 v[0:3], v[160:163], a[96:99], v[0:3]
ds_read_b128 v[192:195], v230 offset:40960
v_fmac_f32 v101, v109, v113
v_mfma_f32_16x16x32_bf16 v[16:19], v[160:163], a[100:103], v[16:19]
buffer_load_dwordx4 v214, s[52:55], s80 offen offset:3072 sc1 lds
v_mfma_f32_16x16x32_bf16 v[4:7], v[164:167], a[96:99], v[4:7]
ds_read_b128 v[196:199], v230 offset:43008
v_lshlrev_b32 v110, 16, v69
v_mfma_f32_16x16x32_bf16 v[20:23], v[164:167], a[100:103], v[20:23]
v_and_b32 v111, s93, v69
v_fmac_f32 v102, v110, v113
v_mfma_f32_16x16x32_bf16 v[216:219], v[248:251], a[96:99], v[216:219]
ds_read_b128 v[200:203], v230 offset:45056
v_fmac_f32 v103, v111, v113
v_mfma_f32_16x16x32_bf16 v[8:11], v[168:171], a[96:99], v[8:11]
buffer_store_dwordx4 v[126:129], v230, s[52:55], s18 offen offset:2048 sc1
v_mfma_f32_16x16x32_bf16 v[24:27], v[168:171], a[100:103], v[24:27]
ds_read_b128 v[204:207], v230 offset:47104
v_lshlrev_b32 v108, 16, v70
v_mfma_f32_16x16x32_bf16 v[12:15], v[172:175], a[96:99], v[12:15]
v_and_b32 v109, s93, v70
v_fmac_f32 v104, v108, v113
v_mfma_f32_16x16x32_bf16 v[28:31], v[172:175], a[100:103], v[28:31]
s_waitcnt lgkmcnt(10)
v_add_f32 v144, v144, v146
v_add_f32 v145, v145, v147
v_mfma_f32_16x16x32_bf16 v[220:223], v[248:251], a[100:103], v[220:223]
v_mov_b32 v147, s78
s_waitcnt lgkmcnt(4)
v_mfma_f32_16x16x32_bf16 v[32:35], v[176:179], a[96:99], v[32:35]
ds_read_b128 v[160:163], v230 offset:41984
s_add_u32 m0, s77, 65536
v_mfma_f32_16x16x32_bf16 v[48:51], v[176:179], a[100:103], v[48:51]
buffer_load_dwordx4 v229, s[32:35], s81 offen offset:1024 lds
v_mfma_f32_16x16x32_bf16 v[36:39], v[180:183], a[96:99], v[36:39]
ds_read_b128 v[164:167], v230 offset:44032
v_add_f32 v148, v148, v150
v_mfma_f32_16x16x32_bf16 v[52:55], v[180:183], a[100:103], v[52:55]
v_add_f32 v149, v149, v151
v_add_f32 v144, v144, v148
v_mfma_f32_16x16x32_bf16 v[40:43], v[184:187], a[96:99], v[40:43]
ds_read_b128 v[168:171], v230 offset:46080
s_xor_b32 s80, s82, 147456
v_mfma_f32_16x16x32_bf16 v[56:59], v[184:187], a[100:103], v[56:59]
buffer_load_dwordx4 v253, s[32:35], s81 offen offset:3072 lds
v_mfma_f32_16x16x32_bf16 v[44:47], v[188:191], a[96:99], v[44:47]
ds_read_b128 v[172:175], v230 offset:48128
s_add_u32 m0, s77, 16384
v_mfma_f32_16x16x32_bf16 v[60:63], v[188:191], a[100:103], v[60:63]
s_waitcnt vmcnt(12) lgkmcnt(4)
s_barrier
v_mfma_f32_16x16x32_bf16 v[0:3], v[192:195], a[104:107], v[0:3]
ds_read_b128 v[176:179], v230 offset:49152
ds_read_b128 a[240:243], v245 offset:49152
v_mfma_f32_16x16x32_bf16 v[16:19], v[192:195], a[108:111], v[16:19]
buffer_load_dwordx4 v214, s[52:55], s80 offen offset:0 sc1 lds
v_mfma_f32_16x16x32_bf16 v[4:7], v[196:199], a[104:107], v[4:7]
ds_read_b128 v[180:183], v230 offset:51200
ds_read_b128 a[244:247], v245 offset:51200
v_mfma_f32_16x16x32_bf16 v[20:23], v[196:199], a[108:111], v[20:23]
v_add_f32 v146, v145, v149
v_mov_b32 v145, s78
v_mfma_f32_16x16x32_bf16 v[216:219], v[248:251], a[104:107], v[216:219]
ds_read_b128 v[184:187], v230 offset:53248
ds_read_b128 a[248:251], v246 offset:49152
v_mfma_f32_16x16x32_bf16 v[8:11], v[200:203], a[104:107], v[8:11]
s_lshl_b64 exec, 0xff, s85
buffer_store_dwordx4 v[144:147], v230, s[52:55], s88 offen sc1
s_mov_b64 exec, -1
v_mfma_f32_16x16x32_bf16 v[24:27], v[200:203], a[108:111], v[24:27]
ds_read_b128 v[188:191], v230 offset:55296
ds_read_b128 a[252:255], v246 offset:51200
v_mfma_f32_16x16x32_bf16 v[12:15], v[204:207], a[104:107], v[12:15]
v_fmac_f32 v105, v109, v113
v_lshlrev_b32 v110, 16, v71
v_mfma_f32_16x16x32_bf16 v[28:31], v[204:207], a[108:111], v[28:31]
v_and_b32 v111, s93, v71
v_fmac_f32 v106, v110, v113
v_mfma_f32_16x16x32_bf16 v[220:223], v[248:251], a[108:111], v[220:223]
v_fmac_f32 v107, v111, v113
s_waitcnt lgkmcnt(8)
v_mfma_f32_16x16x32_bf16 v[32:35], v[160:163], a[104:107], v[32:35]
ds_read_b128 v[192:195], v230 offset:50176
v_mfma_f32_16x16x32_bf16 v[48:51], v[160:163], a[108:111], v[48:51]
buffer_load_dwordx4 v214, s[52:55], s80 offen offset:2048 sc1 lds
v_mfma_f32_16x16x32_bf16 v[36:39], v[164:167], a[104:107], v[36:39]
ds_read_b128 v[196:199], v230 offset:52224
v_mfma_f32_16x16x32_bf16 v[52:55], v[164:167], a[108:111], v[52:55]
v_mfma_f32_16x16x32_bf16 v[40:43], v[168:171], a[104:107], v[40:43]
ds_read_b128 v[200:203], v230 offset:54272
v_mfma_f32_16x16x32_bf16 v[56:59], v[168:171], a[108:111], v[56:59]
buffer_load_dwordx4 v214, s[52:55], s80 offen offset:1024 sc1 lds
v_mfma_f32_16x16x32_bf16 v[44:47], v[172:175], a[104:107], v[44:47]
ds_read_b128 v[204:207], v230 offset:56320
v_mfma_f32_16x16x32_bf16 v[60:63], v[172:175], a[108:111], v[60:63]
s_waitcnt lgkmcnt(5)
v_mfma_f32_16x16x32_bf16 v[0:3], v[176:179], a[112:115], v[0:3]
ds_read_b128 v[160:163], v230 offset:57344
v_mfma_f32_16x16x32_bf16 v[16:19], v[176:179], a[116:119], v[16:19]
buffer_load_dwordx4 v214, s[52:55], s80 offen offset:3072 sc1 lds
v_mfma_f32_16x16x32_bf16 v[4:7], v[180:183], a[112:115], v[4:7]
ds_read_b128 v[164:167], v230 offset:59392
s_xor_b32 s81, s83, 3072
v_mfma_f32_16x16x32_bf16 v[20:23], v[180:183], a[116:119], v[20:23]
v_mfma_f32_16x16x32_bf16 v[216:219], v[248:251], a[112:115], v[216:219]
ds_read_b128 v[168:171], v230 offset:61440
s_add_u32 m0, s77, 81920
v_mfma_f32_16x16x32_bf16 v[8:11], v[184:187], a[112:115], v[8:11]
buffer_load_dwordx4 v228, s[32:35], s81 offen offset:0 lds
v_mfma_f32_16x16x32_bf16 v[24:27], v[184:187], a[116:119], v[24:27]
ds_read_b128 v[172:175], v230 offset:63488
v_mfma_f32_16x16x32_bf16 v[12:15], v[188:191], a[112:115], v[12:15]
v_mfma_f32_16x16x32_bf16 v[28:31], v[188:191], a[116:119], v[28:31]
v_mfma_f32_16x16x32_bf16 v[220:223], v[248:251], a[116:119], v[220:223]
s_waitcnt lgkmcnt(4)
v_mfma_f32_16x16x32_bf16 v[32:35], v[192:195], a[112:115], v[32:35]
ds_read_b128 v[176:179], v230 offset:58368
v_mfma_f32_16x16x32_bf16 v[48:51], v[192:195], a[116:119], v[48:51]
buffer_load_dwordx4 v252, s[32:35], s81 offen offset:2048 lds
v_mfma_f32_16x16x32_bf16 v[36:39], v[196:199], a[112:115], v[36:39]
ds_read_b128 v[180:183], v230 offset:60416
v_mfma_f32_16x16x32_bf16 v[52:55], v[196:199], a[116:119], v[52:55]
v_mfma_f32_16x16x32_bf16 v[40:43], v[200:203], a[112:115], v[40:43]
ds_read_b128 v[184:187], v230 offset:62464
v_mfma_f32_16x16x32_bf16 v[56:59], v[200:203], a[116:119], v[56:59]
buffer_load_dwordx4 v229, s[32:35], s81 offen offset:1024 lds
v_mfma_f32_16x16x32_bf16 v[44:47], v[204:207], a[112:115], v[44:47]
ds_read_b128 v[188:191], v230 offset:64512
s_mov_b32 s87, 40960
v_mfma_f32_16x16x32_bf16 v[60:63], v[204:207], a[116:119], v[60:63]
s_waitcnt vmcnt(11) lgkmcnt(4)
s_barrier
v_mfma_f32_16x16x32_bf16 v[0:3], v[160:163], a[120:123], v[0:3]
ds_read_b128 v[192:195], v230 offset:0
ds_read_b128 a[0:3], v156 offset:0
v_mfma_f32_16x16x32_bf16 v[16:19], v[160:163], a[124:127], v[16:19]
buffer_load_dwordx4 v253, s[32:35], s81 offen offset:3072 lds
v_mfma_f32_16x16x32_bf16 v[4:7], v[164:167], a[120:123], v[4:7]
ds_read_b128 v[196:199], v230 offset:2048
ds_read_b128 a[4:7], v156 offset:2048
v_mfma_f32_16x16x32_bf16 v[20:23], v[164:167], a[124:127], v[20:23]
v_mfma_f32_16x16x32_bf16 v[216:219], v[248:251], a[120:123], v[216:219]
ds_read_b128 v[200:203], v230 offset:4096
ds_read_b128 a[8:11], v157 offset:0
v_mfma_f32_16x16x32_bf16 v[8:11], v[168:171], a[120:123], v[8:11]
buffer_load_dwordx4 v[76:79], v125, s[64:67], s87 offen
v_mfma_f32_16x16x32_bf16 v[24:27], v[168:171], a[124:127], v[24:27]
ds_read_b128 v[204:207], v230 offset:6144
ds_read_b128 a[12:15], v157 offset:2048
v_mfma_f32_16x16x32_bf16 v[12:15], v[172:175], a[120:123], v[12:15]
v_mfma_f32_16x16x32_bf16 v[28:31], v[172:175], a[124:127], v[28:31]
v_mfma_f32_16x16x32_bf16 v[220:223], v[248:251], a[124:127], v[220:223]
s_xor_b32 s80, s82, 163840
s_waitcnt lgkmcnt(8)
v_mfma_f32_16x16x32_bf16 v[32:35], v[176:179], a[120:123], v[32:35]
ds_read_b128 v[160:163], v230 offset:1024
s_add_u32 m0, s77, 32768
v_mfma_f32_16x16x32_bf16 v[48:51], v[176:179], a[124:127], v[48:51]
buffer_load_dwordx4 v214, s[52:55], s80 offen offset:0 sc1 lds
v_mfma_f32_16x16x32_bf16 v[36:39], v[180:183], a[120:123], v[36:39]
ds_read_b128 v[164:167], v230 offset:3072
v_lshlrev_b32 v108, 16, v72
v_mfma_f32_16x16x32_bf16 v[52:55], v[180:183], a[124:127], v[52:55]
v_and_b32 v109, s93, v72
v_fmac_f32 v100, v108, v114
v_mfma_f32_16x16x32_bf16 v[40:43], v[184:187], a[120:123], v[40:43]
ds_read_b128 v[168:171], v230 offset:5120
v_fmac_f32 v101, v109, v114
v_mfma_f32_16x16x32_bf16 v[56:59], v[184:187], a[124:127], v[56:59]
buffer_load_dwordx4 v214, s[52:55], s80 offen offset:2048 sc1 lds
v_mfma_f32_16x16x32_bf16 v[44:47], v[188:191], a[120:123], v[44:47]
ds_read_b128 v[172:175], v230 offset:7168
v_lshlrev_b32 v110, 16, v73
v_mfma_f32_16x16x32_bf16 v[60:63], v[188:191], a[124:127], v[60:63]
v_and_b32 v111, s93, v73
s_waitcnt lgkmcnt(5)
v_mfma_f32_16x16x32_bf16 v[0:3], v[192:195], a[128:131], v[0:3]
ds_read_b128 v[176:179], v230 offset:8192
v_fmac_f32 v102, v110, v114
v_mfma_f32_16x16x32_bf16 v[16:19], v[192:195], a[132:135], v[16:19]
buffer_load_dwordx4 v214, s[52:55], s80 offen offset:1024 sc1 lds
v_mfma_f32_16x16x32_bf16 v[4:7], v[196:199], a[128:131], v[4:7]
ds_read_b128 v[180:183], v230 offset:10240
v_fmac_f32 v103, v111, v114
v_mfma_f32_16x16x32_bf16 v[20:23], v[196:199], a[132:135], v[20:23]
buffer_load_dwordx4 v214, s[52:55], s80 offen offset:3072 sc1 lds
v_mfma_f32_16x16x32_bf16 v[216:219], v[248:251], a[128:131], v[216:219]
ds_read_b128 v[184:187], v230 offset:12288
s_xor_b32 s81, s83, 2304
v_mfma_f32_16x16x32_bf16 v[8:11], v[200:203], a[128:131], v[8:11]
v_lshlrev_b32 v108, 16, v74
v_and_b32 v109, s93, v74
v_mfma_f32_16x16x32_bf16 v[24:27], v[200:203], a[132:135], v[24:27]
ds_read_b128 v[188:191], v230 offset:14336
s_add_u32 m0, s77, 98304
v_mfma_f32_16x16x32_bf16 v[12:15], v[204:207], a[128:131], v[12:15]
buffer_load_dwordx4 v228, s[32:35], s81 offen offset:0 lds
v_mfma_f32_16x16x32_bf16 v[28:31], v[204:207], a[132:135], v[28:31]
v_fmac_f32 v104, v108, v114
v_fmac_f32 v105, v109, v114
v_mfma_f32_16x16x32_bf16 v[220:223], v[248:251], a[132:135], v[220:223]
v_lshlrev_b32 v110, 16, v75
s_waitcnt lgkmcnt(4)
v_mfma_f32_16x16x32_bf16 v[32:35], v[160:163], a[128:131], v[32:35]
ds_read_b128 v[192:195], v230 offset:9216
v_and_b32 v111, s93, v75
v_mfma_f32_16x16x32_bf16 v[48:51], v[160:163], a[132:135], v[48:51]
buffer_load_dwordx4 v252, s[32:35], s81 offen offset:2048 lds
v_mfma_f32_16x16x32_bf16 v[36:39], v[164:167], a[128:131], v[36:39]
ds_read_b128 v[196:199], v230 offset:11264
v_fmac_f32 v106, v110, v114
v_mfma_f32_16x16x32_bf16 v[52:55], v[164:167], a[132:135], v[52:55]
v_fmac_f32 v107, v111, v114
v_mfma_f32_16x16x32_bf16 v[40:43], v[168:171], a[128:131], v[40:43]
ds_read_b128 v[200:203], v230 offset:13312
v_mfma_f32_16x16x32_bf16 v[56:59], v[168:171], a[132:135], v[56:59]
buffer_load_dwordx4 v229, s[32:35], s81 offen offset:1024 lds
v_mfma_f32_16x16x32_bf16 v[44:47], v[172:175], a[128:131], v[44:47]
ds_read_b128 v[204:207], v230 offset:15360
v_mfma_f32_16x16x32_bf16 v[60:63], v[172:175], a[132:135], v[60:63]
s_waitcnt vmcnt(12) lgkmcnt(4)
s_barrier
v_mfma_f32_16x16x32_bf16 v[0:3], v[176:179], a[136:139], v[0:3]
ds_read_b128 v[160:163], v230 offset:16384
ds_read_b128 a[16:19], v245 offset:0
v_mfma_f32_16x16x32_bf16 v[16:19], v[176:179], a[140:143], v[16:19]
buffer_load_dwordx4 v253, s[32:35], s81 offen offset:3072 lds
v_mfma_f32_16x16x32_bf16 v[4:7], v[180:183], a[136:139], v[4:7]
ds_read_b128 v[164:167], v230 offset:18432
ds_read_b128 a[20:23], v245 offset:2048
v_mfma_f32_16x16x32_bf16 v[20:23], v[180:183], a[140:143], v[20:23]
v_mfma_f32_16x16x32_bf16 v[216:219], v[248:251], a[136:139], v[216:219]
ds_read_b128 v[168:171], v230 offset:20480
ds_read_b128 a[24:27], v246 offset:0
v_mfma_f32_16x16x32_bf16 v[8:11], v[184:187], a[136:139], v[8:11]
buffer_store_dwordx4 v[126:129], v230, s[52:55], s18 offen offset:3072 sc1
v_mfma_f32_16x16x32_bf16 v[24:27], v[184:187], a[140:143], v[24:27]
ds_read_b128 v[172:175], v230 offset:22528
ds_read_b128 a[28:31], v246 offset:2048
v_mfma_f32_16x16x32_bf16 v[12:15], v[188:191], a[136:139], v[12:15]
v_mfma_f32_16x16x32_bf16 v[28:31], v[188:191], a[140:143], v[28:31]
v_mfma_f32_16x16x32_bf16 v[220:223], v[248:251], a[140:143], v[220:223]
s_xor_b32 s80, s82, 180224
s_waitcnt lgkmcnt(8)
v_mfma_f32_16x16x32_bf16 v[32:35], v[192:195], a[136:139], v[32:35]
ds_read_b128 v[176:179], v230 offset:17408
s_add_u32 m0, s77, 49152
)"
R"(
v_mfma_f32_16x16x32_bf16 v[48:51], v[192:195], a[140:143], v[48:51]
buffer_load_dwordx4 v214, s[52:55], s80 offen offset:0 sc1 lds
v_mfma_f32_16x16x32_bf16 v[36:39], v[196:199], a[136:139], v[36:39]
ds_read_b128 v[180:183], v230 offset:19456
v_mfma_f32_16x16x32_bf16 v[52:55], v[196:199], a[140:143], v[52:55]
v_mfma_f32_16x16x32_bf16 v[40:43], v[200:203], a[136:139], v[40:43]
ds_read_b128 v[184:187], v230 offset:21504
v_mfma_f32_16x16x32_bf16 v[56:59], v[200:203], a[140:143], v[56:59]
buffer_load_dwordx4 v214, s[52:55], s80 offen offset:2048 sc1 lds
v_mfma_f32_16x16x32_bf16 v[44:47], v[204:207], a[136:139], v[44:47]
ds_read_b128 v[188:191], v230 offset:23552
v_mfma_f32_16x16x32_bf16 v[60:63], v[204:207], a[140:143], v[60:63]
s_waitcnt lgkmcnt(5)
v_mfma_f32_16x16x32_bf16 v[0:3], v[160:163], a[144:147], v[0:3]
ds_read_b128 v[192:195], v230 offset:24576
v_mfma_f32_16x16x32_bf16 v[16:19], v[160:163], a[148:151], v[16:19]
buffer_load_dwordx4 v214, s[52:55], s80 offen offset:1024 sc1 lds
v_mfma_f32_16x16x32_bf16 v[4:7], v[164:167], a[144:147], v[4:7]
ds_read_b128 v[196:199], v230 offset:26624
v_mfma_f32_16x16x32_bf16 v[20:23], v[164:167], a[148:151], v[20:23]
buffer_load_dwordx4 v214, s[52:55], s80 offen offset:3072 sc1 lds
v_mfma_f32_16x16x32_bf16 v[216:219], v[248:251], a[144:147], v[216:219]
ds_read_b128 v[200:203], v230 offset:28672
s_xor_b32 s81, s83, 2432
v_mfma_f32_16x16x32_bf16 v[8:11], v[168:171], a[144:147], v[8:11]
v_mfma_f32_16x16x32_bf16 v[24:27], v[168:171], a[148:151], v[24:27]
ds_read_b128 v[204:207], v230 offset:30720
s_add_u32 m0, s77, 114688
v_mfma_f32_16x16x32_bf16 v[12:15], v[172:175], a[144:147], v[12:15]
buffer_load_dwordx4 v228, s[32:35], s81 offen offset:0 lds
v_mfma_f32_16x16x32_bf16 v[28:31], v[172:175], a[148:151], v[28:31]
v_mfma_f32_16x16x32_bf16 v[220:223], v[248:251], a[148:151], v[220:223]
s_waitcnt lgkmcnt(4)
v_mfma_f32_16x16x32_bf16 v[32:35], v[176:179], a[144:147], v[32:35]
ds_read_b128 v[160:163], v230 offset:25600
v_mfma_f32_16x16x32_bf16 v[48:51], v[176:179], a[148:151], v[48:51]
buffer_load_dwordx4 v252, s[32:35], s81 offen offset:2048 lds
v_mfma_f32_16x16x32_bf16 v[36:39], v[180:183], a[144:147], v[36:39]
ds_read_b128 v[164:167], v230 offset:27648
v_mfma_f32_16x16x32_bf16 v[52:55], v[180:183], a[148:151], v[52:55]
v_mfma_f32_16x16x32_bf16 v[40:43], v[184:187], a[144:147], v[40:43]
ds_read_b128 v[168:171], v230 offset:29696
v_mfma_f32_16x16x32_bf16 v[56:59], v[184:187], a[148:151], v[56:59]
buffer_load_dwordx4 v229, s[32:35], s81 offen offset:1024 lds
v_mfma_f32_16x16x32_bf16 v[44:47], v[188:191], a[144:147], v[44:47]
ds_read_b128 v[172:175], v230 offset:31744
s_mov_b32 s87, 53248
v_mfma_f32_16x16x32_bf16 v[60:63], v[188:191], a[148:151], v[60:63]
s_waitcnt vmcnt(12) lgkmcnt(4)
s_barrier
v_mfma_f32_16x16x32_bf16 v[0:3], v[192:195], a[152:155], v[0:3]
ds_read_b128 v[176:179], v230 offset:32768
ds_read_b128 a[32:35], v245 offset:16384
v_mfma_f32_16x16x32_bf16 v[16:19], v[192:195], a[156:159], v[16:19]
buffer_load_dwordx4 v253, s[32:35], s81 offen offset:3072 lds
v_mfma_f32_16x16x32_bf16 v[4:7], v[196:199], a[152:155], v[4:7]
ds_read_b128 v[180:183], v230 offset:34816
ds_read_b128 a[36:39], v245 offset:18432
v_mfma_f32_16x16x32_bf16 v[20:23], v[196:199], a[156:159], v[20:23]
v_mfma_f32_16x16x32_bf16 v[216:219], v[248:251], a[152:155], v[216:219]
ds_read_b128 v[184:187], v230 offset:36864
ds_read_b128 a[40:43], v246 offset:16384
v_mfma_f32_16x16x32_bf16 v[8:11], v[200:203], a[152:155], v[8:11]
buffer_load_dwordx4 v[80:83], v125, s[64:67], s87 offen
v_mfma_f32_16x16x32_bf16 v[24:27], v[200:203], a[156:159], v[24:27]
ds_read_b128 v[188:191], v230 offset:38912
ds_read_b128 a[44:47], v246 offset:18432
v_mfma_f32_16x16x32_bf16 v[12:15], v[204:207], a[152:155], v[12:15]
v_mfma_f32_16x16x32_bf16 v[28:31], v[204:207], a[156:159], v[28:31]
v_mfma_f32_16x16x32_bf16 v[220:223], v[248:251], a[156:159], v[220:223]
s_xor_b32 s80, s82, 262144
s_waitcnt lgkmcnt(8)
v_mfma_f32_16x16x32_bf16 v[32:35], v[160:163], a[152:155], v[32:35]
ds_read_b128 v[192:195], v230 offset:33792
s_add_u32 m0, s77, 0
v_mfma_f32_16x16x32_bf16 v[48:51], v[160:163], a[156:159], v[48:51]
buffer_load_dwordx4 v214, s[52:55], s80 offen offset:0 sc1 lds
v_mfma_f32_16x16x32_bf16 v[36:39], v[164:167], a[152:155], v[36:39]
ds_read_b128 v[196:199], v230 offset:35840
v_lshlrev_b32 v108, 16, v76
v_mfma_f32_16x16x32_bf16 v[52:55], v[164:167], a[156:159], v[52:55]
v_and_b32 v109, s93, v76
v_fmac_f32 v100, v108, v115
v_mfma_f32_16x16x32_bf16 v[40:43], v[168:171], a[152:155], v[40:43]
ds_read_b128 v[200:203], v230 offset:37888
v_fmac_f32 v101, v109, v115
v_mfma_f32_16x16x32_bf16 v[56:59], v[168:171], a[156:159], v[56:59]
buffer_load_dwordx4 v214, s[52:55], s80 offen offset:2048 sc1 lds
v_mfma_f32_16x16x32_bf16 v[44:47], v[172:175], a[152:155], v[44:47]
ds_read_b128 v[204:207], v230 offset:39936
v_lshlrev_b32 v110, 16, v77
v_mfma_f32_16x16x32_bf16 v[60:63], v[172:175], a[156:159], v[60:63]
v_and_b32 v111, s93, v77
s_waitcnt lgkmcnt(5)
v_mfma_f32_16x16x32_bf16 v[0:3], v[176:179], a[160:163], v[0:3]
ds_read_b128 v[160:163], v230 offset:40960
v_fmac_f32 v102, v110, v115
v_mfma_f32_16x16x32_bf16 v[16:19], v[176:179], a[164:167], v[16:19]
buffer_load_dwordx4 v214, s[52:55], s80 offen offset:1024 sc1 lds
v_mfma_f32_16x16x32_bf16 v[4:7], v[180:183], a[160:163], v[4:7]
ds_read_b128 v[164:167], v230 offset:43008
v_fmac_f32 v103, v111, v115
v_mfma_f32_16x16x32_bf16 v[20:23], v[180:183], a[164:167], v[20:23]
buffer_load_dwordx4 v214, s[52:55], s80 offen offset:3072 sc1 lds
v_mfma_f32_16x16x32_bf16 v[216:219], v[248:251], a[160:163], v[216:219]
ds_read_b128 v[168:171], v230 offset:45056
s_xor_b32 s81, s83, 2560
v_mfma_f32_16x16x32_bf16 v[8:11], v[184:187], a[160:163], v[8:11]
v_lshlrev_b32 v108, 16, v78
v_and_b32 v109, s93, v78
v_mfma_f32_16x16x32_bf16 v[24:27], v[184:187], a[164:167], v[24:27]
ds_read_b128 v[172:175], v230 offset:47104
s_add_u32 m0, s77, 131072
v_mfma_f32_16x16x32_bf16 v[12:15], v[188:191], a[160:163], v[12:15]
buffer_load_dwordx4 v228, s[32:35], s81 offen offset:0 lds
v_mfma_f32_16x16x32_bf16 v[28:31], v[188:191], a[164:167], v[28:31]
v_fmac_f32 v104, v108, v115
v_fmac_f32 v105, v109, v115
v_mfma_f32_16x16x32_bf16 v[220:223], v[248:251], a[164:167], v[220:223]
v_lshlrev_b32 v110, 16, v79
s_waitcnt lgkmcnt(4)
v_mfma_f32_16x16x32_bf16 v[32:35], v[192:195], a[160:163], v[32:35]
ds_read_b128 v[176:179], v230 offset:41984
v_and_b32 v111, s93, v79
v_mfma_f32_16x16x32_bf16 v[48:51], v[192:195], a[164:167], v[48:51]
buffer_load_dwordx4 v252, s[32:35], s81 offen offset:2048 lds
v_mfma_f32_16x16x32_bf16 v[36:39], v[196:199], a[160:163], v[36:39]
ds_read_b128 v[180:183], v230 offset:44032
v_fmac_f32 v106, v110, v115
v_mfma_f32_16x16x32_bf16 v[52:55], v[196:199], a[164:167], v[52:55]
v_fmac_f32 v107, v111, v115
v_mfma_f32_16x16x32_bf16 v[40:43], v[200:203], a[160:163], v[40:43]
ds_read_b128 v[184:187], v230 offset:46080
s_xor_b32 s80, s82, 196608
v_mfma_f32_16x16x32_bf16 v[56:59], v[200:203], a[164:167], v[56:59]
buffer_load_dwordx4 v229, s[32:35], s81 offen offset:1024 lds
v_mfma_f32_16x16x32_bf16 v[44:47], v[204:207], a[160:163], v[44:47]
ds_read_b128 v[188:191], v230 offset:48128
v_mfma_f32_16x16x32_bf16 v[60:63], v[204:207], a[164:167], v[60:63]
s_waitcnt vmcnt(12) lgkmcnt(4)
s_barrier
v_mfma_f32_16x16x32_bf16 v[0:3], v[160:163], a[168:171], v[0:3]
ds_read_b128 v[192:195], v230 offset:49152
ds_read_b128 a[48:51], v245 offset:32768
v_mfma_f32_16x16x32_bf16 v[16:19], v[160:163], a[172:175], v[16:19]
buffer_load_dwordx4 v253, s[32:35], s81 offen offset:3072 lds
v_mfma_f32_16x16x32_bf16 v[4:7], v[164:167], a[168:171], v[4:7]
ds_read_b128 v[196:199], v230 offset:51200
ds_read_b128 a[52:55], v245 offset:34816
v_mfma_f32_16x16x32_bf16 v[20:23], v[164:167], a[172:175], v[20:23]
s_add_u32 m0, s77, 16384
v_mfma_f32_16x16x32_bf16 v[216:219], v[248:251], a[168:171], v[216:219]
ds_read_b128 v[200:203], v230 offset:53248
ds_read_b128 a[56:59], v246 offset:32768
v_mfma_f32_16x16x32_bf16 v[8:11], v[168:171], a[168:171], v[8:11]
buffer_load_dwordx4 v214, s[52:55], s80 offen offset:0 sc1 lds
v_mfma_f32_16x16x32_bf16 v[24:27], v[168:171], a[172:175], v[24:27]
ds_read_b128 v[204:207], v230 offset:55296
ds_read_b128 a[60:63], v246 offset:34816
v_mfma_f32_16x16x32_bf16 v[12:15], v[172:175], a[168:171], v[12:15]
v_mfma_f32_16x16x32_bf16 v[28:31], v[172:175], a[172:175], v[28:31]
v_mfma_f32_16x16x32_bf16 v[220:223], v[248:251], a[172:175], v[220:223]
s_waitcnt lgkmcnt(8)
v_mfma_f32_16x16x32_bf16 v[32:35], v[176:179], a[168:171], v[32:35]
ds_read_b128 v[160:163], v230 offset:50176
v_mfma_f32_16x16x32_bf16 v[48:51], v[176:179], a[172:175], v[48:51]
buffer_load_dwordx4 v214, s[52:55], s80 offen offset:2048 sc1 lds
v_mfma_f32_16x16x32_bf16 v[36:39], v[180:183], a[168:171], v[36:39]
ds_read_b128 v[164:167], v230 offset:52224
v_mfma_f32_16x16x32_bf16 v[52:55], v[180:183], a[172:175], v[52:55]
v_mfma_f32_16x16x32_bf16 v[40:43], v[184:187], a[168:171], v[40:43]
ds_read_b128 v[168:171], v230 offset:54272
v_mfma_f32_16x16x32_bf16 v[56:59], v[184:187], a[172:175], v[56:59]
buffer_load_dwordx4 v214, s[52:55], s80 offen offset:1024 sc1 lds
v_mfma_f32_16x16x32_bf16 v[44:47], v[188:191], a[168:171], v[44:47]
ds_read_b128 v[172:175], v230 offset:56320
v_mfma_f32_16x16x32_bf16 v[60:63], v[188:191], a[172:175], v[60:63]
s_waitcnt lgkmcnt(5)
v_mfma_f32_16x16x32_bf16 v[0:3], v[192:195], a[176:179], v[0:3]
ds_read_b128 v[176:179], v230 offset:57344
v_mfma_f32_16x16x32_bf16 v[16:19], v[192:195], a[180:183], v[16:19]
buffer_load_dwordx4 v214, s[52:55], s80 offen offset:3072 sc1 lds
v_mfma_f32_16x16x32_bf16 v[4:7], v[196:199], a[176:179], v[4:7]
ds_read_b128 v[180:183], v230 offset:59392
v_mfma_f32_16x16x32_bf16 v[20:23], v[196:199], a[180:183], v[20:23]
v_mfma_f32_16x16x32_bf16 v[216:219], v[248:251], a[176:179], v[216:219]
ds_read_b128 v[184:187], v230 offset:61440
s_xor_b32 s81, s83, 2688
v_mfma_f32_16x16x32_bf16 v[8:11], v[200:203], a[176:179], v[8:11]
buffer_store_dwordx4 v[126:129], v230, s[52:55], s19 offen offset:0 sc1
v_mfma_f32_16x16x32_bf16 v[24:27], v[200:203], a[180:183], v[24:27]
ds_read_b128 v[188:191], v230 offset:63488
s_add_u32 m0, s77, 65536
v_mfma_f32_16x16x32_bf16 v[12:15], v[204:207], a[176:179], v[12:15]
v_mfma_f32_16x16x32_bf16 v[28:31], v[204:207], a[180:183], v[28:31]
buffer_load_dwordx4 v228, s[32:35], s81 offen offset:0 lds
v_mfma_f32_16x16x32_bf16 v[220:223], v[248:251], a[180:183], v[220:223]
s_waitcnt lgkmcnt(4)
v_mfma_f32_16x16x32_bf16 v[32:35], v[160:163], a[176:179], v[32:35]
ds_read_b128 v[192:195], v230 offset:58368
v_mfma_f32_16x16x32_bf16 v[48:51], v[160:163], a[180:183], v[48:51]
v_mfma_f32_16x16x32_bf16 v[36:39], v[164:167], a[176:179], v[36:39]
ds_read_b128 v[196:199], v230 offset:60416
v_mfma_f32_16x16x32_bf16 v[52:55], v[164:167], a[180:183], v[52:55]
buffer_load_dwordx4 v252, s[32:35], s81 offen offset:2048 lds
v_mfma_f32_16x16x32_bf16 v[40:43], v[168:171], a[176:179], v[40:43]
ds_read_b128 v[200:203], v230 offset:62464
v_mfma_f32_16x16x32_bf16 v[56:59], v[168:171], a[180:183], v[56:59]
v_mfma_f32_16x16x32_bf16 v[44:47], v[172:175], a[176:179], v[44:47]
ds_read_b128 v[204:207], v230 offset:64512
s_mov_b32 s87, 65536
v_mfma_f32_16x16x32_bf16 v[60:63], v[172:175], a[180:183], v[60:63]
s_waitcnt vmcnt(11) lgkmcnt(4)
s_barrier
v_mfma_f32_16x16x32_bf16 v[0:3], v[176:179], a[184:187], v[0:3]
ds_read_b128 v[160:163], v230 offset:0
ds_read_b128 a[64:67], v245 offset:49152
v_mfma_f32_16x16x32_bf16 v[16:19], v[176:179], a[188:191], v[16:19]
buffer_load_dwordx4 v229, s[32:35], s81 offen offset:1024 lds
v_mfma_f32_16x16x32_bf16 v[4:7], v[180:183], a[184:187], v[4:7]
ds_read_b128 v[164:167], v230 offset:2048
ds_read_b128 a[68:71], v245 offset:51200
v_mfma_f32_16x16x32_bf16 v[20:23], v[180:183], a[188:191], v[20:23]
v_mfma_f32_16x16x32_bf16 v[216:219], v[248:251], a[184:187], v[216:219]
ds_read_b128 v[168:171], v230 offset:4096
ds_read_b128 a[72:75], v246 offset:49152
v_mfma_f32_16x16x32_bf16 v[8:11], v[184:187], a[184:187], v[8:11]
buffer_load_dwordx4 v253, s[32:35], s81 offen offset:3072 lds
v_mfma_f32_16x16x32_bf16 v[24:27], v[184:187], a[188:191], v[24:27]
ds_read_b128 v[172:175], v230 offset:6144
ds_read_b128 a[76:79], v246 offset:51200
v_mfma_f32_16x16x32_bf16 v[12:15], v[188:191], a[184:187], v[12:15]
v_mfma_f32_16x16x32_bf16 v[28:31], v[188:191], a[188:191], v[28:31]
buffer_load_dwordx4 v[84:87], v125, s[64:67], s87 offen
v_mfma_f32_16x16x32_bf16 v[220:223], v[248:251], a[188:191], v[220:223]
s_xor_b32 s80, s82, 212992
s_waitcnt lgkmcnt(8)
v_mfma_f32_16x16x32_bf16 v[32:35], v[192:195], a[184:187], v[32:35]
ds_read_b128 v[176:179], v230 offset:1024
s_add_u32 m0, s77, 32768
v_mfma_f32_16x16x32_bf16 v[48:51], v[192:195], a[188:191], v[48:51]
buffer_load_dwordx4 v214, s[52:55], s80 offen offset:0 sc1 lds
v_mfma_f32_16x16x32_bf16 v[36:39], v[196:199], a[184:187], v[36:39]
ds_read_b128 v[180:183], v230 offset:3072
v_lshlrev_b32 v108, 16, v80
v_mfma_f32_16x16x32_bf16 v[52:55], v[196:199], a[188:191], v[52:55]
v_and_b32 v109, s93, v80
v_fmac_f32 v100, v108, v116
v_mfma_f32_16x16x32_bf16 v[40:43], v[200:203], a[184:187], v[40:43]
ds_read_b128 v[184:187], v230 offset:5120
LC3_%=:
s_cbranch_execz LC7_%=
v_fmac_f32 v101, v109, v116
v_mfma_f32_16x16x32_bf16 v[56:59], v[200:203], a[188:191], v[56:59]
buffer_load_dwordx4 v214, s[52:55], s80 offen offset:2048 sc1 lds
v_mfma_f32_16x16x32_bf16 v[44:47], v[204:207], a[184:187], v[44:47]
ds_read_b128 v[188:191], v230 offset:7168
v_lshlrev_b32 v110, 16, v81
v_mfma_f32_16x16x32_bf16 v[60:63], v[204:207], a[188:191], v[60:63]
v_and_b32 v111, s93, v81
s_waitcnt lgkmcnt(5)
v_mfma_f32_16x16x32_bf16 v[0:3], v[160:163], a[192:195], v[0:3]
ds_read_b128 v[192:195], v230 offset:8192
v_fmac_f32 v102, v110, v116
v_mfma_f32_16x16x32_bf16 v[16:19], v[160:163], a[196:199], v[16:19]
buffer_load_dwordx4 v214, s[52:55], s80 offen offset:1024 sc1 lds
v_mfma_f32_16x16x32_bf16 v[4:7], v[164:167], a[192:195], v[4:7]
ds_read_b128 v[196:199], v230 offset:10240
v_fmac_f32 v103, v111, v116
v_mfma_f32_16x16x32_bf16 v[20:23], v[164:167], a[196:199], v[20:23]
buffer_load_dwordx4 v214, s[52:55], s80 offen offset:3072 sc1 lds
v_mfma_f32_16x16x32_bf16 v[216:219], v[248:251], a[192:195], v[216:219]
ds_read_b128 v[200:203], v230 offset:12288
s_xor_b32 s81, s83, 2816
v_mfma_f32_16x16x32_bf16 v[8:11], v[168:171], a[192:195], v[8:11]
v_lshlrev_b32 v108, 16, v82
v_and_b32 v109, s93, v82
v_mfma_f32_16x16x32_bf16 v[24:27], v[168:171], a[196:199], v[24:27]
ds_read_b128 v[204:207], v230 offset:14336
s_add_u32 m0, s77, 81920
v_mfma_f32_16x16x32_bf16 v[12:15], v[172:175], a[192:195], v[12:15]
buffer_load_dwordx4 v228, s[32:35], s81 offen offset:0 lds
v_mfma_f32_16x16x32_bf16 v[28:31], v[172:175], a[196:199], v[28:31]
v_fmac_f32 v104, v108, v116
v_fmac_f32 v105, v109, v116
v_mfma_f32_16x16x32_bf16 v[220:223], v[248:251], a[196:199], v[220:223]
v_lshlrev_b32 v110, 16, v83
s_waitcnt lgkmcnt(4)
v_mfma_f32_16x16x32_bf16 v[32:35], v[176:179], a[192:195], v[32:35]
ds_read_b128 v[160:163], v230 offset:9216
v_and_b32 v111, s93, v83
v_mfma_f32_16x16x32_bf16 v[48:51], v[176:179], a[196:199], v[48:51]
buffer_load_dwordx4 v252, s[32:35], s81 offen offset:2048 lds
v_mfma_f32_16x16x32_bf16 v[36:39], v[180:183], a[192:195], v[36:39]
ds_read_b128 v[164:167], v230 offset:11264
v_fmac_f32 v106, v110, v116
v_mfma_f32_16x16x32_bf16 v[52:55], v[180:183], a[196:199], v[52:55]
v_fmac_f32 v107, v111, v116
v_mfma_f32_16x16x32_bf16 v[40:43], v[184:187], a[192:195], v[40:43]
ds_read_b128 v[168:171], v230 offset:13312
s_xor_b32 s80, s82, 229376
v_mfma_f32_16x16x32_bf16 v[56:59], v[184:187], a[196:199], v[56:59]
buffer_load_dwordx4 v229, s[32:35], s81 offen offset:1024 lds
v_mfma_f32_16x16x32_bf16 v[44:47], v[188:191], a[192:195], v[44:47]
ds_read_b128 v[172:175], v230 offset:15360
v_mfma_f32_16x16x32_bf16 v[60:63], v[188:191], a[196:199], v[60:63]
s_waitcnt vmcnt(13) lgkmcnt(4)
s_barrier
v_mfma_f32_16x16x32_bf16 v[0:3], v[192:195], a[200:203], v[0:3]
ds_read_b128 v[176:179], v230 offset:16384
ds_read_b128 a[80:83], v156 offset:0
v_mfma_f32_16x16x32_bf16 v[16:19], v[192:195], a[204:207], v[16:19]
buffer_load_dwordx4 v253, s[32:35], s81 offen offset:3072 lds
v_mfma_f32_16x16x32_bf16 v[4:7], v[196:199], a[200:203], v[4:7]
ds_read_b128 v[180:183], v230 offset:18432
ds_read_b128 a[84:87], v156 offset:2048
v_mfma_f32_16x16x32_bf16 v[20:23], v[196:199], a[204:207], v[20:23]
s_mov_b32 s7, 0
v_mfma_f32_16x16x32_bf16 v[216:219], v[248:251], a[200:203], v[216:219]
ds_read_b128 v[184:187], v230 offset:20480
ds_read_b128 a[88:91], v157 offset:0
v_mfma_f32_16x16x32_bf16 v[8:11], v[200:203], a[200:203], v[8:11]
buffer_load_dwordx4 v[128:131], v238, s[52:55], s7 offen sc1
v_mfma_f32_16x16x32_bf16 v[24:27], v[200:203], a[204:207], v[24:27]
ds_read_b128 v[188:191], v230 offset:22528
ds_read_b128 a[92:95], v157 offset:2048
v_mfma_f32_16x16x32_bf16 v[12:15], v[204:207], a[200:203], v[12:15]
s_add_u32 m0, s77, 49152
s_mov_b32 s7, 16384
v_mfma_f32_16x16x32_bf16 v[28:31], v[204:207], a[204:207], v[28:31]
buffer_load_dwordx4 v214, s[52:55], s80 offen offset:0 sc1 lds
v_mfma_f32_16x16x32_bf16 v[220:223], v[248:251], a[204:207], v[220:223]
s_waitcnt lgkmcnt(8)
v_mfma_f32_16x16x32_bf16 v[32:35], v[160:163], a[200:203], v[32:35]
ds_read_b128 v[192:195], v230 offset:17408
v_mfma_f32_16x16x32_bf16 v[48:51], v[160:163], a[204:207], v[48:51]
buffer_load_dwordx4 v[132:135], v238, s[52:55], s7 offen sc1
v_mfma_f32_16x16x32_bf16 v[36:39], v[164:167], a[200:203], v[36:39]
ds_read_b128 v[196:199], v230 offset:19456
s_mov_b32 s7, 32768
v_mfma_f32_16x16x32_bf16 v[52:55], v[164:167], a[204:207], v[52:55]
buffer_load_dwordx4 v[136:139], v238, s[52:55], s7 offen sc1
v_mfma_f32_16x16x32_bf16 v[40:43], v[168:171], a[200:203], v[40:43]
ds_read_b128 v[200:203], v230 offset:21504
s_mov_b32 s7, 49152
v_mfma_f32_16x16x32_bf16 v[56:59], v[168:171], a[204:207], v[56:59]
buffer_load_dwordx4 v214, s[52:55], s80 offen offset:2048 sc1 lds
v_mfma_f32_16x16x32_bf16 v[44:47], v[172:175], a[200:203], v[44:47]
ds_read_b128 v[204:207], v230 offset:23552
v_mfma_f32_16x16x32_bf16 v[60:63], v[172:175], a[204:207], v[60:63]
s_waitcnt lgkmcnt(5)
v_mfma_f32_16x16x32_bf16 v[0:3], v[176:179], a[208:211], v[0:3]
ds_read_b128 v[160:163], v230 offset:24576
v_mfma_f32_16x16x32_bf16 v[16:19], v[176:179], a[212:215], v[16:19]
buffer_load_dwordx4 v[140:143], v238, s[52:55], s7 offen sc1
v_mfma_f32_16x16x32_bf16 v[4:7], v[180:183], a[208:211], v[4:7]
ds_read_b128 v[164:167], v230 offset:26624
v_mfma_f32_16x16x32_bf16 v[20:23], v[180:183], a[212:215], v[20:23]
v_mfma_f32_16x16x32_bf16 v[216:219], v[248:251], a[208:211], v[216:219]
ds_read_b128 v[168:171], v230 offset:28672
v_mfma_f32_16x16x32_bf16 v[8:11], v[184:187], a[208:211], v[8:11]
v_mfma_f32_16x16x32_bf16 v[24:27], v[184:187], a[212:215], v[24:27]
ds_read_b128 v[172:175], v230 offset:30720
v_mfma_f32_16x16x32_bf16 v[12:15], v[188:191], a[208:211], v[12:15]
v_mfma_f32_16x16x32_bf16 v[28:31], v[188:191], a[212:215], v[28:31]
buffer_load_dwordx4 v214, s[52:55], s80 offen offset:1024 sc1 lds
v_mfma_f32_16x16x32_bf16 v[220:223], v[248:251], a[212:215], v[220:223]
s_waitcnt lgkmcnt(4)
v_mfma_f32_16x16x32_bf16 v[32:35], v[192:195], a[208:211], v[32:35]
ds_read_b128 v[176:179], v230 offset:25600
v_mfma_f32_16x16x32_bf16 v[48:51], v[192:195], a[212:215], v[48:51]
buffer_load_dwordx4 v214, s[52:55], s80 offen offset:3072 sc1 lds
v_mfma_f32_16x16x32_bf16 v[36:39], v[196:199], a[208:211], v[36:39]
ds_read_b128 v[180:183], v230 offset:27648
v_mfma_f32_16x16x32_bf16 v[52:55], v[196:199], a[212:215], v[52:55]
v_mfma_f32_16x16x32_bf16 v[40:43], v[200:203], a[208:211], v[40:43]
ds_read_b128 v[184:187], v230 offset:29696
s_xor_b32 s81, s83, 2944
v_mfma_f32_16x16x32_bf16 v[56:59], v[200:203], a[212:215], v[56:59]
buffer_store_dwordx4 v[126:129], v230, s[52:55], s19 offen offset:1024 sc1
v_mfma_f32_16x16x32_bf16 v[44:47], v[204:207], a[208:211], v[44:47]
ds_read_b128 v[188:191], v230 offset:31744
s_add_u32 m0, s77, 98304
v_mfma_f32_16x16x32_bf16 v[60:63], v[204:207], a[212:215], v[60:63]
s_waitcnt vmcnt(13) lgkmcnt(4)
s_barrier
v_mfma_f32_16x16x32_bf16 v[0:3], v[160:163], a[216:219], v[0:3]
ds_read_b128 v[192:195], v230 offset:32768
ds_read_b128 a[96:99], v245 offset:0
v_mfma_f32_16x16x32_bf16 v[16:19], v[160:163], a[220:223], v[16:19]
buffer_load_dwordx4 v228, s[32:35], s81 offen offset:0 lds
v_mfma_f32_16x16x32_bf16 v[4:7], v[164:167], a[216:219], v[4:7]
ds_read_b128 v[196:199], v230 offset:34816
ds_read_b128 a[100:103], v245 offset:2048
v_mfma_f32_16x16x32_bf16 v[20:23], v[164:167], a[220:223], v[20:23]
buffer_load_dwordx4 v252, s[32:35], s81 offen offset:2048 lds
v_mfma_f32_16x16x32_bf16 v[216:219], v[248:251], a[216:219], v[216:219]
ds_read_b128 v[200:203], v230 offset:36864
ds_read_b128 a[104:107], v246 offset:0
v_mfma_f32_16x16x32_bf16 v[8:11], v[168:171], a[216:219], v[8:11]
)"
R"(
buffer_load_dwordx4 v229, s[32:35], s81 offen offset:1024 lds
v_mfma_f32_16x16x32_bf16 v[24:27], v[168:171], a[220:223], v[24:27]
ds_read_b128 v[204:207], v230 offset:38912
ds_read_b128 a[108:111], v246 offset:2048
v_mfma_f32_16x16x32_bf16 v[12:15], v[172:175], a[216:219], v[12:15]
buffer_load_dwordx4 v253, s[32:35], s81 offen offset:3072 lds
v_mfma_f32_16x16x32_bf16 v[28:31], v[172:175], a[220:223], v[28:31]
v_mfma_f32_16x16x32_bf16 v[220:223], v[248:251], a[220:223], v[220:223]
s_xor_b32 s80, s82, 245760
s_waitcnt lgkmcnt(8)
v_mfma_f32_16x16x32_bf16 v[32:35], v[176:179], a[216:219], v[32:35]
ds_read_b128 v[160:163], v230 offset:33792
s_mov_b32 s87, 77824
v_mfma_f32_16x16x32_bf16 v[48:51], v[176:179], a[220:223], v[48:51]
buffer_load_dwordx4 v[88:91], v125, s[64:67], s87 offen
v_mfma_f32_16x16x32_bf16 v[36:39], v[180:183], a[216:219], v[36:39]
ds_read_b128 v[164:167], v230 offset:35840
s_add_u32 m0, s77, 0
v_mfma_f32_16x16x32_bf16 v[52:55], v[180:183], a[220:223], v[52:55]
buffer_load_dwordx4 v214, s[52:55], s80 offen offset:0 sc1 lds
v_mfma_f32_16x16x32_bf16 v[40:43], v[184:187], a[216:219], v[40:43]
ds_read_b128 v[168:171], v230 offset:37888
v_lshlrev_b32 v108, 16, v84
v_mfma_f32_16x16x32_bf16 v[56:59], v[184:187], a[220:223], v[56:59]
v_and_b32 v109, s93, v84
v_fmac_f32 v100, v108, v117
v_mfma_f32_16x16x32_bf16 v[44:47], v[188:191], a[216:219], v[44:47]
ds_read_b128 v[172:175], v230 offset:39936
v_fmac_f32 v101, v109, v117
v_mfma_f32_16x16x32_bf16 v[60:63], v[188:191], a[220:223], v[60:63]
v_lshlrev_b32 v110, 16, v85
s_waitcnt lgkmcnt(5)
v_mfma_f32_16x16x32_bf16 v[0:3], v[192:195], a[224:227], v[0:3]
ds_read_b128 v[176:179], v230 offset:40960
v_and_b32 v111, s93, v85
v_mfma_f32_16x16x32_bf16 v[16:19], v[192:195], a[228:231], v[16:19]
buffer_load_dwordx4 v214, s[52:55], s80 offen offset:2048 sc1 lds
v_mfma_f32_16x16x32_bf16 v[4:7], v[196:199], a[224:227], v[4:7]
ds_read_b128 v[180:183], v230 offset:43008
s_xor_b32 s80, s82, 278528
v_mfma_f32_16x16x32_bf16 v[20:23], v[196:199], a[228:231], v[20:23]
buffer_load_dwordx4 v213, s[52:55], s80 offen offset:1024 sc1 lds
v_mfma_f32_16x16x32_bf16 v[216:219], v[248:251], a[224:227], v[216:219]
ds_read_b128 v[184:187], v230 offset:45056
v_fmac_f32 v102, v110, v117
v_mfma_f32_16x16x32_bf16 v[8:11], v[200:203], a[224:227], v[8:11]
buffer_load_dwordx4 v213, s[52:55], s80 offen offset:3072 sc1 lds
v_mfma_f32_16x16x32_bf16 v[24:27], v[200:203], a[228:231], v[24:27]
ds_read_b128 v[188:191], v230 offset:47104
v_fmac_f32 v103, v111, v117
v_mfma_f32_16x16x32_bf16 v[12:15], v[204:207], a[224:227], v[12:15]
v_lshlrev_b32 v108, 16, v86
v_and_b32 v109, s93, v86
v_mfma_f32_16x16x32_bf16 v[28:31], v[204:207], a[228:231], v[28:31]
v_fmac_f32 v104, v108, v117
v_fmac_f32 v105, v109, v117
v_mfma_f32_16x16x32_bf16 v[220:223], v[248:251], a[228:231], v[220:223]
s_xor_b32 s81, s83, 3200
s_waitcnt lgkmcnt(4)
v_mfma_f32_16x16x32_bf16 v[32:35], v[160:163], a[224:227], v[32:35]
ds_read_b128 v[192:195], v230 offset:41984
s_add_u32 m0, s77, 114688
v_mfma_f32_16x16x32_bf16 v[48:51], v[160:163], a[228:231], v[48:51]
buffer_load_dwordx4 v228, s[32:35], s81 offen offset:0 lds
v_mfma_f32_16x16x32_bf16 v[36:39], v[164:167], a[224:227], v[36:39]
ds_read_b128 v[196:199], v230 offset:44032
v_lshlrev_b32 v110, 16, v87
v_mfma_f32_16x16x32_bf16 v[52:55], v[164:167], a[228:231], v[52:55]
v_and_b32 v111, s93, v87
v_fmac_f32 v106, v110, v117
v_mfma_f32_16x16x32_bf16 v[40:43], v[168:171], a[224:227], v[40:43]
ds_read_b128 v[200:203], v230 offset:46080
v_fmac_f32 v107, v111, v117
v_mfma_f32_16x16x32_bf16 v[56:59], v[168:171], a[228:231], v[56:59]
buffer_load_dwordx4 v252, s[32:35], s81 offen offset:2048 lds
v_mfma_f32_16x16x32_bf16 v[44:47], v[172:175], a[224:227], v[44:47]
ds_read_b128 v[204:207], v230 offset:48128
v_mfma_f32_16x16x32_bf16 v[60:63], v[172:175], a[228:231], v[60:63]
s_waitcnt vmcnt(12) lgkmcnt(4)
s_barrier
v_mfma_f32_16x16x32_bf16 v[0:3], v[176:179], a[232:235], v[0:3]
ds_read_b128 v[160:163], v230 offset:49152
ds_read_b128 a[112:115], v245 offset:16384
v_mfma_f32_16x16x32_bf16 v[16:19], v[176:179], a[236:239], v[16:19]
buffer_load_dwordx4 v229, s[32:35], s81 offen offset:1024 lds
v_mfma_f32_16x16x32_bf16 v[4:7], v[180:183], a[232:235], v[4:7]
ds_read_b128 v[164:167], v230 offset:51200
ds_read_b128 a[116:119], v245 offset:18432
v_mfma_f32_16x16x32_bf16 v[20:23], v[180:183], a[236:239], v[20:23]
v_mfma_f32_16x16x32_bf16 v[216:219], v[248:251], a[232:235], v[216:219]
ds_read_b128 v[168:171], v230 offset:53248
ds_read_b128 a[120:123], v246 offset:16384
v_mfma_f32_16x16x32_bf16 v[8:11], v[184:187], a[232:235], v[8:11]
buffer_load_dwordx4 v253, s[32:35], s81 offen offset:3072 lds
v_mfma_f32_16x16x32_bf16 v[24:27], v[184:187], a[236:239], v[24:27]
ds_read_b128 v[172:175], v230 offset:55296
ds_read_b128 a[124:127], v246 offset:18432
v_mfma_f32_16x16x32_bf16 v[12:15], v[188:191], a[232:235], v[12:15]
v_mfma_f32_16x16x32_bf16 v[28:31], v[188:191], a[236:239], v[28:31]
v_mfma_f32_16x16x32_bf16 v[220:223], v[248:251], a[236:239], v[220:223]
s_xor_b32 s80, s82, 393216
s_waitcnt lgkmcnt(8)
v_mfma_f32_16x16x32_bf16 v[32:35], v[192:195], a[232:235], v[32:35]
ds_read_b128 v[176:179], v230 offset:50176
s_add_u32 m0, s77, 16384
v_mfma_f32_16x16x32_bf16 v[48:51], v[192:195], a[236:239], v[48:51]
buffer_load_dwordx4 v214, s[52:55], s80 offen offset:0 sc1 lds
v_mfma_f32_16x16x32_bf16 v[36:39], v[196:199], a[232:235], v[36:39]
ds_read_b128 v[180:183], v230 offset:52224
v_mfma_f32_16x16x32_bf16 v[52:55], v[196:199], a[236:239], v[52:55]
v_mfma_f32_16x16x32_bf16 v[40:43], v[200:203], a[232:235], v[40:43]
ds_read_b128 v[184:187], v230 offset:54272
v_mfma_f32_16x16x32_bf16 v[56:59], v[200:203], a[236:239], v[56:59]
buffer_load_dwordx4 v214, s[52:55], s80 offen offset:2048 sc1 lds
v_mfma_f32_16x16x32_bf16 v[44:47], v[204:207], a[232:235], v[44:47]
ds_read_b128 v[188:191], v230 offset:56320
v_mfma_f32_16x16x32_bf16 v[60:63], v[204:207], a[236:239], v[60:63]
s_waitcnt lgkmcnt(5)
v_mfma_f32_16x16x32_bf16 v[0:3], v[160:163], a[240:243], v[0:3]
ds_read_b128 v[192:195], v230 offset:57344
s_xor_b32 s80, s82, 294912
v_mfma_f32_16x16x32_bf16 v[16:19], v[160:163], a[244:247], v[16:19]
buffer_load_dwordx4 v213, s[52:55], s80 offen offset:1024 sc1 lds
v_mfma_f32_16x16x32_bf16 v[4:7], v[164:167], a[240:243], v[4:7]
ds_read_b128 v[196:199], v230 offset:59392
v_mfma_f32_16x16x32_bf16 v[20:23], v[164:167], a[244:247], v[20:23]
v_mfma_f32_16x16x32_bf16 v[216:219], v[248:251], a[240:243], v[216:219]
ds_read_b128 v[200:203], v230 offset:61440
v_mfma_f32_16x16x32_bf16 v[8:11], v[168:171], a[240:243], v[8:11]
buffer_load_dwordx4 v213, s[52:55], s80 offen offset:3072 sc1 lds
v_mfma_f32_16x16x32_bf16 v[24:27], v[168:171], a[244:247], v[24:27]
ds_read_b128 v[204:207], v230 offset:63488
v_mfma_f32_16x16x32_bf16 v[12:15], v[172:175], a[240:243], v[12:15]
v_mfma_f32_16x16x32_bf16 v[28:31], v[172:175], a[244:247], v[28:31]
v_mfma_f32_16x16x32_bf16 v[220:223], v[248:251], a[244:247], v[220:223]
s_xor_b32 s81, s83, 3328
s_waitcnt lgkmcnt(4)
v_mfma_f32_16x16x32_bf16 v[32:35], v[176:179], a[240:243], v[32:35]
ds_read_b128 v[160:163], v230 offset:58368
s_add_u32 m0, s77, 131072
v_mfma_f32_16x16x32_bf16 v[48:51], v[176:179], a[244:247], v[48:51]
buffer_load_dwordx4 v228, s[32:35], s81 offen offset:0 lds
v_mfma_f32_16x16x32_bf16 v[36:39], v[180:183], a[240:243], v[36:39]
ds_read_b128 v[164:167], v230 offset:60416
v_mfma_f32_16x16x32_bf16 v[52:55], v[180:183], a[244:247], v[52:55]
v_min3_u32 v236, v129, v131, s78
v_max3_u32 v237, v129, v131, s78
v_mfma_f32_16x16x32_bf16 v[40:43], v[184:187], a[240:243], v[40:43]
ds_read_b128 v[168:171], v230 offset:62464
v_min3_u32 v236, v133, v135, v236
v_mfma_f32_16x16x32_bf16 v[56:59], v[184:187], a[244:247], v[56:59]
buffer_load_dwordx4 v252, s[32:35], s81 offen offset:2048 lds
v_mfma_f32_16x16x32_bf16 v[44:47], v[188:191], a[240:243], v[44:47]
ds_read_b128 v[172:175], v230 offset:64512
v_max3_u32 v237, v133, v135, v237
v_mfma_f32_16x16x32_bf16 v[60:63], v[188:191], a[244:247], v[60:63]
s_waitcnt vmcnt(10) lgkmcnt(4)
s_barrier
v_mfma_f32_16x16x32_bf16 v[0:3], v[192:195], a[248:251], v[0:3]
ds_read_b128 v[176:179], v230 offset:0
ds_read_b128 a[128:131], v245 offset:32768
v_mfma_f32_16x16x32_bf16 v[16:19], v[192:195], a[252:255], v[16:19]
buffer_load_dwordx4 v229, s[32:35], s81 offen offset:1024 lds
v_mfma_f32_16x16x32_bf16 v[4:7], v[196:199], a[248:251], v[4:7]
ds_read_b128 v[180:183], v230 offset:2048
ds_read_b128 a[132:135], v245 offset:34816
v_mfma_f32_16x16x32_bf16 v[20:23], v[196:199], a[252:255], v[20:23]
v_min3_u32 v236, v137, v139, v236
v_max3_u32 v237, v137, v139, v237
v_mfma_f32_16x16x32_bf16 v[216:219], v[248:251], a[248:251], v[216:219]
ds_read_b128 v[184:187], v230 offset:4096
ds_read_b128 a[136:139], v246 offset:32768
v_mfma_f32_16x16x32_bf16 v[8:11], v[200:203], a[248:251], v[8:11]
buffer_load_dwordx4 v253, s[32:35], s81 offen offset:3072 lds
v_mfma_f32_16x16x32_bf16 v[24:27], v[200:203], a[252:255], v[24:27]
ds_read_b128 v[188:191], v230 offset:6144
ds_read_b128 a[140:143], v246 offset:34816
v_mfma_f32_16x16x32_bf16 v[12:15], v[204:207], a[248:251], v[12:15]
v_min3_u32 v236, v141, v143, v236
v_max3_u32 v237, v141, v143, v237
v_mfma_f32_16x16x32_bf16 v[28:31], v[204:207], a[252:255], v[28:31]
v_cmp_ne_u32 vcc, v236, v237
s_nop 0
s_cbranch_vccz LRm0_%=
LSm0_%=:
s_sub_u32 s84, s84, 1
s_cbranch_scc1 LZm0_%=
s_mov_b32 s7, 0
buffer_load_dwordx4 v[128:131], v238, s[52:55], s7 offen sc1
s_mov_b32 s7, 16384
buffer_load_dwordx4 v[132:135], v238, s[52:55], s7 offen sc1
s_mov_b32 s7, 32768
buffer_load_dwordx4 v[136:139], v238, s[52:55], s7 offen sc1
s_mov_b32 s7, 49152
buffer_load_dwordx4 v[140:143], v238, s[52:55], s7 offen sc1
s_waitcnt vmcnt(0)
v_min3_u32 v236, v129, v131, s78
v_max3_u32 v237, v129, v131, s78
v_min3_u32 v236, v133, v135, v236
v_max3_u32 v237, v133, v135, v237
v_min3_u32 v236, v137, v139, v236
v_max3_u32 v237, v137, v139, v237
v_min3_u32 v236, v141, v143, v236
v_max3_u32 v237, v141, v143, v237
v_cmp_ne_u32 vcc, v236, v237
s_nop 0
s_cbranch_vccnz LSm0_%=
LZm0_%=:
s_max_i32 s84, s84, 0
LRm0_%=:
v_mfma_f32_16x16x32_bf16 v[220:223], v[248:251], a[252:255], v[220:223]
v_add_f32 v128, v128, v132
s_waitcnt lgkmcnt(8)
v_mfma_f32_16x16x32_bf16 v[32:35], v[160:163], a[248:251], v[32:35]
ds_read_b128 v[192:195], v230 offset:8192
s_mov_b32 s87, 90112
v_mfma_f32_16x16x32_bf16 v[48:51], v[160:163], a[252:255], v[48:51]
buffer_load_dwordx4 v[92:95], v125, s[64:67], s87 offen
v_mfma_f32_16x16x32_bf16 v[36:39], v[164:167], a[248:251], v[36:39]
ds_read_b128 v[196:199], v230 offset:10240
v_add_f32 v130, v130, v134
v_mfma_f32_16x16x32_bf16 v[52:55], v[164:167], a[252:255], v[52:55]
v_add_f32 v136, v136, v140
v_add_f32 v138, v138, v142
v_mfma_f32_16x16x32_bf16 v[40:43], v[168:171], a[248:251], v[40:43]
ds_read_b128 v[200:203], v230 offset:12288
v_add_f32 v128, v128, v136
v_mfma_f32_16x16x32_bf16 v[56:59], v[168:171], a[252:255], v[56:59]
buffer_store_dwordx4 v[126:129], v230, s[52:55], s19 offen offset:2048 sc1
v_mfma_f32_16x16x32_bf16 v[44:47], v[172:175], a[248:251], v[44:47]
ds_read_b128 v[204:207], v230 offset:14336
v_add_f32 v130, v130, v138
v_mfma_f32_16x16x32_bf16 v[60:63], v[172:175], a[252:255], v[60:63]
s_xor_b32 s80, s82, 311296
s_waitcnt lgkmcnt(5)
v_mfma_f32_16x16x32_bf16 v[0:3], v[176:179], a[0:3], v[0:3]
ds_read_b128 v[160:163], v230 offset:1024
s_add_u32 m0, s77, 32768
v_mfma_f32_16x16x32_bf16 v[16:19], v[176:179], a[4:7], v[16:19]
buffer_load_dwordx4 v214, s[52:55], s80 offen offset:0 sc1 lds
v_mfma_f32_16x16x32_bf16 v[4:7], v[180:183], a[0:3], v[4:7]
ds_read_b128 v[164:167], v230 offset:3072
v_lshrrev_b32 v241, 4, v239
v_mfma_f32_16x16x32_bf16 v[20:23], v[180:183], a[4:7], v[20:23]
v_cvt_f32_u32 v241, v241
v_permlane32_swap_b32 v128, v130
v_mfma_f32_16x16x32_bf16 v[216:219], v[248:251], a[0:3], v[216:219]
ds_read_b128 v[168:171], v230 offset:5120
v_add_f32 v128, v128, v130
v_mfma_f32_16x16x32_bf16 v[8:11], v[184:187], a[0:3], v[8:11]
buffer_load_dwordx4 v214, s[52:55], s80 offen offset:2048 sc1 lds
v_mfma_f32_16x16x32_bf16 v[24:27], v[184:187], a[4:7], v[24:27]
ds_read_b128 v[172:175], v230 offset:7168
s_xor_b32 s80, s82, 327680
v_mfma_f32_16x16x32_bf16 v[12:15], v[188:191], a[0:3], v[12:15]
buffer_load_dwordx4 v213, s[52:55], s80 offen offset:1024 sc1 lds
v_mfma_f32_16x16x32_bf16 v[28:31], v[188:191], a[4:7], v[28:31]
v_cvt_pk_bf16_f32 v128, v128, v128
v_lshlrev_b32 v128, 16, v128
v_mfma_f32_16x16x32_bf16 v[220:223], v[248:251], a[4:7], v[220:223]
v_add_f32 v128, v128, v241
s_waitcnt lgkmcnt(4)
v_mfma_f32_16x16x32_bf16 v[0:3], v[192:195], a[8:11], v[0:3]
ds_read_b128 v[176:179], v230 offset:9216
v_cvt_pk_bf16_f32 v128, v128, v128
v_mfma_f32_16x16x32_bf16 v[16:19], v[192:195], a[12:15], v[16:19]
buffer_load_dwordx4 v213, s[52:55], s80 offen offset:3072 sc1 lds
v_mfma_f32_16x16x32_bf16 v[4:7], v[196:199], a[8:11], v[4:7]
ds_read_b128 v[180:183], v230 offset:11264
s_xor_b32 s81, s83, 3456
v_mfma_f32_16x16x32_bf16 v[20:23], v[196:199], a[12:15], v[20:23]
v_lshlrev_b32 v128, 16, v128
v_xor_b32 v128, s91, v128
v_mfma_f32_16x16x32_bf16 v[216:219], v[248:251], a[8:11], v[216:219]
ds_read_b128 v[184:187], v230 offset:13312
s_add_u32 m0, s77, 65536
v_mfma_f32_16x16x32_bf16 v[8:11], v[200:203], a[8:11], v[8:11]
buffer_load_dwordx4 v228, s[32:35], s81 offen offset:0 lds
v_mfma_f32_16x16x32_bf16 v[24:27], v[200:203], a[12:15], v[24:27]
ds_read_b128 v[188:191], v230 offset:15360
v_and_b32 v238, 31, v231
v_mfma_f32_16x16x32_bf16 v[12:15], v[204:207], a[8:11], v[12:15]
v_lshl_add_u32 v238, v238, 1, v240
s_lshl_b32 s85, s72, 8
v_mfma_f32_16x16x32_bf16 v[28:31], v[204:207], a[12:15], v[28:31]
s_add_u32 s85, s85, 149504
v_lshl_add_u32 v238, v238, 2, s85
v_mfma_f32_16x16x32_bf16 v[220:223], v[248:251], a[12:15], v[220:223]
s_waitcnt vmcnt(11) lgkmcnt(4)
s_barrier
v_mfma_f32_16x16x32_bf16 v[0:3], v[160:163], a[16:19], v[0:3]
ds_read_b128 v[192:195], v230 offset:16384
ds_read_b128 a[144:147], v245 offset:49152
v_mfma_f32_16x16x32_bf16 v[16:19], v[160:163], a[20:23], v[16:19]
buffer_load_dwordx4 v252, s[32:35], s81 offen offset:2048 lds
v_mfma_f32_16x16x32_bf16 v[4:7], v[164:167], a[16:19], v[4:7]
ds_read_b128 v[196:199], v230 offset:18432
ds_read_b128 a[148:151], v245 offset:51200
v_mfma_f32_16x16x32_bf16 v[20:23], v[164:167], a[20:23], v[20:23]
ds_write_b32 v238, v128
v_lshrrev_b32 v242, 4, v231
v_mfma_f32_16x16x32_bf16 v[216:219], v[248:251], a[16:19], v[216:219]
ds_read_b128 v[200:203], v230 offset:20480
ds_read_b128 a[152:155], v246 offset:49152
v_mfma_f32_16x16x32_bf16 v[8:11], v[168:171], a[16:19], v[8:11]
buffer_load_dwordx4 v229, s[32:35], s81 offen offset:1024 lds
v_mfma_f32_16x16x32_bf16 v[24:27], v[168:171], a[20:23], v[24:27]
ds_read_b128 v[204:207], v230 offset:22528
ds_read_b128 a[156:159], v246 offset:51200
v_mfma_f32_16x16x32_bf16 v[12:15], v[172:175], a[16:19], v[12:15]
v_lshlrev_b32 v242, 5, v242
v_add_u32 v242, 149504, v242
v_mfma_f32_16x16x32_bf16 v[28:31], v[172:175], a[20:23], v[28:31]
v_lshlrev_b32 v108, 16, v88
v_and_b32 v109, s93, v88
v_mfma_f32_16x16x32_bf16 v[220:223], v[248:251], a[20:23], v[220:223]
v_fmac_f32 v100, v108, v118
s_waitcnt lgkmcnt(9)
v_mfma_f32_16x16x32_bf16 v[0:3], v[176:179], a[24:27], v[0:3]
ds_read_b128 v[160:163], v230 offset:24576
v_fmac_f32 v101, v109, v118
v_mfma_f32_16x16x32_bf16 v[16:19], v[176:179], a[28:31], v[16:19]
buffer_load_dwordx4 v253, s[32:35], s81 offen offset:3072 lds
v_mfma_f32_16x16x32_bf16 v[4:7], v[180:183], a[24:27], v[4:7]
ds_read_b128 v[164:167], v230 offset:26624
s_xor_b32 s80, s82, 344064
v_mfma_f32_16x16x32_bf16 v[20:23], v[180:183], a[28:31], v[20:23]
v_lshlrev_b32 v110, 16, v89
v_and_b32 v111, s93, v89
v_mfma_f32_16x16x32_bf16 v[216:219], v[248:251], a[24:27], v[216:219]
ds_read_b128 v[168:171], v230 offset:28672
s_add_u32 m0, s77, 49152
v_mfma_f32_16x16x32_bf16 v[8:11], v[184:187], a[24:27], v[8:11]
buffer_load_dwordx4 v214, s[52:55], s80 offen offset:0 sc1 lds
v_mfma_f32_16x16x32_bf16 v[24:27], v[184:187], a[28:31], v[24:27]
ds_read_b128 v[172:175], v230 offset:30720
v_fmac_f32 v102, v110, v118
v_mfma_f32_16x16x32_bf16 v[12:15], v[188:191], a[24:27], v[12:15]
v_fmac_f32 v103, v111, v118
v_lshlrev_b32 v108, 16, v90
v_mfma_f32_16x16x32_bf16 v[28:31], v[188:191], a[28:31], v[28:31]
v_and_b32 v109, s93, v90
v_fmac_f32 v104, v108, v118
v_mfma_f32_16x16x32_bf16 v[220:223], v[248:251], a[28:31], v[220:223]
v_fmac_f32 v105, v109, v118
s_waitcnt lgkmcnt(5)
v_mfma_f32_16x16x32_bf16 v[0:3], v[192:195], a[32:35], v[0:3]
ds_read_b128 v[176:179], v230 offset:17408
v_lshlrev_b32 v110, 16, v91
v_mfma_f32_16x16x32_bf16 v[16:19], v[192:195], a[36:39], v[16:19]
buffer_load_dwordx4 v214, s[52:55], s80 offen offset:2048 sc1 lds
v_mfma_f32_16x16x32_bf16 v[4:7], v[196:199], a[32:35], v[4:7]
ds_read_b128 v[180:183], v230 offset:19456
v_and_b32 v111, s93, v91
v_mfma_f32_16x16x32_bf16 v[20:23], v[196:199], a[36:39], v[20:23]
v_fmac_f32 v106, v110, v118
v_fmac_f32 v107, v111, v118
v_mfma_f32_16x16x32_bf16 v[216:219], v[248:251], a[32:35], v[216:219]
ds_read_b128 v[184:187], v230 offset:21504
s_xor_b32 s80, s82, 360448
v_mfma_f32_16x16x32_bf16 v[8:11], v[200:203], a[32:35], v[8:11]
buffer_load_dwordx4 v213, s[52:55], s80 offen offset:1024 sc1 lds
v_mfma_f32_16x16x32_bf16 v[24:27], v[200:203], a[36:39], v[24:27]
ds_read_b128 v[188:191], v230 offset:23552
v_mfma_f32_16x16x32_bf16 v[12:15], v[204:207], a[32:35], v[12:15]
v_mfma_f32_16x16x32_bf16 v[28:31], v[204:207], a[36:39], v[28:31]
v_mfma_f32_16x16x32_bf16 v[220:223], v[248:251], a[36:39], v[220:223]
s_waitcnt lgkmcnt(4)
v_mfma_f32_16x16x32_bf16 v[0:3], v[160:163], a[40:43], v[0:3]
ds_read_b128 v[192:195], v230 offset:25600
v_mfma_f32_16x16x32_bf16 v[16:19], v[160:163], a[44:47], v[16:19]
buffer_load_dwordx4 v213, s[52:55], s80 offen offset:3072 sc1 lds
v_mfma_f32_16x16x32_bf16 v[4:7], v[164:167], a[40:43], v[4:7]
ds_read_b128 v[196:199], v230 offset:27648
s_xor_b32 s81, s83, 3584
v_mfma_f32_16x16x32_bf16 v[20:23], v[164:167], a[44:47], v[20:23]
v_mfma_f32_16x16x32_bf16 v[216:219], v[248:251], a[40:43], v[216:219]
ds_read_b128 v[200:203], v230 offset:29696
s_add_u32 m0, s77, 81920
v_mfma_f32_16x16x32_bf16 v[8:11], v[168:171], a[40:43], v[8:11]
buffer_load_dwordx4 v228, s[32:35], s81 offen offset:0 lds
v_mfma_f32_16x16x32_bf16 v[24:27], v[168:171], a[44:47], v[24:27]
ds_read_b128 v[204:207], v230 offset:31744
v_mfma_f32_16x16x32_bf16 v[12:15], v[172:175], a[40:43], v[12:15]
v_mfma_f32_16x16x32_bf16 v[28:31], v[172:175], a[44:47], v[28:31]
v_mfma_f32_16x16x32_bf16 v[220:223], v[248:251], a[44:47], v[220:223]
s_waitcnt vmcnt(9) lgkmcnt(4)
s_barrier
v_mfma_f32_16x16x32_bf16 v[0:3], v[176:179], a[48:51], v[0:3]
ds_read_b128 v[160:163], v230 offset:32768
ds_read_b128 a[160:163], v156 offset:0
v_mfma_f32_16x16x32_bf16 v[16:19], v[176:179], a[52:55], v[16:19]
buffer_load_dwordx4 v252, s[32:35], s81 offen offset:2048 lds
v_mfma_f32_16x16x32_bf16 v[4:7], v[180:183], a[48:51], v[4:7]
ds_read_b128 v[164:167], v230 offset:34816
ds_read_b128 a[164:167], v156 offset:2048
v_mfma_f32_16x16x32_bf16 v[20:23], v[180:183], a[52:55], v[20:23]
v_mfma_f32_16x16x32_bf16 v[216:219], v[248:251], a[48:51], v[216:219]
ds_read_b128 v[168:171], v230 offset:36864
ds_read_b128 a[168:171], v157 offset:0
)"
R"(
v_mfma_f32_16x16x32_bf16 v[8:11], v[184:187], a[48:51], v[8:11]
buffer_load_dwordx4 v229, s[32:35], s81 offen offset:1024 lds
v_mfma_f32_16x16x32_bf16 v[24:27], v[184:187], a[52:55], v[24:27]
ds_read_b128 v[172:175], v230 offset:38912
ds_read_b128 a[172:175], v157 offset:2048
v_mfma_f32_16x16x32_bf16 v[12:15], v[188:191], a[48:51], v[12:15]
v_lshlrev_b32 v108, 16, v92
v_and_b32 v109, s93, v92
v_mfma_f32_16x16x32_bf16 v[28:31], v[188:191], a[52:55], v[28:31]
v_fmac_f32 v100, v108, v119
v_fmac_f32 v101, v109, v119
v_mfma_f32_16x16x32_bf16 v[220:223], v[248:251], a[52:55], v[220:223]
v_lshlrev_b32 v110, 16, v93
s_waitcnt lgkmcnt(8)
v_mfma_f32_16x16x32_bf16 v[0:3], v[192:195], a[56:59], v[0:3]
ds_read_b128 v[176:179], v230 offset:40960
v_and_b32 v111, s93, v93
v_mfma_f32_16x16x32_bf16 v[16:19], v[192:195], a[60:63], v[16:19]
buffer_load_dwordx4 v253, s[32:35], s81 offen offset:3072 lds
v_mfma_f32_16x16x32_bf16 v[4:7], v[196:199], a[56:59], v[4:7]
ds_read_b128 v[180:183], v230 offset:43008
v_fmac_f32 v102, v110, v119
v_mfma_f32_16x16x32_bf16 v[20:23], v[196:199], a[60:63], v[20:23]
LC4_%=:
s_cbranch_execz LC8_%=
v_fmac_f32 v103, v111, v119
v_lshlrev_b32 v108, 16, v94
v_mfma_f32_16x16x32_bf16 v[216:219], v[248:251], a[56:59], v[216:219]
ds_read_b128 v[184:187], v230 offset:45056
s_xor_b32 s80, s82, 376832
v_mfma_f32_16x16x32_bf16 v[8:11], v[200:203], a[56:59], v[8:11]
buffer_store_dwordx4 v[126:129], v230, s[52:55], s19 offen offset:3072 sc1
v_mfma_f32_16x16x32_bf16 v[24:27], v[200:203], a[60:63], v[24:27]
ds_read_b128 v[188:191], v230 offset:47104
s_add_u32 m0, s77, 0
v_mfma_f32_16x16x32_bf16 v[12:15], v[204:207], a[56:59], v[12:15]
v_and_b32 v109, s93, v94
v_fmac_f32 v104, v108, v119
v_mfma_f32_16x16x32_bf16 v[28:31], v[204:207], a[60:63], v[28:31]
buffer_load_dwordx4 v214, s[52:55], s80 offen offset:0 sc1 lds
v_mfma_f32_16x16x32_bf16 v[220:223], v[248:251], a[60:63], v[220:223]
v_fmac_f32 v105, v109, v119
s_waitcnt lgkmcnt(5)
v_mfma_f32_16x16x32_bf16 v[0:3], v[160:163], a[64:67], v[0:3]
ds_read_b128 v[192:195], v230 offset:33792
v_lshlrev_b32 v110, 16, v95
v_mfma_f32_16x16x32_bf16 v[16:19], v[160:163], a[68:71], v[16:19]
v_and_b32 v111, s93, v95
v_fmac_f32 v106, v110, v119
v_mfma_f32_16x16x32_bf16 v[4:7], v[164:167], a[64:67], v[4:7]
ds_read_b128 v[196:199], v230 offset:35840
v_fmac_f32 v107, v111, v119
v_mfma_f32_16x16x32_bf16 v[20:23], v[164:167], a[68:71], v[20:23]
buffer_load_dwordx4 v214, s[52:55], s80 offen offset:2048 sc1 lds
v_mfma_f32_16x16x32_bf16 v[216:219], v[248:251], a[64:67], v[216:219]
ds_read_b128 v[200:203], v230 offset:37888
v_permlane32_swap_b32 v100, v104
v_mfma_f32_16x16x32_bf16 v[8:11], v[168:171], a[64:67], v[8:11]
v_permlane32_swap_b32 v101, v105
v_permlane32_swap_b32 v102, v106
v_mfma_f32_16x16x32_bf16 v[24:27], v[168:171], a[68:71], v[24:27]
ds_read_b128 v[204:207], v230 offset:39936
s_xor_b32 s80, s82, 409600
v_mfma_f32_16x16x32_bf16 v[12:15], v[172:175], a[64:67], v[12:15]
buffer_load_dwordx4 v213, s[52:55], s80 offen offset:1024 sc1 lds
v_mfma_f32_16x16x32_bf16 v[28:31], v[172:175], a[68:71], v[28:31]
v_permlane32_swap_b32 v103, v107
v_add_f32 v100, v100, v104
v_mfma_f32_16x16x32_bf16 v[220:223], v[248:251], a[68:71], v[220:223]
v_add_f32 v101, v101, v105
s_waitcnt lgkmcnt(4)
v_mfma_f32_16x16x32_bf16 v[0:3], v[176:179], a[72:75], v[0:3]
ds_read_b128 v[160:163], v230 offset:41984
v_add_f32 v102, v102, v106
v_mfma_f32_16x16x32_bf16 v[16:19], v[176:179], a[76:79], v[16:19]
buffer_load_dwordx4 v213, s[52:55], s80 offen offset:3072 sc1 lds
v_mfma_f32_16x16x32_bf16 v[4:7], v[180:183], a[72:75], v[4:7]
ds_read_b128 v[164:167], v230 offset:44032
s_xor_b32 s81, s83, 3712
v_mfma_f32_16x16x32_bf16 v[20:23], v[180:183], a[76:79], v[20:23]
v_add_f32 v103, v103, v107
v_permlane16_swap_b32 v100, v102
v_mfma_f32_16x16x32_bf16 v[216:219], v[248:251], a[72:75], v[216:219]
ds_read_b128 v[168:171], v230 offset:46080
s_add_u32 m0, s77, 98304
v_mfma_f32_16x16x32_bf16 v[8:11], v[184:187], a[72:75], v[8:11]
buffer_load_dwordx4 v228, s[32:35], s81 offen offset:0 lds
v_mfma_f32_16x16x32_bf16 v[24:27], v[184:187], a[76:79], v[24:27]
ds_read_b128 v[172:175], v230 offset:48128
v_permlane16_swap_b32 v101, v103
v_mfma_f32_16x16x32_bf16 v[12:15], v[188:191], a[72:75], v[12:15]
v_add_f32 v100, v100, v102
v_add_f32 v101, v101, v103
v_mfma_f32_16x16x32_bf16 v[28:31], v[188:191], a[76:79], v[28:31]
v_and_b32 v108, 7, v231
v_lshrrev_b32 v109, 4, v231
v_mfma_f32_16x16x32_bf16 v[220:223], v[248:251], a[76:79], v[220:223]
s_waitcnt vmcnt(10) lgkmcnt(4)
s_barrier
v_mfma_f32_16x16x32_bf16 v[0:3], v[192:195], a[80:83], v[0:3]
ds_read_b128 v[176:179], v230 offset:49152
ds_read_b128 a[176:179], v245 offset:0
v_mfma_f32_16x16x32_bf16 v[16:19], v[192:195], a[84:87], v[16:19]
buffer_load_dwordx4 v252, s[32:35], s81 offen offset:2048 lds
v_mfma_f32_16x16x32_bf16 v[4:7], v[196:199], a[80:83], v[4:7]
ds_read_b128 v[180:183], v230 offset:51200
ds_read_b128 a[180:183], v245 offset:2048
v_mfma_f32_16x16x32_bf16 v[20:23], v[196:199], a[84:87], v[20:23]
v_lshl_add_u32 v108, v108, 2, v109
s_lshl_b32 s5, s72, 8
v_mfma_f32_16x16x32_bf16 v[216:219], v[248:251], a[80:83], v[216:219]
ds_read_b128 v[184:187], v230 offset:53248
ds_read_b128 a[184:187], v246 offset:0
v_mfma_f32_16x16x32_bf16 v[8:11], v[200:203], a[80:83], v[8:11]
buffer_load_dwordx4 v229, s[32:35], s81 offen offset:1024 lds
v_mfma_f32_16x16x32_bf16 v[24:27], v[200:203], a[84:87], v[24:27]
ds_read_b128 v[188:191], v230 offset:55296
ds_read_b128 a[188:191], v246 offset:2048
v_mfma_f32_16x16x32_bf16 v[12:15], v[204:207], a[80:83], v[12:15]
s_add_u32 s5, s5, 148480
v_lshl_add_u32 v108, v108, 3, s5
v_mfma_f32_16x16x32_bf16 v[28:31], v[204:207], a[84:87], v[28:31]
v_add_f32_dpp v100, v100, v100 row_ror:8 row_mask:0xf bank_mask:0xf
v_add_f32_dpp v101, v101, v101 row_ror:8 row_mask:0xf bank_mask:0xf
v_mfma_f32_16x16x32_bf16 v[220:223], v[248:251], a[84:87], v[220:223]
ds_write_b64 v108, v[100:101]
s_waitcnt lgkmcnt(9)
v_mfma_f32_16x16x32_bf16 v[0:3], v[160:163], a[88:91], v[0:3]
ds_read_b128 v[192:195], v230 offset:57344
v_mfma_f32_16x16x32_bf16 v[16:19], v[160:163], a[92:95], v[16:19]
buffer_load_dwordx4 v253, s[32:35], s81 offen offset:3072 lds
v_mfma_f32_16x16x32_bf16 v[4:7], v[164:167], a[88:91], v[4:7]
ds_read_b128 v[196:199], v230 offset:59392
s_xor_b32 s80, s82, 425984
v_mfma_f32_16x16x32_bf16 v[20:23], v[164:167], a[92:95], v[20:23]
v_mfma_f32_16x16x32_bf16 v[216:219], v[248:251], a[88:91], v[216:219]
ds_read_b128 v[200:203], v230 offset:61440
s_add_u32 m0, s77, 16384
v_mfma_f32_16x16x32_bf16 v[8:11], v[168:171], a[88:91], v[8:11]
buffer_load_dwordx4 v214, s[52:55], s80 offen offset:0 sc1 lds
v_mfma_f32_16x16x32_bf16 v[24:27], v[168:171], a[92:95], v[24:27]
ds_read_b128 v[204:207], v230 offset:63488
v_mfma_f32_16x16x32_bf16 v[12:15], v[172:175], a[88:91], v[12:15]
v_mfma_f32_16x16x32_bf16 v[28:31], v[172:175], a[92:95], v[28:31]
v_mfma_f32_16x16x32_bf16 v[220:223], v[248:251], a[92:95], v[220:223]
s_waitcnt lgkmcnt(6)
v_mfma_f32_16x16x32_bf16 v[0:3], v[176:179], a[96:99], v[0:3]
ds_read_b128 v[160:163], v230 offset:50176
v_mfma_f32_16x16x32_bf16 v[16:19], v[176:179], a[100:103], v[16:19]
buffer_load_dwordx4 v214, s[52:55], s80 offen offset:2048 sc1 lds
v_mfma_f32_16x16x32_bf16 v[4:7], v[180:183], a[96:99], v[4:7]
ds_read_b128 v[164:167], v230 offset:52224
v_mfma_f32_16x16x32_bf16 v[20:23], v[180:183], a[100:103], v[20:23]
v_mfma_f32_16x16x32_bf16 v[216:219], v[248:251], a[96:99], v[216:219]
ds_read_b128 v[168:171], v230 offset:54272
s_xor_b32 s80, s82, 442368
v_mfma_f32_16x16x32_bf16 v[8:11], v[184:187], a[96:99], v[8:11]
buffer_load_dwordx4 v213, s[52:55], s80 offen offset:1024 sc1 lds
v_mfma_f32_16x16x32_bf16 v[24:27], v[184:187], a[100:103], v[24:27]
ds_read_b128 v[172:175], v230 offset:56320
v_mfma_f32_16x16x32_bf16 v[12:15], v[188:191], a[96:99], v[12:15]
s_waitcnt lgkmcnt(8)
v_mfma_f32_16x16x32_bf16 v[28:31], v[188:191], a[100:103], v[28:31]
v_mfma_f32_16x16x32_bf16 v[220:223], v[248:251], a[100:103], v[220:223]
s_waitcnt lgkmcnt(4)
v_mfma_f32_16x16x32_bf16 v[0:3], v[192:195], a[104:107], v[0:3]
ds_read_b128 v[176:179], v230 offset:58368
v_mfma_f32_16x16x32_bf16 v[16:19], v[192:195], a[108:111], v[16:19]
buffer_load_dwordx4 v213, s[52:55], s80 offen offset:3072 sc1 lds
v_mfma_f32_16x16x32_bf16 v[4:7], v[196:199], a[104:107], v[4:7]
ds_read_b128 v[180:183], v230 offset:60416
s_xor_b32 s81, s83, 3840
v_mfma_f32_16x16x32_bf16 v[20:23], v[196:199], a[108:111], v[20:23]
v_mfma_f32_16x16x32_bf16 v[216:219], v[248:251], a[104:107], v[216:219]
ds_read_b128 v[184:187], v230 offset:62464
s_add_u32 m0, s77, 114688
v_mfma_f32_16x16x32_bf16 v[8:11], v[200:203], a[104:107], v[8:11]
buffer_load_dwordx4 v228, s[32:35], s81 offen offset:0 lds
v_mfma_f32_16x16x32_bf16 v[24:27], v[200:203], a[108:111], v[24:27]
ds_read_b128 v[188:191], v230 offset:64512
v_mfma_f32_16x16x32_bf16 v[12:15], v[204:207], a[104:107], v[12:15]
v_mfma_f32_16x16x32_bf16 v[28:31], v[204:207], a[108:111], v[28:31]
v_mfma_f32_16x16x32_bf16 v[220:223], v[248:251], a[108:111], v[220:223]
s_waitcnt vmcnt(9) lgkmcnt(4)
s_barrier
v_mfma_f32_16x16x32_bf16 v[0:3], v[160:163], a[112:115], v[0:3]
ds_read_b128 v[192:195], v230 offset:0
ds_read_b128 a[192:195], v245 offset:16384
v_mfma_f32_16x16x32_bf16 v[16:19], v[160:163], a[116:119], v[16:19]
buffer_load_dwordx4 v252, s[32:35], s81 offen offset:2048 lds
v_mfma_f32_16x16x32_bf16 v[4:7], v[164:167], a[112:115], v[4:7]
ds_read_b128 v[196:199], v230 offset:2048
ds_read_b128 a[196:199], v245 offset:18432
v_mfma_f32_16x16x32_bf16 v[20:23], v[164:167], a[116:119], v[20:23]
v_lshlrev_b32 v108, 3, v231
v_and_b32 v108, 255, v108
v_mfma_f32_16x16x32_bf16 v[216:219], v[248:251], a[112:115], v[216:219]
ds_read_b128 v[200:203], v230 offset:4096
ds_read_b128 a[200:203], v246 offset:16384
v_mfma_f32_16x16x32_bf16 v[8:11], v[168:171], a[112:115], v[8:11]
buffer_load_dwordx4 v229, s[32:35], s81 offen offset:1024 lds
v_mfma_f32_16x16x32_bf16 v[24:27], v[168:171], a[116:119], v[24:27]
ds_read_b128 v[204:207], v230 offset:6144
ds_read_b128 a[204:207], v246 offset:18432
v_mfma_f32_16x16x32_bf16 v[12:15], v[172:175], a[112:115], v[12:15]
v_add_u32 v108, 148480, v108
s_lshl_b32 s5, s72, 3
v_mfma_f32_16x16x32_bf16 v[28:31], v[172:175], a[116:119], v[28:31]
ds_read_b64 v[100:101], v108 offset:0
ds_read_b64 v[102:103], v108 offset:256
v_mfma_f32_16x16x32_bf16 v[220:223], v[248:251], a[116:119], v[220:223]
ds_read_b64 v[104:105], v108 offset:512
s_waitcnt lgkmcnt(11)
v_mfma_f32_16x16x32_bf16 v[0:3], v[176:179], a[120:123], v[0:3]
ds_read_b128 v[160:163], v230 offset:8192
ds_read_b64 v[106:107], v108 offset:768
v_mfma_f32_16x16x32_bf16 v[16:19], v[176:179], a[124:127], v[16:19]
buffer_load_dwordx4 v253, s[32:35], s81 offen offset:3072 lds
v_mfma_f32_16x16x32_bf16 v[4:7], v[180:183], a[120:123], v[4:7]
ds_read_b128 v[164:167], v230 offset:10240
s_xor_b32 s80, s82, 458752
v_mfma_f32_16x16x32_bf16 v[20:23], v[180:183], a[124:127], v[20:23]
s_lshl_b32 s87, s6, 9
s_add_u32 s87, s87, 50593792
v_mfma_f32_16x16x32_bf16 v[216:219], v[248:251], a[120:123], v[216:219]
ds_read_b128 v[168:171], v230 offset:12288
s_add_u32 m0, s77, 32768
v_mfma_f32_16x16x32_bf16 v[8:11], v[184:187], a[120:123], v[8:11]
buffer_load_dwordx4 v214, s[52:55], s80 offen offset:0 sc1 lds
v_mfma_f32_16x16x32_bf16 v[24:27], v[184:187], a[124:127], v[24:27]
ds_read_b128 v[172:175], v230 offset:14336
v_mfma_f32_16x16x32_bf16 v[12:15], v[188:191], a[120:123], v[12:15]
v_mfma_f32_16x16x32_bf16 v[28:31], v[188:191], a[124:127], v[28:31]
v_mfma_f32_16x16x32_bf16 v[220:223], v[248:251], a[124:127], v[220:223]
s_waitcnt lgkmcnt(9)
v_mfma_f32_16x16x32_bf16 v[0:3], v[192:195], a[128:131], v[0:3]
ds_read_b128 v[176:179], v230 offset:1024
v_mfma_f32_16x16x32_bf16 v[16:19], v[192:195], a[132:135], v[16:19]
buffer_load_dwordx4 v214, s[52:55], s80 offen offset:2048 sc1 lds
v_mfma_f32_16x16x32_bf16 v[4:7], v[196:199], a[128:131], v[4:7]
ds_read_b128 v[180:183], v230 offset:3072
v_mfma_f32_16x16x32_bf16 v[20:23], v[196:199], a[132:135], v[20:23]
v_mfma_f32_16x16x32_bf16 v[216:219], v[248:251], a[128:131], v[216:219]
ds_read_b128 v[184:187], v230 offset:5120
s_xor_b32 s80, s82, 475136
v_mfma_f32_16x16x32_bf16 v[8:11], v[200:203], a[128:131], v[8:11]
buffer_load_dwordx4 v213, s[52:55], s80 offen offset:1024 sc1 lds
v_mfma_f32_16x16x32_bf16 v[24:27], v[200:203], a[132:135], v[24:27]
ds_read_b128 v[188:191], v230 offset:7168
s_waitcnt lgkmcnt(10)
v_add_f32 v100, v100, v102
v_mfma_f32_16x16x32_bf16 v[12:15], v[204:207], a[128:131], v[12:15]
v_add_f32 v101, v101, v103
v_mov_b32 v103, s78
v_mfma_f32_16x16x32_bf16 v[28:31], v[204:207], a[132:135], v[28:31]
s_waitcnt lgkmcnt(7)
v_add_f32 v104, v104, v106
v_add_f32 v105, v105, v107
v_mfma_f32_16x16x32_bf16 v[220:223], v[248:251], a[132:135], v[220:223]
v_add_f32 v100, v100, v104
s_waitcnt lgkmcnt(4)
v_mfma_f32_16x16x32_bf16 v[0:3], v[160:163], a[136:139], v[0:3]
ds_read_b128 v[192:195], v230 offset:9216
v_add_f32 v102, v101, v105
v_mfma_f32_16x16x32_bf16 v[16:19], v[160:163], a[140:143], v[16:19]
buffer_load_dwordx4 v213, s[52:55], s80 offen offset:3072 sc1 lds
v_mfma_f32_16x16x32_bf16 v[4:7], v[164:167], a[136:139], v[4:7]
ds_read_b128 v[196:199], v230 offset:11264
v_mov_b32 v101, s78
v_mfma_f32_16x16x32_bf16 v[20:23], v[164:167], a[140:143], v[20:23]
s_lshl_b64 exec, 0xff, s5
buffer_store_dwordx4 v[100:103], v230, s[52:55], s87 offen sc1
s_mov_b64 exec, -1
v_mfma_f32_16x16x32_bf16 v[216:219], v[248:251], a[136:139], v[216:219]
ds_read_b128 v[200:203], v230 offset:13312
s_xor_b32 s81, s83, 3968
v_mfma_f32_16x16x32_bf16 v[8:11], v[168:171], a[136:139], v[8:11]
v_mfma_f32_16x16x32_bf16 v[24:27], v[168:171], a[140:143], v[24:27]
ds_read_b128 v[204:207], v230 offset:15360
s_add_u32 m0, s77, 131072
v_mfma_f32_16x16x32_bf16 v[12:15], v[172:175], a[136:139], v[12:15]
v_mfma_f32_16x16x32_bf16 v[28:31], v[172:175], a[140:143], v[28:31]
v_mfma_f32_16x16x32_bf16 v[220:223], v[248:251], a[140:143], v[220:223]
s_waitcnt vmcnt(9) lgkmcnt(4)
s_barrier
v_mfma_f32_16x16x32_bf16 v[0:3], v[176:179], a[144:147], v[0:3]
ds_read_b128 v[160:163], v230 offset:16384
ds_read_b128 a[208:211], v245 offset:32768
v_mfma_f32_16x16x32_bf16 v[16:19], v[176:179], a[148:151], v[16:19]
buffer_load_dwordx4 v228, s[32:35], s81 offen offset:0 lds
v_mfma_f32_16x16x32_bf16 v[4:7], v[180:183], a[144:147], v[4:7]
ds_read_b128 v[164:167], v230 offset:18432
ds_read_b128 a[212:215], v245 offset:34816
v_mfma_f32_16x16x32_bf16 v[20:23], v[180:183], a[148:151], v[20:23]
v_mfma_f32_16x16x32_bf16 v[216:219], v[248:251], a[144:147], v[216:219]
ds_read_b128 v[168:171], v230 offset:20480
ds_read_b128 a[216:219], v246 offset:32768
v_mfma_f32_16x16x32_bf16 v[8:11], v[184:187], a[144:147], v[8:11]
buffer_load_dwordx4 v252, s[32:35], s81 offen offset:2048 lds
v_mfma_f32_16x16x32_bf16 v[24:27], v[184:187], a[148:151], v[24:27]
ds_read_b128 v[172:175], v230 offset:22528
ds_read_b128 a[220:223], v246 offset:34816
v_mfma_f32_16x16x32_bf16 v[12:15], v[188:191], a[144:147], v[12:15]
v_mfma_f32_16x16x32_bf16 v[28:31], v[188:191], a[148:151], v[28:31]
v_mfma_f32_16x16x32_bf16 v[220:223], v[248:251], a[148:151], v[220:223]
s_waitcnt lgkmcnt(8)
v_mfma_f32_16x16x32_bf16 v[0:3], v[192:195], a[152:155], v[0:3]
ds_read_b128 v[176:179], v230 offset:24576
v_mfma_f32_16x16x32_bf16 v[16:19], v[192:195], a[156:159], v[16:19]
buffer_load_dwordx4 v229, s[32:35], s81 offen offset:1024 lds
v_mfma_f32_16x16x32_bf16 v[4:7], v[196:199], a[152:155], v[4:7]
ds_read_b128 v[180:183], v230 offset:26624
v_mfma_f32_16x16x32_bf16 v[20:23], v[196:199], a[156:159], v[20:23]
v_mfma_f32_16x16x32_bf16 v[216:219], v[248:251], a[152:155], v[216:219]
ds_read_b128 v[184:187], v230 offset:28672
s_xor_b32 s80, s82, 491520
v_mfma_f32_16x16x32_bf16 v[8:11], v[200:203], a[152:155], v[8:11]
buffer_load_dwordx4 v253, s[32:35], s81 offen offset:3072 lds
v_mfma_f32_16x16x32_bf16 v[24:27], v[200:203], a[156:159], v[24:27]
ds_read_b128 v[188:191], v230 offset:30720
s_add_u32 m0, s77, 65536
v_mfma_f32_16x16x32_bf16 v[12:15], v[204:207], a[152:155], v[12:15]
v_mfma_f32_16x16x32_bf16 v[28:31], v[204:207], a[156:159], v[28:31]
buffer_load_dwordx4 v214, s[52:55], s80 offen offset:0 sc1 lds
v_mfma_f32_16x16x32_bf16 v[220:223], v[248:251], a[156:159], v[220:223]
s_waitcnt lgkmcnt(5)
v_mfma_f32_16x16x32_bf16 v[0:3], v[160:163], a[160:163], v[0:3]
ds_read_b128 v[192:195], v230 offset:17408
v_mfma_f32_16x16x32_bf16 v[16:19], v[160:163], a[164:167], v[16:19]
v_mfma_f32_16x16x32_bf16 v[4:7], v[164:167], a[160:163], v[4:7]
ds_read_b128 v[196:199], v230 offset:19456
v_mfma_f32_16x16x32_bf16 v[20:23], v[164:167], a[164:167], v[20:23]
buffer_load_dwordx4 v214, s[52:55], s80 offen offset:2048 sc1 lds
v_mfma_f32_16x16x32_bf16 v[216:219], v[248:251], a[160:163], v[216:219]
ds_read_b128 v[200:203], v230 offset:21504
v_mfma_f32_16x16x32_bf16 v[8:11], v[168:171], a[160:163], v[8:11]
v_mfma_f32_16x16x32_bf16 v[24:27], v[168:171], a[164:167], v[24:27]
ds_read_b128 v[204:207], v230 offset:23552
s_xor_b32 s80, s82, 507904
v_mfma_f32_16x16x32_bf16 v[12:15], v[172:175], a[160:163], v[12:15]
buffer_load_dwordx4 v213, s[52:55], s80 offen offset:1024 sc1 lds
v_mfma_f32_16x16x32_bf16 v[28:31], v[172:175], a[164:167], v[28:31]
v_mfma_f32_16x16x32_bf16 v[220:223], v[248:251], a[164:167], v[220:223]
s_waitcnt lgkmcnt(4)
v_mfma_f32_16x16x32_bf16 v[0:3], v[176:179], a[168:171], v[0:3]
ds_read_b128 v[160:163], v230 offset:25600
v_mfma_f32_16x16x32_bf16 v[16:19], v[176:179], a[172:175], v[16:19]
buffer_load_dwordx4 v213, s[52:55], s80 offen offset:3072 sc1 lds
v_mfma_f32_16x16x32_bf16 v[4:7], v[180:183], a[168:171], v[4:7]
ds_read_b128 v[164:167], v230 offset:27648
s_xor_b32 s80, s82, 245760
v_mfma_f32_16x16x32_bf16 v[20:23], v[180:183], a[172:175], v[20:23]
v_mfma_f32_16x16x32_bf16 v[216:219], v[248:251], a[168:171], v[216:219]
ds_read_b128 v[168:171], v230 offset:29696
s_add_u32 m0, s77, 49152
v_mfma_f32_16x16x32_bf16 v[8:11], v[184:187], a[168:171], v[8:11]
buffer_load_dwordx4 v215, s[52:55], s80 offen offset:0 sc1 lds
v_mfma_f32_16x16x32_bf16 v[24:27], v[184:187], a[172:175], v[24:27]
ds_read_b128 v[172:175], v230 offset:31744
v_mfma_f32_16x16x32_bf16 v[12:15], v[188:191], a[168:171], v[12:15]
v_mfma_f32_16x16x32_bf16 v[28:31], v[188:191], a[172:175], v[28:31]
v_mfma_f32_16x16x32_bf16 v[220:223], v[248:251], a[172:175], v[220:223]
s_waitcnt vmcnt(10) lgkmcnt(4)
s_barrier
v_mfma_f32_16x16x32_bf16 v[0:3], v[192:195], a[176:179], v[0:3]
ds_read_b128 v[176:179], v230 offset:32768
ds_read_b128 a[224:227], v245 offset:49152
v_mfma_f32_16x16x32_bf16 v[16:19], v[192:195], a[180:183], v[16:19]
v_mfma_f32_16x16x32_bf16 v[4:7], v[196:199], a[176:179], v[4:7]
ds_read_b128 v[180:183], v230 offset:34816
ds_read_b128 a[228:231], v245 offset:51200
v_mfma_f32_16x16x32_bf16 v[20:23], v[196:199], a[180:183], v[20:23]
s_mov_b64 exec, 1
buffer_store_dword v234, off, s[52:55], s90 sc1
s_mov_b64 exec, -1
v_mfma_f32_16x16x32_bf16 v[216:219], v[248:251], a[176:179], v[216:219]
ds_read_b128 v[184:187], v230 offset:36864
ds_read_b128 a[232:235], v246 offset:49152
v_mfma_f32_16x16x32_bf16 v[8:11], v[200:203], a[176:179], v[8:11]
buffer_load_dwordx4 v215, s[52:55], s80 offen offset:2048 sc1 lds
v_mfma_f32_16x16x32_bf16 v[24:27], v[200:203], a[180:183], v[24:27]
ds_read_b128 v[188:191], v230 offset:38912
ds_read_b128 a[236:239], v246 offset:51200
v_mfma_f32_16x16x32_bf16 v[12:15], v[204:207], a[176:179], v[12:15]
v_mfma_f32_16x16x32_bf16 v[28:31], v[204:207], a[180:183], v[28:31]
v_mfma_f32_16x16x32_bf16 v[220:223], v[248:251], a[180:183], v[220:223]
s_waitcnt lgkmcnt(8)
v_mfma_f32_16x16x32_bf16 v[0:3], v[160:163], a[184:187], v[0:3]
ds_read_b128 v[192:195], v230 offset:40960
s_xor_b32 s80, s82, 278528
v_mfma_f32_16x16x32_bf16 v[16:19], v[160:163], a[188:191], v[16:19]
buffer_load_dwordx4 v214, s[52:55], s80 offen offset:1024 sc1 lds
v_mfma_f32_16x16x32_bf16 v[4:7], v[164:167], a[184:187], v[4:7]
ds_read_b128 v[196:199], v230 offset:43008
v_mfma_f32_16x16x32_bf16 v[20:23], v[164:167], a[188:191], v[20:23]
v_mfma_f32_16x16x32_bf16 v[216:219], v[248:251], a[184:187], v[216:219]
ds_read_b128 v[200:203], v230 offset:45056
v_mfma_f32_16x16x32_bf16 v[8:11], v[168:171], a[184:187], v[8:11]
buffer_load_dwordx4 v214, s[52:55], s80 offen offset:3072 sc1 lds
v_mfma_f32_16x16x32_bf16 v[24:27], v[168:171], a[188:191], v[24:27]
)"
R"(
ds_read_b128 v[204:207], v230 offset:47104
v_mfma_f32_16x16x32_bf16 v[12:15], v[172:175], a[184:187], v[12:15]
v_mfma_f32_16x16x32_bf16 v[28:31], v[172:175], a[188:191], v[28:31]
v_mfma_f32_16x16x32_bf16 v[220:223], v[248:251], a[188:191], v[220:223]
s_xor_b32 s80, s82, 393216
s_waitcnt lgkmcnt(5)
v_mfma_f32_16x16x32_bf16 v[0:3], v[176:179], a[192:195], v[0:3]
ds_read_b128 v[160:163], v230 offset:33792
s_add_u32 m0, s77, 81920
v_mfma_f32_16x16x32_bf16 v[16:19], v[176:179], a[196:199], v[16:19]
buffer_load_dwordx4 v215, s[52:55], s80 offen offset:0 sc1 lds
v_mfma_f32_16x16x32_bf16 v[4:7], v[180:183], a[192:195], v[4:7]
ds_read_b128 v[164:167], v230 offset:35840
v_mfma_f32_16x16x32_bf16 v[20:23], v[180:183], a[196:199], v[20:23]
v_mfma_f32_16x16x32_bf16 v[216:219], v[248:251], a[192:195], v[216:219]
ds_read_b128 v[168:171], v230 offset:37888
v_mfma_f32_16x16x32_bf16 v[8:11], v[184:187], a[192:195], v[8:11]
buffer_load_dwordx4 v215, s[52:55], s80 offen offset:2048 sc1 lds
v_mfma_f32_16x16x32_bf16 v[24:27], v[184:187], a[196:199], v[24:27]
ds_read_b128 v[172:175], v230 offset:39936
v_mfma_f32_16x16x32_bf16 v[12:15], v[188:191], a[192:195], v[12:15]
v_mfma_f32_16x16x32_bf16 v[28:31], v[188:191], a[196:199], v[28:31]
v_mfma_f32_16x16x32_bf16 v[220:223], v[248:251], a[196:199], v[220:223]
s_waitcnt lgkmcnt(4)
v_mfma_f32_16x16x32_bf16 v[0:3], v[192:195], a[200:203], v[0:3]
ds_read_b128 v[176:179], v230 offset:41984
s_xor_b32 s80, s82, 294912
v_mfma_f32_16x16x32_bf16 v[16:19], v[192:195], a[204:207], v[16:19]
buffer_load_dwordx4 v214, s[52:55], s80 offen offset:1024 sc1 lds
v_mfma_f32_16x16x32_bf16 v[4:7], v[196:199], a[200:203], v[4:7]
ds_read_b128 v[180:183], v230 offset:44032
v_mfma_f32_16x16x32_bf16 v[20:23], v[196:199], a[204:207], v[20:23]
v_mfma_f32_16x16x32_bf16 v[216:219], v[248:251], a[200:203], v[216:219]
ds_read_b128 v[184:187], v230 offset:46080
v_mfma_f32_16x16x32_bf16 v[8:11], v[200:203], a[200:203], v[8:11]
buffer_load_dwordx4 v214, s[52:55], s80 offen offset:3072 sc1 lds
v_mfma_f32_16x16x32_bf16 v[24:27], v[200:203], a[204:207], v[24:27]
ds_read_b128 v[188:191], v230 offset:48128
s_xor_b32 s80, s82, 311296
v_mfma_f32_16x16x32_bf16 v[12:15], v[204:207], a[200:203], v[12:15]
v_mfma_f32_16x16x32_bf16 v[28:31], v[204:207], a[204:207], v[28:31]
s_add_u32 m0, s77, 0
v_mfma_f32_16x16x32_bf16 v[220:223], v[248:251], a[204:207], v[220:223]
s_waitcnt vmcnt(9) lgkmcnt(4)
s_barrier
v_mfma_f32_16x16x32_bf16 v[0:3], v[160:163], a[208:211], v[0:3]
ds_read_b128 v[192:195], v212 offset:0
ds_read_b128 a[240:243], v156 offset:0
v_mfma_f32_16x16x32_bf16 v[16:19], v[160:163], a[212:215], v[16:19]
buffer_load_dwordx4 v215, s[52:55], s80 offen offset:0 sc1 lds
v_mfma_f32_16x16x32_bf16 v[4:7], v[164:167], a[208:211], v[4:7]
ds_read_b128 v[196:199], v212 offset:2048
ds_read_b128 a[244:247], v156 offset:2048
v_mfma_f32_16x16x32_bf16 v[20:23], v[164:167], a[212:215], v[20:23]
s_cmp_eq_u32 s72, 2
s_cselect_b32 s92, 1.0, 0
v_mfma_f32_16x16x32_bf16 v[216:219], v[248:251], a[208:211], v[216:219]
ds_read_b128 v[200:203], v212 offset:4096
ds_read_b128 a[248:251], v157 offset:0
v_mfma_f32_16x16x32_bf16 v[8:11], v[168:171], a[208:211], v[8:11]
buffer_load_dwordx4 v215, s[52:55], s80 offen offset:2048 sc1 lds
v_mfma_f32_16x16x32_bf16 v[24:27], v[168:171], a[212:215], v[24:27]
ds_read_b128 v[204:207], v212 offset:6144
ds_read_b128 a[252:255], v157 offset:2048
v_mfma_f32_16x16x32_bf16 v[12:15], v[172:175], a[208:211], v[12:15]
s_lshl4_add_u32 s86, s75, s73
s_lshl3_add_u32 s86, s86, s72
v_mfma_f32_16x16x32_bf16 v[28:31], v[172:175], a[212:215], v[28:31]
s_lshl_b32 s86, s86, 15
s_add_u32 s86, s86, 4194304
v_mfma_f32_16x16x32_bf16 v[220:223], v[248:251], a[212:215], v[220:223]
v_add_u32 v228, s86, v230
s_waitcnt lgkmcnt(8)
v_mfma_f32_16x16x32_bf16 v[0:3], v[176:179], a[216:219], v[0:3]
ds_read_b128 v[160:163], v212 offset:8192
s_xor_b32 s80, s82, 327680
v_mfma_f32_16x16x32_bf16 v[16:19], v[176:179], a[220:223], v[16:19]
buffer_load_dwordx4 v214, s[52:55], s80 offen offset:1024 sc1 lds
v_mfma_f32_16x16x32_bf16 v[4:7], v[180:183], a[216:219], v[4:7]
ds_read_b128 v[164:167], v212 offset:10240
v_add_u32 v229, 131072, v228
v_mfma_f32_16x16x32_bf16 v[20:23], v[180:183], a[220:223], v[20:23]
s_lshl_b32 s79, s76, 12
s_lshl_b32 s85, s72, 3
v_mfma_f32_16x16x32_bf16 v[216:219], v[248:251], a[216:219], v[216:219]
ds_read_b128 v[168:171], v212 offset:12288
s_add_u32 s85, s85, s76
v_mfma_f32_16x16x32_bf16 v[8:11], v[184:187], a[216:219], v[8:11]
buffer_load_dwordx4 v214, s[52:55], s80 offen offset:3072 sc1 lds
v_mfma_f32_16x16x32_bf16 v[24:27], v[184:187], a[220:223], v[24:27]
ds_read_b128 v[172:175], v212 offset:14336
s_lshl_b32 s85, s85, 1
v_mfma_f32_16x16x32_bf16 v[12:15], v[188:191], a[216:219], v[12:15]
s_add_u32 s85, s85, s75
s_lshl_b32 s85, s85, 9
v_mfma_f32_16x16x32_bf16 v[28:31], v[188:191], a[220:223], v[28:31]
s_add_u32 s85, s85, 50593792
v_and_b32 v238, 0x1f0, v230
v_mfma_f32_16x16x32_bf16 v[220:223], v[248:251], a[220:223], v[220:223]
s_xor_b32 s80, s82, 344064
s_waitcnt lgkmcnt(5)
v_mfma_f32_16x16x32_bf16 v[0:3], v[192:195], a[224:227], v[0:3]
ds_read_b128 v[176:179], v212 offset:1024
s_add_u32 m0, s77, 98304
v_mfma_f32_16x16x32_bf16 v[16:19], v[192:195], a[228:231], v[16:19]
buffer_load_dwordx4 v215, s[52:55], s80 offen offset:0 sc1 lds
v_mfma_f32_16x16x32_bf16 v[4:7], v[196:199], a[224:227], v[4:7]
ds_read_b128 v[180:183], v212 offset:3072
v_lshrrev_b32 v240, 5, v231
v_mfma_f32_16x16x32_bf16 v[20:23], v[196:199], a[228:231], v[20:23]
v_lshl_add_u32 v238, v240, 16, v238
v_add_u32 v238, s85, v238
v_mfma_f32_16x16x32_bf16 v[216:219], v[248:251], a[224:227], v[216:219]
ds_read_b128 v[184:187], v212 offset:5120
v_mfma_f32_16x16x32_bf16 v[8:11], v[200:203], a[224:227], v[8:11]
buffer_load_dwordx4 v215, s[52:55], s80 offen offset:2048 sc1 lds
v_mfma_f32_16x16x32_bf16 v[24:27], v[200:203], a[228:231], v[24:27]
ds_read_b128 v[188:191], v212 offset:7168
v_mfma_f32_16x16x32_bf16 v[12:15], v[204:207], a[224:227], v[12:15]
v_mfma_f32_16x16x32_bf16 v[28:31], v[204:207], a[228:231], v[28:31]
v_mfma_f32_16x16x32_bf16 v[220:223], v[248:251], a[228:231], v[220:223]
s_waitcnt lgkmcnt(4)
v_mfma_f32_16x16x32_bf16 v[0:3], v[160:163], a[232:235], v[0:3]
ds_read_b128 v[192:195], v212 offset:9216
s_xor_b32 s80, s82, 360448
v_mfma_f32_16x16x32_bf16 v[16:19], v[160:163], a[236:239], v[16:19]
buffer_load_dwordx4 v214, s[52:55], s80 offen offset:1024 sc1 lds
v_mfma_f32_16x16x32_bf16 v[4:7], v[164:167], a[232:235], v[4:7]
ds_read_b128 v[196:199], v212 offset:11264
ds_read_b128 v[144:147], v242 offset:128
v_mfma_f32_16x16x32_bf16 v[20:23], v[164:167], a[236:239], v[20:23]
ds_read_b128 v[148:151], v242 offset:144
ds_read_b128 v[152:155], v242 offset:384
v_mfma_f32_16x16x32_bf16 v[216:219], v[248:251], a[232:235], v[216:219]
ds_read_b128 v[200:203], v212 offset:13312
ds_read_b128 v[156:159], v242 offset:400
v_mfma_f32_16x16x32_bf16 v[8:11], v[168:171], a[232:235], v[8:11]
buffer_load_dwordx4 v214, s[52:55], s80 offen offset:3072 sc1 lds
v_mfma_f32_16x16x32_bf16 v[24:27], v[168:171], a[236:239], v[24:27]
ds_read_b128 v[204:207], v212 offset:15360
s_xor_b32 s80, s82, 376832
v_mfma_f32_16x16x32_bf16 v[12:15], v[172:175], a[232:235], v[12:15]
ds_read_b128 v[128:131], v242 offset:0
ds_read_b128 v[132:135], v242 offset:16
v_mfma_f32_16x16x32_bf16 v[28:31], v[172:175], a[236:239], v[28:31]
ds_read_b128 v[136:139], v242 offset:256
ds_read_b128 v[140:143], v242 offset:272
v_mfma_f32_16x16x32_bf16 v[220:223], v[248:251], a[236:239], v[220:223]
s_waitcnt vmcnt(12) lgkmcnt(12)
s_barrier
v_mfma_f32_16x16x32_bf16 v[0:3], v[176:179], a[240:243], v[0:3]
ds_read_b128 v[160:163], v230 offset:49152
s_add_u32 m0, s77, 16384
v_mfma_f32_16x16x32_bf16 v[16:19], v[176:179], a[244:247], v[16:19]
buffer_load_dwordx4 v215, s[52:55], s80 offen offset:0 sc1 lds
v_mfma_f32_16x16x32_bf16 v[4:7], v[180:183], a[240:243], v[4:7]
ds_read_b128 v[164:167], v230 offset:51200
v_mfma_f32_16x16x32_bf16 v[20:23], v[180:183], a[244:247], v[20:23]
v_mfma_f32_16x16x32_bf16 v[216:219], v[248:251], a[240:243], v[216:219]
ds_read_b128 v[168:171], v230 offset:53248
v_mfma_f32_16x16x32_bf16 v[8:11], v[184:187], a[240:243], v[8:11]
buffer_load_dwordx4 v215, s[52:55], s80 offen offset:2048 sc1 lds
v_mfma_f32_16x16x32_bf16 v[24:27], v[184:187], a[244:247], v[24:27]
ds_read_b128 v[172:175], v230 offset:55296
v_mfma_f32_16x16x32_bf16 v[12:15], v[188:191], a[240:243], v[12:15]
v_mfma_f32_16x16x32_bf16 v[28:31], v[188:191], a[244:247], v[28:31]
v_mfma_f32_16x16x32_bf16 v[220:223], v[248:251], a[244:247], v[220:223]
s_waitcnt lgkmcnt(8)
v_mfma_f32_16x16x32_bf16 v[0:3], v[192:195], a[248:251], v[0:3]
ds_read_b128 v[176:179], v230 offset:57344
s_xor_b32 s80, s82, 409600
v_mfma_f32_16x16x32_bf16 v[16:19], v[192:195], a[252:255], v[16:19]
buffer_load_dwordx4 v214, s[52:55], s80 offen offset:1024 sc1 lds
v_mfma_f32_16x16x32_bf16 v[4:7], v[196:199], a[248:251], v[4:7]
ds_read_b128 v[180:183], v230 offset:59392
v_mfma_f32_16x16x32_bf16 v[20:23], v[196:199], a[252:255], v[20:23]
v_mfma_f32_16x16x32_bf16 v[216:219], v[248:251], a[248:251], v[216:219]
ds_read_b128 v[184:187], v230 offset:61440
v_mfma_f32_16x16x32_bf16 v[8:11], v[200:203], a[248:251], v[8:11]
buffer_load_dwordx4 v214, s[52:55], s80 offen offset:3072 sc1 lds
LC5_%=:
s_cbranch_execz LRETA_%=
v_mfma_f32_16x16x32_bf16 v[24:27], v[200:203], a[252:255], v[24:27]
ds_read_b128 v[188:191], v230 offset:63488
v_mfma_f32_16x16x32_bf16 v[12:15], v[204:207], a[248:251], v[12:15]
v_mfma_f32_16x16x32_bf16 v[28:31], v[204:207], a[252:255], v[28:31]
v_mfma_f32_16x16x32_bf16 v[220:223], v[248:251], a[252:255], v[220:223]
s_xor_b32 s80, s82, 425984
s_waitcnt lgkmcnt(4)
v_mfma_f32_16x16x32_bf16 v[32:35], v[160:163], a[0:3], v[32:35]
ds_read_b128 v[192:195], v230 offset:50176
s_add_u32 m0, s77, 114688
v_mfma_f32_16x16x32_bf16 v[48:51], v[160:163], a[4:7], v[48:51]
buffer_load_dwordx4 v215, s[52:55], s80 offen offset:0 sc1 lds
v_mfma_f32_16x16x32_bf16 v[36:39], v[164:167], a[0:3], v[36:39]
ds_read_b128 v[196:199], v230 offset:52224
v_mfma_f32_16x16x32_bf16 v[52:55], v[164:167], a[4:7], v[52:55]
v_mfma_f32_16x16x32_bf16 v[40:43], v[168:171], a[0:3], v[40:43]
ds_read_b128 v[200:203], v230 offset:54272
v_mul_f32 v0, v0, v144
v_mul_f32 v1, v1, v145
v_mfma_f32_16x16x32_bf16 v[56:59], v[168:171], a[4:7], v[56:59]
buffer_load_dwordx4 v215, s[52:55], s80 offen offset:2048 sc1 lds
v_mul_f32 v2, v2, v146
v_mul_f32 v3, v3, v147
v_mfma_f32_16x16x32_bf16 v[44:47], v[172:175], a[0:3], v[44:47]
ds_read_b128 v[204:207], v230 offset:56320
v_mul_f32 v4, v4, v148
v_mfma_f32_16x16x32_bf16 v[60:63], v[172:175], a[4:7], v[60:63]
v_mul_f32 v5, v5, v149
v_mul_f32 v6, v6, v150
s_waitcnt lgkmcnt(4)
v_mfma_f32_16x16x32_bf16 v[32:35], v[176:179], a[8:11], v[32:35]
ds_read_b128 v[160:163], v230 offset:58368
s_xor_b32 s80, s82, 442368
v_mul_f32 v7, v7, v151
v_mfma_f32_16x16x32_bf16 v[48:51], v[176:179], a[12:15], v[48:51]
buffer_load_dwordx4 v214, s[52:55], s80 offen offset:1024 sc1 lds
v_fmac_f32 v0, v216, v128
v_fmac_f32 v1, v216, v129
v_mfma_f32_16x16x32_bf16 v[36:39], v[180:183], a[8:11], v[36:39]
ds_read_b128 v[164:167], v230 offset:60416
v_fmac_f32 v2, v216, v130
v_mfma_f32_16x16x32_bf16 v[52:55], v[180:183], a[12:15], v[52:55]
v_fmac_f32 v3, v216, v131
v_fmac_f32 v4, v216, v132
v_mfma_f32_16x16x32_bf16 v[40:43], v[184:187], a[8:11], v[40:43]
ds_read_b128 v[168:171], v230 offset:62464
v_fmac_f32 v5, v216, v133
v_fmac_f32 v6, v216, v134
v_mfma_f32_16x16x32_bf16 v[56:59], v[184:187], a[12:15], v[56:59]
buffer_load_dwordx4 v214, s[52:55], s80 offen offset:3072 sc1 lds
v_fmac_f32 v7, v216, v135
v_mfma_f32_16x16x32_bf16 v[44:47], v[188:191], a[8:11], v[44:47]
ds_read_b128 v[172:175], v230 offset:64512
v_cvt_pk_bf16_f32 v0, v0, v1
v_cvt_pk_bf16_f32 v1, v2, v3
v_mfma_f32_16x16x32_bf16 v[60:63], v[188:191], a[12:15], v[60:63]
v_cvt_pk_bf16_f32 v2, v4, v5
s_waitcnt vmcnt(16) lgkmcnt(4)
s_barrier
v_mfma_f32_16x16x32_bf16 v[32:35], v[192:195], a[16:19], v[32:35]
ds_read_b128 v[176:179], v212 offset:16384
v_cvt_pk_bf16_f32 v3, v6, v7
v_and_b32 v0, -2, v0
v_mfma_f32_16x16x32_bf16 v[48:51], v[192:195], a[20:23], v[48:51]
buffer_store_dwordx4 v[0:3], v228, s[52:55], s79 offen offset:0 sc1
v_mfma_f32_16x16x32_bf16 v[36:39], v[196:199], a[16:19], v[36:39]
ds_read_b128 v[180:183], v212 offset:18432
s_xor_b32 s80, s82, 458752
v_mul_f32 v8, v8, v152
v_mul_f32 v9, v9, v153
v_mfma_f32_16x16x32_bf16 v[52:55], v[196:199], a[20:23], v[52:55]
v_mul_f32 v10, v10, v154
v_mul_f32 v11, v11, v155
v_mfma_f32_16x16x32_bf16 v[40:43], v[200:203], a[16:19], v[40:43]
ds_read_b128 v[184:187], v212 offset:20480
s_add_u32 m0, s77, 32768
v_mul_f32 v12, v12, v156
v_mfma_f32_16x16x32_bf16 v[56:59], v[200:203], a[20:23], v[56:59]
buffer_load_dwordx4 v215, s[52:55], s80 offen offset:0 sc1 lds
v_mul_f32 v13, v13, v157
v_mul_f32 v14, v14, v158
v_mfma_f32_16x16x32_bf16 v[44:47], v[204:207], a[16:19], v[44:47]
ds_read_b128 v[188:191], v212 offset:22528
v_mul_f32 v15, v15, v159
v_mfma_f32_16x16x32_bf16 v[60:63], v[204:207], a[20:23], v[60:63]
v_fmac_f32 v8, v216, v136
v_fmac_f32 v9, v216, v137
s_waitcnt lgkmcnt(4)
v_mfma_f32_16x16x32_bf16 v[32:35], v[160:163], a[24:27], v[32:35]
ds_read_b128 v[192:195], v212 offset:24576
v_fmac_f32 v10, v216, v138
v_mfma_f32_16x16x32_bf16 v[48:51], v[160:163], a[28:31], v[48:51]
buffer_load_dwordx4 v215, s[52:55], s80 offen offset:2048 sc1 lds
v_fmac_f32 v11, v216, v139
v_fmac_f32 v12, v216, v140
v_mfma_f32_16x16x32_bf16 v[36:39], v[164:167], a[24:27], v[36:39]
ds_read_b128 v[196:199], v212 offset:26624
v_fmac_f32 v13, v216, v141
v_fmac_f32 v14, v216, v142
v_mfma_f32_16x16x32_bf16 v[52:55], v[164:167], a[28:31], v[52:55]
v_fmac_f32 v15, v216, v143
v_cvt_pk_bf16_f32 v8, v8, v9
v_mfma_f32_16x16x32_bf16 v[40:43], v[168:171], a[24:27], v[40:43]
ds_read_b128 v[200:203], v212 offset:28672
s_xor_b32 s80, s82, 475136
v_cvt_pk_bf16_f32 v9, v10, v11
v_mfma_f32_16x16x32_bf16 v[56:59], v[168:171], a[28:31], v[56:59]
buffer_load_dwordx4 v214, s[52:55], s80 offen offset:1024 sc1 lds
v_cvt_pk_bf16_f32 v10, v12, v13
v_mfma_f32_16x16x32_bf16 v[44:47], v[172:175], a[24:27], v[44:47]
ds_read_b128 v[204:207], v212 offset:30720
v_cvt_pk_bf16_f32 v11, v14, v15
v_and_b32 v8, -2, v8
v_mfma_f32_16x16x32_bf16 v[60:63], v[172:175], a[28:31], v[60:63]
v_mul_f32 v16, v16, v144
s_waitcnt lgkmcnt(4)
v_mfma_f32_16x16x32_bf16 v[32:35], v[176:179], a[32:35], v[32:35]
ds_read_b128 v[160:163], v212 offset:17408
v_mul_f32 v17, v17, v145
buffer_store_dwordx4 v[8:11], v228, s[52:55], s79 offen offset:1024 sc1
v_mfma_f32_16x16x32_bf16 v[48:51], v[176:179], a[36:39], v[48:51]
v_mul_f32 v18, v18, v146
v_mul_f32 v19, v19, v147
v_mfma_f32_16x16x32_bf16 v[36:39], v[180:183], a[32:35], v[36:39]
ds_read_b128 v[164:167], v212 offset:19456
v_mul_f32 v20, v20, v148
v_mfma_f32_16x16x32_bf16 v[52:55], v[180:183], a[36:39], v[52:55]
v_mul_f32 v21, v21, v149
v_mul_f32 v22, v22, v150
v_mfma_f32_16x16x32_bf16 v[40:43], v[184:187], a[32:35], v[40:43]
ds_read_b128 v[168:171], v212 offset:21504
v_mul_f32 v23, v23, v151
v_mfma_f32_16x16x32_bf16 v[56:59], v[184:187], a[36:39], v[56:59]
buffer_load_dwordx4 v214, s[52:55], s80 offen offset:3072 sc1 lds
v_fmac_f32 v16, v220, v128
v_fmac_f32 v17, v220, v129
v_mfma_f32_16x16x32_bf16 v[44:47], v[188:191], a[32:35], v[44:47]
ds_read_b128 v[172:175], v212 offset:23552
v_fmac_f32 v18, v220, v130
v_mfma_f32_16x16x32_bf16 v[60:63], v[188:191], a[36:39], v[60:63]
s_xor_b32 s80, s82, 491520
v_fmac_f32 v19, v220, v131
v_fmac_f32 v20, v220, v132
s_waitcnt lgkmcnt(4)
v_mfma_f32_16x16x32_bf16 v[32:35], v[192:195], a[40:43], v[32:35]
ds_read_b128 v[176:179], v212 offset:25600
s_add_u32 m0, s77, 65536
v_fmac_f32 v21, v220, v133
v_fmac_f32 v22, v220, v134
v_mfma_f32_16x16x32_bf16 v[48:51], v[192:195], a[44:47], v[48:51]
buffer_load_dwordx4 v215, s[52:55], s80 offen offset:0 sc1 lds
v_fmac_f32 v23, v220, v135
v_mfma_f32_16x16x32_bf16 v[36:39], v[196:199], a[40:43], v[36:39]
ds_read_b128 v[180:183], v212 offset:27648
v_cvt_pk_bf16_f32 v16, v16, v17
v_cvt_pk_bf16_f32 v17, v18, v19
v_mfma_f32_16x16x32_bf16 v[52:55], v[196:199], a[44:47], v[52:55]
v_cvt_pk_bf16_f32 v18, v20, v21
v_cvt_pk_bf16_f32 v19, v22, v23
v_mfma_f32_16x16x32_bf16 v[40:43], v[200:203], a[40:43], v[40:43]
ds_read_b128 v[184:187], v212 offset:29696
v_and_b32 v16, -2, v16
v_mfma_f32_16x16x32_bf16 v[56:59], v[200:203], a[44:47], v[56:59]
buffer_store_dwordx4 v[16:19], v229, s[52:55], s79 offen offset:0 sc1
v_mfma_f32_16x16x32_bf16 v[44:47], v[204:207], a[40:43], v[44:47]
ds_read_b128 v[188:191], v212 offset:31744
v_mul_f32 v24, v24, v152
v_mul_f32 v25, v25, v153
v_mfma_f32_16x16x32_bf16 v[60:63], v[204:207], a[44:47], v[60:63]
v_mul_f32 v26, v26, v154
v_mul_f32 v27, v27, v155
s_waitcnt vmcnt(20) lgkmcnt(4)
s_barrier
v_mfma_f32_16x16x32_bf16 v[32:35], v[160:163], a[48:51], v[32:35]
ds_read_b128 v[192:195], v230 offset:0
v_mul_f32 v28, v28, v156
v_mfma_f32_16x16x32_bf16 v[48:51], v[160:163], a[52:55], v[48:51]
v_mul_f32 v29, v29, v157
v_mul_f32 v30, v30, v158
v_mfma_f32_16x16x32_bf16 v[36:39], v[164:167], a[48:51], v[36:39]
ds_read_b128 v[196:199], v230 offset:2048
v_mul_f32 v31, v31, v159
v_mfma_f32_16x16x32_bf16 v[52:55], v[164:167], a[52:55], v[52:55]
buffer_load_dword v244, v243, s[52:55], s24 offen sc1
v_fmac_f32 v24, v220, v136
v_fmac_f32 v25, v220, v137
v_mfma_f32_16x16x32_bf16 v[40:43], v[168:171], a[48:51], v[40:43]
ds_read_b128 v[200:203], v230 offset:4096
v_fmac_f32 v26, v220, v138
v_mfma_f32_16x16x32_bf16 v[56:59], v[168:171], a[52:55], v[56:59]
buffer_load_dwordx4 v215, s[52:55], s80 offen offset:2048 sc1 lds
v_fmac_f32 v27, v220, v139
v_fmac_f32 v28, v220, v140
v_mfma_f32_16x16x32_bf16 v[44:47], v[172:175], a[48:51], v[44:47]
ds_read_b128 v[204:207], v230 offset:6144
v_fmac_f32 v29, v220, v141
v_fmac_f32 v30, v220, v142
v_mfma_f32_16x16x32_bf16 v[60:63], v[172:175], a[52:55], v[60:63]
v_fmac_f32 v31, v220, v143
s_waitcnt lgkmcnt(4)
v_mfma_f32_16x16x32_bf16 v[32:35], v[176:179], a[56:59], v[32:35]
ds_read_b128 v[160:163], v230 offset:8192
v_cvt_pk_bf16_f32 v24, v24, v25
v_cvt_pk_bf16_f32 v25, v26, v27
v_mfma_f32_16x16x32_bf16 v[48:51], v[176:179], a[60:63], v[48:51]
v_cvt_pk_bf16_f32 v26, v28, v29
v_cvt_pk_bf16_f32 v27, v30, v31
v_mfma_f32_16x16x32_bf16 v[36:39], v[180:183], a[56:59], v[36:39]
ds_read_b128 v[164:167], v230 offset:10240
v_and_b32 v24, -2, v24
v_mfma_f32_16x16x32_bf16 v[52:55], v[180:183], a[60:63], v[52:55]
buffer_store_dwordx4 v[24:27], v229, s[52:55], s79 offen offset:1024 sc1
v_mfma_f32_16x16x32_bf16 v[40:43], v[184:187], a[56:59], v[40:43]
ds_read_b128 v[168:171], v230 offset:12288
s_xor_b32 s80, s82, 507904
v_mfma_f32_16x16x32_bf16 v[56:59], v[184:187], a[60:63], v[56:59]
buffer_load_dwordx4 v214, s[52:55], s80 offen offset:1024 sc1 lds
v_mfma_f32_16x16x32_bf16 v[44:47], v[188:191], a[56:59], v[44:47]
)"
R"(
ds_read_b128 v[172:175], v230 offset:14336
ds_read_b128 v[144:147], v242 offset:640
v_mfma_f32_16x16x32_bf16 v[60:63], v[188:191], a[60:63], v[60:63]
s_bfe_u32 s82, s4, 262148
s_waitcnt lgkmcnt(5)
v_mfma_f32_16x16x32_bf16 v[32:35], v[192:195], a[64:67], v[32:35]
ds_read_b128 v[176:179], v230 offset:1024
s_lshl_b32 s82, s82, 8
v_mfma_f32_16x16x32_bf16 v[48:51], v[192:195], a[68:71], v[48:51]
buffer_load_dwordx4 v214, s[52:55], s80 offen offset:3072 sc1 lds
v_mfma_f32_16x16x32_bf16 v[36:39], v[196:199], a[64:67], v[36:39]
ds_read_b128 v[180:183], v230 offset:3072
s_xor_b32 s80, s82, 0
v_mfma_f32_16x16x32_bf16 v[52:55], v[196:199], a[68:71], v[52:55]
ds_read_b128 v[148:151], v242 offset:656
ds_read_b128 v[152:155], v242 offset:896
v_mfma_f32_16x16x32_bf16 v[40:43], v[200:203], a[64:67], v[40:43]
ds_read_b128 v[184:187], v230 offset:5120
s_add_u32 m0, s77, 49152
v_mfma_f32_16x16x32_bf16 v[56:59], v[200:203], a[68:71], v[56:59]
buffer_load_dwordx4 v224, s[36:39], s80 offen offset:0 lds
v_mfma_f32_16x16x32_bf16 v[44:47], v[204:207], a[64:67], v[44:47]
ds_read_b128 v[188:191], v230 offset:7168
ds_read_b128 v[156:159], v242 offset:912
v_mfma_f32_16x16x32_bf16 v[60:63], v[204:207], a[68:71], v[60:63]
ds_read_b128 v[128:131], v242 offset:512
s_waitcnt lgkmcnt(9)
v_mfma_f32_16x16x32_bf16 v[32:35], v[160:163], a[72:75], v[32:35]
ds_read_b128 v[192:195], v230 offset:9216
ds_read_b128 v[132:135], v242 offset:528
v_mfma_f32_16x16x32_bf16 v[48:51], v[160:163], a[76:79], v[48:51]
buffer_load_dwordx4 v225, s[36:39], s80 offen offset:1024 lds
v_mfma_f32_16x16x32_bf16 v[36:39], v[164:167], a[72:75], v[36:39]
ds_read_b128 v[196:199], v230 offset:11264
ds_read_b128 v[136:139], v242 offset:768
v_mfma_f32_16x16x32_bf16 v[52:55], v[164:167], a[76:79], v[52:55]
ds_read_b128 v[140:143], v242 offset:784
s_lshl_b32 s85, s73, 3
v_mfma_f32_16x16x32_bf16 v[40:43], v[168:171], a[72:75], v[40:43]
ds_read_b128 v[200:203], v230 offset:13312
s_add_u32 s85, s85, s72
v_mfma_f32_16x16x32_bf16 v[56:59], v[168:171], a[76:79], v[56:59]
buffer_load_dwordx4 v226, s[36:39], s80 offen offset:2048 lds
v_mfma_f32_16x16x32_bf16 v[44:47], v[172:175], a[72:75], v[44:47]
ds_read_b128 v[204:207], v230 offset:15360
s_lshl_b32 s85, s85, 7
v_mfma_f32_16x16x32_bf16 v[60:63], v[172:175], a[76:79], v[60:63]
s_waitcnt vmcnt(24) lgkmcnt(9)
s_barrier
v_mfma_f32_16x16x32_bf16 v[32:35], v[176:179], a[80:83], v[32:35]
ds_read_b128 v[160:163], v212 offset:32768
s_add_u32 s85, s85, s76
v_mfma_f32_16x16x32_bf16 v[48:51], v[176:179], a[84:87], v[48:51]
buffer_load_dwordx4 v227, s[36:39], s80 offen offset:3072 lds
v_mfma_f32_16x16x32_bf16 v[36:39], v[180:183], a[80:83], v[36:39]
ds_read_b128 v[164:167], v212 offset:34816
s_xor_b32 s80, s82, 256
v_mfma_f32_16x16x32_bf16 v[52:55], v[180:183], a[84:87], v[52:55]
s_lshl_b32 s85, s85, 1
s_add_u32 s85, s85, s75
v_mfma_f32_16x16x32_bf16 v[40:43], v[184:187], a[80:83], v[40:43]
ds_read_b128 v[168:171], v212 offset:36864
s_add_u32 m0, s77, 81920
v_mfma_f32_16x16x32_bf16 v[56:59], v[184:187], a[84:87], v[56:59]
buffer_load_dwordx4 v224, s[36:39], s80 offen offset:0 lds
v_mfma_f32_16x16x32_bf16 v[44:47], v[188:191], a[80:83], v[44:47]
ds_read_b128 v[172:175], v212 offset:38912
s_lshl_b32 s85, s85, 7
v_mfma_f32_16x16x32_bf16 v[60:63], v[188:191], a[84:87], v[60:63]
v_and_b32 v245, 15, v231
s_waitcnt lgkmcnt(4)
v_mfma_f32_16x16x32_bf16 v[32:35], v[192:195], a[88:91], v[32:35]
ds_read_b128 v[176:179], v212 offset:40960
v_lshl_add_u32 v245, v245, 11, s85
v_mfma_f32_16x16x32_bf16 v[48:51], v[192:195], a[92:95], v[48:51]
buffer_load_dwordx4 v225, s[36:39], s80 offen offset:1024 lds
v_mfma_f32_16x16x32_bf16 v[36:39], v[196:199], a[88:91], v[36:39]
ds_read_b128 v[180:183], v212 offset:43008
v_mul_f32 v0, v216, v128
v_mfma_f32_16x16x32_bf16 v[52:55], v[196:199], a[92:95], v[52:55]
v_mul_f32 v1, v216, v129
v_mul_f32 v2, v216, v130
v_mfma_f32_16x16x32_bf16 v[40:43], v[200:203], a[88:91], v[40:43]
ds_read_b128 v[184:187], v212 offset:45056
v_mul_f32 v3, v216, v131
v_mfma_f32_16x16x32_bf16 v[56:59], v[200:203], a[92:95], v[56:59]
buffer_load_dwordx4 v226, s[36:39], s80 offen offset:2048 lds
v_mfma_f32_16x16x32_bf16 v[44:47], v[204:207], a[88:91], v[44:47]
ds_read_b128 v[188:191], v212 offset:47104
v_mul_f32 v4, v216, v132
v_mfma_f32_16x16x32_bf16 v[60:63], v[204:207], a[92:95], v[60:63]
v_mul_f32 v5, v216, v133
s_waitcnt lgkmcnt(4)
v_mfma_f32_16x16x32_bf16 v[32:35], v[160:163], a[96:99], v[32:35]
ds_read_b128 v[192:195], v212 offset:33792
v_mul_f32 v6, v216, v134
v_mfma_f32_16x16x32_bf16 v[48:51], v[160:163], a[100:103], v[48:51]
buffer_load_dwordx4 v227, s[36:39], s80 offen offset:3072 lds
v_mfma_f32_16x16x32_bf16 v[36:39], v[164:167], a[96:99], v[36:39]
ds_read_b128 v[196:199], v212 offset:35840
v_mul_f32 v7, v216, v135
v_mfma_f32_16x16x32_bf16 v[52:55], v[164:167], a[100:103], v[52:55]
v_mul_f32 v8, v216, v136
v_mul_f32 v9, v216, v137
v_mfma_f32_16x16x32_bf16 v[40:43], v[168:171], a[96:99], v[40:43]
ds_read_b128 v[200:203], v212 offset:37888
v_mul_f32 v10, v216, v138
v_mfma_f32_16x16x32_bf16 v[56:59], v[168:171], a[100:103], v[56:59]
v_mul_f32 v11, v216, v139
v_mul_f32 v12, v216, v140
v_mfma_f32_16x16x32_bf16 v[44:47], v[172:175], a[96:99], v[44:47]
ds_read_b128 v[204:207], v212 offset:39936
v_mul_f32 v13, v216, v141
v_mfma_f32_16x16x32_bf16 v[60:63], v[172:175], a[100:103], v[60:63]
v_mul_f32 v14, v216, v142
s_waitcnt lgkmcnt(4)
v_mfma_f32_16x16x32_bf16 v[32:35], v[176:179], a[104:107], v[32:35]
ds_read_b128 v[160:163], v212 offset:41984
v_mul_f32 v15, v216, v143
v_mfma_f32_16x16x32_bf16 v[48:51], v[176:179], a[108:111], v[48:51]
v_mul_f32 v16, v220, v128
v_mul_f32 v17, v220, v129
v_mfma_f32_16x16x32_bf16 v[36:39], v[180:183], a[104:107], v[36:39]
ds_read_b128 v[164:167], v212 offset:44032
v_mul_f32 v18, v220, v130
v_mfma_f32_16x16x32_bf16 v[52:55], v[180:183], a[108:111], v[52:55]
v_mul_f32 v19, v220, v131
v_mul_f32 v20, v220, v132
v_mfma_f32_16x16x32_bf16 v[40:43], v[184:187], a[104:107], v[40:43]
ds_read_b128 v[168:171], v212 offset:46080
s_xor_b32 s80, s82, 512
v_mfma_f32_16x16x32_bf16 v[56:59], v[184:187], a[108:111], v[56:59]
v_mul_f32 v21, v220, v133
v_mul_f32 v22, v220, v134
v_mfma_f32_16x16x32_bf16 v[44:47], v[188:191], a[104:107], v[44:47]
ds_read_b128 v[172:175], v212 offset:48128
s_add_u32 m0, s77, 0
v_mfma_f32_16x16x32_bf16 v[60:63], v[188:191], a[108:111], v[60:63]
s_waitcnt vmcnt(25) lgkmcnt(4)
s_barrier
v_mfma_f32_16x16x32_bf16 v[32:35], v[192:195], a[112:115], v[32:35]
ds_read_b128 v[176:179], v230 offset:16384
v_and_b32 v254, 31, v231
v_mfma_f32_16x16x32_bf16 v[48:51], v[192:195], a[116:119], v[48:51]
buffer_load_dwordx4 v224, s[36:39], s80 offen offset:0 lds
v_mfma_f32_16x16x32_bf16 v[36:39], v[196:199], a[112:115], v[36:39]
ds_read_b128 v[180:183], v230 offset:18432
v_lshl_add_u32 v254, v254, 7, s21
v_mfma_f32_16x16x32_bf16 v[52:55], v[196:199], a[116:119], v[52:55]
s_waitcnt vmcnt(13)
v_cmp_ne_u32 vcc, s78, v244
s_nop 0
s_cbranch_vccz LRpo_%=
LSpo_%=:
s_sub_u32 s84, s84, 1
s_cbranch_scc1 LZpo_%=
buffer_load_dword v244, v243, s[52:55], s24 offen sc1
s_waitcnt vmcnt(0)
v_cmp_ne_u32 vcc, s78, v244
s_nop 0
s_cbranch_vccnz LSpo_%=
LZpo_%=:
s_max_i32 s84, s84, 0
LRpo_%=:
v_mfma_f32_16x16x32_bf16 v[40:43], v[200:203], a[112:115], v[40:43]
ds_read_b128 v[184:187], v230 offset:20480
v_mul_f32 v23, v220, v135
v_mfma_f32_16x16x32_bf16 v[56:59], v[200:203], a[116:119], v[56:59]
buffer_load_dwordx4 v225, s[36:39], s80 offen offset:1024 lds
v_mfma_f32_16x16x32_bf16 v[44:47], v[204:207], a[112:115], v[44:47]
ds_read_b128 v[188:191], v230 offset:22528
v_mul_f32 v24, v220, v136
v_mfma_f32_16x16x32_bf16 v[60:63], v[204:207], a[116:119], v[60:63]
v_mul_f32 v25, v220, v137
s_waitcnt lgkmcnt(4)
v_mfma_f32_16x16x32_bf16 v[32:35], v[160:163], a[120:123], v[32:35]
ds_read_b128 v[192:195], v230 offset:24576
v_mul_f32 v26, v220, v138
v_mfma_f32_16x16x32_bf16 v[48:51], v[160:163], a[124:127], v[48:51]
buffer_load_dwordx4 v226, s[36:39], s80 offen offset:2048 lds
v_mfma_f32_16x16x32_bf16 v[36:39], v[164:167], a[120:123], v[36:39]
ds_read_b128 v[196:199], v230 offset:26624
v_mul_f32 v27, v220, v139
v_mfma_f32_16x16x32_bf16 v[52:55], v[164:167], a[124:127], v[52:55]
v_mul_f32 v28, v220, v140
v_mul_f32 v29, v220, v141
v_mfma_f32_16x16x32_bf16 v[40:43], v[168:171], a[120:123], v[40:43]
ds_read_b128 v[200:203], v230 offset:28672
v_mul_f32 v30, v220, v142
v_mfma_f32_16x16x32_bf16 v[56:59], v[168:171], a[124:127], v[56:59]
buffer_load_dwordx4 v227, s[36:39], s80 offen offset:3072 lds
v_mfma_f32_16x16x32_bf16 v[44:47], v[172:175], a[120:123], v[44:47]
ds_read_b128 v[204:207], v230 offset:30720
v_mul_f32 v31, v220, v143
v_mfma_f32_16x16x32_bf16 v[60:63], v[172:175], a[124:127], v[60:63]
v_and_b32 v241, 16, v231
s_waitcnt lgkmcnt(4)
v_mfma_f32_16x16x32_bf16 v[32:35], v[176:179], a[128:131], v[32:35]
ds_read_b128 v[160:163], v230 offset:17408
v_lshl_add_u32 v245, v241, 1, v245
v_mfma_f32_16x16x32_bf16 v[48:51], v[176:179], a[132:135], v[48:51]
v_lshrrev_b32 v241, 5, v231
v_lshl_add_u32 v245, v241, 4, v245
v_mfma_f32_16x16x32_bf16 v[36:39], v[180:183], a[128:131], v[36:39]
ds_read_b128 v[164:167], v230 offset:19456
v_add_u32 v246, 131072, v245
v_mfma_f32_16x16x32_bf16 v[52:55], v[180:183], a[132:135], v[52:55]
v_mfma_f32_16x16x32_bf16 v[40:43], v[184:187], a[128:131], v[40:43]
ds_read_b128 v[168:171], v230 offset:21504
v_mfma_f32_16x16x32_bf16 v[56:59], v[184:187], a[132:135], v[56:59]
v_mfma_f32_16x16x32_bf16 v[44:47], v[188:191], a[128:131], v[44:47]
ds_read_b128 v[172:175], v230 offset:23552
v_mfma_f32_16x16x32_bf16 v[60:63], v[188:191], a[132:135], v[60:63]
s_waitcnt lgkmcnt(4)
v_mfma_f32_16x16x32_bf16 v[32:35], v[192:195], a[136:139], v[32:35]
ds_read_b128 v[176:179], v230 offset:25600
v_mfma_f32_16x16x32_bf16 v[48:51], v[192:195], a[140:143], v[48:51]
v_mfma_f32_16x16x32_bf16 v[36:39], v[196:199], a[136:139], v[36:39]
ds_read_b128 v[180:183], v230 offset:27648
v_mfma_f32_16x16x32_bf16 v[52:55], v[196:199], a[140:143], v[52:55]
v_mfma_f32_16x16x32_bf16 v[40:43], v[200:203], a[136:139], v[40:43]
ds_read_b128 v[184:187], v230 offset:29696
v_mfma_f32_16x16x32_bf16 v[56:59], v[200:203], a[140:143], v[56:59]
v_mfma_f32_16x16x32_bf16 v[44:47], v[204:207], a[136:139], v[44:47]
ds_read_b128 v[188:191], v230 offset:31744
s_xor_b32 s80, s82, 768
v_mfma_f32_16x16x32_bf16 v[60:63], v[204:207], a[140:143], v[60:63]
s_waitcnt lgkmcnt(4)
s_barrier
v_mfma_f32_16x16x32_bf16 v[32:35], v[160:163], a[144:147], v[32:35]
ds_read_b128 v[192:195], v212 offset:49152
s_add_u32 m0, s77, 98304
v_mfma_f32_16x16x32_bf16 v[48:51], v[160:163], a[148:151], v[48:51]
buffer_load_dwordx4 v224, s[36:39], s80 offen offset:0 lds
v_mfma_f32_16x16x32_bf16 v[36:39], v[164:167], a[144:147], v[36:39]
ds_read_b128 v[196:199], v212 offset:51200
v_mfma_f32_16x16x32_bf16 v[52:55], v[164:167], a[148:151], v[52:55]
v_mfma_f32_16x16x32_bf16 v[40:43], v[168:171], a[144:147], v[40:43]
ds_read_b128 v[200:203], v212 offset:53248
v_mfma_f32_16x16x32_bf16 v[56:59], v[168:171], a[148:151], v[56:59]
buffer_load_dwordx4 v225, s[36:39], s80 offen offset:1024 lds
v_mfma_f32_16x16x32_bf16 v[44:47], v[172:175], a[144:147], v[44:47]
ds_read_b128 v[204:207], v212 offset:55296
s_bfe_u32 s83, s4, 262148
v_mfma_f32_16x16x32_bf16 v[60:63], v[172:175], a[148:151], v[60:63]
s_lshl_b32 s83, s83, 12
s_waitcnt lgkmcnt(4)
v_mfma_f32_16x16x32_bf16 v[32:35], v[176:179], a[152:155], v[32:35]
ds_read_b128 v[160:163], v212 offset:57344
s_xor_b32 s81, s83, 0
v_mfma_f32_16x16x32_bf16 v[48:51], v[176:179], a[156:159], v[48:51]
buffer_load_dwordx4 a[0:3], v228, s[52:55], s81 offen offset:0 sc1
v_mfma_f32_16x16x32_bf16 v[36:39], v[180:183], a[152:155], v[36:39]
ds_read_b128 v[164:167], v212 offset:59392
v_mfma_f32_16x16x32_bf16 v[52:55], v[180:183], a[156:159], v[52:55]
v_mfma_f32_16x16x32_bf16 v[40:43], v[184:187], a[152:155], v[40:43]
ds_read_b128 v[168:171], v212 offset:61440
v_mfma_f32_16x16x32_bf16 v[56:59], v[184:187], a[156:159], v[56:59]
buffer_load_dwordx4 a[4:7], v229, s[52:55], s81 offen offset:0 sc1
v_mfma_f32_16x16x32_bf16 v[44:47], v[188:191], a[152:155], v[44:47]
ds_read_b128 v[172:175], v212 offset:63488
v_mfma_f32_16x16x32_bf16 v[60:63], v[188:191], a[156:159], v[60:63]
s_waitcnt lgkmcnt(4)
v_mfma_f32_16x16x32_bf16 v[32:35], v[192:195], a[160:163], v[32:35]
ds_read_b128 v[176:179], v212 offset:50176
v_mfma_f32_16x16x32_bf16 v[48:51], v[192:195], a[164:167], v[48:51]
buffer_load_dwordx4 a[8:11], v228, s[52:55], s81 offen offset:1024 sc1
v_mfma_f32_16x16x32_bf16 v[36:39], v[196:199], a[160:163], v[36:39]
ds_read_b128 v[180:183], v212 offset:52224
v_mfma_f32_16x16x32_bf16 v[52:55], v[196:199], a[164:167], v[52:55]
v_mfma_f32_16x16x32_bf16 v[40:43], v[200:203], a[160:163], v[40:43]
ds_read_b128 v[184:187], v212 offset:54272
v_mfma_f32_16x16x32_bf16 v[56:59], v[200:203], a[164:167], v[56:59]
buffer_load_dwordx4 a[12:15], v229, s[52:55], s81 offen offset:1024 sc1
v_mfma_f32_16x16x32_bf16 v[44:47], v[204:207], a[160:163], v[44:47]
ds_read_b128 v[188:191], v212 offset:56320
v_mfma_f32_16x16x32_bf16 v[60:63], v[204:207], a[164:167], v[60:63]
s_waitcnt lgkmcnt(4)
v_mfma_f32_16x16x32_bf16 v[32:35], v[160:163], a[168:171], v[32:35]
ds_read_b128 v[192:195], v212 offset:58368
s_xor_b32 s81, s83, 4096
v_mfma_f32_16x16x32_bf16 v[48:51], v[160:163], a[172:175], v[48:51]
buffer_load_dwordx4 a[16:19], v228, s[52:55], s81 offen offset:0 sc1
v_mfma_f32_16x16x32_bf16 v[36:39], v[164:167], a[168:171], v[36:39]
ds_read_b128 v[196:199], v212 offset:60416
v_mfma_f32_16x16x32_bf16 v[52:55], v[164:167], a[172:175], v[52:55]
v_mfma_f32_16x16x32_bf16 v[40:43], v[168:171], a[168:171], v[40:43]
ds_read_b128 v[200:203], v212 offset:62464
v_mfma_f32_16x16x32_bf16 v[56:59], v[168:171], a[172:175], v[56:59]
buffer_load_dwordx4 a[20:23], v229, s[52:55], s81 offen offset:0 sc1
v_mfma_f32_16x16x32_bf16 v[44:47], v[172:175], a[168:171], v[44:47]
ds_read_b128 v[204:207], v212 offset:64512
v_mfma_f32_16x16x32_bf16 v[60:63], v[172:175], a[172:175], v[60:63]
s_waitcnt lgkmcnt(4)
s_barrier
v_mfma_f32_16x16x32_bf16 v[32:35], v[176:179], a[176:179], v[32:35]
ds_read_b128 v[160:163], v230 offset:32768
v_mfma_f32_16x16x32_bf16 v[48:51], v[176:179], a[180:183], v[48:51]
buffer_load_dwordx4 a[24:27], v228, s[52:55], s81 offen offset:1024 sc1
v_mfma_f32_16x16x32_bf16 v[36:39], v[180:183], a[176:179], v[36:39]
ds_read_b128 v[164:167], v230 offset:34816
v_mfma_f32_16x16x32_bf16 v[52:55], v[180:183], a[180:183], v[52:55]
v_mfma_f32_16x16x32_bf16 v[40:43], v[184:187], a[176:179], v[40:43]
ds_read_b128 v[168:171], v230 offset:36864
v_mfma_f32_16x16x32_bf16 v[56:59], v[184:187], a[180:183], v[56:59]
buffer_load_dwordx4 a[28:31], v229, s[52:55], s81 offen offset:1024 sc1
v_mfma_f32_16x16x32_bf16 v[44:47], v[188:191], a[176:179], v[44:47]
ds_read_b128 v[172:175], v230 offset:38912
v_mfma_f32_16x16x32_bf16 v[60:63], v[188:191], a[180:183], v[60:63]
s_waitcnt lgkmcnt(4)
v_mfma_f32_16x16x32_bf16 v[32:35], v[192:195], a[184:187], v[32:35]
ds_read_b128 v[176:179], v230 offset:40960
s_xor_b32 s81, s83, 8192
v_mfma_f32_16x16x32_bf16 v[48:51], v[192:195], a[188:191], v[48:51]
buffer_load_dwordx4 a[32:35], v228, s[52:55], s81 offen offset:0 sc1
v_mfma_f32_16x16x32_bf16 v[36:39], v[196:199], a[184:187], v[36:39]
ds_read_b128 v[180:183], v230 offset:43008
v_mfma_f32_16x16x32_bf16 v[52:55], v[196:199], a[188:191], v[52:55]
v_mfma_f32_16x16x32_bf16 v[40:43], v[200:203], a[184:187], v[40:43]
ds_read_b128 v[184:187], v230 offset:45056
v_mfma_f32_16x16x32_bf16 v[56:59], v[200:203], a[188:191], v[56:59]
buffer_load_dwordx4 a[36:39], v229, s[52:55], s81 offen offset:0 sc1
v_mfma_f32_16x16x32_bf16 v[44:47], v[204:207], a[184:187], v[44:47]
ds_read_b128 v[188:191], v230 offset:47104
v_mfma_f32_16x16x32_bf16 v[60:63], v[204:207], a[188:191], v[60:63]
s_waitcnt lgkmcnt(4)
v_mfma_f32_16x16x32_bf16 v[32:35], v[160:163], a[192:195], v[32:35]
ds_read_b128 v[192:195], v230 offset:33792
v_mfma_f32_16x16x32_bf16 v[48:51], v[160:163], a[196:199], v[48:51]
buffer_load_dwordx4 a[40:43], v228, s[52:55], s81 offen offset:1024 sc1
v_mfma_f32_16x16x32_bf16 v[36:39], v[164:167], a[192:195], v[36:39]
ds_read_b128 v[196:199], v230 offset:35840
v_mfma_f32_16x16x32_bf16 v[52:55], v[164:167], a[196:199], v[52:55]
v_mfma_f32_16x16x32_bf16 v[40:43], v[168:171], a[192:195], v[40:43]
ds_read_b128 v[200:203], v230 offset:37888
v_mfma_f32_16x16x32_bf16 v[56:59], v[168:171], a[196:199], v[56:59]
buffer_load_dwordx4 a[44:47], v229, s[52:55], s81 offen offset:1024 sc1
v_mfma_f32_16x16x32_bf16 v[44:47], v[172:175], a[192:195], v[44:47]
ds_read_b128 v[204:207], v230 offset:39936
v_mfma_f32_16x16x32_bf16 v[60:63], v[172:175], a[196:199], v[60:63]
s_waitcnt lgkmcnt(4)
v_mfma_f32_16x16x32_bf16 v[32:35], v[176:179], a[200:203], v[32:35]
ds_read_b128 v[160:163], v230 offset:41984
s_xor_b32 s81, s83, 12288
v_mfma_f32_16x16x32_bf16 v[48:51], v[176:179], a[204:207], v[48:51]
buffer_load_dwordx4 a[48:51], v228, s[52:55], s81 offen offset:0 sc1
v_mfma_f32_16x16x32_bf16 v[36:39], v[180:183], a[200:203], v[36:39]
ds_read_b128 v[164:167], v230 offset:44032
v_mfma_f32_16x16x32_bf16 v[52:55], v[180:183], a[204:207], v[52:55]
v_mfma_f32_16x16x32_bf16 v[40:43], v[184:187], a[200:203], v[40:43]
ds_read_b128 v[168:171], v230 offset:46080
v_mfma_f32_16x16x32_bf16 v[56:59], v[184:187], a[204:207], v[56:59]
buffer_load_dwordx4 a[52:55], v229, s[52:55], s81 offen offset:0 sc1
v_mfma_f32_16x16x32_bf16 v[44:47], v[188:191], a[200:203], v[44:47]
ds_read_b128 v[172:175], v230 offset:48128
v_mfma_f32_16x16x32_bf16 v[60:63], v[188:191], a[204:207], v[60:63]
s_waitcnt vmcnt(28) lgkmcnt(4)
s_barrier
v_mfma_f32_16x16x32_bf16 v[32:35], v[192:195], a[208:211], v[32:35]
ds_read_b128 v[176:179], v212 offset:0
v_mfma_f32_16x16x32_bf16 v[48:51], v[192:195], a[212:215], v[48:51]
v_mfma_f32_16x16x32_bf16 v[36:39], v[196:199], a[208:211], v[36:39]
ds_read_b128 v[180:183], v212 offset:2048
v_mfma_f32_16x16x32_bf16 v[52:55], v[196:199], a[212:215], v[52:55]
s_mov_b64 exec, 1
buffer_store_dword v234, off, s[52:55], s22 sc1
s_mov_b64 exec, -1
v_mfma_f32_16x16x32_bf16 v[40:43], v[200:203], a[208:211], v[40:43]
ds_read_b128 v[184:187], v212 offset:4096
v_mfma_f32_16x16x32_bf16 v[56:59], v[200:203], a[212:215], v[56:59]
buffer_load_dwordx4 a[56:59], v228, s[52:55], s81 offen offset:1024 sc1
v_mfma_f32_16x16x32_bf16 v[44:47], v[204:207], a[208:211], v[44:47]
ds_read_b128 v[188:191], v212 offset:6144
v_mfma_f32_16x16x32_bf16 v[60:63], v[204:207], a[212:215], v[60:63]
s_waitcnt lgkmcnt(4)
v_mfma_f32_16x16x32_bf16 v[32:35], v[160:163], a[216:219], v[32:35]
ds_read_b128 v[192:195], v212 offset:8192
v_mfma_f32_16x16x32_bf16 v[48:51], v[160:163], a[220:223], v[48:51]
buffer_load_dwordx4 a[60:63], v229, s[52:55], s81 offen offset:1024 sc1
LC6_%=:
s_cbranch_execz LRETA_%=
v_mfma_f32_16x16x32_bf16 v[36:39], v[164:167], a[216:219], v[36:39]
ds_read_b128 v[196:199], v212 offset:10240
v_mfma_f32_16x16x32_bf16 v[52:55], v[164:167], a[220:223], v[52:55]
v_mfma_f32_16x16x32_bf16 v[40:43], v[168:171], a[216:219], v[40:43]
ds_read_b128 v[200:203], v212 offset:12288
v_mfma_f32_16x16x32_bf16 v[56:59], v[168:171], a[220:223], v[56:59]
buffer_load_dwordx4 v226, s[36:39], s80 offen offset:2048 lds
v_mfma_f32_16x16x32_bf16 v[44:47], v[172:175], a[216:219], v[44:47]
ds_read_b128 v[204:207], v212 offset:14336
v_mfma_f32_16x16x32_bf16 v[60:63], v[172:175], a[220:223], v[60:63]
s_waitcnt lgkmcnt(4)
v_mfma_f32_16x16x32_bf16 v[32:35], v[176:179], a[224:227], v[32:35]
ds_read_b128 v[160:163], v212 offset:1024
v_mfma_f32_16x16x32_bf16 v[48:51], v[176:179], a[228:231], v[48:51]
buffer_load_dwordx4 v227, s[36:39], s80 offen offset:3072 lds
v_mfma_f32_16x16x32_bf16 v[36:39], v[180:183], a[224:227], v[36:39]
ds_read_b128 v[164:167], v212 offset:3072
v_mfma_f32_16x16x32_bf16 v[52:55], v[180:183], a[228:231], v[52:55]
v_mfma_f32_16x16x32_bf16 v[40:43], v[184:187], a[224:227], v[40:43]
)"
R"(
ds_read_b128 v[168:171], v212 offset:5120
s_xor_b32 s81, s83, 16384
v_mfma_f32_16x16x32_bf16 v[56:59], v[184:187], a[228:231], v[56:59]
buffer_load_dwordx4 a[64:67], v228, s[52:55], s81 offen offset:0 sc1
v_mfma_f32_16x16x32_bf16 v[44:47], v[188:191], a[224:227], v[44:47]
ds_read_b128 v[172:175], v212 offset:7168
v_mfma_f32_16x16x32_bf16 v[60:63], v[188:191], a[228:231], v[60:63]
s_waitcnt lgkmcnt(4)
v_mfma_f32_16x16x32_bf16 v[32:35], v[192:195], a[232:235], v[32:35]
ds_read_b128 v[176:179], v212 offset:9216
v_mfma_f32_16x16x32_bf16 v[48:51], v[192:195], a[236:239], v[48:51]
buffer_load_dwordx4 a[68:71], v229, s[52:55], s81 offen offset:0 sc1
v_mfma_f32_16x16x32_bf16 v[36:39], v[196:199], a[232:235], v[36:39]
ds_read_b128 v[180:183], v212 offset:11264
v_mfma_f32_16x16x32_bf16 v[52:55], v[196:199], a[236:239], v[52:55]
v_mfma_f32_16x16x32_bf16 v[40:43], v[200:203], a[232:235], v[40:43]
ds_read_b128 v[184:187], v212 offset:13312
s_xor_b32 s80, s82, 1024
v_mfma_f32_16x16x32_bf16 v[56:59], v[200:203], a[236:239], v[56:59]
buffer_load_dwordx4 a[72:75], v228, s[52:55], s81 offen offset:1024 sc1
v_mfma_f32_16x16x32_bf16 v[44:47], v[204:207], a[232:235], v[44:47]
ds_read_b128 v[188:191], v212 offset:15360
s_add_u32 m0, s77, 16384
v_mfma_f32_16x16x32_bf16 v[60:63], v[204:207], a[236:239], v[60:63]
s_waitcnt vmcnt(18) lgkmcnt(4)
s_barrier
v_mfma_f32_16x16x32_bf16 v[32:35], v[160:163], a[240:243], v[32:35]
ds_read_b128 v[192:195], v208 offset:49152
v_accvgpr_read_b32 v248, a0
v_mfma_f32_16x16x32_bf16 v[48:51], v[160:163], a[244:247], v[48:51]
buffer_load_dwordx4 a[76:79], v229, s[52:55], s81 offen offset:1024 sc1
v_mfma_f32_16x16x32_bf16 v[36:39], v[164:167], a[240:243], v[36:39]
ds_read_b128 v[196:199], v208 offset:53248
v_accvgpr_read_b32 v249, a4
v_mfma_f32_16x16x32_bf16 v[52:55], v[164:167], a[244:247], v[52:55]
v_accvgpr_read_b32 v250, a8
v_or3_b32 v248, v248, v249, v250
v_mfma_f32_16x16x32_bf16 v[40:43], v[168:171], a[240:243], v[40:43]
ds_read_b128 v[200:203], v208 offset:57344
v_accvgpr_read_b32 v251, a12
v_mfma_f32_16x16x32_bf16 v[56:59], v[168:171], a[244:247], v[56:59]
buffer_load_dwordx4 v224, s[36:39], s80 offen offset:0 lds
v_mfma_f32_16x16x32_bf16 v[44:47], v[172:175], a[240:243], v[44:47]
ds_read_b128 v[204:207], v208 offset:61440
v_bitop3_b32 v248, v248, v251, 1 bitop3:0xa8
v_mfma_f32_16x16x32_bf16 v[60:63], v[172:175], a[244:247], v[60:63]
s_waitcnt lgkmcnt(4)
v_mfma_f32_16x16x32_bf16 v[32:35], v[176:179], a[248:251], v[32:35]
ds_read_b128 v[160:163], v208 offset:51200
v_mfma_f32_16x16x32_bf16 v[48:51], v[176:179], a[252:255], v[48:51]
buffer_load_dwordx4 v225, s[36:39], s80 offen offset:1024 lds
v_mfma_f32_16x16x32_bf16 v[36:39], v[180:183], a[248:251], v[36:39]
ds_read_b128 v[164:167], v208 offset:55296
v_mfma_f32_16x16x32_bf16 v[52:55], v[180:183], a[252:255], v[52:55]
v_cmp_ne_u32 vcc, 0, v248
s_nop 0
s_cbranch_vccnz LSt2k0_%=
LRt2k0_%=:
v_mfma_f32_16x16x32_bf16 v[40:43], v[184:187], a[248:251], v[40:43]
ds_read_b128 v[168:171], v208 offset:59392
v_mfma_f32_16x16x32_bf16 v[56:59], v[184:187], a[252:255], v[56:59]
buffer_load_dwordx4 v226, s[36:39], s80 offen offset:2048 lds
v_mfma_f32_16x16x32_bf16 v[44:47], v[188:191], a[248:251], v[44:47]
ds_read_b128 v[172:175], v208 offset:63488
v_mfma_f32_16x16x32_bf16 v[60:63], v[188:191], a[252:255], v[60:63]
s_waitcnt lgkmcnt(4)
v_mfma_f32_16x16x32_bf16 v[64:67], v[192:195], a[0:3], 0
ds_read_b128 v[176:179], v209 offset:49152
v_mfma_f32_16x16x32_bf16 v[80:83], v[192:195], a[4:7], 0
buffer_load_dwordx4 v227, s[36:39], s80 offen offset:3072 lds
v_mfma_f32_16x16x32_bf16 v[68:71], v[196:199], a[0:3], 0
ds_read_b128 v[180:183], v209 offset:53248
v_mfma_f32_16x16x32_bf16 v[84:87], v[196:199], a[4:7], 0
v_mfma_f32_16x16x32_bf16 v[72:75], v[200:203], a[0:3], 0
ds_read_b128 v[184:187], v209 offset:57344
s_xor_b32 s81, s83, 20480
v_fmac_f32 v0, v32, v144
v_fmac_f32 v1, v33, v145
v_mfma_f32_16x16x32_bf16 v[88:91], v[200:203], a[4:7], 0
buffer_load_dwordx4 a[80:83], v228, s[52:55], s81 offen offset:0 sc1
v_fmac_f32 v2, v34, v146
v_fmac_f32 v3, v35, v147
v_mfma_f32_16x16x32_bf16 v[76:79], v[204:207], a[0:3], 0
ds_read_b128 v[188:191], v209 offset:61440
v_fmac_f32 v4, v36, v148
v_mfma_f32_16x16x32_bf16 v[92:95], v[204:207], a[4:7], 0
v_fmac_f32 v5, v37, v149
v_fmac_f32 v6, v38, v150
s_waitcnt lgkmcnt(4)
v_mfma_f32_16x16x32_bf16 v[96:99], v[160:163], a[0:3], 0
ds_read_b128 v[192:195], v209 offset:51200
v_fmac_f32 v7, v39, v151
v_mfma_f32_16x16x32_bf16 v[112:115], v[160:163], a[4:7], 0
buffer_load_dwordx4 a[84:87], v229, s[52:55], s81 offen offset:0 sc1
v_cvt_pk_bf16_f32 v0, v0, v1
v_cvt_pk_bf16_f32 v1, v2, v3
v_mfma_f32_16x16x32_bf16 v[100:103], v[164:167], a[0:3], 0
ds_read_b128 v[196:199], v209 offset:55296
v_cvt_pk_bf16_f32 v2, v4, v5
v_mfma_f32_16x16x32_bf16 v[116:119], v[164:167], a[4:7], 0
v_cvt_pk_bf16_f32 v3, v6, v7
v_and_b32 v0, -2, v0
v_mfma_f32_16x16x32_bf16 v[104:107], v[168:171], a[0:3], 0
ds_read_b128 v[200:203], v209 offset:59392
v_fmac_f32 v8, v40, v152
v_mfma_f32_16x16x32_bf16 v[120:123], v[168:171], a[4:7], 0
buffer_store_dwordx4 v[0:3], v228, s[52:55], s79 offen offset:2048 sc1
v_fmac_f32 v9, v41, v153
v_mfma_f32_16x16x32_bf16 v[108:111], v[172:175], a[0:3], 0
ds_read_b128 v[204:207], v209 offset:63488
v_fmac_f32 v10, v42, v154
v_fmac_f32 v11, v43, v155
v_mfma_f32_16x16x32_bf16 v[124:127], v[172:175], a[4:7], 0
v_fmac_f32 v12, v44, v156
s_waitcnt vmcnt(22) lgkmcnt(4)
s_barrier
v_mfma_f32_16x16x32_bf16 v[64:67], v[176:179], a[8:11], v[64:67]
ds_read_b128 v[160:163], v210 offset:16384
v_accvgpr_read_b32 v248, a16
v_fmac_f32 v13, v45, v157
v_fmac_f32 v14, v46, v158
v_mfma_f32_16x16x32_bf16 v[80:83], v[176:179], a[12:15], v[80:83]
buffer_load_dwordx4 a[88:91], v228, s[52:55], s81 offen offset:1024 sc1
v_fmac_f32 v15, v47, v159
v_mfma_f32_16x16x32_bf16 v[68:71], v[180:183], a[8:11], v[68:71]
ds_read_b128 v[164:167], v210 offset:20480
v_accvgpr_read_b32 v249, a20
v_cvt_pk_bf16_f32 v8, v8, v9
v_cvt_pk_bf16_f32 v9, v10, v11
v_mfma_f32_16x16x32_bf16 v[84:87], v[180:183], a[12:15], v[84:87]
v_accvgpr_read_b32 v250, a24
v_or3_b32 v248, v248, v249, v250
v_cvt_pk_bf16_f32 v10, v12, v13
v_mfma_f32_16x16x32_bf16 v[72:75], v[184:187], a[8:11], v[72:75]
ds_read_b128 v[168:171], v210 offset:24576
v_accvgpr_read_b32 v251, a28
v_cvt_pk_bf16_f32 v11, v14, v15
v_and_b32 v8, -2, v8
v_mfma_f32_16x16x32_bf16 v[88:91], v[184:187], a[12:15], v[88:91]
buffer_store_dwordx4 v[8:11], v228, s[52:55], s79 offen offset:3072 sc1
v_mfma_f32_16x16x32_bf16 v[76:79], v[188:191], a[8:11], v[76:79]
ds_read_b128 v[172:175], v210 offset:28672
v_bitop3_b32 v248, v248, v251, 1 bitop3:0xa8
v_fmac_f32 v16, v48, v144
v_fmac_f32 v17, v49, v145
v_mfma_f32_16x16x32_bf16 v[92:95], v[188:191], a[12:15], v[92:95]
v_fmac_f32 v18, v50, v146
v_fmac_f32 v19, v51, v147
s_waitcnt lgkmcnt(4)
v_mfma_f32_16x16x32_bf16 v[96:99], v[192:195], a[8:11], v[96:99]
ds_read_b128 v[176:179], v210 offset:18432
v_fmac_f32 v20, v52, v148
v_mfma_f32_16x16x32_bf16 v[112:115], v[192:195], a[12:15], v[112:115]
buffer_load_dwordx4 a[92:95], v229, s[52:55], s81 offen offset:1024 sc1
v_fmac_f32 v21, v53, v149
v_fmac_f32 v22, v54, v150
v_mfma_f32_16x16x32_bf16 v[100:103], v[196:199], a[8:11], v[100:103]
ds_read_b128 v[180:183], v210 offset:22528
v_fmac_f32 v23, v55, v151
v_mfma_f32_16x16x32_bf16 v[116:119], v[196:199], a[12:15], v[116:119]
v_cmp_ne_u32 vcc, 0, v248
s_nop 0
s_cbranch_vccnz LSt2k1_%=
LRt2k1_%=:
v_cvt_pk_bf16_f32 v16, v16, v17
v_cvt_pk_bf16_f32 v17, v18, v19
v_mfma_f32_16x16x32_bf16 v[104:107], v[200:203], a[8:11], v[104:107]
ds_read_b128 v[184:187], v210 offset:26624
v_cvt_pk_bf16_f32 v18, v20, v21
v_mfma_f32_16x16x32_bf16 v[120:123], v[200:203], a[12:15], v[120:123]
v_cvt_pk_bf16_f32 v19, v22, v23
v_and_b32 v16, -2, v16
v_mfma_f32_16x16x32_bf16 v[108:111], v[204:207], a[8:11], v[108:111]
ds_read_b128 v[188:191], v210 offset:30720
v_fmac_f32 v24, v56, v152
v_mfma_f32_16x16x32_bf16 v[124:127], v[204:207], a[12:15], v[124:127]
s_xor_b32 s80, s82, 1280
buffer_store_dwordx4 v[16:19], v229, s[52:55], s79 offen offset:2048 sc1
v_fmac_f32 v25, v57, v153
s_waitcnt lgkmcnt(4)
v_mfma_f32_16x16x32_bf16 v[64:67], v[160:163], a[16:19], v[64:67]
ds_read_b128 v[192:195], v211 offset:16384
s_add_u32 m0, s77, 114688
v_fmac_f32 v26, v58, v154
v_fmac_f32 v27, v59, v155
v_mfma_f32_16x16x32_bf16 v[80:83], v[160:163], a[20:23], v[80:83]
buffer_load_dwordx4 v224, s[36:39], s80 offen offset:0 lds
v_fmac_f32 v28, v60, v156
v_mfma_f32_16x16x32_bf16 v[68:71], v[164:167], a[16:19], v[68:71]
ds_read_b128 v[196:199], v211 offset:20480
v_fmac_f32 v29, v61, v157
v_fmac_f32 v30, v62, v158
v_mfma_f32_16x16x32_bf16 v[84:87], v[164:167], a[20:23], v[84:87]
v_fmac_f32 v31, v63, v159
v_cvt_pk_bf16_f32 v24, v24, v25
v_mfma_f32_16x16x32_bf16 v[72:75], v[168:171], a[16:19], v[72:75]
ds_read_b128 v[200:203], v211 offset:24576
v_cvt_pk_bf16_f32 v25, v26, v27
v_mfma_f32_16x16x32_bf16 v[88:91], v[168:171], a[20:23], v[88:91]
v_cvt_pk_bf16_f32 v26, v28, v29
v_cvt_pk_bf16_f32 v27, v30, v31
v_mfma_f32_16x16x32_bf16 v[76:79], v[172:175], a[16:19], v[76:79]
ds_read_b128 v[204:207], v211 offset:28672
v_and_b32 v24, -2, v24
v_mfma_f32_16x16x32_bf16 v[92:95], v[172:175], a[20:23], v[92:95]
buffer_store_dwordx4 v[24:27], v229, s[52:55], s79 offen offset:3072 sc1
s_waitcnt lgkmcnt(4)
v_mfma_f32_16x16x32_bf16 v[96:99], v[176:179], a[16:19], v[96:99]
ds_read_b128 v[160:163], v211 offset:18432
s_mov_b32 s7, 0
v_mfma_f32_16x16x32_bf16 v[112:115], v[176:179], a[20:23], v[112:115]
buffer_load_dwordx4 v[48:51], v238, s[52:55], s7 offen sc1
v_mfma_f32_16x16x32_bf16 v[100:103], v[180:183], a[16:19], v[100:103]
ds_read_b128 v[164:167], v211 offset:22528
s_mov_b32 s7, 32768
v_mfma_f32_16x16x32_bf16 v[116:119], v[180:183], a[20:23], v[116:119]
buffer_load_dwordx4 v[52:55], v238, s[52:55], s7 offen sc1
v_mfma_f32_16x16x32_bf16 v[104:107], v[184:187], a[16:19], v[104:107]
ds_read_b128 v[168:171], v211 offset:26624
s_mov_b32 s66, LE_%=-LB_%=
v_mfma_f32_16x16x32_bf16 v[120:123], v[184:187], a[20:23], v[120:123]
buffer_load_dwordx4 v225, s[36:39], s80 offen offset:1024 lds
v_mfma_f32_16x16x32_bf16 v[108:111], v[188:191], a[16:19], v[108:111]
ds_read_b128 v[172:175], v211 offset:30720
s_mov_b32 s67, 0x20000
v_mfma_f32_16x16x32_bf16 v[124:127], v[188:191], a[20:23], v[124:127]
s_waitcnt vmcnt(27) lgkmcnt(4)
s_barrier
v_mfma_f32_16x16x32_bf16 v[64:67], v[192:195], a[24:27], v[64:67]
ds_read_b128 v[176:179], v208 offset:0
v_accvgpr_read_b32 v248, a32
v_mfma_f32_16x16x32_bf16 v[80:83], v[192:195], a[28:31], v[80:83]
buffer_load_dwordx4 v226, s[36:39], s80 offen offset:2048 lds
v_mfma_f32_16x16x32_bf16 v[68:71], v[196:199], a[24:27], v[68:71]
ds_read_b128 v[180:183], v208 offset:4096
v_accvgpr_read_b32 v249, a36
v_mfma_f32_16x16x32_bf16 v[84:87], v[196:199], a[28:31], v[84:87]
v_accvgpr_read_b32 v250, a40
v_or3_b32 v248, v248, v249, v250
v_mfma_f32_16x16x32_bf16 v[72:75], v[200:203], a[24:27], v[72:75]
ds_read_b128 v[184:187], v208 offset:8192
v_accvgpr_read_b32 v251, a44
v_mfma_f32_16x16x32_bf16 v[88:91], v[200:203], a[28:31], v[88:91]
buffer_load_dwordx4 v227, s[36:39], s80 offen offset:3072 lds
v_mfma_f32_16x16x32_bf16 v[76:79], v[204:207], a[24:27], v[76:79]
ds_read_b128 v[188:191], v208 offset:12288
v_bitop3_b32 v248, v248, v251, 1 bitop3:0xa8
v_mfma_f32_16x16x32_bf16 v[92:95], v[204:207], a[28:31], v[92:95]
s_lshr_b32 s87, s6, 3
s_waitcnt lgkmcnt(4)
v_mfma_f32_16x16x32_bf16 v[96:99], v[160:163], a[24:27], v[96:99]
ds_read_b128 v[192:195], v208 offset:2048
s_xor_b32 s81, s83, 24576
v_mfma_f32_16x16x32_bf16 v[112:115], v[160:163], a[28:31], v[112:115]
buffer_load_dwordx4 a[96:99], v228, s[52:55], s81 offen offset:0 sc1
v_mfma_f32_16x16x32_bf16 v[100:103], v[164:167], a[24:27], v[100:103]
ds_read_b128 v[196:199], v208 offset:6144
s_lshl2_add_u32 s87, s87, s72
v_mfma_f32_16x16x32_bf16 v[116:119], v[164:167], a[28:31], v[116:119]
v_cmp_ne_u32 vcc, 0, v248
s_nop 0
s_cbranch_vccnz LSt2k2_%=
LRt2k2_%=:
v_mfma_f32_16x16x32_bf16 v[104:107], v[168:171], a[24:27], v[104:107]
ds_read_b128 v[200:203], v208 offset:10240
s_lshl_b32 s87, s87, 10
v_mfma_f32_16x16x32_bf16 v[120:123], v[168:171], a[28:31], v[120:123]
buffer_load_dwordx4 a[100:103], v229, s[52:55], s81 offen offset:0 sc1
v_mfma_f32_16x16x32_bf16 v[108:111], v[172:175], a[24:27], v[108:111]
ds_read_b128 v[204:207], v208 offset:14336
v_and_b32 v247, 7, v231
v_mfma_f32_16x16x32_bf16 v[124:127], v[172:175], a[28:31], v[124:127]
v_lshlrev_b32 v247, 7, v247
s_waitcnt lgkmcnt(4)
v_mfma_f32_16x16x32_bf16 v[64:67], v[176:179], a[32:35], v[64:67]
ds_read_b128 v[160:163], v209 offset:0
v_mfma_f32_16x16x32_bf16 v[80:83], v[176:179], a[36:39], v[80:83]
buffer_load_dwordx4 v[32:35], v245, s[56:59], 0 offen offset:0
v_mfma_f32_16x16x32_bf16 v[68:71], v[180:183], a[32:35], v[68:71]
ds_read_b128 v[164:167], v209 offset:4096
v_mfma_f32_16x16x32_bf16 v[84:87], v[180:183], a[36:39], v[84:87]
buffer_load_dwordx4 v[36:39], v245, s[56:59], 0 offen offset:64
v_mfma_f32_16x16x32_bf16 v[72:75], v[184:187], a[32:35], v[72:75]
ds_read_b128 v[168:171], v209 offset:8192
v_mfma_f32_16x16x32_bf16 v[88:91], v[184:187], a[36:39], v[88:91]
buffer_load_dwordx4 a[104:107], v228, s[52:55], s81 offen offset:1024 sc1
v_mfma_f32_16x16x32_bf16 v[76:79], v[188:191], a[32:35], v[76:79]
ds_read_b128 v[172:175], v209 offset:12288
v_mfma_f32_16x16x32_bf16 v[92:95], v[188:191], a[36:39], v[92:95]
s_waitcnt lgkmcnt(4)
v_mfma_f32_16x16x32_bf16 v[96:99], v[192:195], a[32:35], v[96:99]
ds_read_b128 v[176:179], v209 offset:2048
v_mfma_f32_16x16x32_bf16 v[112:115], v[192:195], a[36:39], v[112:115]
buffer_load_dwordx4 v[40:43], v246, s[56:59], 0 offen offset:0
v_mfma_f32_16x16x32_bf16 v[100:103], v[196:199], a[32:35], v[100:103]
ds_read_b128 v[180:183], v209 offset:6144
v_mfma_f32_16x16x32_bf16 v[116:119], v[196:199], a[36:39], v[116:119]
buffer_load_dwordx4 v[44:47], v246, s[56:59], 0 offen offset:64
v_mfma_f32_16x16x32_bf16 v[104:107], v[200:203], a[32:35], v[104:107]
ds_read_b128 v[184:187], v209 offset:10240
s_xor_b32 s80, s82, 1536
v_mfma_f32_16x16x32_bf16 v[120:123], v[200:203], a[36:39], v[120:123]
buffer_load_dwordx4 a[108:111], v229, s[52:55], s81 offen offset:1024 sc1
v_mfma_f32_16x16x32_bf16 v[108:111], v[204:207], a[32:35], v[108:111]
ds_read_b128 v[188:191], v209 offset:14336
s_add_u32 m0, s77, 32768
v_mfma_f32_16x16x32_bf16 v[124:127], v[204:207], a[36:39], v[124:127]
s_waitcnt vmcnt(30) lgkmcnt(4)
s_barrier
v_mfma_f32_16x16x32_bf16 v[64:67], v[160:163], a[40:43], v[64:67]
ds_read_b128 v[192:195], v210 offset:32768
v_accvgpr_read_b32 v248, a48
v_mfma_f32_16x16x32_bf16 v[80:83], v[160:163], a[44:47], v[80:83]
buffer_load_dwordx4 v224, s[36:39], s80 offen offset:0 lds
v_mfma_f32_16x16x32_bf16 v[68:71], v[164:167], a[40:43], v[68:71]
ds_read_b128 v[196:199], v210 offset:36864
v_accvgpr_read_b32 v249, a52
v_mfma_f32_16x16x32_bf16 v[84:87], v[164:167], a[44:47], v[84:87]
v_accvgpr_read_b32 v250, a56
v_or3_b32 v248, v248, v249, v250
v_mfma_f32_16x16x32_bf16 v[72:75], v[168:171], a[40:43], v[72:75]
ds_read_b128 v[200:203], v210 offset:40960
v_accvgpr_read_b32 v251, a60
v_mfma_f32_16x16x32_bf16 v[88:91], v[168:171], a[44:47], v[88:91]
buffer_load_dwordx4 v225, s[36:39], s80 offen offset:1024 lds
v_mfma_f32_16x16x32_bf16 v[76:79], v[172:175], a[40:43], v[76:79]
ds_read_b128 v[204:207], v210 offset:45056
v_bitop3_b32 v248, v248, v251, 1 bitop3:0xa8
v_mfma_f32_16x16x32_bf16 v[92:95], v[172:175], a[44:47], v[92:95]
s_waitcnt lgkmcnt(4)
v_mfma_f32_16x16x32_bf16 v[96:99], v[176:179], a[40:43], v[96:99]
ds_read_b128 v[160:163], v210 offset:34816
v_mfma_f32_16x16x32_bf16 v[112:115], v[176:179], a[44:47], v[112:115]
buffer_load_dwordx4 v226, s[36:39], s80 offen offset:2048 lds
v_mfma_f32_16x16x32_bf16 v[100:103], v[180:183], a[40:43], v[100:103]
ds_read_b128 v[164:167], v210 offset:38912
v_mfma_f32_16x16x32_bf16 v[116:119], v[180:183], a[44:47], v[116:119]
v_cmp_ne_u32 vcc, 0, v248
s_nop 0
s_cbranch_vccnz LSt2k3_%=
LRt2k3_%=:
v_mfma_f32_16x16x32_bf16 v[104:107], v[184:187], a[40:43], v[104:107]
ds_read_b128 v[168:171], v210 offset:43008
v_mfma_f32_16x16x32_bf16 v[120:123], v[184:187], a[44:47], v[120:123]
buffer_load_dwordx4 v227, s[36:39], s80 offen offset:3072 lds
v_mfma_f32_16x16x32_bf16 v[108:111], v[188:191], a[40:43], v[108:111]
ds_read_b128 v[172:175], v210 offset:47104
v_mfma_f32_16x16x32_bf16 v[124:127], v[188:191], a[44:47], v[124:127]
s_waitcnt lgkmcnt(4)
v_mfma_f32_16x16x32_bf16 v[64:67], v[192:195], a[48:51], v[64:67]
ds_read_b128 v[176:179], v211 offset:32768
v_mfma_f32_16x16x32_bf16 v[80:83], v[192:195], a[52:55], v[80:83]
s_getpc_b64 s[64:65]
LT1_%=:
s_sub_u32 s64, s64, LT1_%=-LB_%=
s_subb_u32 s65, s65, 0
v_mfma_f32_16x16x32_bf16 v[68:71], v[196:199], a[48:51], v[68:71]
ds_read_b128 v[180:183], v211 offset:36864
v_mfma_f32_16x16x32_bf16 v[84:87], v[196:199], a[52:55], v[84:87]
buffer_load_dword v247, v247, s[64:67], s87 offen
v_mfma_f32_16x16x32_bf16 v[72:75], v[200:203], a[48:51], v[72:75]
ds_read_b128 v[184:187], v211 offset:40960
s_xor_b32 s81, s83, 28672
v_mfma_f32_16x16x32_bf16 v[88:91], v[200:203], a[52:55], v[88:91]
buffer_load_dwordx4 a[112:115], v228, s[52:55], s81 offen offset:0 sc1
v_mfma_f32_16x16x32_bf16 v[76:79], v[204:207], a[48:51], v[76:79]
ds_read_b128 v[188:191], v211 offset:45056
v_mfma_f32_16x16x32_bf16 v[92:95], v[204:207], a[52:55], v[92:95]
s_waitcnt lgkmcnt(4)
v_mfma_f32_16x16x32_bf16 v[96:99], v[160:163], a[48:51], v[96:99]
ds_read_b128 v[192:195], v211 offset:34816
v_mfma_f32_16x16x32_bf16 v[112:115], v[160:163], a[52:55], v[112:115]
v_mfma_f32_16x16x32_bf16 v[100:103], v[164:167], a[48:51], v[100:103]
ds_read_b128 v[196:199], v211 offset:38912
v_mfma_f32_16x16x32_bf16 v[116:119], v[164:167], a[52:55], v[116:119]
v_mfma_f32_16x16x32_bf16 v[104:107], v[168:171], a[48:51], v[104:107]
ds_read_b128 v[200:203], v211 offset:43008
v_mfma_f32_16x16x32_bf16 v[120:123], v[168:171], a[52:55], v[120:123]
buffer_load_dwordx4 a[116:119], v229, s[52:55], s81 offen offset:0 sc1
v_mfma_f32_16x16x32_bf16 v[108:111], v[172:175], a[48:51], v[108:111]
ds_read_b128 v[204:207], v211 offset:47104
v_mfma_f32_16x16x32_bf16 v[124:127], v[172:175], a[52:55], v[124:127]
s_waitcnt vmcnt(29) lgkmcnt(4)
s_barrier
v_mfma_f32_16x16x32_bf16 v[64:67], v[176:179], a[56:59], v[64:67]
ds_read_b128 v[160:163], v208 offset:16384
v_accvgpr_read_b32 v248, a64
v_mfma_f32_16x16x32_bf16 v[80:83], v[176:179], a[60:63], v[80:83]
buffer_load_dwordx4 a[120:123], v228, s[52:55], s81 offen offset:1024 sc1
v_mfma_f32_16x16x32_bf16 v[68:71], v[180:183], a[56:59], v[68:71]
ds_read_b128 v[164:167], v208 offset:20480
v_accvgpr_read_b32 v249, a68
v_mfma_f32_16x16x32_bf16 v[84:87], v[180:183], a[60:63], v[84:87]
v_accvgpr_read_b32 v250, a72
v_or3_b32 v248, v248, v249, v250
v_mfma_f32_16x16x32_bf16 v[72:75], v[184:187], a[56:59], v[72:75]
ds_read_b128 v[168:171], v208 offset:24576
v_accvgpr_read_b32 v251, a76
v_mfma_f32_16x16x32_bf16 v[88:91], v[184:187], a[60:63], v[88:91]
buffer_load_dwordx4 a[124:127], v229, s[52:55], s81 offen offset:1024 sc1
)"
R"(
v_mfma_f32_16x16x32_bf16 v[76:79], v[188:191], a[56:59], v[76:79]
ds_read_b128 v[172:175], v208 offset:28672
v_bitop3_b32 v248, v248, v251, 1 bitop3:0xa8
v_mfma_f32_16x16x32_bf16 v[92:95], v[188:191], a[60:63], v[92:95]
s_xor_b32 s80, s82, 1792
s_waitcnt lgkmcnt(4)
v_mfma_f32_16x16x32_bf16 v[96:99], v[192:195], a[56:59], v[96:99]
ds_read_b128 v[176:179], v208 offset:18432
s_add_u32 m0, s77, 65536
v_mfma_f32_16x16x32_bf16 v[112:115], v[192:195], a[60:63], v[112:115]
buffer_load_dwordx4 v224, s[36:39], s80 offen offset:0 lds
v_mfma_f32_16x16x32_bf16 v[100:103], v[196:199], a[56:59], v[100:103]
ds_read_b128 v[180:183], v208 offset:22528
v_mfma_f32_16x16x32_bf16 v[116:119], v[196:199], a[60:63], v[116:119]
v_cmp_ne_u32 vcc, 0, v248
s_nop 0
s_cbranch_vccnz LSt2k4_%=
LRt2k4_%=:
v_mfma_f32_16x16x32_bf16 v[104:107], v[200:203], a[56:59], v[104:107]
ds_read_b128 v[184:187], v208 offset:26624
v_mfma_f32_16x16x32_bf16 v[120:123], v[200:203], a[60:63], v[120:123]
buffer_load_dwordx4 v225, s[36:39], s80 offen offset:1024 lds
v_mfma_f32_16x16x32_bf16 v[108:111], v[204:207], a[56:59], v[108:111]
ds_read_b128 v[188:191], v208 offset:30720
v_mfma_f32_16x16x32_bf16 v[124:127], v[204:207], a[60:63], v[124:127]
s_waitcnt lgkmcnt(4)
v_mfma_f32_16x16x32_bf16 v[64:67], v[160:163], a[64:67], v[64:67]
ds_read_b128 v[192:195], v209 offset:16384
v_mfma_f32_16x16x32_bf16 v[80:83], v[160:163], a[68:71], v[80:83]
v_mfma_f32_16x16x32_bf16 v[68:71], v[164:167], a[64:67], v[68:71]
ds_read_b128 v[196:199], v209 offset:20480
v_mfma_f32_16x16x32_bf16 v[84:87], v[164:167], a[68:71], v[84:87]
v_mfma_f32_16x16x32_bf16 v[72:75], v[168:171], a[64:67], v[72:75]
ds_read_b128 v[200:203], v209 offset:24576
v_mfma_f32_16x16x32_bf16 v[88:91], v[168:171], a[68:71], v[88:91]
buffer_load_dwordx4 v226, s[36:39], s80 offen offset:2048 lds
v_mfma_f32_16x16x32_bf16 v[76:79], v[172:175], a[64:67], v[76:79]
ds_read_b128 v[204:207], v209 offset:28672
v_mfma_f32_16x16x32_bf16 v[92:95], v[172:175], a[68:71], v[92:95]
s_waitcnt lgkmcnt(4)
v_mfma_f32_16x16x32_bf16 v[96:99], v[176:179], a[64:67], v[96:99]
ds_read_b128 v[160:163], v209 offset:18432
v_mfma_f32_16x16x32_bf16 v[112:115], v[176:179], a[68:71], v[112:115]
buffer_load_dwordx4 v227, s[36:39], s80 offen offset:3072 lds
v_mfma_f32_16x16x32_bf16 v[100:103], v[180:183], a[64:67], v[100:103]
ds_read_b128 v[164:167], v209 offset:22528
v_mfma_f32_16x16x32_bf16 v[116:119], v[180:183], a[68:71], v[116:119]
v_mfma_f32_16x16x32_bf16 v[104:107], v[184:187], a[64:67], v[104:107]
ds_read_b128 v[168:171], v209 offset:26624
s_xor_b32 s81, s83, 2048
v_mfma_f32_16x16x32_bf16 v[120:123], v[184:187], a[68:71], v[120:123]
buffer_load_dwordx4 a[128:131], v228, s[52:55], s81 offen offset:0 sc1
v_mfma_f32_16x16x32_bf16 v[108:111], v[188:191], a[64:67], v[108:111]
ds_read_b128 v[172:175], v209 offset:30720
v_mfma_f32_16x16x32_bf16 v[124:127], v[188:191], a[68:71], v[124:127]
s_waitcnt vmcnt(22) lgkmcnt(4)
s_barrier
v_mfma_f32_16x16x32_bf16 v[64:67], v[192:195], a[72:75], v[64:67]
ds_read_b128 v[176:179], v210 offset:49152
v_accvgpr_read_b32 v248, a80
v_mfma_f32_16x16x32_bf16 v[80:83], v[192:195], a[76:79], v[80:83]
buffer_load_dwordx4 a[132:135], v229, s[52:55], s81 offen offset:0 sc1
v_mfma_f32_16x16x32_bf16 v[68:71], v[196:199], a[72:75], v[68:71]
ds_read_b128 v[180:183], v210 offset:53248
v_accvgpr_read_b32 v249, a84
v_mfma_f32_16x16x32_bf16 v[84:87], v[196:199], a[76:79], v[84:87]
v_accvgpr_read_b32 v250, a88
v_or3_b32 v248, v248, v249, v250
v_mfma_f32_16x16x32_bf16 v[72:75], v[200:203], a[72:75], v[72:75]
ds_read_b128 v[184:187], v210 offset:57344
v_accvgpr_read_b32 v251, a92
v_mfma_f32_16x16x32_bf16 v[88:91], v[200:203], a[76:79], v[88:91]
buffer_load_dwordx4 a[136:139], v228, s[52:55], s81 offen offset:1024 sc1
v_mfma_f32_16x16x32_bf16 v[76:79], v[204:207], a[72:75], v[76:79]
ds_read_b128 v[188:191], v210 offset:61440
v_bitop3_b32 v248, v248, v251, 1 bitop3:0xa8
v_mfma_f32_16x16x32_bf16 v[92:95], v[204:207], a[76:79], v[92:95]
s_waitcnt lgkmcnt(4)
v_mfma_f32_16x16x32_bf16 v[96:99], v[160:163], a[72:75], v[96:99]
ds_read_b128 v[192:195], v210 offset:51200
v_mfma_f32_16x16x32_bf16 v[112:115], v[160:163], a[76:79], v[112:115]
buffer_load_dwordx4 a[140:143], v229, s[52:55], s81 offen offset:1024 sc1
v_mfma_f32_16x16x32_bf16 v[100:103], v[164:167], a[72:75], v[100:103]
ds_read_b128 v[196:199], v210 offset:55296
v_mfma_f32_16x16x32_bf16 v[116:119], v[164:167], a[76:79], v[116:119]
v_cmp_ne_u32 vcc, 0, v248
s_nop 0
s_cbranch_vccnz LSt2k5_%=
LRt2k5_%=:
v_mfma_f32_16x16x32_bf16 v[104:107], v[168:171], a[72:75], v[104:107]
ds_read_b128 v[200:203], v210 offset:59392
s_xor_b32 s81, s83, 6144
v_mfma_f32_16x16x32_bf16 v[120:123], v[168:171], a[76:79], v[120:123]
buffer_load_dwordx4 a[144:147], v228, s[52:55], s81 offen offset:0 sc1
v_mfma_f32_16x16x32_bf16 v[108:111], v[172:175], a[72:75], v[108:111]
ds_read_b128 v[204:207], v210 offset:63488
v_mfma_f32_16x16x32_bf16 v[124:127], v[172:175], a[76:79], v[124:127]
s_waitcnt lgkmcnt(4)
v_mfma_f32_16x16x32_bf16 v[64:67], v[176:179], a[80:83], v[64:67]
ds_read_b128 v[160:163], v211 offset:49152
v_mfma_f32_16x16x32_bf16 v[80:83], v[176:179], a[84:87], v[80:83]
v_mfma_f32_16x16x32_bf16 v[68:71], v[180:183], a[80:83], v[68:71]
ds_read_b128 v[164:167], v211 offset:53248
v_mfma_f32_16x16x32_bf16 v[84:87], v[180:183], a[84:87], v[84:87]
v_mfma_f32_16x16x32_bf16 v[72:75], v[184:187], a[80:83], v[72:75]
ds_read_b128 v[168:171], v211 offset:57344
v_mfma_f32_16x16x32_bf16 v[88:91], v[184:187], a[84:87], v[88:91]
buffer_load_dwordx4 a[148:151], v229, s[52:55], s81 offen offset:0 sc1
v_mfma_f32_16x16x32_bf16 v[76:79], v[188:191], a[80:83], v[76:79]
ds_read_b128 v[172:175], v211 offset:61440
v_mfma_f32_16x16x32_bf16 v[92:95], v[188:191], a[84:87], v[92:95]
s_waitcnt lgkmcnt(4)
v_mfma_f32_16x16x32_bf16 v[96:99], v[192:195], a[80:83], v[96:99]
ds_read_b128 v[176:179], v211 offset:51200
v_mfma_f32_16x16x32_bf16 v[112:115], v[192:195], a[84:87], v[112:115]
buffer_load_dwordx4 a[152:155], v228, s[52:55], s81 offen offset:1024 sc1
v_mfma_f32_16x16x32_bf16 v[100:103], v[196:199], a[80:83], v[100:103]
ds_read_b128 v[180:183], v211 offset:55296
v_subrev_u32 v226, s30, v226
v_mfma_f32_16x16x32_bf16 v[116:119], v[196:199], a[84:87], v[116:119]
v_mfma_f32_16x16x32_bf16 v[104:107], v[200:203], a[80:83], v[104:107]
ds_read_b128 v[184:187], v211 offset:59392
s_xor_b32 s80, s82, 128
v_mfma_f32_16x16x32_bf16 v[120:123], v[200:203], a[84:87], v[120:123]
buffer_load_dwordx4 a[156:159], v229, s[52:55], s81 offen offset:1024 sc1
v_mfma_f32_16x16x32_bf16 v[108:111], v[204:207], a[80:83], v[108:111]
ds_read_b128 v[188:191], v211 offset:63488
s_add_u32 m0, s77, 49152
v_mfma_f32_16x16x32_bf16 v[124:127], v[204:207], a[84:87], v[124:127]
s_waitcnt vmcnt(17) lgkmcnt(4)
s_barrier
v_mfma_f32_16x16x32_bf16 v[64:67], v[160:163], a[88:91], v[64:67]
ds_read_b128 v[192:195], v208 offset:32768
v_accvgpr_read_b32 v248, a96
v_mfma_f32_16x16x32_bf16 v[80:83], v[160:163], a[92:95], v[80:83]
buffer_load_dwordx4 v224, s[36:39], s80 offen offset:0 lds
v_mfma_f32_16x16x32_bf16 v[68:71], v[164:167], a[88:91], v[68:71]
ds_read_b128 v[196:199], v208 offset:36864
v_accvgpr_read_b32 v249, a100
v_mfma_f32_16x16x32_bf16 v[84:87], v[164:167], a[92:95], v[84:87]
v_accvgpr_read_b32 v250, a104
v_or3_b32 v248, v248, v249, v250
v_mfma_f32_16x16x32_bf16 v[72:75], v[168:171], a[88:91], v[72:75]
ds_read_b128 v[200:203], v208 offset:40960
v_accvgpr_read_b32 v251, a108
v_mfma_f32_16x16x32_bf16 v[88:91], v[168:171], a[92:95], v[88:91]
buffer_load_dwordx4 v225, s[36:39], s80 offen offset:1024 lds
v_mfma_f32_16x16x32_bf16 v[76:79], v[172:175], a[88:91], v[76:79]
LC7_%=:
s_cbranch_execz LRETA_%=
ds_read_b128 v[204:207], v208 offset:45056
v_bitop3_b32 v248, v248, v251, 1 bitop3:0xa8
v_mfma_f32_16x16x32_bf16 v[92:95], v[172:175], a[92:95], v[92:95]
v_subrev_u32 v227, s30, v227
s_waitcnt lgkmcnt(4)
v_mfma_f32_16x16x32_bf16 v[96:99], v[176:179], a[88:91], v[96:99]
ds_read_b128 v[160:163], v208 offset:34816
s_xor_b32 s80, s82, 384
v_mfma_f32_16x16x32_bf16 v[112:115], v[176:179], a[92:95], v[112:115]
buffer_load_dwordx4 v226, s[36:39], s80 offen offset:2048 lds
v_mfma_f32_16x16x32_bf16 v[100:103], v[180:183], a[88:91], v[100:103]
ds_read_b128 v[164:167], v208 offset:38912
v_min3_u32 v236, v49, v51, s78
v_mfma_f32_16x16x32_bf16 v[116:119], v[180:183], a[92:95], v[116:119]
v_cmp_ne_u32 vcc, 0, v248
s_nop 0
s_cbranch_vccnz LSt2k6_%=
LRt2k6_%=:
v_mfma_f32_16x16x32_bf16 v[104:107], v[184:187], a[88:91], v[104:107]
ds_read_b128 v[168:171], v208 offset:43008
v_max3_u32 v237, v49, v51, s78
v_mfma_f32_16x16x32_bf16 v[120:123], v[184:187], a[92:95], v[120:123]
buffer_load_dwordx4 v227, s[36:39], s80 offen offset:3072 lds
v_mfma_f32_16x16x32_bf16 v[108:111], v[188:191], a[88:91], v[108:111]
ds_read_b128 v[172:175], v208 offset:47104
v_min3_u32 v236, v53, v55, v236
v_mfma_f32_16x16x32_bf16 v[124:127], v[188:191], a[92:95], v[124:127]
v_max3_u32 v237, v53, v55, v237
s_waitcnt lgkmcnt(4)
v_mfma_f32_16x16x32_bf16 v[64:67], v[192:195], a[96:99], v[64:67]
ds_read_b128 v[176:179], v209 offset:32768
v_mfma_f32_16x16x32_bf16 v[80:83], v[192:195], a[100:103], v[80:83]
v_cmp_ne_u32 vcc, v236, v237
s_nop 0
s_cbranch_vccz LRm1_%=
LSm1_%=:
s_sub_u32 s84, s84, 1
s_cbranch_scc1 LZm1_%=
s_mov_b32 s7, 0
buffer_load_dwordx4 v[48:51], v238, s[52:55], s7 offen sc1
s_mov_b32 s7, 32768
buffer_load_dwordx4 v[52:55], v238, s[52:55], s7 offen sc1
s_waitcnt vmcnt(0)
v_min3_u32 v236, v49, v51, s78
v_max3_u32 v237, v49, v51, s78
v_min3_u32 v236, v53, v55, v236
v_max3_u32 v237, v53, v55, v237
v_cmp_ne_u32 vcc, v236, v237
s_nop 0
s_cbranch_vccnz LSm1_%=
LZm1_%=:
s_max_i32 s84, s84, 0
LRm1_%=:
v_mfma_f32_16x16x32_bf16 v[68:71], v[196:199], a[96:99], v[68:71]
ds_read_b128 v[180:183], v209 offset:36864
v_add_f32 v48, v48, v52
v_mfma_f32_16x16x32_bf16 v[84:87], v[196:199], a[100:103], v[84:87]
buffer_load_dword v253, v254, s[52:55], 0 offen sc1
v_mfma_f32_16x16x32_bf16 v[72:75], v[200:203], a[96:99], v[72:75]
ds_read_b128 v[184:187], v209 offset:40960
s_xor_b32 s81, s83, 10240
v_mfma_f32_16x16x32_bf16 v[88:91], v[200:203], a[100:103], v[88:91]
buffer_load_dwordx4 a[160:163], v228, s[52:55], s81 offen offset:0 sc1
v_mfma_f32_16x16x32_bf16 v[76:79], v[204:207], a[96:99], v[76:79]
ds_read_b128 v[188:191], v209 offset:45056
v_add_f32 v50, v50, v54
v_mfma_f32_16x16x32_bf16 v[92:95], v[204:207], a[100:103], v[92:95]
v_and_b32 v238, 31, v231
s_waitcnt lgkmcnt(4)
v_mfma_f32_16x16x32_bf16 v[96:99], v[160:163], a[96:99], v[96:99]
ds_read_b128 v[192:195], v209 offset:34816
v_lshl_add_u32 v238, v238, 1, v240
v_mfma_f32_16x16x32_bf16 v[112:115], v[160:163], a[100:103], v[112:115]
buffer_load_dwordx4 a[164:167], v229, s[52:55], s81 offen offset:0 sc1
v_mfma_f32_16x16x32_bf16 v[100:103], v[164:167], a[96:99], v[100:103]
ds_read_b128 v[196:199], v209 offset:38912
v_permlane32_swap_b32 v48, v50
v_mfma_f32_16x16x32_bf16 v[116:119], v[164:167], a[100:103], v[116:119]
buffer_load_dwordx4 a[168:171], v228, s[52:55], s81 offen offset:1024 sc1
v_mfma_f32_16x16x32_bf16 v[104:107], v[168:171], a[96:99], v[104:107]
ds_read_b128 v[200:203], v209 offset:43008
v_add_f32 v48, v48, v50
v_mfma_f32_16x16x32_bf16 v[120:123], v[168:171], a[100:103], v[120:123]
buffer_load_dwordx4 a[172:175], v229, s[52:55], s81 offen offset:1024 sc1
v_mfma_f32_16x16x32_bf16 v[108:111], v[172:175], a[96:99], v[108:111]
ds_read_b128 v[204:207], v209 offset:47104
s_xor_b32 s81, s83, 14336
v_mfma_f32_16x16x32_bf16 v[124:127], v[172:175], a[100:103], v[124:127]
s_waitcnt vmcnt(17) lgkmcnt(4)
s_barrier
v_mfma_f32_16x16x32_bf16 v[64:67], v[176:179], a[104:107], v[64:67]
ds_read_b128 v[160:163], v210 offset:0
v_accvgpr_read_b32 v248, a112
v_mfma_f32_16x16x32_bf16 v[80:83], v[176:179], a[108:111], v[80:83]
buffer_load_dwordx4 a[176:179], v228, s[52:55], s81 offen offset:0 sc1
v_mfma_f32_16x16x32_bf16 v[68:71], v[180:183], a[104:107], v[68:71]
ds_read_b128 v[164:167], v210 offset:4096
v_accvgpr_read_b32 v249, a116
v_mfma_f32_16x16x32_bf16 v[84:87], v[180:183], a[108:111], v[84:87]
v_accvgpr_read_b32 v250, a120
v_or3_b32 v248, v248, v249, v250
v_mfma_f32_16x16x32_bf16 v[72:75], v[184:187], a[104:107], v[72:75]
ds_read_b128 v[168:171], v210 offset:8192
v_accvgpr_read_b32 v251, a124
v_mfma_f32_16x16x32_bf16 v[88:91], v[184:187], a[108:111], v[88:91]
buffer_load_dwordx4 a[180:183], v229, s[52:55], s81 offen offset:0 sc1
v_mfma_f32_16x16x32_bf16 v[76:79], v[188:191], a[104:107], v[76:79]
ds_read_b128 v[172:175], v210 offset:12288
v_bitop3_b32 v248, v248, v251, 1 bitop3:0xa8
v_mfma_f32_16x16x32_bf16 v[92:95], v[188:191], a[108:111], v[92:95]
v_cvt_pk_bf16_f32 v48, v48, v48
s_waitcnt lgkmcnt(4)
v_mfma_f32_16x16x32_bf16 v[96:99], v[192:195], a[104:107], v[96:99]
ds_read_b128 v[176:179], v210 offset:2048
v_lshlrev_b32 v48, 16, v48
v_mfma_f32_16x16x32_bf16 v[112:115], v[192:195], a[108:111], v[112:115]
buffer_load_dwordx4 a[184:187], v228, s[52:55], s81 offen offset:1024 sc1
v_mfma_f32_16x16x32_bf16 v[100:103], v[196:199], a[104:107], v[100:103]
ds_read_b128 v[180:183], v210 offset:6144
v_add_f32 v48, s92, v48
v_mfma_f32_16x16x32_bf16 v[116:119], v[196:199], a[108:111], v[116:119]
v_cmp_ne_u32 vcc, 0, v248
s_nop 0
s_cbranch_vccnz LSt2k7_%=
LRt2k7_%=:
v_mfma_f32_16x16x32_bf16 v[104:107], v[200:203], a[104:107], v[104:107]
ds_read_b128 v[184:187], v210 offset:10240
v_cvt_pk_bf16_f32 v48, v48, v48
v_mfma_f32_16x16x32_bf16 v[120:123], v[200:203], a[108:111], v[120:123]
buffer_load_dwordx4 a[188:191], v229, s[52:55], s81 offen offset:1024 sc1
v_mfma_f32_16x16x32_bf16 v[108:111], v[204:207], a[104:107], v[108:111]
ds_read_b128 v[188:191], v210 offset:14336
v_lshlrev_b32 v48, 16, v48
v_mfma_f32_16x16x32_bf16 v[124:127], v[204:207], a[108:111], v[124:127]
s_xor_b32 s80, s82, 640
s_waitcnt lgkmcnt(4)
v_mfma_f32_16x16x32_bf16 v[64:67], v[160:163], a[112:115], v[64:67]
ds_read_b128 v[192:195], v211 offset:0
s_add_u32 m0, s77, 81920
v_mfma_f32_16x16x32_bf16 v[80:83], v[160:163], a[116:119], v[80:83]
buffer_load_dwordx4 v224, s[36:39], s80 offen offset:0 lds
v_mfma_f32_16x16x32_bf16 v[68:71], v[164:167], a[112:115], v[68:71]
ds_read_b128 v[196:199], v211 offset:4096
s_lshl_b32 s85, s72, 8
v_mfma_f32_16x16x32_bf16 v[84:87], v[164:167], a[116:119], v[84:87]
buffer_load_dwordx4 v225, s[36:39], s80 offen offset:1024 lds
v_mfma_f32_16x16x32_bf16 v[72:75], v[168:171], a[112:115], v[72:75]
ds_read_b128 v[200:203], v211 offset:8192
s_xor_b32 s80, s82, 896
v_mfma_f32_16x16x32_bf16 v[88:91], v[168:171], a[116:119], v[88:91]
buffer_load_dwordx4 v226, s[36:39], s80 offen offset:2048 lds
v_mfma_f32_16x16x32_bf16 v[76:79], v[172:175], a[112:115], v[76:79]
ds_read_b128 v[204:207], v211 offset:12288
s_add_u32 s85, s85, 150528
v_mfma_f32_16x16x32_bf16 v[92:95], v[172:175], a[116:119], v[92:95]
v_lshl_add_u32 v238, v238, 2, s85
s_waitcnt lgkmcnt(4)
v_mfma_f32_16x16x32_bf16 v[96:99], v[176:179], a[112:115], v[96:99]
ds_read_b128 v[160:163], v211 offset:2048
ds_write_b32 v238, v48
v_mfma_f32_16x16x32_bf16 v[112:115], v[176:179], a[116:119], v[112:115]
buffer_load_dwordx4 v227, s[36:39], s80 offen offset:3072 lds
v_mfma_f32_16x16x32_bf16 v[100:103], v[180:183], a[112:115], v[100:103]
ds_read_b128 v[164:167], v211 offset:6144
s_xor_b32 s81, s83, 18432
v_mfma_f32_16x16x32_bf16 v[116:119], v[180:183], a[116:119], v[116:119]
buffer_load_dwordx4 a[192:195], v228, s[52:55], s81 offen offset:0 sc1
v_mfma_f32_16x16x32_bf16 v[104:107], v[184:187], a[112:115], v[104:107]
ds_read_b128 v[168:171], v211 offset:10240
v_permlane16_swap_b32 v32, v34
v_mfma_f32_16x16x32_bf16 v[120:123], v[184:187], a[116:119], v[120:123]
buffer_load_dwordx4 a[196:199], v229, s[52:55], s81 offen offset:0 sc1
v_mfma_f32_16x16x32_bf16 v[108:111], v[188:191], a[112:115], v[108:111]
ds_read_b128 v[172:175], v211 offset:14336
v_permlane16_swap_b32 v33, v35
v_mfma_f32_16x16x32_bf16 v[124:127], v[188:191], a[116:119], v[124:127]
s_waitcnt vmcnt(15) lgkmcnt(5)
s_barrier
v_mfma_f32_16x16x32_bf16 v[64:67], v[192:195], a[120:123], v[64:67]
ds_read_b128 v[176:179], v208 offset:49152
v_accvgpr_read_b32 v248, a128
v_mfma_f32_16x16x32_bf16 v[80:83], v[192:195], a[124:127], v[80:83]
buffer_load_dwordx4 a[200:203], v228, s[52:55], s81 offen offset:1024 sc1
v_mfma_f32_16x16x32_bf16 v[68:71], v[196:199], a[120:123], v[68:71]
ds_read_b128 v[180:183], v208 offset:53248
v_accvgpr_read_b32 v249, a132
v_accvgpr_read_b32 v250, a136
v_mfma_f32_16x16x32_bf16 v[84:87], v[196:199], a[124:127], v[84:87]
v_or3_b32 v248, v248, v249, v250
v_accvgpr_read_b32 v251, a140
v_mfma_f32_16x16x32_bf16 v[72:75], v[200:203], a[120:123], v[72:75]
ds_read_b128 v[184:187], v208 offset:57344
v_accvgpr_read_b32 v252, a144
v_mfma_f32_16x16x32_bf16 v[88:91], v[200:203], a[124:127], v[88:91]
buffer_load_dwordx4 a[204:207], v229, s[52:55], s81 offen offset:1024 sc1
v_mfma_f32_16x16x32_bf16 v[76:79], v[204:207], a[120:123], v[76:79]
ds_read_b128 v[188:191], v208 offset:61440
v_or3_b32 v248, v248, v251, v252
v_accvgpr_read_b32 v249, a148
v_mfma_f32_16x16x32_bf16 v[92:95], v[204:207], a[124:127], v[92:95]
v_accvgpr_read_b32 v250, a152
s_waitcnt lgkmcnt(4)
v_mfma_f32_16x16x32_bf16 v[96:99], v[160:163], a[120:123], v[96:99]
ds_read_b128 v[192:195], v209 offset:49152
v_or3_b32 v248, v248, v249, v250
s_xor_b32 s81, s83, 22528
v_mfma_f32_16x16x32_bf16 v[112:115], v[160:163], a[124:127], v[112:115]
buffer_load_dwordx4 a[208:211], v228, s[52:55], s81 offen offset:0 sc1
v_mfma_f32_16x16x32_bf16 v[100:103], v[164:167], a[120:123], v[100:103]
ds_read_b128 v[196:199], v209 offset:53248
v_accvgpr_read_b32 v251, a156
v_bitop3_b32 v248, v248, v251, 1 bitop3:0xa8
v_mfma_f32_16x16x32_bf16 v[116:119], v[164:167], a[124:127], v[116:119]
v_cmp_ne_u32 vcc, 0, v248
s_nop 0
s_cbranch_vccnz LSt2k8_%=
LRt2k8_%=:
v_mfma_f32_16x16x32_bf16 v[104:107], v[168:171], a[120:123], v[104:107]
ds_read_b128 v[200:203], v209 offset:57344
v_permlane16_swap_b32 v36, v38
v_mfma_f32_16x16x32_bf16 v[120:123], v[168:171], a[124:127], v[120:123]
buffer_load_dwordx4 a[212:215], v229, s[52:55], s81 offen offset:0 sc1
v_mfma_f32_16x16x32_bf16 v[108:111], v[172:175], a[120:123], v[108:111]
ds_read_b128 v[204:207], v209 offset:61440
v_permlane16_swap_b32 v37, v39
v_mfma_f32_16x16x32_bf16 v[124:127], v[172:175], a[124:127], v[124:127]
v_permlane16_swap_b32 v40, v42
s_waitcnt lgkmcnt(4)
v_mfma_f32_16x16x32_bf16 v[64:67], v[176:179], a[128:131], v[64:67]
ds_read_b128 v[160:163], v208 offset:51200
v_permlane16_swap_b32 v41, v43
v_mfma_f32_16x16x32_bf16 v[80:83], v[176:179], a[132:135], v[80:83]
buffer_load_dwordx4 a[216:219], v228, s[52:55], s81 offen offset:1024 sc1
v_mfma_f32_16x16x32_bf16 v[68:71], v[180:183], a[128:131], v[68:71]
ds_read_b128 v[164:167], v208 offset:55296
s_xor_b32 s80, s82, 1152
v_mfma_f32_16x16x32_bf16 v[84:87], v[180:183], a[132:135], v[84:87]
buffer_load_dwordx4 a[220:223], v229, s[52:55], s81 offen offset:1024 sc1
v_mfma_f32_16x16x32_bf16 v[72:75], v[184:187], a[128:131], v[72:75]
ds_read_b128 v[168:171], v208 offset:59392
s_add_u32 m0, s77, 0
v_mfma_f32_16x16x32_bf16 v[88:91], v[184:187], a[132:135], v[88:91]
buffer_load_dwordx4 v224, s[36:39], s80 offen offset:0 lds
v_mfma_f32_16x16x32_bf16 v[76:79], v[188:191], a[128:131], v[76:79]
ds_read_b128 v[172:175], v208 offset:63488
v_permlane16_swap_b32 v44, v46
v_mfma_f32_16x16x32_bf16 v[92:95], v[188:191], a[132:135], v[92:95]
v_permlane16_swap_b32 v45, v47
s_waitcnt lgkmcnt(4)
v_mfma_f32_16x16x32_bf16 v[64:67], v[192:195], a[136:139], v[64:67]
)"
R"(
ds_read_b128 v[176:179], v209 offset:51200
v_lshlrev_b32 v0, 16, v32
v_mfma_f32_16x16x32_bf16 v[80:83], v[192:195], a[140:143], v[80:83]
buffer_load_dwordx4 v225, s[36:39], s80 offen offset:1024 lds
v_mfma_f32_16x16x32_bf16 v[68:71], v[196:199], a[136:139], v[68:71]
ds_read_b128 v[180:183], v209 offset:55296
s_xor_b32 s80, s82, 1408
v_mfma_f32_16x16x32_bf16 v[84:87], v[196:199], a[140:143], v[84:87]
buffer_load_dwordx4 v226, s[36:39], s80 offen offset:2048 lds
v_mfma_f32_16x16x32_bf16 v[72:75], v[200:203], a[136:139], v[72:75]
ds_read_b128 v[184:187], v209 offset:59392
v_and_b32 v1, s93, v32
v_mfma_f32_16x16x32_bf16 v[88:91], v[200:203], a[140:143], v[88:91]
buffer_load_dwordx4 v227, s[36:39], s80 offen offset:3072 lds
v_mfma_f32_16x16x32_bf16 v[76:79], v[204:207], a[136:139], v[76:79]
ds_read_b128 v[188:191], v209 offset:63488
s_xor_b32 s81, s83, 26624
v_mfma_f32_16x16x32_bf16 v[92:95], v[204:207], a[140:143], v[92:95]
s_waitcnt vmcnt(12) lgkmcnt(4)
s_barrier
v_mfma_f32_16x16x32_bf16 v[64:67], v[160:163], a[144:147], v[64:67]
ds_read_b128 v[192:195], v210 offset:16384
v_accvgpr_read_b32 v248, a160
v_mfma_f32_16x16x32_bf16 v[80:83], v[160:163], a[148:151], v[80:83]
buffer_load_dwordx4 a[224:227], v228, s[52:55], s81 offen offset:0 sc1
v_mfma_f32_16x16x32_bf16 v[68:71], v[164:167], a[144:147], v[68:71]
ds_read_b128 v[196:199], v210 offset:20480
v_accvgpr_read_b32 v249, a164
v_accvgpr_read_b32 v250, a168
v_mfma_f32_16x16x32_bf16 v[84:87], v[164:167], a[148:151], v[84:87]
v_or3_b32 v248, v248, v249, v250
v_accvgpr_read_b32 v251, a172
v_mfma_f32_16x16x32_bf16 v[72:75], v[168:171], a[144:147], v[72:75]
ds_read_b128 v[200:203], v210 offset:24576
v_accvgpr_read_b32 v252, a176
v_mfma_f32_16x16x32_bf16 v[88:91], v[168:171], a[148:151], v[88:91]
buffer_load_dwordx4 a[228:231], v229, s[52:55], s81 offen offset:0 sc1
v_mfma_f32_16x16x32_bf16 v[76:79], v[172:175], a[144:147], v[76:79]
ds_read_b128 v[204:207], v210 offset:28672
v_or3_b32 v248, v248, v251, v252
v_accvgpr_read_b32 v249, a180
v_mfma_f32_16x16x32_bf16 v[92:95], v[172:175], a[148:151], v[92:95]
v_accvgpr_read_b32 v250, a184
s_waitcnt lgkmcnt(4)
v_mfma_f32_16x16x32_bf16 v[64:67], v[176:179], a[152:155], v[64:67]
ds_read_b128 v[160:163], v211 offset:16384
v_or3_b32 v248, v248, v249, v250
v_mfma_f32_16x16x32_bf16 v[80:83], v[176:179], a[156:159], v[80:83]
buffer_load_dwordx4 a[232:235], v228, s[52:55], s81 offen offset:1024 sc1
v_mfma_f32_16x16x32_bf16 v[68:71], v[180:183], a[152:155], v[68:71]
ds_read_b128 v[164:167], v211 offset:20480
v_accvgpr_read_b32 v251, a188
v_bitop3_b32 v248, v248, v251, 1 bitop3:0xa8
v_mfma_f32_16x16x32_bf16 v[84:87], v[180:183], a[156:159], v[84:87]
v_cmp_ne_u32 vcc, 0, v248
s_nop 0
s_cbranch_vccnz LSt2k10_%=
LRt2k10_%=:
v_mfma_f32_16x16x32_bf16 v[72:75], v[184:187], a[152:155], v[72:75]
ds_read_b128 v[168:171], v211 offset:24576
v_lshlrev_b32 v2, 16, v33
v_mfma_f32_16x16x32_bf16 v[88:91], v[184:187], a[156:159], v[88:91]
buffer_load_dwordx4 a[236:239], v229, s[52:55], s81 offen offset:1024 sc1
v_mfma_f32_16x16x32_bf16 v[76:79], v[188:191], a[152:155], v[76:79]
ds_read_b128 v[172:175], v211 offset:28672
v_and_b32 v3, s93, v33
v_mfma_f32_16x16x32_bf16 v[92:95], v[188:191], a[156:159], v[92:95]
v_lshlrev_b32 v4, 16, v34
s_waitcnt lgkmcnt(4)
v_mfma_f32_16x16x32_bf16 v[64:67], v[192:195], a[160:163], v[64:67]
ds_read_b128 v[176:179], v210 offset:18432
s_xor_b32 s81, s83, 30720
v_mfma_f32_16x16x32_bf16 v[80:83], v[192:195], a[164:167], v[80:83]
buffer_load_dwordx4 a[240:243], v228, s[52:55], s81 offen offset:0 sc1
v_mfma_f32_16x16x32_bf16 v[68:71], v[196:199], a[160:163], v[68:71]
ds_read_b128 v[180:183], v210 offset:22528
v_and_b32 v5, s93, v34
v_mfma_f32_16x16x32_bf16 v[84:87], v[196:199], a[164:167], v[84:87]
v_cmp_ne_u32 vcc, s78, v253
s_nop 0
s_cbranch_vccz LRxd_%=
LSxd_%=:
s_sub_u32 s84, s84, 1
s_cbranch_scc1 LZxd_%=
buffer_load_dword v253, v254, s[52:55], 0 offen sc1
s_waitcnt vmcnt(0)
v_cmp_ne_u32 vcc, s78, v253
s_nop 0
s_cbranch_vccnz LSxd_%=
LZxd_%=:
s_max_i32 s84, s84, 0
LRxd_%=:
v_mfma_f32_16x16x32_bf16 v[72:75], v[200:203], a[160:163], v[72:75]
ds_read_b128 v[184:187], v210 offset:26624
v_lshlrev_b32 v6, 16, v35
v_mfma_f32_16x16x32_bf16 v[88:91], v[200:203], a[164:167], v[88:91]
buffer_load_dwordx4 a[244:247], v229, s[52:55], s81 offen offset:0 sc1
v_mfma_f32_16x16x32_bf16 v[76:79], v[204:207], a[160:163], v[76:79]
ds_read_b128 v[188:191], v210 offset:30720
v_and_b32 v7, s93, v35
v_mfma_f32_16x16x32_bf16 v[92:95], v[204:207], a[164:167], v[92:95]
v_lshlrev_b32 v8, 16, v36
s_waitcnt lgkmcnt(4)
v_mfma_f32_16x16x32_bf16 v[64:67], v[160:163], a[168:171], v[64:67]
ds_read_b128 v[192:195], v211 offset:18432
v_and_b32 v9, s93, v36
v_mfma_f32_16x16x32_bf16 v[80:83], v[160:163], a[172:175], v[80:83]
buffer_load_dwordx4 a[248:251], v228, s[52:55], s81 offen offset:1024 sc1
v_mfma_f32_16x16x32_bf16 v[68:71], v[164:167], a[168:171], v[68:71]
ds_read_b128 v[196:199], v211 offset:22528
s_xor_b32 s80, s82, 1664
v_mfma_f32_16x16x32_bf16 v[84:87], v[164:167], a[172:175], v[84:87]
buffer_load_dwordx4 a[252:255], v229, s[52:55], s81 offen offset:1024 sc1
v_mfma_f32_16x16x32_bf16 v[72:75], v[168:171], a[168:171], v[72:75]
ds_read_b128 v[200:203], v211 offset:26624
s_add_u32 m0, s77, 98304
v_mfma_f32_16x16x32_bf16 v[88:91], v[168:171], a[172:175], v[88:91]
buffer_load_dwordx4 v224, s[36:39], s80 offen offset:0 lds
v_mfma_f32_16x16x32_bf16 v[76:79], v[172:175], a[168:171], v[76:79]
ds_read_b128 v[204:207], v211 offset:30720
v_add_u32 v224, s30, v224
v_mfma_f32_16x16x32_bf16 v[92:95], v[172:175], a[172:175], v[92:95]
s_waitcnt vmcnt(9) lgkmcnt(4)
s_barrier
v_mfma_f32_16x16x32_bf16 v[64:67], v[176:179], a[176:179], v[64:67]
ds_read_b128 v[160:163], v208 offset:0
v_accvgpr_read_b32 v248, a192
v_mfma_f32_16x16x32_bf16 v[80:83], v[176:179], a[180:183], v[80:83]
buffer_load_dwordx4 v225, s[36:39], s80 offen offset:1024 lds
v_mfma_f32_16x16x32_bf16 v[68:71], v[180:183], a[176:179], v[68:71]
ds_read_b128 v[164:167], v208 offset:4096
v_accvgpr_read_b32 v249, a196
v_accvgpr_read_b32 v250, a200
v_mfma_f32_16x16x32_bf16 v[84:87], v[180:183], a[180:183], v[84:87]
v_or3_b32 v248, v248, v249, v250
v_accvgpr_read_b32 v251, a204
v_mfma_f32_16x16x32_bf16 v[72:75], v[184:187], a[176:179], v[72:75]
ds_read_b128 v[168:171], v208 offset:8192
v_accvgpr_read_b32 v252, a208
s_xor_b32 s80, s82, 1920
v_mfma_f32_16x16x32_bf16 v[88:91], v[184:187], a[180:183], v[88:91]
buffer_load_dwordx4 v226, s[36:39], s80 offen offset:2048 lds
v_mfma_f32_16x16x32_bf16 v[76:79], v[188:191], a[176:179], v[76:79]
ds_read_b128 v[172:175], v208 offset:12288
v_or3_b32 v248, v248, v251, v252
v_accvgpr_read_b32 v249, a212
v_mfma_f32_16x16x32_bf16 v[92:95], v[188:191], a[180:183], v[92:95]
v_accvgpr_read_b32 v250, a216
s_waitcnt lgkmcnt(4)
v_mfma_f32_16x16x32_bf16 v[64:67], v[192:195], a[184:187], v[64:67]
ds_read_b128 v[176:179], v209 offset:0
v_or3_b32 v248, v248, v249, v250
v_mfma_f32_16x16x32_bf16 v[80:83], v[192:195], a[188:191], v[80:83]
buffer_load_dwordx4 v227, s[36:39], s80 offen offset:3072 lds
v_mfma_f32_16x16x32_bf16 v[68:71], v[196:199], a[184:187], v[68:71]
ds_read_b128 v[180:183], v209 offset:4096
v_accvgpr_read_b32 v251, a220
v_bitop3_b32 v248, v248, v251, 1 bitop3:0xa8
v_mfma_f32_16x16x32_bf16 v[84:87], v[196:199], a[188:191], v[84:87]
v_cmp_ne_u32 vcc, 0, v248
s_nop 0
s_cbranch_vccnz LSt2k12_%=
LRt2k12_%=:
v_mfma_f32_16x16x32_bf16 v[72:75], v[200:203], a[184:187], v[72:75]
ds_read_b128 v[184:187], v209 offset:8192
v_add_u32 v225, s30, v225
s_xor_b32 s80, s82, 128
s_add_u32 m0, s77, 16384
v_mfma_f32_16x16x32_bf16 v[88:91], v[200:203], a[188:191], v[88:91]
buffer_load_dwordx4 v224, s[36:39], s80 offen offset:0 lds
v_mfma_f32_16x16x32_bf16 v[76:79], v[204:207], a[184:187], v[76:79]
ds_read_b128 v[188:191], v209 offset:12288
v_lshlrev_b32 v10, 16, v37
v_mfma_f32_16x16x32_bf16 v[92:95], v[204:207], a[188:191], v[92:95]
v_add_u32 v226, s30, v226
s_waitcnt lgkmcnt(4)
v_mfma_f32_16x16x32_bf16 v[64:67], v[160:163], a[192:195], v[64:67]
ds_read_b128 v[192:195], v208 offset:2048
v_add_u32 v227, s30, v227
v_mfma_f32_16x16x32_bf16 v[80:83], v[160:163], a[196:199], v[80:83]
buffer_load_dwordx4 v225, s[36:39], s80 offen offset:1024 lds
v_mfma_f32_16x16x32_bf16 v[68:71], v[164:167], a[192:195], v[68:71]
ds_read_b128 v[196:199], v208 offset:6144
s_xor_b32 s80, s82, 384
v_mfma_f32_16x16x32_bf16 v[84:87], v[164:167], a[196:199], v[84:87]
buffer_load_dwordx4 v226, s[36:39], s80 offen offset:2048 lds
v_mfma_f32_16x16x32_bf16 v[72:75], v[168:171], a[192:195], v[72:75]
ds_read_b128 v[200:203], v208 offset:10240
v_and_b32 v11, s93, v37
v_mfma_f32_16x16x32_bf16 v[88:91], v[168:171], a[196:199], v[88:91]
buffer_load_dwordx4 v227, s[36:39], s80 offen offset:3072 lds
v_mfma_f32_16x16x32_bf16 v[76:79], v[172:175], a[192:195], v[76:79]
ds_read_b128 v[204:207], v208 offset:14336
v_lshlrev_b32 v12, 16, v38
v_mfma_f32_16x16x32_bf16 v[92:95], v[172:175], a[196:199], v[92:95]
s_xor_b32 s80, s82, 640
s_waitcnt lgkmcnt(4)
v_mfma_f32_16x16x32_bf16 v[64:67], v[176:179], a[200:203], v[64:67]
ds_read_b128 v[160:163], v209 offset:2048
s_add_u32 m0, s77, 114688
v_mfma_f32_16x16x32_bf16 v[80:83], v[176:179], a[204:207], v[80:83]
buffer_load_dwordx4 v224, s[36:39], s80 offen offset:0 lds
v_mfma_f32_16x16x32_bf16 v[68:71], v[180:183], a[200:203], v[68:71]
ds_read_b128 v[164:167], v209 offset:6144
v_and_b32 v13, s93, v38
v_mfma_f32_16x16x32_bf16 v[84:87], v[180:183], a[204:207], v[84:87]
v_lshlrev_b32 v14, 16, v39
v_and_b32 v15, s93, v39
v_mfma_f32_16x16x32_bf16 v[72:75], v[184:187], a[200:203], v[72:75]
ds_read_b128 v[168:171], v209 offset:10240
v_lshlrev_b32 v16, 16, v40
v_mfma_f32_16x16x32_bf16 v[88:91], v[184:187], a[204:207], v[88:91]
buffer_load_dwordx4 v225, s[36:39], s80 offen offset:1024 lds
v_mfma_f32_16x16x32_bf16 v[76:79], v[188:191], a[200:203], v[76:79]
ds_read_b128 v[172:175], v209 offset:14336
s_xor_b32 s80, s82, 896
v_mfma_f32_16x16x32_bf16 v[92:95], v[188:191], a[204:207], v[92:95]
s_waitcnt vmcnt(6) lgkmcnt(4)
s_barrier
v_mfma_f32_16x16x32_bf16 v[64:67], v[192:195], a[208:211], v[64:67]
ds_read_b128 v[176:179], v210 offset:32768
v_accvgpr_read_b32 v248, a224
v_mfma_f32_16x16x32_bf16 v[80:83], v[192:195], a[212:215], v[80:83]
buffer_load_dwordx4 v226, s[36:39], s80 offen offset:2048 lds
v_mfma_f32_16x16x32_bf16 v[68:71], v[196:199], a[208:211], v[68:71]
ds_read_b128 v[180:183], v210 offset:36864
v_accvgpr_read_b32 v249, a228
v_accvgpr_read_b32 v250, a232
v_mfma_f32_16x16x32_bf16 v[84:87], v[196:199], a[212:215], v[84:87]
v_or3_b32 v248, v248, v249, v250
v_accvgpr_read_b32 v251, a236
v_mfma_f32_16x16x32_bf16 v[72:75], v[200:203], a[208:211], v[72:75]
ds_read_b128 v[184:187], v210 offset:40960
v_accvgpr_read_b32 v252, a240
v_mfma_f32_16x16x32_bf16 v[88:91], v[200:203], a[212:215], v[88:91]
buffer_load_dwordx4 v227, s[36:39], s80 offen offset:3072 lds
v_mfma_f32_16x16x32_bf16 v[76:79], v[204:207], a[208:211], v[76:79]
ds_read_b128 v[188:191], v210 offset:45056
v_or3_b32 v248, v248, v251, v252
v_accvgpr_read_b32 v249, a244
v_mfma_f32_16x16x32_bf16 v[92:95], v[204:207], a[212:215], v[92:95]
v_accvgpr_read_b32 v250, a248
s_waitcnt lgkmcnt(4)
v_mfma_f32_16x16x32_bf16 v[64:67], v[160:163], a[216:219], v[64:67]
ds_read_b128 v[192:195], v211 offset:32768
v_or3_b32 v248, v248, v249, v250
s_xor_b32 s80, s82, 1152
s_add_u32 m0, s77, 32768
v_mfma_f32_16x16x32_bf16 v[80:83], v[160:163], a[220:223], v[80:83]
buffer_load_dwordx4 v224, s[36:39], s80 offen offset:0 lds
v_mfma_f32_16x16x32_bf16 v[68:71], v[164:167], a[216:219], v[68:71]
ds_read_b128 v[196:199], v211 offset:36864
v_accvgpr_read_b32 v251, a252
v_bitop3_b32 v248, v248, v251, 1 bitop3:0xa8
v_mfma_f32_16x16x32_bf16 v[84:87], v[164:167], a[220:223], v[84:87]
v_cmp_ne_u32 vcc, 0, v248
s_nop 0
s_cbranch_vccnz LSt2k14_%=
LRt2k14_%=:
v_mfma_f32_16x16x32_bf16 v[72:75], v[168:171], a[216:219], v[72:75]
ds_read_b128 v[200:203], v211 offset:40960
v_and_b32 v17, s93, v40
v_mfma_f32_16x16x32_bf16 v[88:91], v[168:171], a[220:223], v[88:91]
buffer_load_dwordx4 v225, s[36:39], s80 offen offset:1024 lds
v_mfma_f32_16x16x32_bf16 v[76:79], v[172:175], a[216:219], v[76:79]
ds_read_b128 v[204:207], v211 offset:45056
v_lshlrev_b32 v18, 16, v41
v_mfma_f32_16x16x32_bf16 v[92:95], v[172:175], a[220:223], v[92:95]
v_and_b32 v19, s93, v41
s_waitcnt lgkmcnt(4)
v_mfma_f32_16x16x32_bf16 v[64:67], v[176:179], a[224:227], v[64:67]
ds_read_b128 v[160:163], v210 offset:34816
s_xor_b32 s80, s82, 1408
v_mfma_f32_16x16x32_bf16 v[80:83], v[176:179], a[228:231], v[80:83]
buffer_load_dwordx4 v226, s[36:39], s80 offen offset:2048 lds
v_mfma_f32_16x16x32_bf16 v[68:71], v[180:183], a[224:227], v[68:71]
ds_read_b128 v[164:167], v210 offset:38912
v_lshlrev_b32 v20, 16, v42
v_mfma_f32_16x16x32_bf16 v[84:87], v[180:183], a[228:231], v[84:87]
v_and_b32 v21, s93, v42
v_lshlrev_b32 v22, 16, v43
v_mfma_f32_16x16x32_bf16 v[72:75], v[184:187], a[224:227], v[72:75]
ds_read_b128 v[168:171], v210 offset:43008
v_and_b32 v23, s93, v43
v_mfma_f32_16x16x32_bf16 v[88:91], v[184:187], a[228:231], v[88:91]
buffer_load_dwordx4 v227, s[36:39], s80 offen offset:3072 lds
v_mfma_f32_16x16x32_bf16 v[76:79], v[188:191], a[224:227], v[76:79]
ds_read_b128 v[172:175], v210 offset:47104
v_lshlrev_b32 v24, 16, v44
v_mfma_f32_16x16x32_bf16 v[92:95], v[188:191], a[228:231], v[92:95]
s_xor_b32 s80, s82, 1664
s_waitcnt lgkmcnt(4)
v_mfma_f32_16x16x32_bf16 v[64:67], v[192:195], a[232:235], v[64:67]
ds_read_b128 v[176:179], v211 offset:34816
s_add_u32 m0, s77, 65536
v_mfma_f32_16x16x32_bf16 v[80:83], v[192:195], a[236:239], v[80:83]
buffer_load_dwordx4 v224, s[36:39], s80 offen offset:0 lds
v_mfma_f32_16x16x32_bf16 v[68:71], v[196:199], a[232:235], v[68:71]
ds_read_b128 v[180:183], v211 offset:38912
v_and_b32 v25, s93, v44
v_mfma_f32_16x16x32_bf16 v[84:87], v[196:199], a[236:239], v[84:87]
v_lshlrev_b32 v26, 16, v45
v_and_b32 v27, s93, v45
v_mfma_f32_16x16x32_bf16 v[72:75], v[200:203], a[232:235], v[72:75]
ds_read_b128 v[184:187], v211 offset:43008
v_lshlrev_b32 v28, 16, v46
v_mfma_f32_16x16x32_bf16 v[88:91], v[200:203], a[236:239], v[88:91]
buffer_load_dwordx4 v225, s[36:39], s80 offen offset:1024 lds
v_mfma_f32_16x16x32_bf16 v[76:79], v[204:207], a[232:235], v[76:79]
ds_read_b128 v[188:191], v211 offset:47104
v_and_b32 v29, s93, v46
v_mfma_f32_16x16x32_bf16 v[92:95], v[204:207], a[236:239], v[92:95]
s_waitcnt vmcnt(10) lgkmcnt(4)
s_barrier
v_mfma_f32_16x16x32_bf16 v[64:67], v[160:163], a[240:243], v[64:67]
ds_read_b128 v[192:195], v208 offset:16384
s_xor_b32 s80, s82, 1920
v_mfma_f32_16x16x32_bf16 v[80:83], v[160:163], a[244:247], v[80:83]
buffer_load_dwordx4 v226, s[36:39], s80 offen offset:2048 lds
v_mfma_f32_16x16x32_bf16 v[68:71], v[164:167], a[240:243], v[68:71]
ds_read_b128 v[196:199], v208 offset:20480
v_lshlrev_b32 v30, 16, v47
v_mfma_f32_16x16x32_bf16 v[84:87], v[164:167], a[244:247], v[84:87]
v_and_b32 v31, s93, v47
v_mfma_f32_16x16x32_bf16 v[72:75], v[168:171], a[240:243], v[72:75]
ds_read_b128 v[200:203], v208 offset:24576
v_mfma_f32_16x16x32_bf16 v[88:91], v[168:171], a[244:247], v[88:91]
buffer_load_dwordx4 v227, s[36:39], s80 offen offset:3072 lds
v_mfma_f32_16x16x32_bf16 v[76:79], v[172:175], a[240:243], v[76:79]
ds_read_b128 v[204:207], v208 offset:28672
v_mfma_f32_16x16x32_bf16 v[92:95], v[172:175], a[244:247], v[92:95]
s_waitcnt lgkmcnt(4)
v_mfma_f32_16x16x32_bf16 v[64:67], v[176:179], a[248:251], v[64:67]
ds_read_b128 v[160:163], v209 offset:16384
v_mfma_f32_16x16x32_bf16 v[80:83], v[176:179], a[252:255], v[80:83]
v_mfma_f32_16x16x32_bf16 v[68:71], v[180:183], a[248:251], v[68:71]
LC8_%=:
s_cbranch_execz LRETA_%=
ds_read_b128 v[164:167], v209 offset:20480
v_mfma_f32_16x16x32_bf16 v[84:87], v[180:183], a[252:255], v[84:87]
v_mfma_f32_16x16x32_bf16 v[72:75], v[184:187], a[248:251], v[72:75]
ds_read_b128 v[168:171], v209 offset:24576
v_mfma_f32_16x16x32_bf16 v[88:91], v[184:187], a[252:255], v[88:91]
v_mfma_f32_16x16x32_bf16 v[76:79], v[188:191], a[248:251], v[76:79]
ds_read_b128 v[172:175], v209 offset:28672
v_mfma_f32_16x16x32_bf16 v[92:95], v[188:191], a[252:255], v[92:95]
s_waitcnt lgkmcnt(4)
v_mfma_f32_16x16x32_bf16 v[96:99], v[192:195], a[128:131], v[96:99]
ds_read_b128 v[176:179], v208 offset:18432
v_mfma_f32_16x16x32_bf16 v[112:115], v[192:195], a[132:135], v[112:115]
v_mfma_f32_16x16x32_bf16 v[100:103], v[196:199], a[128:131], v[100:103]
ds_read_b128 v[180:183], v208 offset:22528
v_mfma_f32_16x16x32_bf16 v[116:119], v[196:199], a[132:135], v[116:119]
v_cvt_pk_bf16_f32 v128, v64, v65
v_cvt_pk_bf16_f32 v129, v66, v67
v_mfma_f32_16x16x32_bf16 v[104:107], v[200:203], a[128:131], v[104:107]
ds_read_b128 v[184:187], v208 offset:26624
v_cvt_pk_bf16_f32 v130, v68, v69
v_mfma_f32_16x16x32_bf16 v[120:123], v[200:203], a[132:135], v[120:123]
v_cvt_pk_bf16_f32 v131, v70, v71
v_or_b32 v128, 1, v128
v_mfma_f32_16x16x32_bf16 v[108:111], v[204:207], a[128:131], v[108:111]
ds_read_b128 v[188:191], v208 offset:30720
v_cvt_pk_bf16_f32 v132, v72, v73
v_mfma_f32_16x16x32_bf16 v[124:127], v[204:207], a[132:135], v[124:127]
v_cvt_pk_bf16_f32 v133, v74, v75
s_waitcnt lgkmcnt(4)
v_mfma_f32_16x16x32_bf16 v[96:99], v[160:163], a[136:139], v[96:99]
ds_read_b128 v[192:195], v209 offset:18432
v_cvt_pk_bf16_f32 v134, v76, v77
v_mfma_f32_16x16x32_bf16 v[112:115], v[160:163], a[140:143], v[112:115]
buffer_store_dwordx4 v[128:131], v230, s[52:55], s20 offen offset:0 sc1
v_mfma_f32_16x16x32_bf16 v[100:103], v[164:167], a[136:139], v[100:103]
ds_read_b128 v[196:199], v209 offset:22528
v_cvt_pk_bf16_f32 v135, v78, v79
v_mfma_f32_16x16x32_bf16 v[116:119], v[164:167], a[140:143], v[116:119]
v_or_b32 v132, 1, v132
v_cvt_pk_bf16_f32 v136, v80, v81
v_mfma_f32_16x16x32_bf16 v[104:107], v[168:171], a[136:139], v[104:107]
ds_read_b128 v[200:203], v209 offset:26624
v_cvt_pk_bf16_f32 v137, v82, v83
v_mfma_f32_16x16x32_bf16 v[120:123], v[168:171], a[140:143], v[120:123]
buffer_store_dwordx4 v[132:135], v230, s[52:55], s20 offen offset:1024 sc1
v_mfma_f32_16x16x32_bf16 v[108:111], v[172:175], a[136:139], v[108:111]
ds_read_b128 v[204:207], v209 offset:30720
v_cvt_pk_bf16_f32 v138, v84, v85
v_mfma_f32_16x16x32_bf16 v[124:127], v[172:175], a[140:143], v[124:127]
s_waitcnt vmcnt(10) lgkmcnt(4)
s_barrier
v_mfma_f32_16x16x32_bf16 v[96:99], v[176:179], a[144:147], v[96:99]
ds_read_b128 v[160:163], v210 offset:49152
v_cvt_pk_bf16_f32 v139, v86, v87
v_mfma_f32_16x16x32_bf16 v[112:115], v[176:179], a[148:151], v[112:115]
v_or_b32 v136, 1, v136
v_cvt_pk_bf16_f32 v140, v88, v89
v_mfma_f32_16x16x32_bf16 v[100:103], v[180:183], a[144:147], v[100:103]
ds_read_b128 v[164:167], v210 offset:53248
v_cvt_pk_bf16_f32 v141, v90, v91
v_mfma_f32_16x16x32_bf16 v[116:119], v[180:183], a[148:151], v[116:119]
buffer_store_dwordx4 v[136:139], v230, s[52:55], s20 offen offset:2048 sc1
)"
R"(
v_mfma_f32_16x16x32_bf16 v[104:107], v[184:187], a[144:147], v[104:107]
ds_read_b128 v[168:171], v210 offset:57344
v_cvt_pk_bf16_f32 v142, v92, v93
v_mfma_f32_16x16x32_bf16 v[120:123], v[184:187], a[148:151], v[120:123]
v_cvt_pk_bf16_f32 v143, v94, v95
v_or_b32 v140, 1, v140
v_mfma_f32_16x16x32_bf16 v[108:111], v[188:191], a[144:147], v[108:111]
ds_read_b128 v[172:175], v210 offset:61440
v_lshrrev_b32 v238, 4, v231
v_mfma_f32_16x16x32_bf16 v[124:127], v[188:191], a[148:151], v[124:127]
v_lshlrev_b32 v238, 4, v238
s_waitcnt lgkmcnt(4)
v_mfma_f32_16x16x32_bf16 v[96:99], v[192:195], a[152:155], v[96:99]
ds_read_b128 v[176:179], v211 offset:49152
v_add_u32 v238, 150528, v238
v_mfma_f32_16x16x32_bf16 v[112:115], v[192:195], a[156:159], v[112:115]
buffer_store_dwordx4 v[140:143], v230, s[52:55], s20 offen offset:3072 sc1
v_mfma_f32_16x16x32_bf16 v[100:103], v[196:199], a[152:155], v[100:103]
ds_read_b128 v[180:183], v211 offset:53248
ds_read_b128 v[48:51], v238 offset:0
v_mfma_f32_16x16x32_bf16 v[116:119], v[196:199], a[156:159], v[116:119]
ds_read_b128 v[52:55], v238 offset:64
ds_read_b128 v[56:59], v238 offset:128
v_mfma_f32_16x16x32_bf16 v[104:107], v[200:203], a[152:155], v[104:107]
ds_read_b128 v[184:187], v211 offset:57344
ds_read_b128 v[60:63], v238 offset:192
v_mfma_f32_16x16x32_bf16 v[120:123], v[200:203], a[156:159], v[120:123]
s_xor_b32 s88, s20, 16384
v_mfma_f32_16x16x32_bf16 v[108:111], v[204:207], a[152:155], v[108:111]
ds_read_b128 v[188:191], v211 offset:61440
v_mfma_f32_16x16x32_bf16 v[124:127], v[204:207], a[156:159], v[124:127]
s_waitcnt lgkmcnt(8)
v_mfma_f32_16x16x32_bf16 v[96:99], v[160:163], a[160:163], v[96:99]
ds_read_b128 v[192:195], v210 offset:51200
v_mfma_f32_16x16x32_bf16 v[112:115], v[160:163], a[164:167], v[112:115]
v_mfma_f32_16x16x32_bf16 v[100:103], v[164:167], a[160:163], v[100:103]
ds_read_b128 v[196:199], v210 offset:55296
v_mfma_f32_16x16x32_bf16 v[116:119], v[164:167], a[164:167], v[116:119]
v_mfma_f32_16x16x32_bf16 v[104:107], v[168:171], a[160:163], v[104:107]
ds_read_b128 v[200:203], v210 offset:59392
v_mfma_f32_16x16x32_bf16 v[120:123], v[168:171], a[164:167], v[120:123]
v_mfma_f32_16x16x32_bf16 v[108:111], v[172:175], a[160:163], v[108:111]
ds_read_b128 v[204:207], v210 offset:63488
v_mfma_f32_16x16x32_bf16 v[124:127], v[172:175], a[164:167], v[124:127]
s_waitcnt lgkmcnt(4)
v_mfma_f32_16x16x32_bf16 v[96:99], v[176:179], a[168:171], v[96:99]
ds_read_b128 v[160:163], v211 offset:51200
v_mfma_f32_16x16x32_bf16 v[112:115], v[176:179], a[172:175], v[112:115]
v_mfma_f32_16x16x32_bf16 v[100:103], v[180:183], a[168:171], v[100:103]
ds_read_b128 v[164:167], v211 offset:55296
v_mfma_f32_16x16x32_bf16 v[116:119], v[180:183], a[172:175], v[116:119]
v_mfma_f32_16x16x32_bf16 v[104:107], v[184:187], a[168:171], v[104:107]
ds_read_b128 v[168:171], v211 offset:59392
v_mfma_f32_16x16x32_bf16 v[120:123], v[184:187], a[172:175], v[120:123]
v_mfma_f32_16x16x32_bf16 v[108:111], v[188:191], a[168:171], v[108:111]
ds_read_b128 v[172:175], v211 offset:63488
v_mfma_f32_16x16x32_bf16 v[124:127], v[188:191], a[172:175], v[124:127]
s_waitcnt vmcnt(8) lgkmcnt(4)
s_barrier
v_mfma_f32_16x16x32_bf16 v[96:99], v[192:195], a[176:179], v[96:99]
ds_read_b128 v[176:179], v208 offset:32768
v_mfma_f32_16x16x32_bf16 v[112:115], v[192:195], a[180:183], v[112:115]
v_mfma_f32_16x16x32_bf16 v[100:103], v[196:199], a[176:179], v[100:103]
ds_read_b128 v[180:183], v208 offset:36864
v_mfma_f32_16x16x32_bf16 v[116:119], v[196:199], a[180:183], v[116:119]
v_mfma_f32_16x16x32_bf16 v[104:107], v[200:203], a[176:179], v[104:107]
ds_read_b128 v[184:187], v208 offset:40960
v_mfma_f32_16x16x32_bf16 v[120:123], v[200:203], a[180:183], v[120:123]
v_mfma_f32_16x16x32_bf16 v[108:111], v[204:207], a[176:179], v[108:111]
ds_read_b128 v[188:191], v208 offset:45056
v_mfma_f32_16x16x32_bf16 v[124:127], v[204:207], a[180:183], v[124:127]
s_waitcnt lgkmcnt(4)
v_mfma_f32_16x16x32_bf16 v[96:99], v[160:163], a[184:187], v[96:99]
ds_read_b128 v[192:195], v209 offset:32768
v_mfma_f32_16x16x32_bf16 v[112:115], v[160:163], a[188:191], v[112:115]
v_mfma_f32_16x16x32_bf16 v[100:103], v[164:167], a[184:187], v[100:103]
ds_read_b128 v[196:199], v209 offset:36864
v_mfma_f32_16x16x32_bf16 v[116:119], v[164:167], a[188:191], v[116:119]
v_mfma_f32_16x16x32_bf16 v[104:107], v[168:171], a[184:187], v[104:107]
ds_read_b128 v[200:203], v209 offset:40960
v_mfma_f32_16x16x32_bf16 v[120:123], v[168:171], a[188:191], v[120:123]
v_mfma_f32_16x16x32_bf16 v[108:111], v[172:175], a[184:187], v[108:111]
ds_read_b128 v[204:207], v209 offset:45056
v_mfma_f32_16x16x32_bf16 v[124:127], v[172:175], a[188:191], v[124:127]
s_waitcnt lgkmcnt(4)
v_mfma_f32_16x16x32_bf16 v[96:99], v[176:179], a[192:195], v[96:99]
ds_read_b128 v[160:163], v208 offset:34816
v_mfma_f32_16x16x32_bf16 v[112:115], v[176:179], a[196:199], v[112:115]
buffer_load_dwordx4 v[144:147], v230, s[52:55], s88 offen offset:0 sc1
v_mfma_f32_16x16x32_bf16 v[100:103], v[180:183], a[192:195], v[100:103]
ds_read_b128 v[164:167], v208 offset:38912
v_mfma_f32_16x16x32_bf16 v[116:119], v[180:183], a[196:199], v[116:119]
buffer_load_dwordx4 v[148:151], v230, s[52:55], s88 offen offset:1024 sc1
v_mfma_f32_16x16x32_bf16 v[104:107], v[184:187], a[192:195], v[104:107]
ds_read_b128 v[168:171], v208 offset:43008
v_mfma_f32_16x16x32_bf16 v[120:123], v[184:187], a[196:199], v[120:123]
buffer_load_dwordx4 v[152:155], v230, s[52:55], s88 offen offset:2048 sc1
v_mfma_f32_16x16x32_bf16 v[108:111], v[188:191], a[192:195], v[108:111]
ds_read_b128 v[172:175], v208 offset:47104
v_mfma_f32_16x16x32_bf16 v[124:127], v[188:191], a[196:199], v[124:127]
s_waitcnt lgkmcnt(4)
v_mfma_f32_16x16x32_bf16 v[96:99], v[192:195], a[200:203], v[96:99]
ds_read_b128 v[176:179], v209 offset:34816
v_mfma_f32_16x16x32_bf16 v[112:115], v[192:195], a[204:207], v[112:115]
buffer_load_dwordx4 v[156:159], v230, s[52:55], s88 offen offset:3072 sc1
v_mfma_f32_16x16x32_bf16 v[100:103], v[196:199], a[200:203], v[100:103]
ds_read_b128 v[180:183], v209 offset:38912
v_mfma_f32_16x16x32_bf16 v[116:119], v[196:199], a[204:207], v[116:119]
v_mfma_f32_16x16x32_bf16 v[104:107], v[200:203], a[200:203], v[104:107]
ds_read_b128 v[184:187], v209 offset:43008
v_mfma_f32_16x16x32_bf16 v[120:123], v[200:203], a[204:207], v[120:123]
v_mfma_f32_16x16x32_bf16 v[108:111], v[204:207], a[200:203], v[108:111]
ds_read_b128 v[188:191], v209 offset:47104
v_mfma_f32_16x16x32_bf16 v[124:127], v[204:207], a[204:207], v[124:127]
s_waitcnt vmcnt(8) lgkmcnt(4)
s_barrier
v_mfma_f32_16x16x32_bf16 v[96:99], v[160:163], a[208:211], v[96:99]
ds_read_b128 v[192:195], v210 offset:0
v_mfma_f32_16x16x32_bf16 v[112:115], v[160:163], a[212:215], v[112:115]
v_mfma_f32_16x16x32_bf16 v[100:103], v[164:167], a[208:211], v[100:103]
ds_read_b128 v[196:199], v210 offset:4096
v_mfma_f32_16x16x32_bf16 v[116:119], v[164:167], a[212:215], v[116:119]
v_mfma_f32_16x16x32_bf16 v[104:107], v[168:171], a[208:211], v[104:107]
ds_read_b128 v[200:203], v210 offset:8192
v_mfma_f32_16x16x32_bf16 v[120:123], v[168:171], a[212:215], v[120:123]
v_mfma_f32_16x16x32_bf16 v[108:111], v[172:175], a[208:211], v[108:111]
ds_read_b128 v[204:207], v210 offset:12288
v_mfma_f32_16x16x32_bf16 v[124:127], v[172:175], a[212:215], v[124:127]
s_waitcnt lgkmcnt(4)
v_mfma_f32_16x16x32_bf16 v[96:99], v[176:179], a[216:219], v[96:99]
ds_read_b128 v[160:163], v211 offset:0
v_mfma_f32_16x16x32_bf16 v[112:115], v[176:179], a[220:223], v[112:115]
v_mfma_f32_16x16x32_bf16 v[100:103], v[180:183], a[216:219], v[100:103]
ds_read_b128 v[164:167], v211 offset:4096
v_mfma_f32_16x16x32_bf16 v[116:119], v[180:183], a[220:223], v[116:119]
v_mfma_f32_16x16x32_bf16 v[104:107], v[184:187], a[216:219], v[104:107]
ds_read_b128 v[168:171], v211 offset:8192
v_mfma_f32_16x16x32_bf16 v[120:123], v[184:187], a[220:223], v[120:123]
v_mfma_f32_16x16x32_bf16 v[108:111], v[188:191], a[216:219], v[108:111]
ds_read_b128 v[172:175], v211 offset:12288
v_mfma_f32_16x16x32_bf16 v[124:127], v[188:191], a[220:223], v[124:127]
s_waitcnt lgkmcnt(4)
v_mfma_f32_16x16x32_bf16 v[96:99], v[192:195], a[224:227], v[96:99]
ds_read_b128 v[176:179], v210 offset:2048
v_mfma_f32_16x16x32_bf16 v[112:115], v[192:195], a[228:231], v[112:115]
v_mfma_f32_16x16x32_bf16 v[100:103], v[196:199], a[224:227], v[100:103]
ds_read_b128 v[180:183], v210 offset:6144
v_mfma_f32_16x16x32_bf16 v[116:119], v[196:199], a[228:231], v[116:119]
v_mfma_f32_16x16x32_bf16 v[104:107], v[200:203], a[224:227], v[104:107]
ds_read_b128 v[184:187], v210 offset:10240
v_mfma_f32_16x16x32_bf16 v[120:123], v[200:203], a[228:231], v[120:123]
v_mfma_f32_16x16x32_bf16 v[108:111], v[204:207], a[224:227], v[108:111]
ds_read_b128 v[188:191], v210 offset:14336
v_mfma_f32_16x16x32_bf16 v[124:127], v[204:207], a[228:231], v[124:127]
s_waitcnt lgkmcnt(4)
v_mfma_f32_16x16x32_bf16 v[96:99], v[160:163], a[232:235], v[96:99]
ds_read_b128 v[192:195], v211 offset:2048
v_mfma_f32_16x16x32_bf16 v[112:115], v[160:163], a[236:239], v[112:115]
v_mfma_f32_16x16x32_bf16 v[100:103], v[164:167], a[232:235], v[100:103]
ds_read_b128 v[196:199], v211 offset:6144
v_mfma_f32_16x16x32_bf16 v[116:119], v[164:167], a[236:239], v[116:119]
v_mfma_f32_16x16x32_bf16 v[104:107], v[168:171], a[232:235], v[104:107]
ds_read_b128 v[200:203], v211 offset:10240
v_mfma_f32_16x16x32_bf16 v[120:123], v[168:171], a[236:239], v[120:123]
v_mfma_f32_16x16x32_bf16 v[108:111], v[172:175], a[232:235], v[108:111]
ds_read_b128 v[204:207], v211 offset:14336
v_mfma_f32_16x16x32_bf16 v[124:127], v[172:175], a[236:239], v[124:127]
s_waitcnt lgkmcnt(4)
v_mfma_f32_16x16x32_bf16 v[96:99], v[176:179], a[240:243], v[96:99]
s_waitcnt vmcnt(1)
v_bitop3_b32 v236, v144, v148, v152 bitop3:0x80
s_waitcnt vmcnt(0)
v_bitop3_b32 v236, v236, v156, 1 bitop3:0x80
v_cmp_ne_u32 vcc, 1, v236
s_nop 0
s_cbranch_vccz LRr2_%=
LSr2_%=:
s_sub_u32 s84, s84, 1
s_cbranch_scc1 LZr2_%=
buffer_load_dwordx4 v[144:147], v230, s[52:55], s88 offen offset:0 sc1
buffer_load_dwordx4 v[148:151], v230, s[52:55], s88 offen offset:1024 sc1
buffer_load_dwordx4 v[152:155], v230, s[52:55], s88 offen offset:2048 sc1
buffer_load_dwordx4 v[156:159], v230, s[52:55], s88 offen offset:3072 sc1
s_waitcnt vmcnt(0)
v_bitop3_b32 v236, v144, v148, v152 bitop3:0x80
v_bitop3_b32 v236, v236, v156, 1 bitop3:0x80
v_cmp_ne_u32 vcc, 1, v236
s_nop 0
s_cbranch_vccnz LSr2_%=
LZr2_%=:
s_max_i32 s84, s84, 0
LRr2_%=:
v_and_b32 v128, s93, v144
v_lshlrev_b32 v144, 16, v144
v_mfma_f32_16x16x32_bf16 v[112:115], v[176:179], a[244:247], v[112:115]
v_fmac_f32 v0, v144, v48
v_fmac_f32 v1, v128, v49
v_and_b32 v129, s93, v145
v_lshlrev_b32 v145, 16, v145
v_mfma_f32_16x16x32_bf16 v[100:103], v[180:183], a[240:243], v[100:103]
v_fmac_f32 v2, v145, v50
v_fmac_f32 v3, v129, v51
v_and_b32 v130, s93, v146
v_lshlrev_b32 v146, 16, v146
v_mfma_f32_16x16x32_bf16 v[116:119], v[180:183], a[244:247], v[116:119]
v_fmac_f32 v4, v146, v52
v_fmac_f32 v5, v130, v53
v_and_b32 v131, s93, v147
v_lshlrev_b32 v147, 16, v147
v_mfma_f32_16x16x32_bf16 v[104:107], v[184:187], a[240:243], v[104:107]
v_fmac_f32 v6, v147, v54
v_fmac_f32 v7, v131, v55
v_and_b32 v132, s93, v148
v_lshlrev_b32 v148, 16, v148
v_mfma_f32_16x16x32_bf16 v[120:123], v[184:187], a[244:247], v[120:123]
v_fmac_f32 v8, v148, v56
v_fmac_f32 v9, v132, v57
v_and_b32 v133, s93, v149
v_lshlrev_b32 v149, 16, v149
v_fmac_f32 v10, v149, v58
v_mfma_f32_16x16x32_bf16 v[108:111], v[188:191], a[240:243], v[108:111]
v_fmac_f32 v11, v133, v59
v_and_b32 v134, s93, v150
v_lshlrev_b32 v150, 16, v150
v_fmac_f32 v12, v150, v60
v_mfma_f32_16x16x32_bf16 v[124:127], v[188:191], a[244:247], v[124:127]
v_fmac_f32 v13, v134, v61
v_and_b32 v135, s93, v151
v_lshlrev_b32 v151, 16, v151
v_fmac_f32 v14, v151, v62
s_waitcnt lgkmcnt(0)
v_mfma_f32_16x16x32_bf16 v[96:99], v[192:195], a[248:251], v[96:99]
v_fmac_f32 v15, v135, v63
v_and_b32 v136, s93, v152
v_lshlrev_b32 v152, 16, v152
v_fmac_f32 v16, v152, v48
v_mfma_f32_16x16x32_bf16 v[112:115], v[192:195], a[252:255], v[112:115]
v_fmac_f32 v17, v136, v49
v_and_b32 v137, s93, v153
v_lshlrev_b32 v153, 16, v153
v_fmac_f32 v18, v153, v50
v_mfma_f32_16x16x32_bf16 v[100:103], v[196:199], a[248:251], v[100:103]
v_fmac_f32 v19, v137, v51
v_and_b32 v138, s93, v154
v_lshlrev_b32 v154, 16, v154
v_fmac_f32 v20, v154, v52
v_fmac_f32 v21, v138, v53
v_mfma_f32_16x16x32_bf16 v[116:119], v[196:199], a[252:255], v[116:119]
v_and_b32 v139, s93, v155
v_lshlrev_b32 v155, 16, v155
v_fmac_f32 v22, v155, v54
v_fmac_f32 v23, v139, v55
v_mfma_f32_16x16x32_bf16 v[104:107], v[200:203], a[248:251], v[104:107]
v_and_b32 v140, s93, v156
v_lshlrev_b32 v156, 16, v156
v_fmac_f32 v24, v156, v56
v_fmac_f32 v25, v140, v57
v_mfma_f32_16x16x32_bf16 v[120:123], v[200:203], a[252:255], v[120:123]
v_and_b32 v141, s93, v157
v_lshlrev_b32 v157, 16, v157
v_fmac_f32 v26, v157, v58
v_fmac_f32 v27, v141, v59
v_mfma_f32_16x16x32_bf16 v[108:111], v[204:207], a[248:251], v[108:111]
v_and_b32 v142, s93, v158
v_lshlrev_b32 v158, 16, v158
v_fmac_f32 v28, v158, v60
v_fmac_f32 v29, v142, v61
v_mfma_f32_16x16x32_bf16 v[124:127], v[204:207], a[252:255], v[124:127]
v_and_b32 v143, s93, v159
v_lshlrev_b32 v159, 16, v159
v_fmac_f32 v30, v159, v62
v_fmac_f32 v31, v143, v63
s_nop 3
v_pk_fma_f32 v[0:1], v[96:97], v[48:49], v[0:1]
v_pk_fma_f32 v[2:3], v[98:99], v[50:51], v[2:3]
v_pk_fma_f32 v[4:5], v[100:101], v[52:53], v[4:5]
v_pk_fma_f32 v[6:7], v[102:103], v[54:55], v[6:7]
v_pk_fma_f32 v[8:9], v[104:105], v[56:57], v[8:9]
v_pk_fma_f32 v[10:11], v[106:107], v[58:59], v[10:11]
v_pk_fma_f32 v[12:13], v[108:109], v[60:61], v[12:13]
v_pk_fma_f32 v[14:15], v[110:111], v[62:63], v[14:15]
v_pk_fma_f32 v[16:17], v[112:113], v[48:49], v[16:17]
v_pk_fma_f32 v[18:19], v[114:115], v[50:51], v[18:19]
v_pk_fma_f32 v[20:21], v[116:117], v[52:53], v[20:21]
v_pk_fma_f32 v[22:23], v[118:119], v[54:55], v[22:23]
v_pk_fma_f32 v[24:25], v[120:121], v[56:57], v[24:25]
v_pk_fma_f32 v[26:27], v[122:123], v[58:59], v[26:27]
v_pk_fma_f32 v[28:29], v[124:125], v[60:61], v[28:29]
v_pk_fma_f32 v[30:31], v[126:127], v[62:63], v[30:31]
v_pk_add_f32 v[236:237], v[0:1], v[2:3]
v_pk_add_f32 v[240:241], v[16:17], v[18:19]
v_pk_add_f32 v[238:239], v[4:5], v[6:7]
v_pk_add_f32 v[242:243], v[20:21], v[22:23]
v_pk_add_f32 v[236:237], v[236:237], v[8:9]
v_pk_add_f32 v[240:241], v[240:241], v[24:25]
v_pk_add_f32 v[238:239], v[238:239], v[10:11]
v_pk_add_f32 v[242:243], v[242:243], v[26:27]
v_pk_add_f32 v[236:237], v[236:237], v[12:13]
v_pk_add_f32 v[240:241], v[240:241], v[28:29]
v_pk_add_f32 v[238:239], v[238:239], v[14:15]
v_pk_add_f32 v[242:243], v[242:243], v[30:31]
v_pk_add_f32 v[236:237], v[236:237], v[238:239]
v_pk_add_f32 v[240:241], v[240:241], v[242:243]
v_add_f32 v244, v236, v237
v_add_f32 v245, v240, v241
s_nop 1
v_permlane16_swap_b32 v244, v245
v_add_f32 v244, v244, v245
v_mov_b32 v245, v244
s_nop 1
v_permlane32_swap_b32 v244, v245
v_add_f32 v244, v244, v245
v_mul_f32 v248, 0x3c800000, v244
v_mul_f32 v246, 0xbc800000, v244
v_mul_f32 v247, 0xbc800000, v244
v_mov_b32 v249, s78
v_mov_b32 v251, s78
v_permlane16_swap_b32 v246, v247
v_pk_add_f32 v[96:97], v[0:1], v[246:247] op_sel:[0,0] op_sel_hi:[1,0]
v_pk_add_f32 v[98:99], v[2:3], v[246:247] op_sel:[0,0] op_sel_hi:[1,0]
v_pk_add_f32 v[100:101], v[4:5], v[246:247] op_sel:[0,0] op_sel_hi:[1,0]
v_pk_add_f32 v[102:103], v[6:7], v[246:247] op_sel:[0,0] op_sel_hi:[1,0]
v_pk_add_f32 v[104:105], v[8:9], v[246:247] op_sel:[0,0] op_sel_hi:[1,0]
v_pk_add_f32 v[106:107], v[10:11], v[246:247] op_sel:[0,0] op_sel_hi:[1,0]
v_pk_add_f32 v[108:109], v[12:13], v[246:247] op_sel:[0,0] op_sel_hi:[1,0]
v_pk_add_f32 v[110:111], v[14:15], v[246:247] op_sel:[0,0] op_sel_hi:[1,0]
v_pk_add_f32 v[112:113], v[16:17], v[246:247] op_sel:[0,1] op_sel_hi:[1,1]
v_pk_add_f32 v[114:115], v[18:19], v[246:247] op_sel:[0,1] op_sel_hi:[1,1]
v_pk_add_f32 v[116:117], v[20:21], v[246:247] op_sel:[0,1] op_sel_hi:[1,1]
v_pk_add_f32 v[118:119], v[22:23], v[246:247] op_sel:[0,1] op_sel_hi:[1,1]
v_pk_add_f32 v[120:121], v[24:25], v[246:247] op_sel:[0,1] op_sel_hi:[1,1]
v_pk_add_f32 v[122:123], v[26:27], v[246:247] op_sel:[0,1] op_sel_hi:[1,1]
v_pk_add_f32 v[124:125], v[28:29], v[246:247] op_sel:[0,1] op_sel_hi:[1,1]
v_pk_add_f32 v[126:127], v[30:31], v[246:247] op_sel:[0,1] op_sel_hi:[1,1]
s_lshl_b32 s85, s73, 2
s_add_u32 s85, s85, s72
s_lshl_b32 s89, s85, 13
s_add_u32 s89, s89, 50855936
s_lshl_b32 s86, s74, 9
s_add_u32 s86, s86, s89
v_pk_mul_f32 v[236:237], v[96:97], v[96:97]
v_pk_mul_f32 v[240:241], v[112:113], v[112:113]
v_pk_mul_f32 v[238:239], v[98:99], v[98:99]
v_pk_mul_f32 v[242:243], v[114:115], v[114:115]
v_pk_fma_f32 v[236:237], v[100:101], v[100:101], v[236:237]
v_pk_fma_f32 v[240:241], v[116:117], v[116:117], v[240:241]
v_pk_fma_f32 v[238:239], v[102:103], v[102:103], v[238:239]
v_pk_fma_f32 v[242:243], v[118:119], v[118:119], v[242:243]
v_pk_fma_f32 v[236:237], v[104:105], v[104:105], v[236:237]
v_pk_fma_f32 v[240:241], v[120:121], v[120:121], v[240:241]
v_pk_fma_f32 v[238:239], v[106:107], v[106:107], v[238:239]
v_pk_fma_f32 v[242:243], v[122:123], v[122:123], v[242:243]
v_pk_fma_f32 v[236:237], v[108:109], v[108:109], v[236:237]
v_pk_fma_f32 v[240:241], v[124:125], v[124:125], v[240:241]
v_pk_fma_f32 v[238:239], v[110:111], v[110:111], v[238:239]
v_pk_fma_f32 v[242:243], v[126:127], v[126:127], v[242:243]
v_pk_add_f32 v[236:237], v[236:237], v[238:239]
v_pk_add_f32 v[240:241], v[240:241], v[242:243]
v_add_f32 v244, v236, v237
v_add_f32 v245, v240, v241
s_nop 1
v_permlane16_swap_b32 v244, v245
v_add_f32 v244, v244, v245
v_mov_b32 v245, v244
s_nop 1
v_permlane32_swap_b32 v244, v245
v_add_f32 v250, v244, v245
s_mov_b32 exec_hi, 0
buffer_store_dwordx4 v[248:251], v230, s[52:55], s86 offen sc1
s_mov_b64 exec, -1
s_add_u32 m0, s77, 49152
s_nop 0
buffer_load_dwordx4 v232, s[40:43], s10 offen offset:0 lds
buffer_load_dwordx4 v233, s[40:43], s10 offen offset:1024 lds
buffer_load_dwordx4 v232, s[44:47], s10 offen offset:2048 lds
buffer_load_dwordx4 v233, s[44:47], s10 offen offset:3072 lds
v_and_b32 v238, 0x1f0, v230
v_lshrrev_b32 v239, 5, v231
v_lshl_add_u32 v238, v239, 12, v238
v_lshrrev_b32 v240, 4, v231
v_lshlrev_b32 v240, 4, v240
v_add_u32 v240, 151040, v240
ds_read_b128 v[64:67], v240 offset:0
ds_read_b128 v[68:71], v240 offset:64
ds_read_b128 v[72:75], v240 offset:128
ds_read_b128 v[76:79], v240 offset:192
v_lshrrev_b32 v241, 4, v231
v_lshlrev_b32 v241, 4, v241
v_add_u32 v241, 150784, v241
ds_read_b128 v[80:83], v241 offset:0
ds_read_b128 v[84:87], v241 offset:64
ds_read_b128 v[88:91], v241 offset:128
ds_read_b128 v[92:95], v241 offset:192
v_cvt_pk_bf16_f32 v32, v0, v1
v_cvt_pk_bf16_f32 v33, v2, v3
v_cvt_pk_bf16_f32 v34, v4, v5
v_cvt_pk_bf16_f32 v35, v6, v7
v_cvt_pk_bf16_f32 v36, v8, v9
v_cvt_pk_bf16_f32 v37, v10, v11
v_cvt_pk_bf16_f32 v38, v12, v13
)"
R"(
v_cvt_pk_bf16_f32 v39, v14, v15
v_cvt_pk_bf16_f32 v40, v16, v17
v_cvt_pk_bf16_f32 v41, v18, v19
v_cvt_pk_bf16_f32 v42, v20, v21
v_cvt_pk_bf16_f32 v43, v22, v23
v_cvt_pk_bf16_f32 v44, v24, v25
v_cvt_pk_bf16_f32 v45, v26, v27
v_cvt_pk_bf16_f32 v46, v28, v29
v_cvt_pk_bf16_f32 v47, v30, v31
s_add_u32 s86, s77, 131072
v_add_u32 v240, s86, v230
ds_write_b128 v240, v[32:35] offset:0
ds_write_b128 v240, v[36:39] offset:1024
ds_write_b128 v240, v[40:43] offset:2048
ds_write_b128 v240, v[44:47] offset:3072
v_lshrrev_b32 v240, 2, v231
v_and_b32 v241, 3, v231
v_lshlrev_b32 v241, 3, v241
v_lshl_add_u32 v242, v240, 7, v241
v_lshl_add_u32 v243, v240, 6, v241
v_and_b32 v241, 6, v240
v_lshl_add_u32 v242, v241, 4, v242
v_and_b32 v241, 4, v240
v_lshl_add_u32 v243, v241, 3, v243
v_xor_b32 v208, 0, v242
v_xor_b32 v209, 32, v242
v_xor_b32 v210, 64, v242
v_xor_b32 v211, 96, v242
v_xor_b32 v216, 0, v243
v_xor_b32 v217, 32, v243
v_xor_b32 v212, 65536, v242
v_xor_b32 v213, 65568, v242
v_xor_b32 v214, 65600, v242
v_xor_b32 v215, 65632, v242
v_xor_b32 v218, 65536, v243
v_xor_b32 v219, 65568, v243
v_mov_b32 v223, 0xbdd2d3e8
s_mul_i32 s86, s85, 8192
s_add_u32 s86, s86, 49020928
s_mul_i32 s79, s74, 128
s_add_u32 s79, s79, s86
s_mul_i32 s88, s75, 1024
s_add_u32 s86, s86, s88
v_and_b32 v222, 7, v231
v_mul_u32_u24 v222, 128, v222
v_add_u32 v222, s86, v222
s_lshl_b32 s86, s73, 3
s_add_u32 s86, s86, s72
s_mul_i32 s86, s86, 114688
s_add_u32 s86, s86, 16777216
v_add_u32 v220, s86, v230
v_add_u32 v221, 458752, v220
s_mul_i32 s92, s74, 7168
s_and_b32 s88, s74, 1
s_cselect_b32 s90, 0, 6144
s_add_u32 s90, s90, s92
s_lshl_b32 s88, s88, 10
s_add_u32 s92, s92, s88
s_ashr_i32 s30, s30, 10
s_mul_i32 s85, s75, 3669888
s_lshl_b32 s86, s72, 15
s_add_u32 s85, s85, s86
s_lshl_b32 s86, s76, 8
s_add_u32 s85, s85, s86
s_add_u32 s85, s85, 4224
v_lshrrev_b32 v240, 3, v231
v_and_b32 v241, 6, v240
v_xor_b32 v241, v241, v231
v_and_b32 v241, 7, v241
v_mul_u32_u24 v240, 2048, v240
v_lshl_add_u32 v240, v241, 4, v240
v_add_u32 v224, s85, v240
v_add_u32 v225, 15360, v224
s_sub_u32 s85, s30, 2048
v_add_u32 v226, s85, v224
v_add_u32 v227, s85, v225
s_mov_b32 s7, s20
s_bfe_u32 s85, s4, 262156
s_xor_b32 s87, s85, 0
s_and_b32 s88, s87, 1
s_lshr_b32 s87, s87, 1
s_mul_i32 s87, s87, 7
s_add_u32 s86, s87, 1
s_lshl_b32 s26, s86, 17
s_lshl2_add_u32 s87, s88, s87
s_lshl_b32 s18, s87, 17
s_lshl_b32 s94, s87, 11
s_xor_b32 s87, s85, 1
s_and_b32 s88, s87, 1
s_lshr_b32 s87, s87, 1
s_mul_i32 s87, s87, 7
s_lshl2_add_u32 s87, s88, s87
s_lshl_b32 s19, s87, 17
s_lshl_b32 s95, s87, 11
s_xor_b32 s87, s85, 2
s_and_b32 s88, s87, 1
s_lshr_b32 s87, s87, 1
s_mul_i32 s87, s87, 7
s_add_u32 s86, s87, 1
s_lshl_b32 s27, s86, 17
s_lshl2_add_u32 s87, s88, s87
s_lshl_b32 s20, s87, 17
s_lshl_b32 s96, s87, 11
s_xor_b32 s87, s85, 3
s_and_b32 s88, s87, 1
s_lshr_b32 s87, s87, 1
s_mul_i32 s87, s87, 7
s_lshl2_add_u32 s87, s88, s87
s_lshl_b32 s21, s87, 17
s_lshl_b32 s97, s87, 11
s_xor_b32 s87, s85, 4
s_and_b32 s88, s87, 1
s_lshr_b32 s87, s87, 1
s_mul_i32 s87, s87, 7
s_add_u32 s86, s87, 1
s_lshl_b32 s28, s86, 17
s_lshl2_add_u32 s87, s88, s87
s_lshl_b32 s22, s87, 17
s_lshl_b32 s98, s87, 11
s_xor_b32 s87, s85, 5
s_and_b32 s88, s87, 1
s_lshr_b32 s87, s87, 1
s_mul_i32 s87, s87, 7
s_lshl2_add_u32 s87, s88, s87
s_lshl_b32 s23, s87, 17
s_lshl_b32 s99, s87, 11
s_xor_b32 s87, s85, 6
s_and_b32 s88, s87, 1
s_lshr_b32 s87, s87, 1
s_mul_i32 s87, s87, 7
s_add_u32 s86, s87, 1
s_lshl_b32 s29, s86, 17
s_lshl2_add_u32 s87, s88, s87
s_lshl_b32 s24, s87, 17
s_lshl_b32 s100, s87, 11
s_xor_b32 s87, s85, 7
s_and_b32 s88, s87, 1
s_lshr_b32 s87, s87, 1
s_mul_i32 s87, s87, 7
s_lshl2_add_u32 s87, s88, s87
s_lshl_b32 s25, s87, 17
s_lshl_b32 s101, s87, 11
s_and_b32 s85, s85, 1
s_lshl_b32 s85, s85, 13
s_sub_u32 s31, 2048, s85
s_mov_b64 exec, 0
s_cmp_eq_u32 s72, 0
s_cbranch_scc1 LC9_%=
s_cmp_eq_u32 s72, 1
s_cbranch_scc1 LC10_%=
s_cmp_eq_u32 s72, 2
s_cbranch_scc1 LC11_%=
s_branch LC12_%=
LRETB_%=:
s_mov_b64 exec, -1
s_lshl3_add_u32 s86, s73, s72
s_lshl_b32 s86, s86, 15
s_add_u32 s86, s86, 4194304
v_add_u32 v228, s86, v230
v_add_u32 v229, 131072, v228
s_lshl_b32 s86, s76, 1
s_add_u32 s86, s86, s75
s_lshl_b32 s86, s86, 11
s_sleep 2
buffer_load_dwordx4 v[128:131], v238, s[52:55], s89 offen offset:0 sc1
buffer_load_dwordx4 v[132:135], v238, s[52:55], s89 offen offset:512 sc1
buffer_load_dwordx4 v[136:139], v238, s[52:55], s89 offen offset:1024 sc1
buffer_load_dwordx4 v[140:143], v238, s[52:55], s89 offen offset:1536 sc1
buffer_load_dwordx4 v[144:147], v238, s[52:55], s89 offen offset:2048 sc1
buffer_load_dwordx4 v[148:151], v238, s[52:55], s89 offen offset:2560 sc1
buffer_load_dwordx4 v[152:155], v238, s[52:55], s89 offen offset:3072 sc1
buffer_load_dwordx4 v[156:159], v238, s[52:55], s89 offen offset:3584 sc1
s_waitcnt vmcnt(7)
v_min3_u32 v236, v129, v131, s78
v_max3_u32 v237, v129, v131, s78
s_waitcnt vmcnt(6)
v_min3_u32 v236, v133, v135, v236
v_max3_u32 v237, v133, v135, v237
s_waitcnt vmcnt(5)
v_min3_u32 v236, v137, v139, v236
v_max3_u32 v237, v137, v139, v237
LC9_%=:
s_cbranch_execz LC13_%=
s_waitcnt vmcnt(4)
v_min3_u32 v236, v141, v143, v236
v_max3_u32 v237, v141, v143, v237
s_waitcnt vmcnt(3)
v_min3_u32 v236, v145, v147, v236
v_max3_u32 v237, v145, v147, v237
s_waitcnt vmcnt(2)
v_min3_u32 v236, v149, v151, v236
v_max3_u32 v237, v149, v151, v237
s_waitcnt vmcnt(1)
v_min3_u32 v236, v153, v155, v236
v_max3_u32 v237, v153, v155, v237
s_waitcnt vmcnt(0)
v_min3_u32 v236, v157, v159, v236
v_max3_u32 v237, v157, v159, v237
v_cmp_ne_u32 vcc, v236, v237
s_nop 0
s_cbranch_vccz LRst_%=
LSst_%=:
s_sub_u32 s84, s84, 1
s_cbranch_scc1 LZst_%=
buffer_load_dwordx4 v[128:131], v238, s[52:55], s89 offen offset:0 sc1
buffer_load_dwordx4 v[132:135], v238, s[52:55], s89 offen offset:512 sc1
buffer_load_dwordx4 v[136:139], v238, s[52:55], s89 offen offset:1024 sc1
buffer_load_dwordx4 v[140:143], v238, s[52:55], s89 offen offset:1536 sc1
buffer_load_dwordx4 v[144:147], v238, s[52:55], s89 offen offset:2048 sc1
buffer_load_dwordx4 v[148:151], v238, s[52:55], s89 offen offset:2560 sc1
buffer_load_dwordx4 v[152:155], v238, s[52:55], s89 offen offset:3072 sc1
buffer_load_dwordx4 v[156:159], v238, s[52:55], s89 offen offset:3584 sc1
s_waitcnt vmcnt(0)
v_min3_u32 v236, v129, v131, s78
v_max3_u32 v237, v129, v131, s78
v_min3_u32 v236, v133, v135, v236
v_max3_u32 v237, v133, v135, v237
v_min3_u32 v236, v137, v139, v236
v_max3_u32 v237, v137, v139, v237
v_min3_u32 v236, v141, v143, v236
v_max3_u32 v237, v141, v143, v237
v_min3_u32 v236, v145, v147, v236
v_max3_u32 v237, v145, v147, v237
v_min3_u32 v236, v149, v151, v236
v_max3_u32 v237, v149, v151, v237
v_min3_u32 v236, v153, v155, v236
v_max3_u32 v237, v153, v155, v237
v_min3_u32 v236, v157, v159, v236
v_max3_u32 v237, v157, v159, v237
v_cmp_ne_u32 vcc, v236, v237
s_nop 0
s_cbranch_vccnz LSst_%=
LZst_%=:
s_max_i32 s84, s84, 0
LRst_%=:
v_add_f32 v236, v128, v132
v_add_f32 v237, v136, v140
v_add_f32 v238, v144, v148
v_add_f32 v239, v152, v156
v_add_f32 v240, v130, v134
v_add_f32 v241, v138, v142
v_add_f32 v242, v146, v150
v_add_f32 v243, v154, v158
v_pk_add_f32 v[236:237], v[236:237], v[238:239]
v_pk_add_f32 v[240:241], v[240:241], v[242:243]
v_add_f32 v244, v236, v237
v_add_f32 v245, v236, v237
v_add_f32 v240, v240, v241
s_nop 0
v_permlane32_swap_b32 v244, v245
v_add_f32 v244, v244, v245
v_mul_f32 v244, 0xbd800000, v244
v_add_f32 v128, v128, v244
v_add_f32 v132, v132, v244
v_add_f32 v136, v136, v244
v_add_f32 v140, v140, v244
v_add_f32 v144, v144, v244
v_add_f32 v148, v148, v244
v_add_f32 v152, v152, v244
v_add_f32 v156, v156, v244
v_mul_f32 v236, v128, v128
v_mul_f32 v237, v132, v132
v_fmac_f32 v236, v136, v136
v_fmac_f32 v237, v140, v140
v_fmac_f32 v236, v144, v144
v_fmac_f32 v237, v148, v148
v_fmac_f32 v236, v152, v152
v_fmac_f32 v237, v156, v156
v_add_f32 v236, v236, v237
v_fmac_f32 v240, 0x42800000, v236
v_mov_b32 v241, v240
v_mov_b32 v242, 0x3727c5ac
s_nop 0
v_permlane32_swap_b32 v240, v241
v_add_f32 v240, v240, v241
v_fmac_f32 v242, 0x3a800000, v240
v_rsq_f32 v236, v242
v_rsq_f32 v237, v242
v_mul_f32 v238, v236, v244
v_mul_f32 v239, v236, v244
v_permlane16_swap_b32 v236, v237
s_nop 0
v_permlane16_swap_b32 v238, v239
v_pk_fma_f32 v[0:1], v[0:1], v[236:237], v[238:239] op_sel:[0,0,0] op_sel_hi:[1,0,0]
v_pk_fma_f32 v[2:3], v[2:3], v[236:237], v[238:239] op_sel:[0,0,0] op_sel_hi:[1,0,0]
v_pk_fma_f32 v[4:5], v[4:5], v[236:237], v[238:239] op_sel:[0,0,0] op_sel_hi:[1,0,0]
v_pk_fma_f32 v[6:7], v[6:7], v[236:237], v[238:239] op_sel:[0,0,0] op_sel_hi:[1,0,0]
s_waitcnt lgkmcnt(7)
v_pk_fma_f32 v[0:1], v[0:1], v[64:65], v[80:81]
v_pk_fma_f32 v[2:3], v[2:3], v[66:67], v[82:83]
s_waitcnt lgkmcnt(6)
v_pk_fma_f32 v[4:5], v[4:5], v[68:69], v[84:85]
v_pk_fma_f32 v[6:7], v[6:7], v[70:71], v[86:87]
v_cvt_pk_bf16_f32 v0, v0, v1
v_cvt_pk_bf16_f32 v1, v2, v3
v_cvt_pk_bf16_f32 v2, v4, v5
v_cvt_pk_bf16_f32 v3, v6, v7
v_or_b32 v0, 1, v0
buffer_store_dwordx4 v[0:3], v228, s[52:55], s86 offen offset:0 sc1
v_pk_fma_f32 v[8:9], v[8:9], v[236:237], v[238:239] op_sel:[0,0,0] op_sel_hi:[1,0,0]
v_pk_fma_f32 v[10:11], v[10:11], v[236:237], v[238:239] op_sel:[0,0,0] op_sel_hi:[1,0,0]
v_pk_fma_f32 v[12:13], v[12:13], v[236:237], v[238:239] op_sel:[0,0,0] op_sel_hi:[1,0,0]
v_pk_fma_f32 v[14:15], v[14:15], v[236:237], v[238:239] op_sel:[0,0,0] op_sel_hi:[1,0,0]
s_waitcnt lgkmcnt(5)
v_pk_fma_f32 v[8:9], v[8:9], v[72:73], v[88:89]
v_pk_fma_f32 v[10:11], v[10:11], v[74:75], v[90:91]
s_waitcnt lgkmcnt(4)
v_pk_fma_f32 v[12:13], v[12:13], v[76:77], v[92:93]
v_pk_fma_f32 v[14:15], v[14:15], v[78:79], v[94:95]
v_cvt_pk_bf16_f32 v8, v8, v9
v_cvt_pk_bf16_f32 v9, v10, v11
v_cvt_pk_bf16_f32 v10, v12, v13
v_cvt_pk_bf16_f32 v11, v14, v15
v_or_b32 v8, 1, v8
buffer_store_dwordx4 v[8:11], v228, s[52:55], s86 offen offset:1024 sc1
v_pk_fma_f32 v[16:17], v[16:17], v[236:237], v[238:239] op_sel:[0,1,1] op_sel_hi:[1,1,1]
v_pk_fma_f32 v[18:19], v[18:19], v[236:237], v[238:239] op_sel:[0,1,1] op_sel_hi:[1,1,1]
v_pk_fma_f32 v[20:21], v[20:21], v[236:237], v[238:239] op_sel:[0,1,1] op_sel_hi:[1,1,1]
v_pk_fma_f32 v[22:23], v[22:23], v[236:237], v[238:239] op_sel:[0,1,1] op_sel_hi:[1,1,1]
v_pk_fma_f32 v[16:17], v[16:17], v[64:65], v[80:81]
v_pk_fma_f32 v[18:19], v[18:19], v[66:67], v[82:83]
v_pk_fma_f32 v[20:21], v[20:21], v[68:69], v[84:85]
v_pk_fma_f32 v[22:23], v[22:23], v[70:71], v[86:87]
v_cvt_pk_bf16_f32 v16, v16, v17
v_cvt_pk_bf16_f32 v17, v18, v19
v_cvt_pk_bf16_f32 v18, v20, v21
v_cvt_pk_bf16_f32 v19, v22, v23
v_or_b32 v16, 1, v16
buffer_store_dwordx4 v[16:19], v229, s[52:55], s86 offen offset:0 sc1
v_pk_fma_f32 v[24:25], v[24:25], v[236:237], v[238:239] op_sel:[0,1,1] op_sel_hi:[1,1,1]
v_pk_fma_f32 v[26:27], v[26:27], v[236:237], v[238:239] op_sel:[0,1,1] op_sel_hi:[1,1,1]
v_pk_fma_f32 v[28:29], v[28:29], v[236:237], v[238:239] op_sel:[0,1,1] op_sel_hi:[1,1,1]
v_pk_fma_f32 v[30:31], v[30:31], v[236:237], v[238:239] op_sel:[0,1,1] op_sel_hi:[1,1,1]
v_pk_fma_f32 v[24:25], v[24:25], v[72:73], v[88:89]
v_pk_fma_f32 v[26:27], v[26:27], v[74:75], v[90:91]
v_pk_fma_f32 v[28:29], v[28:29], v[76:77], v[92:93]
v_pk_fma_f32 v[30:31], v[30:31], v[78:79], v[94:95]
v_cvt_pk_bf16_f32 v24, v24, v25
v_cvt_pk_bf16_f32 v25, v26, v27
v_cvt_pk_bf16_f32 v26, v28, v29
v_cvt_pk_bf16_f32 v27, v30, v31
v_or_b32 v24, 1, v24
buffer_store_dwordx4 v[24:27], v229, s[52:55], s86 offen offset:1024 sc1
s_waitcnt vmcnt(0)
s_sleep 7
s_add_u32 m0, s77, 81920
s_nop 0
buffer_load_dwordx4 v232, s[40:43], s11 offen offset:0 lds
buffer_load_dwordx4 v233, s[40:43], s11 offen offset:1024 lds
buffer_load_dwordx4 v232, s[44:47], s11 offen offset:2048 lds
buffer_load_dwordx4 v233, s[44:47], s11 offen offset:3072 lds
s_add_u32 m0, s77, 0
s_nop 0
buffer_load_dwordx4 v232, s[40:43], s12 offen offset:0 lds
buffer_load_dwordx4 v233, s[40:43], s12 offen offset:1024 lds
buffer_load_dwordx4 v232, s[44:47], s12 offen offset:2048 lds
buffer_load_dwordx4 v233, s[44:47], s12 offen offset:3072 lds
s_bfe_u32 s83, s4, 262152
s_lshl_b32 s83, s83, 11
s_xor_b32 s81, s83, 0
buffer_load_dwordx4 a[0:3], v228, s[52:55], s81 offen offset:0 sc1
buffer_load_dwordx4 a[4:7], v229, s[52:55], s81 offen offset:0 sc1
buffer_load_dwordx4 a[8:11], v228, s[52:55], s81 offen offset:1024 sc1
buffer_load_dwordx4 a[12:15], v229, s[52:55], s81 offen offset:1024 sc1
s_xor_b32 s81, s83, 2048
buffer_load_dwordx4 a[16:19], v228, s[52:55], s81 offen offset:0 sc1
buffer_load_dwordx4 a[20:23], v229, s[52:55], s81 offen offset:0 sc1
buffer_load_dwordx4 a[24:27], v228, s[52:55], s81 offen offset:1024 sc1
buffer_load_dwordx4 a[28:31], v229, s[52:55], s81 offen offset:1024 sc1
s_xor_b32 s81, s83, 4096
buffer_load_dwordx4 a[32:35], v228, s[52:55], s81 offen offset:0 sc1
buffer_load_dwordx4 a[36:39], v229, s[52:55], s81 offen offset:0 sc1
buffer_load_dwordx4 a[40:43], v228, s[52:55], s81 offen offset:1024 sc1
buffer_load_dwordx4 a[44:47], v229, s[52:55], s81 offen offset:1024 sc1
s_waitcnt vmcnt(11)
v_accvgpr_read_b32 v248, a0
s_waitcnt vmcnt(10)
v_accvgpr_read_b32 v249, a4
s_waitcnt vmcnt(9)
v_accvgpr_read_b32 v250, a8
v_bitop3_b32 v248, v248, v249, v250 bitop3:0x80
s_waitcnt vmcnt(8)
v_accvgpr_read_b32 v251, a12
v_bitop3_b32 v248, v248, v251, 1 bitop3:0x80
v_cmp_ne_u32 vcc, 1, v248
s_nop 0
s_cbranch_vccnz LSt3k0_%=
LRt3k0_%=:
s_barrier
ds_read_b64_tr_b16 v[160:161], v208 offset:49152
ds_read_b64_tr_b16 v[162:163], v208 offset:53248
ds_read_b64_tr_b16 v[164:165], v209 offset:49152
ds_read_b64_tr_b16 v[166:167], v209 offset:53248
ds_read_b64_tr_b16 v[168:169], v210 offset:49152
ds_read_b64_tr_b16 v[170:171], v210 offset:53248
ds_read_b64_tr_b16 v[172:173], v211 offset:49152
ds_read_b64_tr_b16 v[174:175], v211 offset:53248
ds_read_b64_tr_b16 v[176:177], v208 offset:51200
ds_read_b64_tr_b16 v[178:179], v208 offset:55296
ds_read_b64_tr_b16 v[180:181], v209 offset:51200
)"
R"(
ds_read_b64_tr_b16 v[182:183], v209 offset:55296
ds_read_b64_tr_b16 v[184:185], v210 offset:51200
ds_read_b64_tr_b16 v[186:187], v210 offset:55296
ds_read_b64_tr_b16 v[188:189], v211 offset:51200
ds_read_b64_tr_b16 v[190:191], v211 offset:55296
s_waitcnt lgkmcnt(8)
v_mfma_f32_16x16x32_bf16 v[0:3], v[160:163], a[0:3], 0
ds_read_b64_tr_b16 v[192:193], v208 offset:57344
ds_read_b64_tr_b16 v[194:195], v208 offset:61440
s_add_u32 m0, s77, 98304
v_mfma_f32_16x16x32_bf16 v[16:19], v[160:163], a[4:7], 0
buffer_load_dwordx4 v232, s[40:43], s13 offen offset:0 lds
v_mfma_f32_16x16x32_bf16 v[4:7], v[164:167], a[0:3], 0
ds_read_b64_tr_b16 v[196:197], v209 offset:57344
ds_read_b64_tr_b16 v[198:199], v209 offset:61440
s_xor_b32 s81, s83, 6144
v_mfma_f32_16x16x32_bf16 v[20:23], v[164:167], a[4:7], 0
buffer_load_dwordx4 a[48:51], v228, s[52:55], s81 offen offset:0 sc1
v_mfma_f32_16x16x32_bf16 v[8:11], v[168:171], a[0:3], 0
ds_read_b64_tr_b16 v[200:201], v210 offset:57344
ds_read_b64_tr_b16 v[202:203], v210 offset:61440
v_mfma_f32_16x16x32_bf16 v[24:27], v[168:171], a[4:7], 0
buffer_load_dwordx4 v233, s[40:43], s13 offen offset:1024 lds
v_mfma_f32_16x16x32_bf16 v[12:15], v[172:175], a[0:3], 0
ds_read_b64_tr_b16 v[204:205], v211 offset:57344
ds_read_b64_tr_b16 v[206:207], v211 offset:61440
v_mfma_f32_16x16x32_bf16 v[28:31], v[172:175], a[4:7], 0
s_waitcnt lgkmcnt(8)
v_mfma_f32_16x16x32_bf16 v[32:35], v[176:179], a[0:3], 0
ds_read_b64_tr_b16 v[160:161], v208 offset:59392
ds_read_b64_tr_b16 v[162:163], v208 offset:63488
v_mfma_f32_16x16x32_bf16 v[48:51], v[176:179], a[4:7], 0
buffer_load_dwordx4 v232, s[44:47], s13 offen offset:2048 lds
v_mfma_f32_16x16x32_bf16 v[36:39], v[180:183], a[0:3], 0
ds_read_b64_tr_b16 v[164:165], v209 offset:59392
ds_read_b64_tr_b16 v[166:167], v209 offset:63488
v_mfma_f32_16x16x32_bf16 v[52:55], v[180:183], a[4:7], 0
buffer_load_dwordx4 a[52:55], v229, s[52:55], s81 offen offset:0 sc1
v_mfma_f32_16x16x32_bf16 v[40:43], v[184:187], a[0:3], 0
ds_read_b64_tr_b16 v[168:169], v210 offset:59392
ds_read_b64_tr_b16 v[170:171], v210 offset:63488
v_mfma_f32_16x16x32_bf16 v[56:59], v[184:187], a[4:7], 0
buffer_load_dwordx4 v233, s[44:47], s13 offen offset:3072 lds
v_mfma_f32_16x16x32_bf16 v[44:47], v[188:191], a[0:3], 0
ds_read_b64_tr_b16 v[172:173], v211 offset:59392
ds_read_b64_tr_b16 v[174:175], v211 offset:63488
v_mfma_f32_16x16x32_bf16 v[60:63], v[188:191], a[4:7], 0
buffer_load_dwordx4 a[56:59], v228, s[52:55], s81 offen offset:1024 sc1
s_waitcnt vmcnt(11) lgkmcnt(8)
s_barrier
v_mfma_f32_16x16x32_bf16 v[0:3], v[192:195], a[8:11], v[0:3]
ds_read_b64_tr_b16 v[176:177], v212 offset:16384
ds_read_b64_tr_b16 v[178:179], v212 offset:20480
v_mfma_f32_16x16x32_bf16 v[16:19], v[192:195], a[12:15], v[16:19]
v_accvgpr_read_b32 v248, a16
v_accvgpr_read_b32 v249, a20
v_mfma_f32_16x16x32_bf16 v[4:7], v[196:199], a[8:11], v[4:7]
ds_read_b64_tr_b16 v[180:181], v213 offset:16384
ds_read_b64_tr_b16 v[182:183], v213 offset:20480
v_mfma_f32_16x16x32_bf16 v[20:23], v[196:199], a[12:15], v[20:23]
v_accvgpr_read_b32 v250, a24
v_bitop3_b32 v248, v248, v249, v250 bitop3:0x80
v_mfma_f32_16x16x32_bf16 v[8:11], v[200:203], a[8:11], v[8:11]
ds_read_b64_tr_b16 v[184:185], v214 offset:16384
ds_read_b64_tr_b16 v[186:187], v214 offset:20480
v_mfma_f32_16x16x32_bf16 v[24:27], v[200:203], a[12:15], v[24:27]
v_accvgpr_read_b32 v251, a28
v_bitop3_b32 v248, v248, v251, 1 bitop3:0x80
v_mfma_f32_16x16x32_bf16 v[12:15], v[204:207], a[8:11], v[12:15]
ds_read_b64_tr_b16 v[188:189], v215 offset:16384
ds_read_b64_tr_b16 v[190:191], v215 offset:20480
v_mfma_f32_16x16x32_bf16 v[28:31], v[204:207], a[12:15], v[28:31]
s_add_u32 m0, s77, 16384
s_waitcnt lgkmcnt(8)
v_mfma_f32_16x16x32_bf16 v[32:35], v[160:163], a[8:11], v[32:35]
ds_read_b64_tr_b16 v[192:193], v212 offset:18432
ds_read_b64_tr_b16 v[194:195], v212 offset:22528
v_mfma_f32_16x16x32_bf16 v[48:51], v[160:163], a[12:15], v[48:51]
v_cmp_ne_u32 vcc, 1, v248
s_nop 0
s_cbranch_vccnz LSt3k1_%=
LRt3k1_%=:
v_mfma_f32_16x16x32_bf16 v[36:39], v[164:167], a[8:11], v[36:39]
ds_read_b64_tr_b16 v[196:197], v213 offset:18432
ds_read_b64_tr_b16 v[198:199], v213 offset:22528
v_mfma_f32_16x16x32_bf16 v[52:55], v[164:167], a[12:15], v[52:55]
buffer_load_dwordx4 a[60:63], v229, s[52:55], s81 offen offset:1024 sc1
v_mfma_f32_16x16x32_bf16 v[40:43], v[168:171], a[8:11], v[40:43]
ds_read_b64_tr_b16 v[200:201], v214 offset:18432
ds_read_b64_tr_b16 v[202:203], v214 offset:22528
v_mfma_f32_16x16x32_bf16 v[56:59], v[168:171], a[12:15], v[56:59]
buffer_load_dwordx4 v232, s[40:43], s14 offen offset:0 lds
v_mfma_f32_16x16x32_bf16 v[44:47], v[172:175], a[8:11], v[44:47]
ds_read_b64_tr_b16 v[204:205], v215 offset:18432
ds_read_b64_tr_b16 v[206:207], v215 offset:22528
v_mfma_f32_16x16x32_bf16 v[60:63], v[172:175], a[12:15], v[60:63]
s_xor_b32 s81, s83, 8192
s_waitcnt lgkmcnt(8)
v_mfma_f32_16x16x32_bf16 v[0:3], v[176:179], a[16:19], v[0:3]
ds_read_b64_tr_b16 v[160:161], v212 offset:24576
ds_read_b64_tr_b16 v[162:163], v212 offset:28672
v_mfma_f32_16x16x32_bf16 v[16:19], v[176:179], a[20:23], v[16:19]
buffer_load_dwordx4 a[64:67], v228, s[52:55], s81 offen offset:0 sc1
v_mfma_f32_16x16x32_bf16 v[4:7], v[180:183], a[16:19], v[4:7]
ds_read_b64_tr_b16 v[164:165], v213 offset:24576
ds_read_b64_tr_b16 v[166:167], v213 offset:28672
v_mfma_f32_16x16x32_bf16 v[20:23], v[180:183], a[20:23], v[20:23]
buffer_load_dwordx4 v233, s[40:43], s14 offen offset:1024 lds
v_mfma_f32_16x16x32_bf16 v[8:11], v[184:187], a[16:19], v[8:11]
ds_read_b64_tr_b16 v[168:169], v214 offset:24576
ds_read_b64_tr_b16 v[170:171], v214 offset:28672
v_mfma_f32_16x16x32_bf16 v[24:27], v[184:187], a[20:23], v[24:27]
buffer_load_dwordx4 a[68:71], v229, s[52:55], s81 offen offset:0 sc1
v_mfma_f32_16x16x32_bf16 v[12:15], v[188:191], a[16:19], v[12:15]
ds_read_b64_tr_b16 v[172:173], v215 offset:24576
ds_read_b64_tr_b16 v[174:175], v215 offset:28672
v_mfma_f32_16x16x32_bf16 v[28:31], v[188:191], a[20:23], v[28:31]
s_waitcnt lgkmcnt(8)
v_mfma_f32_16x16x32_bf16 v[32:35], v[192:195], a[16:19], v[32:35]
ds_read_b64_tr_b16 v[176:177], v212 offset:26624
ds_read_b64_tr_b16 v[178:179], v212 offset:30720
v_mfma_f32_16x16x32_bf16 v[48:51], v[192:195], a[20:23], v[48:51]
buffer_load_dwordx4 v232, s[44:47], s14 offen offset:2048 lds
v_mfma_f32_16x16x32_bf16 v[36:39], v[196:199], a[16:19], v[36:39]
ds_read_b64_tr_b16 v[180:181], v213 offset:26624
ds_read_b64_tr_b16 v[182:183], v213 offset:30720
v_mfma_f32_16x16x32_bf16 v[52:55], v[196:199], a[20:23], v[52:55]
v_mfma_f32_16x16x32_bf16 v[40:43], v[200:203], a[16:19], v[40:43]
ds_read_b64_tr_b16 v[184:185], v214 offset:26624
ds_read_b64_tr_b16 v[186:187], v214 offset:30720
v_mfma_f32_16x16x32_bf16 v[56:59], v[200:203], a[20:23], v[56:59]
buffer_load_dwordx4 v233, s[44:47], s14 offen offset:3072 lds
buffer_load_dwordx4 a[72:75], v228, s[52:55], s81 offen offset:1024 sc1
v_mfma_f32_16x16x32_bf16 v[44:47], v[204:207], a[16:19], v[44:47]
ds_read_b64_tr_b16 v[188:189], v215 offset:26624
ds_read_b64_tr_b16 v[190:191], v215 offset:30720
v_mfma_f32_16x16x32_bf16 v[60:63], v[204:207], a[20:23], v[60:63]
s_waitcnt vmcnt(15) lgkmcnt(8)
s_barrier
v_mfma_f32_16x16x32_bf16 v[0:3], v[160:163], a[24:27], v[0:3]
ds_read_b64_tr_b16 v[192:193], v208 offset:0
ds_read_b64_tr_b16 v[194:195], v208 offset:4096
v_mfma_f32_16x16x32_bf16 v[16:19], v[160:163], a[28:31], v[16:19]
v_accvgpr_read_b32 v248, a32
v_accvgpr_read_b32 v249, a36
v_mfma_f32_16x16x32_bf16 v[4:7], v[164:167], a[24:27], v[4:7]
ds_read_b64_tr_b16 v[196:197], v209 offset:0
ds_read_b64_tr_b16 v[198:199], v209 offset:4096
v_mfma_f32_16x16x32_bf16 v[20:23], v[164:167], a[28:31], v[20:23]
v_accvgpr_read_b32 v250, a40
v_bitop3_b32 v248, v248, v249, v250 bitop3:0x80
v_mfma_f32_16x16x32_bf16 v[8:11], v[168:171], a[24:27], v[8:11]
ds_read_b64_tr_b16 v[200:201], v210 offset:0
ds_read_b64_tr_b16 v[202:203], v210 offset:4096
v_mfma_f32_16x16x32_bf16 v[24:27], v[168:171], a[28:31], v[24:27]
v_accvgpr_read_b32 v251, a44
v_bitop3_b32 v248, v248, v251, 1 bitop3:0x80
v_mfma_f32_16x16x32_bf16 v[12:15], v[172:175], a[24:27], v[12:15]
ds_read_b64_tr_b16 v[204:205], v211 offset:0
ds_read_b64_tr_b16 v[206:207], v211 offset:4096
v_mfma_f32_16x16x32_bf16 v[28:31], v[172:175], a[28:31], v[28:31]
s_add_u32 m0, s77, 114688
s_waitcnt lgkmcnt(8)
v_mfma_f32_16x16x32_bf16 v[32:35], v[176:179], a[24:27], v[32:35]
ds_read_b64_tr_b16 v[160:161], v208 offset:2048
ds_read_b64_tr_b16 v[162:163], v208 offset:6144
v_mfma_f32_16x16x32_bf16 v[48:51], v[176:179], a[28:31], v[48:51]
v_cmp_ne_u32 vcc, 1, v248
s_nop 0
s_cbranch_vccnz LSt3k2_%=
LRt3k2_%=:
v_mfma_f32_16x16x32_bf16 v[36:39], v[180:183], a[24:27], v[36:39]
ds_read_b64_tr_b16 v[164:165], v209 offset:2048
ds_read_b64_tr_b16 v[166:167], v209 offset:6144
v_mfma_f32_16x16x32_bf16 v[52:55], v[180:183], a[28:31], v[52:55]
buffer_load_dwordx4 a[76:79], v229, s[52:55], s81 offen offset:1024 sc1
v_mfma_f32_16x16x32_bf16 v[40:43], v[184:187], a[24:27], v[40:43]
ds_read_b64_tr_b16 v[168:169], v210 offset:2048
ds_read_b64_tr_b16 v[170:171], v210 offset:6144
v_mfma_f32_16x16x32_bf16 v[56:59], v[184:187], a[28:31], v[56:59]
buffer_load_dwordx4 v232, s[40:43], s15 offen offset:0 lds
v_mfma_f32_16x16x32_bf16 v[44:47], v[188:191], a[24:27], v[44:47]
ds_read_b64_tr_b16 v[172:173], v211 offset:2048
ds_read_b64_tr_b16 v[174:175], v211 offset:6144
v_mfma_f32_16x16x32_bf16 v[60:63], v[188:191], a[28:31], v[60:63]
s_xor_b32 s81, s83, 10240
s_waitcnt lgkmcnt(8)
v_mfma_f32_16x16x32_bf16 v[0:3], v[192:195], a[32:35], v[0:3]
ds_read_b64_tr_b16 v[176:177], v208 offset:8192
ds_read_b64_tr_b16 v[178:179], v208 offset:12288
v_mfma_f32_16x16x32_bf16 v[16:19], v[192:195], a[36:39], v[16:19]
buffer_load_dwordx4 a[80:83], v228, s[52:55], s81 offen offset:0 sc1
v_mfma_f32_16x16x32_bf16 v[4:7], v[196:199], a[32:35], v[4:7]
ds_read_b64_tr_b16 v[180:181], v209 offset:8192
ds_read_b64_tr_b16 v[182:183], v209 offset:12288
v_mfma_f32_16x16x32_bf16 v[20:23], v[196:199], a[36:39], v[20:23]
buffer_load_dwordx4 v233, s[40:43], s15 offen offset:1024 lds
v_mfma_f32_16x16x32_bf16 v[8:11], v[200:203], a[32:35], v[8:11]
ds_read_b64_tr_b16 v[184:185], v210 offset:8192
ds_read_b64_tr_b16 v[186:187], v210 offset:12288
v_mfma_f32_16x16x32_bf16 v[24:27], v[200:203], a[36:39], v[24:27]
buffer_load_dwordx4 a[84:87], v229, s[52:55], s81 offen offset:0 sc1
v_mfma_f32_16x16x32_bf16 v[12:15], v[204:207], a[32:35], v[12:15]
ds_read_b64_tr_b16 v[188:189], v211 offset:8192
ds_read_b64_tr_b16 v[190:191], v211 offset:12288
v_mfma_f32_16x16x32_bf16 v[28:31], v[204:207], a[36:39], v[28:31]
s_waitcnt lgkmcnt(8)
v_mfma_f32_16x16x32_bf16 v[32:35], v[160:163], a[32:35], v[32:35]
ds_read_b64_tr_b16 v[192:193], v208 offset:10240
ds_read_b64_tr_b16 v[194:195], v208 offset:14336
v_mfma_f32_16x16x32_bf16 v[48:51], v[160:163], a[36:39], v[48:51]
buffer_load_dwordx4 v232, s[44:47], s15 offen offset:2048 lds
v_mfma_f32_16x16x32_bf16 v[36:39], v[164:167], a[32:35], v[36:39]
ds_read_b64_tr_b16 v[196:197], v209 offset:10240
ds_read_b64_tr_b16 v[198:199], v209 offset:14336
v_mfma_f32_16x16x32_bf16 v[52:55], v[164:167], a[36:39], v[52:55]
buffer_load_dwordx4 a[88:91], v228, s[52:55], s81 offen offset:1024 sc1
v_mfma_f32_16x16x32_bf16 v[40:43], v[168:171], a[32:35], v[40:43]
ds_read_b64_tr_b16 v[200:201], v210 offset:10240
ds_read_b64_tr_b16 v[202:203], v210 offset:14336
v_mfma_f32_16x16x32_bf16 v[56:59], v[168:171], a[36:39], v[56:59]
buffer_load_dwordx4 v233, s[44:47], s15 offen offset:3072 lds
v_mfma_f32_16x16x32_bf16 v[44:47], v[172:175], a[32:35], v[44:47]
ds_read_b64_tr_b16 v[204:205], v211 offset:10240
ds_read_b64_tr_b16 v[206:207], v211 offset:14336
v_mfma_f32_16x16x32_bf16 v[60:63], v[172:175], a[36:39], v[60:63]
s_waitcnt vmcnt(15) lgkmcnt(8)
s_barrier
v_mfma_f32_16x16x32_bf16 v[0:3], v[176:179], a[40:43], v[0:3]
ds_read_b64_tr_b16 v[160:161], v212 offset:32768
ds_read_b64_tr_b16 v[162:163], v212 offset:36864
v_mfma_f32_16x16x32_bf16 v[16:19], v[176:179], a[44:47], v[16:19]
v_accvgpr_read_b32 v248, a48
v_accvgpr_read_b32 v249, a52
v_mfma_f32_16x16x32_bf16 v[4:7], v[180:183], a[40:43], v[4:7]
ds_read_b64_tr_b16 v[164:165], v213 offset:32768
ds_read_b64_tr_b16 v[166:167], v213 offset:36864
v_mfma_f32_16x16x32_bf16 v[20:23], v[180:183], a[44:47], v[20:23]
v_accvgpr_read_b32 v250, a56
v_bitop3_b32 v248, v248, v249, v250 bitop3:0x80
v_mfma_f32_16x16x32_bf16 v[8:11], v[184:187], a[40:43], v[8:11]
ds_read_b64_tr_b16 v[168:169], v214 offset:32768
ds_read_b64_tr_b16 v[170:171], v214 offset:36864
v_mfma_f32_16x16x32_bf16 v[24:27], v[184:187], a[44:47], v[24:27]
v_accvgpr_read_b32 v251, a60
v_bitop3_b32 v248, v248, v251, 1 bitop3:0x80
v_mfma_f32_16x16x32_bf16 v[12:15], v[188:191], a[40:43], v[12:15]
ds_read_b64_tr_b16 v[172:173], v215 offset:32768
ds_read_b64_tr_b16 v[174:175], v215 offset:36864
v_mfma_f32_16x16x32_bf16 v[28:31], v[188:191], a[44:47], v[28:31]
buffer_load_dwordx4 a[92:95], v229, s[52:55], s81 offen offset:1024 sc1
s_waitcnt lgkmcnt(8)
v_mfma_f32_16x16x32_bf16 v[32:35], v[192:195], a[40:43], v[32:35]
ds_read_b64_tr_b16 v[176:177], v212 offset:34816
ds_read_b64_tr_b16 v[178:179], v212 offset:38912
v_mfma_f32_16x16x32_bf16 v[48:51], v[192:195], a[44:47], v[48:51]
v_cmp_ne_u32 vcc, 1, v248
s_nop 0
s_cbranch_vccnz LSt3k3_%=
LRt3k3_%=:
v_mfma_f32_16x16x32_bf16 v[36:39], v[196:199], a[40:43], v[36:39]
ds_read_b64_tr_b16 v[180:181], v213 offset:34816
ds_read_b64_tr_b16 v[182:183], v213 offset:38912
s_xor_b32 s81, s83, 12288
v_mfma_f32_16x16x32_bf16 v[52:55], v[196:199], a[44:47], v[52:55]
buffer_load_dwordx4 a[96:99], v228, s[52:55], s81 offen offset:0 sc1
v_mfma_f32_16x16x32_bf16 v[40:43], v[200:203], a[40:43], v[40:43]
ds_read_b64_tr_b16 v[184:185], v214 offset:34816
ds_read_b64_tr_b16 v[186:187], v214 offset:38912
s_add_u32 m0, s77, 32768
v_mfma_f32_16x16x32_bf16 v[56:59], v[200:203], a[44:47], v[56:59]
buffer_load_dwordx4 v232, s[40:43], s16 offen offset:0 lds
v_mfma_f32_16x16x32_bf16 v[44:47], v[204:207], a[40:43], v[44:47]
ds_read_b64_tr_b16 v[188:189], v215 offset:34816
ds_read_b64_tr_b16 v[190:191], v215 offset:38912
v_mfma_f32_16x16x32_bf16 v[60:63], v[204:207], a[44:47], v[60:63]
s_waitcnt lgkmcnt(8)
v_mfma_f32_16x16x32_bf16 v[0:3], v[160:163], a[48:51], v[0:3]
ds_read_b64_tr_b16 v[192:193], v212 offset:40960
ds_read_b64_tr_b16 v[194:195], v212 offset:45056
v_mfma_f32_16x16x32_bf16 v[16:19], v[160:163], a[52:55], v[16:19]
buffer_load_dwordx4 a[100:103], v229, s[52:55], s81 offen offset:0 sc1
v_mfma_f32_16x16x32_bf16 v[4:7], v[164:167], a[48:51], v[4:7]
ds_read_b64_tr_b16 v[196:197], v213 offset:40960
ds_read_b64_tr_b16 v[198:199], v213 offset:45056
v_mfma_f32_16x16x32_bf16 v[20:23], v[164:167], a[52:55], v[20:23]
buffer_load_dwordx4 v233, s[40:43], s16 offen offset:1024 lds
v_mfma_f32_16x16x32_bf16 v[8:11], v[168:171], a[48:51], v[8:11]
ds_read_b64_tr_b16 v[200:201], v214 offset:40960
ds_read_b64_tr_b16 v[202:203], v214 offset:45056
v_mfma_f32_16x16x32_bf16 v[24:27], v[168:171], a[52:55], v[24:27]
v_mfma_f32_16x16x32_bf16 v[12:15], v[172:175], a[48:51], v[12:15]
ds_read_b64_tr_b16 v[204:205], v215 offset:40960
ds_read_b64_tr_b16 v[206:207], v215 offset:45056
v_mfma_f32_16x16x32_bf16 v[28:31], v[172:175], a[52:55], v[28:31]
s_waitcnt lgkmcnt(8)
v_mfma_f32_16x16x32_bf16 v[32:35], v[176:179], a[48:51], v[32:35]
ds_read_b64_tr_b16 v[160:161], v212 offset:43008
ds_read_b64_tr_b16 v[162:163], v212 offset:47104
v_mfma_f32_16x16x32_bf16 v[48:51], v[176:179], a[52:55], v[48:51]
buffer_load_dwordx4 v232, s[44:47], s16 offen offset:2048 lds
v_mfma_f32_16x16x32_bf16 v[36:39], v[180:183], a[48:51], v[36:39]
ds_read_b64_tr_b16 v[164:165], v213 offset:43008
ds_read_b64_tr_b16 v[166:167], v213 offset:47104
v_mfma_f32_16x16x32_bf16 v[52:55], v[180:183], a[52:55], v[52:55]
buffer_load_dwordx4 a[104:107], v228, s[52:55], s81 offen offset:1024 sc1
v_mfma_f32_16x16x32_bf16 v[40:43], v[184:187], a[48:51], v[40:43]
ds_read_b64_tr_b16 v[168:169], v214 offset:43008
ds_read_b64_tr_b16 v[170:171], v214 offset:47104
v_mfma_f32_16x16x32_bf16 v[56:59], v[184:187], a[52:55], v[56:59]
buffer_load_dwordx4 v233, s[44:47], s16 offen offset:3072 lds
v_mfma_f32_16x16x32_bf16 v[44:47], v[188:191], a[48:51], v[44:47]
ds_read_b64_tr_b16 v[172:173], v215 offset:43008
ds_read_b64_tr_b16 v[174:175], v215 offset:47104
v_mfma_f32_16x16x32_bf16 v[60:63], v[188:191], a[52:55], v[60:63]
s_waitcnt vmcnt(15) lgkmcnt(8)
s_barrier
v_mfma_f32_16x16x32_bf16 v[0:3], v[192:195], a[56:59], v[0:3]
ds_read_b64_tr_b16 v[176:177], v208 offset:16384
ds_read_b64_tr_b16 v[178:179], v208 offset:20480
v_mfma_f32_16x16x32_bf16 v[16:19], v[192:195], a[60:63], v[16:19]
v_accvgpr_read_b32 v248, a64
v_accvgpr_read_b32 v249, a68
v_mfma_f32_16x16x32_bf16 v[4:7], v[196:199], a[56:59], v[4:7]
ds_read_b64_tr_b16 v[180:181], v209 offset:16384
ds_read_b64_tr_b16 v[182:183], v209 offset:20480
v_mfma_f32_16x16x32_bf16 v[20:23], v[196:199], a[60:63], v[20:23]
v_accvgpr_read_b32 v250, a72
v_bitop3_b32 v248, v248, v249, v250 bitop3:0x80
v_mfma_f32_16x16x32_bf16 v[8:11], v[200:203], a[56:59], v[8:11]
ds_read_b64_tr_b16 v[184:185], v210 offset:16384
ds_read_b64_tr_b16 v[186:187], v210 offset:20480
v_mfma_f32_16x16x32_bf16 v[24:27], v[200:203], a[60:63], v[24:27]
v_accvgpr_read_b32 v251, a76
v_bitop3_b32 v248, v248, v251, 1 bitop3:0x80
v_mfma_f32_16x16x32_bf16 v[12:15], v[204:207], a[56:59], v[12:15]
ds_read_b64_tr_b16 v[188:189], v211 offset:16384
ds_read_b64_tr_b16 v[190:191], v211 offset:20480
v_mfma_f32_16x16x32_bf16 v[28:31], v[204:207], a[60:63], v[28:31]
buffer_load_dwordx4 a[108:111], v229, s[52:55], s81 offen offset:1024 sc1
s_waitcnt lgkmcnt(8)
v_mfma_f32_16x16x32_bf16 v[32:35], v[160:163], a[56:59], v[32:35]
ds_read_b64_tr_b16 v[192:193], v208 offset:18432
ds_read_b64_tr_b16 v[194:195], v208 offset:22528
v_mfma_f32_16x16x32_bf16 v[48:51], v[160:163], a[60:63], v[48:51]
v_cmp_ne_u32 vcc, 1, v248
s_nop 0
s_cbranch_vccnz LSt3k4_%=
LRt3k4_%=:
v_mfma_f32_16x16x32_bf16 v[36:39], v[164:167], a[56:59], v[36:39]
ds_read_b64_tr_b16 v[196:197], v209 offset:18432
ds_read_b64_tr_b16 v[198:199], v209 offset:22528
s_xor_b32 s81, s83, 14336
v_mfma_f32_16x16x32_bf16 v[52:55], v[164:167], a[60:63], v[52:55]
buffer_load_dwordx4 a[112:115], v228, s[52:55], s81 offen offset:0 sc1
v_mfma_f32_16x16x32_bf16 v[40:43], v[168:171], a[56:59], v[40:43]
ds_read_b64_tr_b16 v[200:201], v210 offset:18432
ds_read_b64_tr_b16 v[202:203], v210 offset:22528
s_add_u32 m0, s77, 65536
LC10_%=:
s_cbranch_execz LC14_%=
v_mfma_f32_16x16x32_bf16 v[56:59], v[168:171], a[60:63], v[56:59]
buffer_load_dwordx4 v232, s[40:43], s17 offen offset:0 lds
v_mfma_f32_16x16x32_bf16 v[44:47], v[172:175], a[56:59], v[44:47]
ds_read_b64_tr_b16 v[204:205], v211 offset:18432
ds_read_b64_tr_b16 v[206:207], v211 offset:22528
v_mfma_f32_16x16x32_bf16 v[60:63], v[172:175], a[60:63], v[60:63]
s_waitcnt lgkmcnt(8)
v_mfma_f32_16x16x32_bf16 v[0:3], v[176:179], a[64:67], v[0:3]
ds_read_b64_tr_b16 v[160:161], v208 offset:24576
ds_read_b64_tr_b16 v[162:163], v208 offset:28672
v_mfma_f32_16x16x32_bf16 v[16:19], v[176:179], a[68:71], v[16:19]
buffer_load_dwordx4 a[116:119], v229, s[52:55], s81 offen offset:0 sc1
v_mfma_f32_16x16x32_bf16 v[4:7], v[180:183], a[64:67], v[4:7]
ds_read_b64_tr_b16 v[164:165], v209 offset:24576
ds_read_b64_tr_b16 v[166:167], v209 offset:28672
v_mfma_f32_16x16x32_bf16 v[20:23], v[180:183], a[68:71], v[20:23]
buffer_load_dwordx4 v233, s[40:43], s17 offen offset:1024 lds
v_mfma_f32_16x16x32_bf16 v[8:11], v[184:187], a[64:67], v[8:11]
ds_read_b64_tr_b16 v[168:169], v210 offset:24576
ds_read_b64_tr_b16 v[170:171], v210 offset:28672
v_mfma_f32_16x16x32_bf16 v[24:27], v[184:187], a[68:71], v[24:27]
v_mfma_f32_16x16x32_bf16 v[12:15], v[188:191], a[64:67], v[12:15]
ds_read_b64_tr_b16 v[172:173], v211 offset:24576
ds_read_b64_tr_b16 v[174:175], v211 offset:28672
v_mfma_f32_16x16x32_bf16 v[28:31], v[188:191], a[68:71], v[28:31]
s_waitcnt lgkmcnt(8)
v_mfma_f32_16x16x32_bf16 v[32:35], v[192:195], a[64:67], v[32:35]
ds_read_b64_tr_b16 v[176:177], v208 offset:26624
ds_read_b64_tr_b16 v[178:179], v208 offset:30720
v_mfma_f32_16x16x32_bf16 v[48:51], v[192:195], a[68:71], v[48:51]
buffer_load_dwordx4 v232, s[44:47], s17 offen offset:2048 lds
v_mfma_f32_16x16x32_bf16 v[36:39], v[196:199], a[64:67], v[36:39]
ds_read_b64_tr_b16 v[180:181], v209 offset:26624
ds_read_b64_tr_b16 v[182:183], v209 offset:30720
v_mfma_f32_16x16x32_bf16 v[52:55], v[196:199], a[68:71], v[52:55]
buffer_load_dwordx4 a[120:123], v228, s[52:55], s81 offen offset:1024 sc1
v_mfma_f32_16x16x32_bf16 v[40:43], v[200:203], a[64:67], v[40:43]
ds_read_b64_tr_b16 v[184:185], v210 offset:26624
ds_read_b64_tr_b16 v[186:187], v210 offset:30720
)"
R"(
v_mfma_f32_16x16x32_bf16 v[56:59], v[200:203], a[68:71], v[56:59]
buffer_load_dwordx4 v233, s[44:47], s17 offen offset:3072 lds
v_mfma_f32_16x16x32_bf16 v[44:47], v[204:207], a[64:67], v[44:47]
ds_read_b64_tr_b16 v[188:189], v211 offset:26624
ds_read_b64_tr_b16 v[190:191], v211 offset:30720
buffer_load_dwordx4 a[124:127], v229, s[52:55], s81 offen offset:1024 sc1
v_mfma_f32_16x16x32_bf16 v[60:63], v[204:207], a[68:71], v[60:63]
s_waitcnt vmcnt(16) lgkmcnt(8)
s_barrier
v_mfma_f32_16x16x32_bf16 v[0:3], v[160:163], a[72:75], v[0:3]
ds_read_b64_tr_b16 v[192:193], v212 offset:49152
ds_read_b64_tr_b16 v[194:195], v212 offset:53248
v_mfma_f32_16x16x32_bf16 v[16:19], v[160:163], a[76:79], v[16:19]
v_accvgpr_read_b32 v248, a80
v_accvgpr_read_b32 v249, a84
v_mfma_f32_16x16x32_bf16 v[4:7], v[164:167], a[72:75], v[4:7]
ds_read_b64_tr_b16 v[196:197], v213 offset:49152
ds_read_b64_tr_b16 v[198:199], v213 offset:53248
v_mfma_f32_16x16x32_bf16 v[20:23], v[164:167], a[76:79], v[20:23]
v_accvgpr_read_b32 v250, a88
v_bitop3_b32 v248, v248, v249, v250 bitop3:0x80
v_mfma_f32_16x16x32_bf16 v[8:11], v[168:171], a[72:75], v[8:11]
ds_read_b64_tr_b16 v[200:201], v214 offset:49152
ds_read_b64_tr_b16 v[202:203], v214 offset:53248
v_mfma_f32_16x16x32_bf16 v[24:27], v[168:171], a[76:79], v[24:27]
v_accvgpr_read_b32 v251, a92
v_bitop3_b32 v248, v248, v251, 1 bitop3:0x80
v_mfma_f32_16x16x32_bf16 v[12:15], v[172:175], a[72:75], v[12:15]
ds_read_b64_tr_b16 v[204:205], v215 offset:49152
ds_read_b64_tr_b16 v[206:207], v215 offset:53248
v_mfma_f32_16x16x32_bf16 v[28:31], v[172:175], a[76:79], v[28:31]
s_xor_b32 s81, s83, 16384
s_waitcnt lgkmcnt(8)
v_mfma_f32_16x16x32_bf16 v[32:35], v[176:179], a[72:75], v[32:35]
ds_read_b64_tr_b16 v[160:161], v212 offset:51200
ds_read_b64_tr_b16 v[162:163], v212 offset:55296
v_mfma_f32_16x16x32_bf16 v[48:51], v[176:179], a[76:79], v[48:51]
v_cmp_ne_u32 vcc, 1, v248
s_nop 0
s_cbranch_vccnz LSt3k5_%=
LRt3k5_%=:
v_mfma_f32_16x16x32_bf16 v[36:39], v[180:183], a[72:75], v[36:39]
ds_read_b64_tr_b16 v[164:165], v213 offset:51200
ds_read_b64_tr_b16 v[166:167], v213 offset:55296
v_mfma_f32_16x16x32_bf16 v[52:55], v[180:183], a[76:79], v[52:55]
buffer_load_dwordx4 a[128:131], v228, s[52:55], s81 offen offset:0 sc1
v_mfma_f32_16x16x32_bf16 v[40:43], v[184:187], a[72:75], v[40:43]
ds_read_b64_tr_b16 v[168:169], v214 offset:51200
ds_read_b64_tr_b16 v[170:171], v214 offset:55296
v_add_u32 v232, 3670016, v232
v_add_u32 v233, 3670016, v233
s_add_u32 m0, s77, 49152
v_mfma_f32_16x16x32_bf16 v[56:59], v[184:187], a[76:79], v[56:59]
buffer_load_dwordx4 v232, s[40:43], s10 offen offset:0 lds
v_mfma_f32_16x16x32_bf16 v[44:47], v[188:191], a[72:75], v[44:47]
ds_read_b64_tr_b16 v[172:173], v215 offset:51200
ds_read_b64_tr_b16 v[174:175], v215 offset:55296
v_mfma_f32_16x16x32_bf16 v[60:63], v[188:191], a[76:79], v[60:63]
s_waitcnt lgkmcnt(8)
v_mfma_f32_16x16x32_bf16 v[0:3], v[192:195], a[80:83], v[0:3]
ds_read_b64_tr_b16 v[176:177], v212 offset:57344
ds_read_b64_tr_b16 v[178:179], v212 offset:61440
v_mfma_f32_16x16x32_bf16 v[16:19], v[192:195], a[84:87], v[16:19]
buffer_load_dwordx4 a[132:135], v229, s[52:55], s81 offen offset:0 sc1
v_mfma_f32_16x16x32_bf16 v[4:7], v[196:199], a[80:83], v[4:7]
ds_read_b64_tr_b16 v[180:181], v213 offset:57344
ds_read_b64_tr_b16 v[182:183], v213 offset:61440
v_mfma_f32_16x16x32_bf16 v[20:23], v[196:199], a[84:87], v[20:23]
buffer_load_dwordx4 v233, s[40:43], s10 offen offset:1024 lds
v_mfma_f32_16x16x32_bf16 v[8:11], v[200:203], a[80:83], v[8:11]
ds_read_b64_tr_b16 v[184:185], v214 offset:57344
ds_read_b64_tr_b16 v[186:187], v214 offset:61440
v_mfma_f32_16x16x32_bf16 v[24:27], v[200:203], a[84:87], v[24:27]
buffer_load_dwordx4 a[136:139], v228, s[52:55], s81 offen offset:1024 sc1
v_mfma_f32_16x16x32_bf16 v[12:15], v[204:207], a[80:83], v[12:15]
ds_read_b64_tr_b16 v[188:189], v215 offset:57344
ds_read_b64_tr_b16 v[190:191], v215 offset:61440
v_mfma_f32_16x16x32_bf16 v[28:31], v[204:207], a[84:87], v[28:31]
s_waitcnt lgkmcnt(8)
v_mfma_f32_16x16x32_bf16 v[32:35], v[160:163], a[80:83], v[32:35]
ds_read_b64_tr_b16 v[192:193], v212 offset:59392
ds_read_b64_tr_b16 v[194:195], v212 offset:63488
v_mfma_f32_16x16x32_bf16 v[48:51], v[160:163], a[84:87], v[48:51]
buffer_load_dwordx4 v232, s[44:47], s10 offen offset:2048 lds
v_mfma_f32_16x16x32_bf16 v[36:39], v[164:167], a[80:83], v[36:39]
ds_read_b64_tr_b16 v[196:197], v213 offset:59392
ds_read_b64_tr_b16 v[198:199], v213 offset:63488
v_mfma_f32_16x16x32_bf16 v[52:55], v[164:167], a[84:87], v[52:55]
v_mfma_f32_16x16x32_bf16 v[40:43], v[168:171], a[80:83], v[40:43]
ds_read_b64_tr_b16 v[200:201], v214 offset:59392
ds_read_b64_tr_b16 v[202:203], v214 offset:63488
buffer_load_dwordx4 a[140:143], v229, s[52:55], s81 offen offset:1024 sc1
v_mfma_f32_16x16x32_bf16 v[56:59], v[168:171], a[84:87], v[56:59]
buffer_load_dwordx4 v233, s[44:47], s10 offen offset:3072 lds
v_mfma_f32_16x16x32_bf16 v[44:47], v[172:175], a[80:83], v[44:47]
ds_read_b64_tr_b16 v[204:205], v215 offset:59392
ds_read_b64_tr_b16 v[206:207], v215 offset:63488
v_mfma_f32_16x16x32_bf16 v[60:63], v[172:175], a[84:87], v[60:63]
s_waitcnt vmcnt(16) lgkmcnt(8)
s_barrier
v_mfma_f32_16x16x32_bf16 v[0:3], v[176:179], a[88:91], v[0:3]
ds_read_b64_tr_b16 v[160:161], v208 offset:32768
ds_read_b64_tr_b16 v[162:163], v208 offset:36864
v_mfma_f32_16x16x32_bf16 v[16:19], v[176:179], a[92:95], v[16:19]
v_accvgpr_read_b32 v248, a96
v_accvgpr_read_b32 v249, a100
v_mfma_f32_16x16x32_bf16 v[4:7], v[180:183], a[88:91], v[4:7]
ds_read_b64_tr_b16 v[164:165], v209 offset:32768
ds_read_b64_tr_b16 v[166:167], v209 offset:36864
v_mfma_f32_16x16x32_bf16 v[20:23], v[180:183], a[92:95], v[20:23]
v_accvgpr_read_b32 v250, a104
v_bitop3_b32 v248, v248, v249, v250 bitop3:0x80
v_mfma_f32_16x16x32_bf16 v[8:11], v[184:187], a[88:91], v[8:11]
ds_read_b64_tr_b16 v[168:169], v210 offset:32768
ds_read_b64_tr_b16 v[170:171], v210 offset:36864
v_mfma_f32_16x16x32_bf16 v[24:27], v[184:187], a[92:95], v[24:27]
v_accvgpr_read_b32 v251, a108
v_bitop3_b32 v248, v248, v251, 1 bitop3:0x80
v_mfma_f32_16x16x32_bf16 v[12:15], v[188:191], a[88:91], v[12:15]
ds_read_b64_tr_b16 v[172:173], v211 offset:32768
ds_read_b64_tr_b16 v[174:175], v211 offset:36864
v_mfma_f32_16x16x32_bf16 v[28:31], v[188:191], a[92:95], v[28:31]
s_xor_b32 s81, s83, 18432
s_waitcnt lgkmcnt(8)
v_mfma_f32_16x16x32_bf16 v[32:35], v[192:195], a[88:91], v[32:35]
ds_read_b64_tr_b16 v[176:177], v208 offset:34816
ds_read_b64_tr_b16 v[178:179], v208 offset:38912
v_mfma_f32_16x16x32_bf16 v[48:51], v[192:195], a[92:95], v[48:51]
v_cmp_ne_u32 vcc, 1, v248
s_nop 0
s_cbranch_vccnz LSt3k6_%=
LRt3k6_%=:
v_mfma_f32_16x16x32_bf16 v[36:39], v[196:199], a[88:91], v[36:39]
ds_read_b64_tr_b16 v[180:181], v209 offset:34816
ds_read_b64_tr_b16 v[182:183], v209 offset:38912
v_mfma_f32_16x16x32_bf16 v[52:55], v[196:199], a[92:95], v[52:55]
buffer_load_dwordx4 a[144:147], v228, s[52:55], s81 offen offset:0 sc1
v_mfma_f32_16x16x32_bf16 v[40:43], v[200:203], a[88:91], v[40:43]
ds_read_b64_tr_b16 v[184:185], v210 offset:34816
ds_read_b64_tr_b16 v[186:187], v210 offset:38912
s_add_u32 m0, s77, 81920
v_mfma_f32_16x16x32_bf16 v[56:59], v[200:203], a[92:95], v[56:59]
buffer_load_dwordx4 v232, s[40:43], s11 offen offset:0 lds
v_mfma_f32_16x16x32_bf16 v[44:47], v[204:207], a[88:91], v[44:47]
ds_read_b64_tr_b16 v[188:189], v211 offset:34816
ds_read_b64_tr_b16 v[190:191], v211 offset:38912
v_mfma_f32_16x16x32_bf16 v[60:63], v[204:207], a[92:95], v[60:63]
s_waitcnt lgkmcnt(8)
v_mfma_f32_16x16x32_bf16 v[0:3], v[160:163], a[96:99], v[0:3]
ds_read_b64_tr_b16 v[192:193], v208 offset:40960
ds_read_b64_tr_b16 v[194:195], v208 offset:45056
v_mfma_f32_16x16x32_bf16 v[16:19], v[160:163], a[100:103], v[16:19]
buffer_load_dwordx4 a[148:151], v229, s[52:55], s81 offen offset:0 sc1
v_mfma_f32_16x16x32_bf16 v[4:7], v[164:167], a[96:99], v[4:7]
ds_read_b64_tr_b16 v[196:197], v209 offset:40960
ds_read_b64_tr_b16 v[198:199], v209 offset:45056
v_mfma_f32_16x16x32_bf16 v[20:23], v[164:167], a[100:103], v[20:23]
buffer_load_dwordx4 v233, s[40:43], s11 offen offset:1024 lds
v_mfma_f32_16x16x32_bf16 v[8:11], v[168:171], a[96:99], v[8:11]
ds_read_b64_tr_b16 v[200:201], v210 offset:40960
ds_read_b64_tr_b16 v[202:203], v210 offset:45056
v_mfma_f32_16x16x32_bf16 v[24:27], v[168:171], a[100:103], v[24:27]
buffer_load_dwordx4 a[152:155], v228, s[52:55], s81 offen offset:1024 sc1
v_mfma_f32_16x16x32_bf16 v[12:15], v[172:175], a[96:99], v[12:15]
ds_read_b64_tr_b16 v[204:205], v211 offset:40960
ds_read_b64_tr_b16 v[206:207], v211 offset:45056
v_mfma_f32_16x16x32_bf16 v[28:31], v[172:175], a[100:103], v[28:31]
s_waitcnt lgkmcnt(8)
v_mfma_f32_16x16x32_bf16 v[32:35], v[176:179], a[96:99], v[32:35]
ds_read_b64_tr_b16 v[160:161], v208 offset:43008
ds_read_b64_tr_b16 v[162:163], v208 offset:47104
v_mfma_f32_16x16x32_bf16 v[48:51], v[176:179], a[100:103], v[48:51]
buffer_load_dwordx4 v232, s[44:47], s11 offen offset:2048 lds
v_mfma_f32_16x16x32_bf16 v[36:39], v[180:183], a[96:99], v[36:39]
ds_read_b64_tr_b16 v[164:165], v209 offset:43008
ds_read_b64_tr_b16 v[166:167], v209 offset:47104
v_mfma_f32_16x16x32_bf16 v[52:55], v[180:183], a[100:103], v[52:55]
buffer_load_dwordx4 a[156:159], v229, s[52:55], s81 offen offset:1024 sc1
v_mfma_f32_16x16x32_bf16 v[40:43], v[184:187], a[96:99], v[40:43]
ds_read_b64_tr_b16 v[168:169], v210 offset:43008
ds_read_b64_tr_b16 v[170:171], v210 offset:47104
v_mfma_f32_16x16x32_bf16 v[56:59], v[184:187], a[100:103], v[56:59]
buffer_load_dwordx4 v233, s[44:47], s11 offen offset:3072 lds
v_mfma_f32_16x16x32_bf16 v[44:47], v[188:191], a[96:99], v[44:47]
ds_read_b64_tr_b16 v[172:173], v211 offset:43008
ds_read_b64_tr_b16 v[174:175], v211 offset:47104
v_mfma_f32_16x16x32_bf16 v[60:63], v[188:191], a[100:103], v[60:63]
s_waitcnt vmcnt(16) lgkmcnt(8)
s_barrier
v_mfma_f32_16x16x32_bf16 v[0:3], v[192:195], a[104:107], v[0:3]
ds_read_b64_tr_b16 v[176:177], v212 offset:0
ds_read_b64_tr_b16 v[178:179], v212 offset:4096
v_mfma_f32_16x16x32_bf16 v[16:19], v[192:195], a[108:111], v[16:19]
v_accvgpr_read_b32 v248, a112
v_accvgpr_read_b32 v249, a116
v_mfma_f32_16x16x32_bf16 v[4:7], v[196:199], a[104:107], v[4:7]
ds_read_b64_tr_b16 v[180:181], v213 offset:0
ds_read_b64_tr_b16 v[182:183], v213 offset:4096
v_mfma_f32_16x16x32_bf16 v[20:23], v[196:199], a[108:111], v[20:23]
v_accvgpr_read_b32 v250, a120
v_bitop3_b32 v248, v248, v249, v250 bitop3:0x80
v_mfma_f32_16x16x32_bf16 v[8:11], v[200:203], a[104:107], v[8:11]
ds_read_b64_tr_b16 v[184:185], v214 offset:0
ds_read_b64_tr_b16 v[186:187], v214 offset:4096
v_mfma_f32_16x16x32_bf16 v[24:27], v[200:203], a[108:111], v[24:27]
v_accvgpr_read_b32 v251, a124
v_bitop3_b32 v248, v248, v251, 1 bitop3:0x80
v_mfma_f32_16x16x32_bf16 v[12:15], v[204:207], a[104:107], v[12:15]
ds_read_b64_tr_b16 v[188:189], v215 offset:0
ds_read_b64_tr_b16 v[190:191], v215 offset:4096
s_xor_b32 s81, s83, 20480
v_mfma_f32_16x16x32_bf16 v[28:31], v[204:207], a[108:111], v[28:31]
buffer_load_dwordx4 a[160:163], v228, s[52:55], s81 offen offset:0 sc1
s_waitcnt lgkmcnt(8)
v_mfma_f32_16x16x32_bf16 v[32:35], v[160:163], a[104:107], v[32:35]
ds_read_b64_tr_b16 v[192:193], v212 offset:2048
ds_read_b64_tr_b16 v[194:195], v212 offset:6144
v_mfma_f32_16x16x32_bf16 v[48:51], v[160:163], a[108:111], v[48:51]
v_cmp_ne_u32 vcc, 1, v248
s_nop 0
s_cbranch_vccnz LSt3k7_%=
LRt3k7_%=:
v_mfma_f32_16x16x32_bf16 v[36:39], v[164:167], a[104:107], v[36:39]
ds_read_b64_tr_b16 v[196:197], v213 offset:2048
ds_read_b64_tr_b16 v[198:199], v213 offset:6144
v_mfma_f32_16x16x32_bf16 v[52:55], v[164:167], a[108:111], v[52:55]
buffer_load_dwordx4 a[164:167], v229, s[52:55], s81 offen offset:0 sc1
v_mfma_f32_16x16x32_bf16 v[40:43], v[168:171], a[104:107], v[40:43]
ds_read_b64_tr_b16 v[200:201], v214 offset:2048
ds_read_b64_tr_b16 v[202:203], v214 offset:6144
s_add_u32 m0, s77, 0
v_mfma_f32_16x16x32_bf16 v[56:59], v[168:171], a[108:111], v[56:59]
buffer_load_dwordx4 v232, s[40:43], s12 offen offset:0 lds
v_mfma_f32_16x16x32_bf16 v[44:47], v[172:175], a[104:107], v[44:47]
ds_read_b64_tr_b16 v[204:205], v215 offset:2048
ds_read_b64_tr_b16 v[206:207], v215 offset:6144
v_mfma_f32_16x16x32_bf16 v[60:63], v[172:175], a[108:111], v[60:63]
s_waitcnt lgkmcnt(8)
v_mfma_f32_16x16x32_bf16 v[0:3], v[176:179], a[112:115], v[0:3]
ds_read_b64_tr_b16 v[160:161], v212 offset:8192
ds_read_b64_tr_b16 v[162:163], v212 offset:12288
v_mfma_f32_16x16x32_bf16 v[16:19], v[176:179], a[116:119], v[16:19]
buffer_load_dwordx4 a[168:171], v228, s[52:55], s81 offen offset:1024 sc1
v_mfma_f32_16x16x32_bf16 v[4:7], v[180:183], a[112:115], v[4:7]
ds_read_b64_tr_b16 v[164:165], v213 offset:8192
ds_read_b64_tr_b16 v[166:167], v213 offset:12288
v_mfma_f32_16x16x32_bf16 v[20:23], v[180:183], a[116:119], v[20:23]
buffer_load_dwordx4 v233, s[40:43], s12 offen offset:1024 lds
v_mfma_f32_16x16x32_bf16 v[8:11], v[184:187], a[112:115], v[8:11]
ds_read_b64_tr_b16 v[168:169], v214 offset:8192
ds_read_b64_tr_b16 v[170:171], v214 offset:12288
v_mfma_f32_16x16x32_bf16 v[24:27], v[184:187], a[116:119], v[24:27]
v_mfma_f32_16x16x32_bf16 v[12:15], v[188:191], a[112:115], v[12:15]
ds_read_b64_tr_b16 v[172:173], v215 offset:8192
ds_read_b64_tr_b16 v[174:175], v215 offset:12288
v_mfma_f32_16x16x32_bf16 v[28:31], v[188:191], a[116:119], v[28:31]
s_waitcnt lgkmcnt(8)
v_mfma_f32_16x16x32_bf16 v[32:35], v[192:195], a[112:115], v[32:35]
ds_read_b64_tr_b16 v[176:177], v212 offset:10240
ds_read_b64_tr_b16 v[178:179], v212 offset:14336
v_mfma_f32_16x16x32_bf16 v[48:51], v[192:195], a[116:119], v[48:51]
buffer_load_dwordx4 v232, s[44:47], s12 offen offset:2048 lds
v_mfma_f32_16x16x32_bf16 v[36:39], v[196:199], a[112:115], v[36:39]
ds_read_b64_tr_b16 v[180:181], v213 offset:10240
ds_read_b64_tr_b16 v[182:183], v213 offset:14336
v_mfma_f32_16x16x32_bf16 v[52:55], v[196:199], a[116:119], v[52:55]
buffer_load_dwordx4 a[172:175], v229, s[52:55], s81 offen offset:1024 sc1
v_mfma_f32_16x16x32_bf16 v[40:43], v[200:203], a[112:115], v[40:43]
ds_read_b64_tr_b16 v[184:185], v214 offset:10240
ds_read_b64_tr_b16 v[186:187], v214 offset:14336
v_mfma_f32_16x16x32_bf16 v[56:59], v[200:203], a[116:119], v[56:59]
buffer_load_dwordx4 v233, s[44:47], s12 offen offset:3072 lds
v_mfma_f32_16x16x32_bf16 v[44:47], v[204:207], a[112:115], v[44:47]
ds_read_b64_tr_b16 v[188:189], v215 offset:10240
ds_read_b64_tr_b16 v[190:191], v215 offset:14336
v_mfma_f32_16x16x32_bf16 v[60:63], v[204:207], a[116:119], v[60:63]
s_waitcnt vmcnt(16) lgkmcnt(8)
s_barrier
v_mfma_f32_16x16x32_bf16 v[0:3], v[160:163], a[120:123], v[0:3]
ds_read_b64_tr_b16 v[192:193], v208 offset:49152
ds_read_b64_tr_b16 v[194:195], v208 offset:53248
v_mfma_f32_16x16x32_bf16 v[16:19], v[160:163], a[124:127], v[16:19]
v_accvgpr_read_b32 v248, a128
v_accvgpr_read_b32 v249, a132
v_mfma_f32_16x16x32_bf16 v[4:7], v[164:167], a[120:123], v[4:7]
ds_read_b64_tr_b16 v[196:197], v209 offset:49152
ds_read_b64_tr_b16 v[198:199], v209 offset:53248
v_mfma_f32_16x16x32_bf16 v[20:23], v[164:167], a[124:127], v[20:23]
v_accvgpr_read_b32 v250, a136
v_bitop3_b32 v248, v248, v249, v250 bitop3:0x80
v_mfma_f32_16x16x32_bf16 v[8:11], v[168:171], a[120:123], v[8:11]
ds_read_b64_tr_b16 v[200:201], v210 offset:49152
ds_read_b64_tr_b16 v[202:203], v210 offset:53248
v_mfma_f32_16x16x32_bf16 v[24:27], v[168:171], a[124:127], v[24:27]
v_accvgpr_read_b32 v251, a140
v_bitop3_b32 v248, v248, v251, 1 bitop3:0x80
v_mfma_f32_16x16x32_bf16 v[12:15], v[172:175], a[120:123], v[12:15]
ds_read_b64_tr_b16 v[204:205], v211 offset:49152
ds_read_b64_tr_b16 v[206:207], v211 offset:53248
s_xor_b32 s81, s83, 22528
v_mfma_f32_16x16x32_bf16 v[28:31], v[172:175], a[124:127], v[28:31]
buffer_load_dwordx4 a[176:179], v228, s[52:55], s81 offen offset:0 sc1
s_waitcnt lgkmcnt(8)
v_mfma_f32_16x16x32_bf16 v[32:35], v[176:179], a[120:123], v[32:35]
ds_read_b64_tr_b16 v[160:161], v208 offset:51200
ds_read_b64_tr_b16 v[162:163], v208 offset:55296
v_mfma_f32_16x16x32_bf16 v[48:51], v[176:179], a[124:127], v[48:51]
v_cmp_ne_u32 vcc, 1, v248
s_nop 0
s_cbranch_vccnz LSt3k8_%=
LRt3k8_%=:
v_mfma_f32_16x16x32_bf16 v[36:39], v[180:183], a[120:123], v[36:39]
ds_read_b64_tr_b16 v[164:165], v209 offset:51200
ds_read_b64_tr_b16 v[166:167], v209 offset:55296
v_mfma_f32_16x16x32_bf16 v[52:55], v[180:183], a[124:127], v[52:55]
buffer_load_dwordx4 a[180:183], v229, s[52:55], s81 offen offset:0 sc1
v_mfma_f32_16x16x32_bf16 v[40:43], v[184:187], a[120:123], v[40:43]
ds_read_b64_tr_b16 v[168:169], v210 offset:51200
ds_read_b64_tr_b16 v[170:171], v210 offset:55296
s_add_u32 m0, s77, 98304
v_mfma_f32_16x16x32_bf16 v[56:59], v[184:187], a[124:127], v[56:59]
buffer_load_dwordx4 v232, s[40:43], s13 offen offset:0 lds
v_mfma_f32_16x16x32_bf16 v[44:47], v[188:191], a[120:123], v[44:47]
ds_read_b64_tr_b16 v[172:173], v211 offset:51200
ds_read_b64_tr_b16 v[174:175], v211 offset:55296
v_mfma_f32_16x16x32_bf16 v[60:63], v[188:191], a[124:127], v[60:63]
s_waitcnt lgkmcnt(8)
v_mfma_f32_16x16x32_bf16 v[0:3], v[192:195], a[128:131], v[0:3]
ds_read_b64_tr_b16 v[176:177], v208 offset:57344
ds_read_b64_tr_b16 v[178:179], v208 offset:61440
v_mfma_f32_16x16x32_bf16 v[16:19], v[192:195], a[132:135], v[16:19]
buffer_load_dwordx4 a[184:187], v228, s[52:55], s81 offen offset:1024 sc1
v_mfma_f32_16x16x32_bf16 v[4:7], v[196:199], a[128:131], v[4:7]
ds_read_b64_tr_b16 v[180:181], v209 offset:57344
ds_read_b64_tr_b16 v[182:183], v209 offset:61440
v_mfma_f32_16x16x32_bf16 v[20:23], v[196:199], a[132:135], v[20:23]
buffer_load_dwordx4 v233, s[40:43], s13 offen offset:1024 lds
v_mfma_f32_16x16x32_bf16 v[8:11], v[200:203], a[128:131], v[8:11]
ds_read_b64_tr_b16 v[184:185], v210 offset:57344
ds_read_b64_tr_b16 v[186:187], v210 offset:61440
v_mfma_f32_16x16x32_bf16 v[24:27], v[200:203], a[132:135], v[24:27]
v_mfma_f32_16x16x32_bf16 v[12:15], v[204:207], a[128:131], v[12:15]
ds_read_b64_tr_b16 v[188:189], v211 offset:57344
ds_read_b64_tr_b16 v[190:191], v211 offset:61440
v_mfma_f32_16x16x32_bf16 v[28:31], v[204:207], a[132:135], v[28:31]
s_waitcnt lgkmcnt(8)
v_mfma_f32_16x16x32_bf16 v[32:35], v[160:163], a[128:131], v[32:35]
ds_read_b64_tr_b16 v[192:193], v208 offset:59392
ds_read_b64_tr_b16 v[194:195], v208 offset:63488
v_mfma_f32_16x16x32_bf16 v[48:51], v[160:163], a[132:135], v[48:51]
buffer_load_dwordx4 v232, s[44:47], s13 offen offset:2048 lds
v_mfma_f32_16x16x32_bf16 v[36:39], v[164:167], a[128:131], v[36:39]
ds_read_b64_tr_b16 v[196:197], v209 offset:59392
ds_read_b64_tr_b16 v[198:199], v209 offset:63488
v_mfma_f32_16x16x32_bf16 v[52:55], v[164:167], a[132:135], v[52:55]
buffer_load_dwordx4 a[188:191], v229, s[52:55], s81 offen offset:1024 sc1
v_mfma_f32_16x16x32_bf16 v[40:43], v[168:171], a[128:131], v[40:43]
ds_read_b64_tr_b16 v[200:201], v210 offset:59392
ds_read_b64_tr_b16 v[202:203], v210 offset:63488
v_mfma_f32_16x16x32_bf16 v[56:59], v[168:171], a[132:135], v[56:59]
buffer_load_dwordx4 v233, s[44:47], s13 offen offset:3072 lds
s_xor_b32 s81, s83, 24576
v_mfma_f32_16x16x32_bf16 v[44:47], v[172:175], a[128:131], v[44:47]
ds_read_b64_tr_b16 v[204:205], v211 offset:59392
ds_read_b64_tr_b16 v[206:207], v211 offset:63488
buffer_load_dwordx4 a[192:195], v228, s[52:55], s81 offen offset:0 sc1
v_mfma_f32_16x16x32_bf16 v[60:63], v[172:175], a[132:135], v[60:63]
s_waitcnt vmcnt(17) lgkmcnt(8)
s_barrier
v_mfma_f32_16x16x32_bf16 v[0:3], v[176:179], a[136:139], v[0:3]
ds_read_b64_tr_b16 v[160:161], v212 offset:16384
ds_read_b64_tr_b16 v[162:163], v212 offset:20480
v_mfma_f32_16x16x32_bf16 v[16:19], v[176:179], a[140:143], v[16:19]
v_accvgpr_read_b32 v248, a144
v_accvgpr_read_b32 v249, a148
v_mfma_f32_16x16x32_bf16 v[4:7], v[180:183], a[136:139], v[4:7]
ds_read_b64_tr_b16 v[164:165], v213 offset:16384
ds_read_b64_tr_b16 v[166:167], v213 offset:20480
v_mfma_f32_16x16x32_bf16 v[20:23], v[180:183], a[140:143], v[20:23]
v_accvgpr_read_b32 v250, a152
v_bitop3_b32 v248, v248, v249, v250 bitop3:0x80
v_mfma_f32_16x16x32_bf16 v[8:11], v[184:187], a[136:139], v[8:11]
ds_read_b64_tr_b16 v[168:169], v214 offset:16384
ds_read_b64_tr_b16 v[170:171], v214 offset:20480
v_mfma_f32_16x16x32_bf16 v[24:27], v[184:187], a[140:143], v[24:27]
v_accvgpr_read_b32 v251, a156
v_bitop3_b32 v248, v248, v251, 1 bitop3:0x80
v_mfma_f32_16x16x32_bf16 v[12:15], v[188:191], a[136:139], v[12:15]
ds_read_b64_tr_b16 v[172:173], v215 offset:16384
ds_read_b64_tr_b16 v[174:175], v215 offset:20480
v_mfma_f32_16x16x32_bf16 v[28:31], v[188:191], a[140:143], v[28:31]
s_add_u32 m0, s77, 16384
s_waitcnt lgkmcnt(8)
v_mfma_f32_16x16x32_bf16 v[32:35], v[192:195], a[136:139], v[32:35]
ds_read_b64_tr_b16 v[176:177], v212 offset:18432
ds_read_b64_tr_b16 v[178:179], v212 offset:22528
v_mfma_f32_16x16x32_bf16 v[48:51], v[192:195], a[140:143], v[48:51]
v_cmp_ne_u32 vcc, 1, v248
s_nop 0
s_cbranch_vccnz LSt3k9_%=
)"
R"(
LRt3k9_%=:
v_mfma_f32_16x16x32_bf16 v[36:39], v[196:199], a[136:139], v[36:39]
ds_read_b64_tr_b16 v[180:181], v213 offset:18432
ds_read_b64_tr_b16 v[182:183], v213 offset:22528
v_mfma_f32_16x16x32_bf16 v[52:55], v[196:199], a[140:143], v[52:55]
buffer_load_dwordx4 a[196:199], v229, s[52:55], s81 offen offset:0 sc1
v_mfma_f32_16x16x32_bf16 v[40:43], v[200:203], a[136:139], v[40:43]
ds_read_b64_tr_b16 v[184:185], v214 offset:18432
ds_read_b64_tr_b16 v[186:187], v214 offset:22528
v_mfma_f32_16x16x32_bf16 v[56:59], v[200:203], a[140:143], v[56:59]
buffer_load_dwordx4 v232, s[40:43], s14 offen offset:0 lds
v_mfma_f32_16x16x32_bf16 v[44:47], v[204:207], a[136:139], v[44:47]
ds_read_b64_tr_b16 v[188:189], v215 offset:18432
ds_read_b64_tr_b16 v[190:191], v215 offset:22528
v_mfma_f32_16x16x32_bf16 v[60:63], v[204:207], a[140:143], v[60:63]
s_waitcnt lgkmcnt(8)
v_mfma_f32_16x16x32_bf16 v[0:3], v[160:163], a[144:147], v[0:3]
ds_read_b64_tr_b16 v[192:193], v212 offset:24576
ds_read_b64_tr_b16 v[194:195], v212 offset:28672
v_mfma_f32_16x16x32_bf16 v[16:19], v[160:163], a[148:151], v[16:19]
buffer_load_dwordx4 a[200:203], v228, s[52:55], s81 offen offset:1024 sc1
v_mfma_f32_16x16x32_bf16 v[4:7], v[164:167], a[144:147], v[4:7]
ds_read_b64_tr_b16 v[196:197], v213 offset:24576
ds_read_b64_tr_b16 v[198:199], v213 offset:28672
v_mfma_f32_16x16x32_bf16 v[20:23], v[164:167], a[148:151], v[20:23]
buffer_load_dwordx4 v233, s[40:43], s14 offen offset:1024 lds
v_mfma_f32_16x16x32_bf16 v[8:11], v[168:171], a[144:147], v[8:11]
ds_read_b64_tr_b16 v[200:201], v214 offset:24576
ds_read_b64_tr_b16 v[202:203], v214 offset:28672
v_mfma_f32_16x16x32_bf16 v[24:27], v[168:171], a[148:151], v[24:27]
buffer_load_dwordx4 a[204:207], v229, s[52:55], s81 offen offset:1024 sc1
v_mfma_f32_16x16x32_bf16 v[12:15], v[172:175], a[144:147], v[12:15]
ds_read_b64_tr_b16 v[204:205], v215 offset:24576
ds_read_b64_tr_b16 v[206:207], v215 offset:28672
v_mfma_f32_16x16x32_bf16 v[28:31], v[172:175], a[148:151], v[28:31]
s_xor_b32 s81, s83, 26624
s_waitcnt lgkmcnt(8)
v_mfma_f32_16x16x32_bf16 v[32:35], v[176:179], a[144:147], v[32:35]
ds_read_b64_tr_b16 v[160:161], v212 offset:26624
ds_read_b64_tr_b16 v[162:163], v212 offset:30720
v_mfma_f32_16x16x32_bf16 v[48:51], v[176:179], a[148:151], v[48:51]
buffer_load_dwordx4 v232, s[44:47], s14 offen offset:2048 lds
v_mfma_f32_16x16x32_bf16 v[36:39], v[180:183], a[144:147], v[36:39]
ds_read_b64_tr_b16 v[164:165], v213 offset:26624
ds_read_b64_tr_b16 v[166:167], v213 offset:30720
v_mfma_f32_16x16x32_bf16 v[52:55], v[180:183], a[148:151], v[52:55]
v_mfma_f32_16x16x32_bf16 v[40:43], v[184:187], a[144:147], v[40:43]
ds_read_b64_tr_b16 v[168:169], v214 offset:26624
ds_read_b64_tr_b16 v[170:171], v214 offset:30720
buffer_load_dwordx4 a[208:211], v228, s[52:55], s81 offen offset:0 sc1
v_mfma_f32_16x16x32_bf16 v[56:59], v[184:187], a[148:151], v[56:59]
buffer_load_dwordx4 v233, s[44:47], s14 offen offset:3072 lds
v_mfma_f32_16x16x32_bf16 v[44:47], v[188:191], a[144:147], v[44:47]
ds_read_b64_tr_b16 v[172:173], v215 offset:26624
ds_read_b64_tr_b16 v[174:175], v215 offset:30720
v_mfma_f32_16x16x32_bf16 v[60:63], v[188:191], a[148:151], v[60:63]
s_waitcnt vmcnt(17) lgkmcnt(8)
s_barrier
v_mfma_f32_16x16x32_bf16 v[0:3], v[192:195], a[152:155], v[0:3]
ds_read_b64_tr_b16 v[176:177], v208 offset:0
ds_read_b64_tr_b16 v[178:179], v208 offset:4096
v_mfma_f32_16x16x32_bf16 v[16:19], v[192:195], a[156:159], v[16:19]
v_accvgpr_read_b32 v248, a160
v_accvgpr_read_b32 v249, a164
v_mfma_f32_16x16x32_bf16 v[4:7], v[196:199], a[152:155], v[4:7]
ds_read_b64_tr_b16 v[180:181], v209 offset:0
ds_read_b64_tr_b16 v[182:183], v209 offset:4096
v_mfma_f32_16x16x32_bf16 v[20:23], v[196:199], a[156:159], v[20:23]
v_accvgpr_read_b32 v250, a168
v_bitop3_b32 v248, v248, v249, v250 bitop3:0x80
v_mfma_f32_16x16x32_bf16 v[8:11], v[200:203], a[152:155], v[8:11]
ds_read_b64_tr_b16 v[184:185], v210 offset:0
ds_read_b64_tr_b16 v[186:187], v210 offset:4096
v_mfma_f32_16x16x32_bf16 v[24:27], v[200:203], a[156:159], v[24:27]
v_accvgpr_read_b32 v251, a172
v_bitop3_b32 v248, v248, v251, 1 bitop3:0x80
v_mfma_f32_16x16x32_bf16 v[12:15], v[204:207], a[152:155], v[12:15]
ds_read_b64_tr_b16 v[188:189], v211 offset:0
ds_read_b64_tr_b16 v[190:191], v211 offset:4096
v_mfma_f32_16x16x32_bf16 v[28:31], v[204:207], a[156:159], v[28:31]
s_add_u32 m0, s77, 114688
s_waitcnt lgkmcnt(8)
v_mfma_f32_16x16x32_bf16 v[32:35], v[160:163], a[152:155], v[32:35]
ds_read_b64_tr_b16 v[192:193], v208 offset:2048
ds_read_b64_tr_b16 v[194:195], v208 offset:6144
v_mfma_f32_16x16x32_bf16 v[48:51], v[160:163], a[156:159], v[48:51]
v_cmp_ne_u32 vcc, 1, v248
s_nop 0
s_cbranch_vccnz LSt3k10_%=
LRt3k10_%=:
v_mfma_f32_16x16x32_bf16 v[36:39], v[164:167], a[152:155], v[36:39]
ds_read_b64_tr_b16 v[196:197], v209 offset:2048
ds_read_b64_tr_b16 v[198:199], v209 offset:6144
v_mfma_f32_16x16x32_bf16 v[52:55], v[164:167], a[156:159], v[52:55]
buffer_load_dwordx4 a[212:215], v229, s[52:55], s81 offen offset:0 sc1
v_mfma_f32_16x16x32_bf16 v[40:43], v[168:171], a[152:155], v[40:43]
ds_read_b64_tr_b16 v[200:201], v210 offset:2048
ds_read_b64_tr_b16 v[202:203], v210 offset:6144
v_mfma_f32_16x16x32_bf16 v[56:59], v[168:171], a[156:159], v[56:59]
buffer_load_dwordx4 v232, s[40:43], s15 offen offset:0 lds
v_mfma_f32_16x16x32_bf16 v[44:47], v[172:175], a[152:155], v[44:47]
ds_read_b64_tr_b16 v[204:205], v211 offset:2048
ds_read_b64_tr_b16 v[206:207], v211 offset:6144
LC11_%=:
s_cbranch_execz LC15_%=
v_mfma_f32_16x16x32_bf16 v[60:63], v[172:175], a[156:159], v[60:63]
s_waitcnt lgkmcnt(8)
v_mfma_f32_16x16x32_bf16 v[0:3], v[176:179], a[160:163], v[0:3]
ds_read_b64_tr_b16 v[160:161], v208 offset:8192
ds_read_b64_tr_b16 v[162:163], v208 offset:12288
v_mfma_f32_16x16x32_bf16 v[16:19], v[176:179], a[164:167], v[16:19]
buffer_load_dwordx4 a[216:219], v228, s[52:55], s81 offen offset:1024 sc1
v_mfma_f32_16x16x32_bf16 v[4:7], v[180:183], a[160:163], v[4:7]
ds_read_b64_tr_b16 v[164:165], v209 offset:8192
ds_read_b64_tr_b16 v[166:167], v209 offset:12288
v_mfma_f32_16x16x32_bf16 v[20:23], v[180:183], a[164:167], v[20:23]
buffer_load_dwordx4 v233, s[40:43], s15 offen offset:1024 lds
v_mfma_f32_16x16x32_bf16 v[8:11], v[184:187], a[160:163], v[8:11]
ds_read_b64_tr_b16 v[168:169], v210 offset:8192
ds_read_b64_tr_b16 v[170:171], v210 offset:12288
v_mfma_f32_16x16x32_bf16 v[24:27], v[184:187], a[164:167], v[24:27]
buffer_load_dwordx4 a[220:223], v229, s[52:55], s81 offen offset:1024 sc1
v_mfma_f32_16x16x32_bf16 v[12:15], v[188:191], a[160:163], v[12:15]
ds_read_b64_tr_b16 v[172:173], v211 offset:8192
ds_read_b64_tr_b16 v[174:175], v211 offset:12288
v_mfma_f32_16x16x32_bf16 v[28:31], v[188:191], a[164:167], v[28:31]
s_xor_b32 s81, s83, 28672
s_waitcnt lgkmcnt(8)
v_mfma_f32_16x16x32_bf16 v[32:35], v[192:195], a[160:163], v[32:35]
ds_read_b64_tr_b16 v[176:177], v208 offset:10240
ds_read_b64_tr_b16 v[178:179], v208 offset:14336
v_mfma_f32_16x16x32_bf16 v[48:51], v[192:195], a[164:167], v[48:51]
buffer_load_dwordx4 v232, s[44:47], s15 offen offset:2048 lds
v_mfma_f32_16x16x32_bf16 v[36:39], v[196:199], a[160:163], v[36:39]
ds_read_b64_tr_b16 v[180:181], v209 offset:10240
ds_read_b64_tr_b16 v[182:183], v209 offset:14336
v_mfma_f32_16x16x32_bf16 v[52:55], v[196:199], a[164:167], v[52:55]
buffer_load_dwordx4 a[224:227], v228, s[52:55], s81 offen offset:0 sc1
v_mfma_f32_16x16x32_bf16 v[40:43], v[200:203], a[160:163], v[40:43]
ds_read_b64_tr_b16 v[184:185], v210 offset:10240
ds_read_b64_tr_b16 v[186:187], v210 offset:14336
v_mfma_f32_16x16x32_bf16 v[56:59], v[200:203], a[164:167], v[56:59]
buffer_load_dwordx4 v233, s[44:47], s15 offen offset:3072 lds
v_mfma_f32_16x16x32_bf16 v[44:47], v[204:207], a[160:163], v[44:47]
ds_read_b64_tr_b16 v[188:189], v211 offset:10240
ds_read_b64_tr_b16 v[190:191], v211 offset:14336
v_mfma_f32_16x16x32_bf16 v[60:63], v[204:207], a[164:167], v[60:63]
s_waitcnt vmcnt(17) lgkmcnt(8)
s_barrier
v_mfma_f32_16x16x32_bf16 v[0:3], v[160:163], a[168:171], v[0:3]
ds_read_b64_tr_b16 v[192:193], v212 offset:32768
ds_read_b64_tr_b16 v[194:195], v212 offset:36864
v_mfma_f32_16x16x32_bf16 v[16:19], v[160:163], a[172:175], v[16:19]
v_accvgpr_read_b32 v248, a176
v_accvgpr_read_b32 v249, a180
v_mfma_f32_16x16x32_bf16 v[4:7], v[164:167], a[168:171], v[4:7]
ds_read_b64_tr_b16 v[196:197], v213 offset:32768
ds_read_b64_tr_b16 v[198:199], v213 offset:36864
v_mfma_f32_16x16x32_bf16 v[20:23], v[164:167], a[172:175], v[20:23]
v_accvgpr_read_b32 v250, a184
v_bitop3_b32 v248, v248, v249, v250 bitop3:0x80
v_mfma_f32_16x16x32_bf16 v[8:11], v[168:171], a[168:171], v[8:11]
ds_read_b64_tr_b16 v[200:201], v214 offset:32768
ds_read_b64_tr_b16 v[202:203], v214 offset:36864
v_mfma_f32_16x16x32_bf16 v[24:27], v[168:171], a[172:175], v[24:27]
v_accvgpr_read_b32 v251, a188
v_bitop3_b32 v248, v248, v251, 1 bitop3:0x80
v_mfma_f32_16x16x32_bf16 v[12:15], v[172:175], a[168:171], v[12:15]
ds_read_b64_tr_b16 v[204:205], v215 offset:32768
ds_read_b64_tr_b16 v[206:207], v215 offset:36864
v_mfma_f32_16x16x32_bf16 v[28:31], v[172:175], a[172:175], v[28:31]
buffer_load_dwordx4 a[228:231], v229, s[52:55], s81 offen offset:0 sc1
s_waitcnt lgkmcnt(8)
v_mfma_f32_16x16x32_bf16 v[32:35], v[176:179], a[168:171], v[32:35]
ds_read_b64_tr_b16 v[160:161], v212 offset:34816
ds_read_b64_tr_b16 v[162:163], v212 offset:38912
v_mfma_f32_16x16x32_bf16 v[48:51], v[176:179], a[172:175], v[48:51]
v_cmp_ne_u32 vcc, 1, v248
s_nop 0
s_cbranch_vccnz LSt3k11_%=
LRt3k11_%=:
v_mfma_f32_16x16x32_bf16 v[36:39], v[180:183], a[168:171], v[36:39]
ds_read_b64_tr_b16 v[164:165], v213 offset:34816
ds_read_b64_tr_b16 v[166:167], v213 offset:38912
v_mfma_f32_16x16x32_bf16 v[52:55], v[180:183], a[172:175], v[52:55]
buffer_load_dwordx4 a[232:235], v228, s[52:55], s81 offen offset:1024 sc1
v_mfma_f32_16x16x32_bf16 v[40:43], v[184:187], a[168:171], v[40:43]
ds_read_b64_tr_b16 v[168:169], v214 offset:34816
ds_read_b64_tr_b16 v[170:171], v214 offset:38912
s_add_u32 m0, s77, 32768
v_mfma_f32_16x16x32_bf16 v[56:59], v[184:187], a[172:175], v[56:59]
buffer_load_dwordx4 v232, s[40:43], s16 offen offset:0 lds
v_mfma_f32_16x16x32_bf16 v[44:47], v[188:191], a[168:171], v[44:47]
ds_read_b64_tr_b16 v[172:173], v215 offset:34816
ds_read_b64_tr_b16 v[174:175], v215 offset:38912
v_mfma_f32_16x16x32_bf16 v[60:63], v[188:191], a[172:175], v[60:63]
s_waitcnt lgkmcnt(8)
v_mfma_f32_16x16x32_bf16 v[0:3], v[192:195], a[176:179], v[0:3]
ds_read_b64_tr_b16 v[176:177], v212 offset:40960
ds_read_b64_tr_b16 v[178:179], v212 offset:45056
v_mfma_f32_16x16x32_bf16 v[16:19], v[192:195], a[180:183], v[16:19]
buffer_load_dwordx4 a[236:239], v229, s[52:55], s81 offen offset:1024 sc1
v_mfma_f32_16x16x32_bf16 v[4:7], v[196:199], a[176:179], v[4:7]
ds_read_b64_tr_b16 v[180:181], v213 offset:40960
ds_read_b64_tr_b16 v[182:183], v213 offset:45056
v_mfma_f32_16x16x32_bf16 v[20:23], v[196:199], a[180:183], v[20:23]
buffer_load_dwordx4 v233, s[40:43], s16 offen offset:1024 lds
v_mfma_f32_16x16x32_bf16 v[8:11], v[200:203], a[176:179], v[8:11]
ds_read_b64_tr_b16 v[184:185], v214 offset:40960
ds_read_b64_tr_b16 v[186:187], v214 offset:45056
v_mfma_f32_16x16x32_bf16 v[24:27], v[200:203], a[180:183], v[24:27]
v_mfma_f32_16x16x32_bf16 v[12:15], v[204:207], a[176:179], v[12:15]
ds_read_b64_tr_b16 v[188:189], v215 offset:40960
ds_read_b64_tr_b16 v[190:191], v215 offset:45056
v_mfma_f32_16x16x32_bf16 v[28:31], v[204:207], a[180:183], v[28:31]
s_xor_b32 s81, s83, 30720
s_waitcnt lgkmcnt(8)
v_mfma_f32_16x16x32_bf16 v[32:35], v[160:163], a[176:179], v[32:35]
ds_read_b64_tr_b16 v[192:193], v212 offset:43008
ds_read_b64_tr_b16 v[194:195], v212 offset:47104
v_mfma_f32_16x16x32_bf16 v[48:51], v[160:163], a[180:183], v[48:51]
buffer_load_dwordx4 v232, s[44:47], s16 offen offset:2048 lds
v_mfma_f32_16x16x32_bf16 v[36:39], v[164:167], a[176:179], v[36:39]
ds_read_b64_tr_b16 v[196:197], v213 offset:43008
ds_read_b64_tr_b16 v[198:199], v213 offset:47104
v_mfma_f32_16x16x32_bf16 v[52:55], v[164:167], a[180:183], v[52:55]
buffer_load_dwordx4 a[240:243], v228, s[52:55], s81 offen offset:0 sc1
v_mfma_f32_16x16x32_bf16 v[40:43], v[168:171], a[176:179], v[40:43]
ds_read_b64_tr_b16 v[200:201], v214 offset:43008
ds_read_b64_tr_b16 v[202:203], v214 offset:47104
v_mfma_f32_16x16x32_bf16 v[56:59], v[168:171], a[180:183], v[56:59]
buffer_load_dwordx4 v233, s[44:47], s16 offen offset:3072 lds
v_mfma_f32_16x16x32_bf16 v[44:47], v[172:175], a[176:179], v[44:47]
ds_read_b64_tr_b16 v[204:205], v215 offset:43008
ds_read_b64_tr_b16 v[206:207], v215 offset:47104
v_mfma_f32_16x16x32_bf16 v[60:63], v[172:175], a[180:183], v[60:63]
s_waitcnt vmcnt(16) lgkmcnt(8)
s_barrier
v_mfma_f32_16x16x32_bf16 v[0:3], v[176:179], a[184:187], v[0:3]
ds_read_b64_tr_b16 v[160:161], v208 offset:16384
ds_read_b64_tr_b16 v[162:163], v208 offset:20480
v_mfma_f32_16x16x32_bf16 v[16:19], v[176:179], a[188:191], v[16:19]
v_accvgpr_read_b32 v248, a192
v_accvgpr_read_b32 v249, a196
v_mfma_f32_16x16x32_bf16 v[4:7], v[180:183], a[184:187], v[4:7]
ds_read_b64_tr_b16 v[164:165], v209 offset:16384
ds_read_b64_tr_b16 v[166:167], v209 offset:20480
v_mfma_f32_16x16x32_bf16 v[20:23], v[180:183], a[188:191], v[20:23]
v_accvgpr_read_b32 v250, a200
v_bitop3_b32 v248, v248, v249, v250 bitop3:0x80
v_mfma_f32_16x16x32_bf16 v[8:11], v[184:187], a[184:187], v[8:11]
ds_read_b64_tr_b16 v[168:169], v210 offset:16384
ds_read_b64_tr_b16 v[170:171], v210 offset:20480
v_mfma_f32_16x16x32_bf16 v[24:27], v[184:187], a[188:191], v[24:27]
v_accvgpr_read_b32 v251, a204
v_bitop3_b32 v248, v248, v251, 1 bitop3:0x80
v_mfma_f32_16x16x32_bf16 v[12:15], v[188:191], a[184:187], v[12:15]
ds_read_b64_tr_b16 v[172:173], v211 offset:16384
ds_read_b64_tr_b16 v[174:175], v211 offset:20480
v_mfma_f32_16x16x32_bf16 v[28:31], v[188:191], a[188:191], v[28:31]
buffer_load_dwordx4 a[244:247], v229, s[52:55], s81 offen offset:0 sc1
s_waitcnt lgkmcnt(8)
v_mfma_f32_16x16x32_bf16 v[32:35], v[192:195], a[184:187], v[32:35]
ds_read_b64_tr_b16 v[176:177], v208 offset:18432
ds_read_b64_tr_b16 v[178:179], v208 offset:22528
v_mfma_f32_16x16x32_bf16 v[48:51], v[192:195], a[188:191], v[48:51]
v_cmp_ne_u32 vcc, 1, v248
s_nop 0
s_cbranch_vccnz LSt3k12_%=
LRt3k12_%=:
v_mfma_f32_16x16x32_bf16 v[36:39], v[196:199], a[184:187], v[36:39]
ds_read_b64_tr_b16 v[180:181], v209 offset:18432
ds_read_b64_tr_b16 v[182:183], v209 offset:22528
v_mfma_f32_16x16x32_bf16 v[52:55], v[196:199], a[188:191], v[52:55]
buffer_load_dwordx4 a[248:251], v228, s[52:55], s81 offen offset:1024 sc1
v_mfma_f32_16x16x32_bf16 v[40:43], v[200:203], a[184:187], v[40:43]
ds_read_b64_tr_b16 v[184:185], v210 offset:18432
ds_read_b64_tr_b16 v[186:187], v210 offset:22528
s_add_u32 m0, s77, 65536
v_mfma_f32_16x16x32_bf16 v[56:59], v[200:203], a[188:191], v[56:59]
buffer_load_dwordx4 v232, s[40:43], s17 offen offset:0 lds
v_mfma_f32_16x16x32_bf16 v[44:47], v[204:207], a[184:187], v[44:47]
ds_read_b64_tr_b16 v[188:189], v211 offset:18432
ds_read_b64_tr_b16 v[190:191], v211 offset:22528
v_mfma_f32_16x16x32_bf16 v[60:63], v[204:207], a[188:191], v[60:63]
s_waitcnt lgkmcnt(8)
v_mfma_f32_16x16x32_bf16 v[0:3], v[160:163], a[192:195], v[0:3]
ds_read_b64_tr_b16 v[192:193], v208 offset:24576
ds_read_b64_tr_b16 v[194:195], v208 offset:28672
v_mfma_f32_16x16x32_bf16 v[16:19], v[160:163], a[196:199], v[16:19]
buffer_load_dwordx4 a[252:255], v229, s[52:55], s81 offen offset:1024 sc1
v_mfma_f32_16x16x32_bf16 v[4:7], v[164:167], a[192:195], v[4:7]
ds_read_b64_tr_b16 v[196:197], v209 offset:24576
ds_read_b64_tr_b16 v[198:199], v209 offset:28672
v_mfma_f32_16x16x32_bf16 v[20:23], v[164:167], a[196:199], v[20:23]
buffer_load_dwordx4 v233, s[40:43], s17 offen offset:1024 lds
v_mfma_f32_16x16x32_bf16 v[8:11], v[168:171], a[192:195], v[8:11]
ds_read_b64_tr_b16 v[200:201], v210 offset:24576
ds_read_b64_tr_b16 v[202:203], v210 offset:28672
v_mfma_f32_16x16x32_bf16 v[24:27], v[168:171], a[196:199], v[24:27]
v_mfma_f32_16x16x32_bf16 v[12:15], v[172:175], a[192:195], v[12:15]
ds_read_b64_tr_b16 v[204:205], v211 offset:24576
ds_read_b64_tr_b16 v[206:207], v211 offset:28672
v_mfma_f32_16x16x32_bf16 v[28:31], v[172:175], a[196:199], v[28:31]
s_waitcnt lgkmcnt(8)
v_mfma_f32_16x16x32_bf16 v[32:35], v[176:179], a[192:195], v[32:35]
ds_read_b64_tr_b16 v[160:161], v208 offset:26624
ds_read_b64_tr_b16 v[162:163], v208 offset:30720
v_mfma_f32_16x16x32_bf16 v[48:51], v[176:179], a[196:199], v[48:51]
buffer_load_dwordx4 v232, s[44:47], s17 offen offset:2048 lds
v_mfma_f32_16x16x32_bf16 v[36:39], v[180:183], a[192:195], v[36:39]
ds_read_b64_tr_b16 v[164:165], v209 offset:26624
ds_read_b64_tr_b16 v[166:167], v209 offset:30720
v_mfma_f32_16x16x32_bf16 v[52:55], v[180:183], a[196:199], v[52:55]
v_mfma_f32_16x16x32_bf16 v[40:43], v[184:187], a[192:195], v[40:43]
ds_read_b64_tr_b16 v[168:169], v210 offset:26624
ds_read_b64_tr_b16 v[170:171], v210 offset:30720
v_mfma_f32_16x16x32_bf16 v[56:59], v[184:187], a[196:199], v[56:59]
buffer_load_dwordx4 v233, s[44:47], s17 offen offset:3072 lds
v_mfma_f32_16x16x32_bf16 v[44:47], v[188:191], a[192:195], v[44:47]
ds_read_b64_tr_b16 v[172:173], v211 offset:26624
ds_read_b64_tr_b16 v[174:175], v211 offset:30720
v_mfma_f32_16x16x32_bf16 v[60:63], v[188:191], a[196:199], v[60:63]
s_waitcnt vmcnt(15) lgkmcnt(8)
s_barrier
v_mfma_f32_16x16x32_bf16 v[0:3], v[192:195], a[200:203], v[0:3]
ds_read_b64_tr_b16 v[176:177], v212 offset:49152
ds_read_b64_tr_b16 v[178:179], v212 offset:53248
v_mfma_f32_16x16x32_bf16 v[16:19], v[192:195], a[204:207], v[16:19]
v_accvgpr_read_b32 v248, a208
v_accvgpr_read_b32 v249, a212
v_mfma_f32_16x16x32_bf16 v[4:7], v[196:199], a[200:203], v[4:7]
ds_read_b64_tr_b16 v[180:181], v213 offset:49152
ds_read_b64_tr_b16 v[182:183], v213 offset:53248
v_mfma_f32_16x16x32_bf16 v[20:23], v[196:199], a[204:207], v[20:23]
v_accvgpr_read_b32 v250, a216
v_bitop3_b32 v248, v248, v249, v250 bitop3:0x80
v_mfma_f32_16x16x32_bf16 v[8:11], v[200:203], a[200:203], v[8:11]
ds_read_b64_tr_b16 v[184:185], v214 offset:49152
ds_read_b64_tr_b16 v[186:187], v214 offset:53248
v_mfma_f32_16x16x32_bf16 v[24:27], v[200:203], a[204:207], v[24:27]
v_accvgpr_read_b32 v251, a220
v_bitop3_b32 v248, v248, v251, 1 bitop3:0x80
v_mfma_f32_16x16x32_bf16 v[12:15], v[204:207], a[200:203], v[12:15]
ds_read_b64_tr_b16 v[188:189], v215 offset:49152
ds_read_b64_tr_b16 v[190:191], v215 offset:53248
v_mfma_f32_16x16x32_bf16 v[28:31], v[204:207], a[204:207], v[28:31]
v_add_u32 v232, 4291297408, v232
s_waitcnt lgkmcnt(8)
v_mfma_f32_16x16x32_bf16 v[32:35], v[160:163], a[200:203], v[32:35]
ds_read_b64_tr_b16 v[192:193], v212 offset:51200
ds_read_b64_tr_b16 v[194:195], v212 offset:55296
v_mfma_f32_16x16x32_bf16 v[48:51], v[160:163], a[204:207], v[48:51]
v_cmp_ne_u32 vcc, 1, v248
s_nop 0
s_cbranch_vccnz LSt3k13_%=
LRt3k13_%=:
v_mfma_f32_16x16x32_bf16 v[36:39], v[164:167], a[200:203], v[36:39]
ds_read_b64_tr_b16 v[196:197], v213 offset:51200
ds_read_b64_tr_b16 v[198:199], v213 offset:55296
v_add_u32 v233, 4291297408, v233
s_add_u32 m0, s77, 49152
v_mfma_f32_16x16x32_bf16 v[52:55], v[164:167], a[204:207], v[52:55]
buffer_load_dwordx4 v232, s[40:43], s10 offen offset:0 lds
v_mfma_f32_16x16x32_bf16 v[40:43], v[168:171], a[200:203], v[40:43]
ds_read_b64_tr_b16 v[200:201], v214 offset:51200
ds_read_b64_tr_b16 v[202:203], v214 offset:55296
v_mfma_f32_16x16x32_bf16 v[56:59], v[168:171], a[204:207], v[56:59]
buffer_load_dwordx4 v233, s[40:43], s10 offen offset:1024 lds
v_mfma_f32_16x16x32_bf16 v[44:47], v[172:175], a[200:203], v[44:47]
ds_read_b64_tr_b16 v[204:205], v215 offset:51200
ds_read_b64_tr_b16 v[206:207], v215 offset:55296
v_mfma_f32_16x16x32_bf16 v[60:63], v[172:175], a[204:207], v[60:63]
s_waitcnt lgkmcnt(8)
v_mfma_f32_16x16x32_bf16 v[0:3], v[176:179], a[208:211], v[0:3]
ds_read_b64_tr_b16 v[160:161], v212 offset:57344
ds_read_b64_tr_b16 v[162:163], v212 offset:61440
v_mfma_f32_16x16x32_bf16 v[16:19], v[176:179], a[212:215], v[16:19]
buffer_load_dwordx4 v232, s[44:47], s10 offen offset:2048 lds
v_mfma_f32_16x16x32_bf16 v[4:7], v[180:183], a[208:211], v[4:7]
ds_read_b64_tr_b16 v[164:165], v213 offset:57344
ds_read_b64_tr_b16 v[166:167], v213 offset:61440
v_mfma_f32_16x16x32_bf16 v[20:23], v[180:183], a[212:215], v[20:23]
buffer_load_dwordx4 v233, s[44:47], s10 offen offset:3072 lds
v_mfma_f32_16x16x32_bf16 v[8:11], v[184:187], a[208:211], v[8:11]
ds_read_b64_tr_b16 v[168:169], v214 offset:57344
ds_read_b64_tr_b16 v[170:171], v214 offset:61440
s_add_u32 m0, s77, 81920
v_mfma_f32_16x16x32_bf16 v[24:27], v[184:187], a[212:215], v[24:27]
buffer_load_dwordx4 v232, s[40:43], s11 offen offset:0 lds
v_mfma_f32_16x16x32_bf16 v[12:15], v[188:191], a[208:211], v[12:15]
ds_read_b64_tr_b16 v[172:173], v215 offset:57344
ds_read_b64_tr_b16 v[174:175], v215 offset:61440
v_mfma_f32_16x16x32_bf16 v[28:31], v[188:191], a[212:215], v[28:31]
s_waitcnt lgkmcnt(8)
v_mfma_f32_16x16x32_bf16 v[32:35], v[192:195], a[208:211], v[32:35]
ds_read_b64_tr_b16 v[176:177], v212 offset:59392
ds_read_b64_tr_b16 v[178:179], v212 offset:63488
v_mfma_f32_16x16x32_bf16 v[48:51], v[192:195], a[212:215], v[48:51]
buffer_load_dwordx4 v233, s[40:43], s11 offen offset:1024 lds
)"
R"(
v_mfma_f32_16x16x32_bf16 v[36:39], v[196:199], a[208:211], v[36:39]
ds_read_b64_tr_b16 v[180:181], v213 offset:59392
ds_read_b64_tr_b16 v[182:183], v213 offset:63488
v_mfma_f32_16x16x32_bf16 v[52:55], v[196:199], a[212:215], v[52:55]
buffer_load_dwordx4 v232, s[44:47], s11 offen offset:2048 lds
v_mfma_f32_16x16x32_bf16 v[40:43], v[200:203], a[208:211], v[40:43]
ds_read_b64_tr_b16 v[184:185], v214 offset:59392
ds_read_b64_tr_b16 v[186:187], v214 offset:63488
v_mfma_f32_16x16x32_bf16 v[56:59], v[200:203], a[212:215], v[56:59]
buffer_load_dwordx4 v233, s[44:47], s11 offen offset:3072 lds
v_mfma_f32_16x16x32_bf16 v[44:47], v[204:207], a[208:211], v[44:47]
ds_read_b64_tr_b16 v[188:189], v215 offset:59392
ds_read_b64_tr_b16 v[190:191], v215 offset:63488
v_mfma_f32_16x16x32_bf16 v[60:63], v[204:207], a[212:215], v[60:63]
s_waitcnt vmcnt(15) lgkmcnt(8)
s_barrier
v_mfma_f32_16x16x32_bf16 v[0:3], v[160:163], a[216:219], v[0:3]
ds_read_b64_tr_b16 v[192:193], v208 offset:32768
ds_read_b64_tr_b16 v[194:195], v208 offset:36864
v_mfma_f32_16x16x32_bf16 v[16:19], v[160:163], a[220:223], v[16:19]
v_accvgpr_read_b32 v248, a224
v_accvgpr_read_b32 v249, a228
v_mfma_f32_16x16x32_bf16 v[4:7], v[164:167], a[216:219], v[4:7]
ds_read_b64_tr_b16 v[196:197], v209 offset:32768
ds_read_b64_tr_b16 v[198:199], v209 offset:36864
v_mfma_f32_16x16x32_bf16 v[20:23], v[164:167], a[220:223], v[20:23]
v_accvgpr_read_b32 v250, a232
v_bitop3_b32 v248, v248, v249, v250 bitop3:0x80
v_mfma_f32_16x16x32_bf16 v[8:11], v[168:171], a[216:219], v[8:11]
ds_read_b64_tr_b16 v[200:201], v210 offset:32768
ds_read_b64_tr_b16 v[202:203], v210 offset:36864
v_mfma_f32_16x16x32_bf16 v[24:27], v[168:171], a[220:223], v[24:27]
v_accvgpr_read_b32 v251, a236
v_bitop3_b32 v248, v248, v251, 1 bitop3:0x80
v_mfma_f32_16x16x32_bf16 v[12:15], v[172:175], a[216:219], v[12:15]
ds_read_b64_tr_b16 v[204:205], v211 offset:32768
ds_read_b64_tr_b16 v[206:207], v211 offset:36864
v_mfma_f32_16x16x32_bf16 v[28:31], v[172:175], a[220:223], v[28:31]
s_add_u32 m0, s77, 0
s_waitcnt lgkmcnt(8)
v_mfma_f32_16x16x32_bf16 v[32:35], v[176:179], a[216:219], v[32:35]
ds_read_b64_tr_b16 v[160:161], v208 offset:34816
ds_read_b64_tr_b16 v[162:163], v208 offset:38912
v_mfma_f32_16x16x32_bf16 v[48:51], v[176:179], a[220:223], v[48:51]
v_cmp_ne_u32 vcc, 1, v248
s_nop 0
s_cbranch_vccnz LSt3k14_%=
LRt3k14_%=:
v_mfma_f32_16x16x32_bf16 v[36:39], v[180:183], a[216:219], v[36:39]
ds_read_b64_tr_b16 v[164:165], v209 offset:34816
ds_read_b64_tr_b16 v[166:167], v209 offset:38912
v_mfma_f32_16x16x32_bf16 v[52:55], v[180:183], a[220:223], v[52:55]
buffer_load_dwordx4 v232, s[40:43], s12 offen offset:0 lds
v_mfma_f32_16x16x32_bf16 v[40:43], v[184:187], a[216:219], v[40:43]
ds_read_b64_tr_b16 v[168:169], v210 offset:34816
ds_read_b64_tr_b16 v[170:171], v210 offset:38912
v_mfma_f32_16x16x32_bf16 v[56:59], v[184:187], a[220:223], v[56:59]
buffer_load_dwordx4 v233, s[40:43], s12 offen offset:1024 lds
v_mfma_f32_16x16x32_bf16 v[44:47], v[188:191], a[216:219], v[44:47]
ds_read_b64_tr_b16 v[172:173], v211 offset:34816
ds_read_b64_tr_b16 v[174:175], v211 offset:38912
v_mfma_f32_16x16x32_bf16 v[60:63], v[188:191], a[220:223], v[60:63]
s_waitcnt lgkmcnt(8)
v_mfma_f32_16x16x32_bf16 v[0:3], v[192:195], a[224:227], v[0:3]
ds_read_b64_tr_b16 v[176:177], v208 offset:40960
ds_read_b64_tr_b16 v[178:179], v208 offset:45056
v_mfma_f32_16x16x32_bf16 v[16:19], v[192:195], a[228:231], v[16:19]
buffer_load_dwordx4 v232, s[44:47], s12 offen offset:2048 lds
v_mfma_f32_16x16x32_bf16 v[4:7], v[196:199], a[224:227], v[4:7]
ds_read_b64_tr_b16 v[180:181], v209 offset:40960
ds_read_b64_tr_b16 v[182:183], v209 offset:45056
v_mfma_f32_16x16x32_bf16 v[20:23], v[196:199], a[228:231], v[20:23]
buffer_load_dwordx4 v233, s[44:47], s12 offen offset:3072 lds
v_mfma_f32_16x16x32_bf16 v[8:11], v[200:203], a[224:227], v[8:11]
ds_read_b64_tr_b16 v[184:185], v210 offset:40960
ds_read_b64_tr_b16 v[186:187], v210 offset:45056
s_add_u32 m0, s77, 98304
v_mfma_f32_16x16x32_bf16 v[24:27], v[200:203], a[228:231], v[24:27]
buffer_load_dwordx4 v232, s[40:43], s13 offen offset:0 lds
v_mfma_f32_16x16x32_bf16 v[12:15], v[204:207], a[224:227], v[12:15]
ds_read_b64_tr_b16 v[188:189], v211 offset:40960
ds_read_b64_tr_b16 v[190:191], v211 offset:45056
v_mfma_f32_16x16x32_bf16 v[28:31], v[204:207], a[228:231], v[28:31]
s_waitcnt lgkmcnt(8)
v_mfma_f32_16x16x32_bf16 v[32:35], v[160:163], a[224:227], v[32:35]
ds_read_b64_tr_b16 v[192:193], v208 offset:43008
ds_read_b64_tr_b16 v[194:195], v208 offset:47104
v_mfma_f32_16x16x32_bf16 v[48:51], v[160:163], a[228:231], v[48:51]
buffer_load_dwordx4 v233, s[40:43], s13 offen offset:1024 lds
v_mfma_f32_16x16x32_bf16 v[36:39], v[164:167], a[224:227], v[36:39]
ds_read_b64_tr_b16 v[196:197], v209 offset:43008
ds_read_b64_tr_b16 v[198:199], v209 offset:47104
v_mfma_f32_16x16x32_bf16 v[52:55], v[164:167], a[228:231], v[52:55]
buffer_load_dwordx4 v232, s[44:47], s13 offen offset:2048 lds
v_mfma_f32_16x16x32_bf16 v[40:43], v[168:171], a[224:227], v[40:43]
ds_read_b64_tr_b16 v[200:201], v210 offset:43008
ds_read_b64_tr_b16 v[202:203], v210 offset:47104
v_mfma_f32_16x16x32_bf16 v[56:59], v[168:171], a[228:231], v[56:59]
buffer_load_dwordx4 v233, s[44:47], s13 offen offset:3072 lds
v_mfma_f32_16x16x32_bf16 v[44:47], v[172:175], a[224:227], v[44:47]
ds_read_b64_tr_b16 v[204:205], v211 offset:43008
ds_read_b64_tr_b16 v[206:207], v211 offset:47104
v_mfma_f32_16x16x32_bf16 v[60:63], v[172:175], a[228:231], v[60:63]
s_waitcnt vmcnt(16) lgkmcnt(8)
s_barrier
v_mfma_f32_16x16x32_bf16 v[0:3], v[176:179], a[232:235], v[0:3]
ds_read_b64_tr_b16 v[160:161], v212 offset:0
ds_read_b64_tr_b16 v[162:163], v212 offset:4096
v_mfma_f32_16x16x32_bf16 v[16:19], v[176:179], a[236:239], v[16:19]
v_accvgpr_read_b32 v248, a240
v_accvgpr_read_b32 v249, a244
v_mfma_f32_16x16x32_bf16 v[4:7], v[180:183], a[232:235], v[4:7]
ds_read_b64_tr_b16 v[164:165], v213 offset:0
ds_read_b64_tr_b16 v[166:167], v213 offset:4096
v_mfma_f32_16x16x32_bf16 v[20:23], v[180:183], a[236:239], v[20:23]
v_accvgpr_read_b32 v250, a248
v_bitop3_b32 v248, v248, v249, v250 bitop3:0x80
v_mfma_f32_16x16x32_bf16 v[8:11], v[184:187], a[232:235], v[8:11]
ds_read_b64_tr_b16 v[168:169], v214 offset:0
ds_read_b64_tr_b16 v[170:171], v214 offset:4096
v_mfma_f32_16x16x32_bf16 v[24:27], v[184:187], a[236:239], v[24:27]
v_accvgpr_read_b32 v251, a252
v_bitop3_b32 v248, v248, v251, 1 bitop3:0x80
v_mfma_f32_16x16x32_bf16 v[12:15], v[188:191], a[232:235], v[12:15]
ds_read_b64_tr_b16 v[172:173], v215 offset:0
ds_read_b64_tr_b16 v[174:175], v215 offset:4096
v_mfma_f32_16x16x32_bf16 v[28:31], v[188:191], a[236:239], v[28:31]
s_add_u32 m0, s77, 16384
s_waitcnt lgkmcnt(8)
v_mfma_f32_16x16x32_bf16 v[32:35], v[192:195], a[232:235], v[32:35]
ds_read_b64_tr_b16 v[176:177], v212 offset:2048
ds_read_b64_tr_b16 v[178:179], v212 offset:6144
v_mfma_f32_16x16x32_bf16 v[48:51], v[192:195], a[236:239], v[48:51]
v_cmp_ne_u32 vcc, 1, v248
s_nop 0
s_cbranch_vccnz LSt3k15_%=
LRt3k15_%=:
v_mfma_f32_16x16x32_bf16 v[36:39], v[196:199], a[232:235], v[36:39]
ds_read_b64_tr_b16 v[180:181], v213 offset:2048
ds_read_b64_tr_b16 v[182:183], v213 offset:6144
v_mfma_f32_16x16x32_bf16 v[52:55], v[196:199], a[236:239], v[52:55]
buffer_load_dwordx4 v232, s[40:43], s14 offen offset:0 lds
v_mfma_f32_16x16x32_bf16 v[40:43], v[200:203], a[232:235], v[40:43]
ds_read_b64_tr_b16 v[184:185], v214 offset:2048
ds_read_b64_tr_b16 v[186:187], v214 offset:6144
v_mfma_f32_16x16x32_bf16 v[56:59], v[200:203], a[236:239], v[56:59]
buffer_load_dwordx4 v233, s[40:43], s14 offen offset:1024 lds
v_mfma_f32_16x16x32_bf16 v[44:47], v[204:207], a[232:235], v[44:47]
ds_read_b64_tr_b16 v[188:189], v215 offset:2048
ds_read_b64_tr_b16 v[190:191], v215 offset:6144
v_mfma_f32_16x16x32_bf16 v[60:63], v[204:207], a[236:239], v[60:63]
s_waitcnt lgkmcnt(8)
v_mfma_f32_16x16x32_bf16 v[0:3], v[160:163], a[240:243], v[0:3]
ds_read_b64_tr_b16 v[192:193], v212 offset:8192
ds_read_b64_tr_b16 v[194:195], v212 offset:12288
v_mfma_f32_16x16x32_bf16 v[16:19], v[160:163], a[244:247], v[16:19]
buffer_load_dwordx4 v232, s[44:47], s14 offen offset:2048 lds
v_mfma_f32_16x16x32_bf16 v[4:7], v[164:167], a[240:243], v[4:7]
ds_read_b64_tr_b16 v[196:197], v213 offset:8192
ds_read_b64_tr_b16 v[198:199], v213 offset:12288
v_mfma_f32_16x16x32_bf16 v[20:23], v[164:167], a[244:247], v[20:23]
buffer_load_dwordx4 v233, s[44:47], s14 offen offset:3072 lds
v_mfma_f32_16x16x32_bf16 v[8:11], v[168:171], a[240:243], v[8:11]
ds_read_b64_tr_b16 v[200:201], v214 offset:8192
ds_read_b64_tr_b16 v[202:203], v214 offset:12288
s_add_u32 m0, s77, 114688
v_mfma_f32_16x16x32_bf16 v[24:27], v[168:171], a[244:247], v[24:27]
buffer_load_dwordx4 v232, s[40:43], s15 offen offset:0 lds
v_mfma_f32_16x16x32_bf16 v[12:15], v[172:175], a[240:243], v[12:15]
ds_read_b64_tr_b16 v[204:205], v215 offset:8192
ds_read_b64_tr_b16 v[206:207], v215 offset:12288
v_mfma_f32_16x16x32_bf16 v[28:31], v[172:175], a[244:247], v[28:31]
s_waitcnt lgkmcnt(8)
v_mfma_f32_16x16x32_bf16 v[32:35], v[176:179], a[240:243], v[32:35]
ds_read_b64_tr_b16 v[160:161], v212 offset:10240
ds_read_b64_tr_b16 v[162:163], v212 offset:14336
v_mfma_f32_16x16x32_bf16 v[48:51], v[176:179], a[244:247], v[48:51]
buffer_load_dwordx4 v233, s[40:43], s15 offen offset:1024 lds
v_mfma_f32_16x16x32_bf16 v[36:39], v[180:183], a[240:243], v[36:39]
ds_read_b64_tr_b16 v[164:165], v213 offset:10240
ds_read_b64_tr_b16 v[166:167], v213 offset:14336
v_mfma_f32_16x16x32_bf16 v[52:55], v[180:183], a[244:247], v[52:55]
buffer_load_dwordx4 v232, s[44:47], s15 offen offset:2048 lds
v_mfma_f32_16x16x32_bf16 v[40:43], v[184:187], a[240:243], v[40:43]
ds_read_b64_tr_b16 v[168:169], v214 offset:10240
ds_read_b64_tr_b16 v[170:171], v214 offset:14336
v_mfma_f32_16x16x32_bf16 v[56:59], v[184:187], a[244:247], v[56:59]
buffer_load_dwordx4 v233, s[44:47], s15 offen offset:3072 lds
v_mfma_f32_16x16x32_bf16 v[44:47], v[188:191], a[240:243], v[44:47]
ds_read_b64_tr_b16 v[172:173], v215 offset:10240
ds_read_b64_tr_b16 v[174:175], v215 offset:14336
v_mfma_f32_16x16x32_bf16 v[60:63], v[188:191], a[244:247], v[60:63]
s_waitcnt vmcnt(20) lgkmcnt(8)
s_barrier
v_mfma_f32_16x16x32_bf16 v[0:3], v[192:195], a[248:251], v[0:3]
ds_read_b64_tr_b16 v[176:177], v208 offset:49152
ds_read_b64_tr_b16 v[178:179], v208 offset:53248
s_add_u32 m0, s77, 32768
v_mfma_f32_16x16x32_bf16 v[16:19], v[192:195], a[252:255], v[16:19]
buffer_load_dwordx4 v232, s[40:43], s16 offen offset:0 lds
v_mfma_f32_16x16x32_bf16 v[4:7], v[196:199], a[248:251], v[4:7]
ds_read_b64_tr_b16 v[180:181], v209 offset:49152
ds_read_b64_tr_b16 v[182:183], v209 offset:53248
v_mfma_f32_16x16x32_bf16 v[20:23], v[196:199], a[252:255], v[20:23]
v_mfma_f32_16x16x32_bf16 v[8:11], v[200:203], a[248:251], v[8:11]
ds_read_b64_tr_b16 v[184:185], v210 offset:49152
ds_read_b64_tr_b16 v[186:187], v210 offset:53248
v_mfma_f32_16x16x32_bf16 v[24:27], v[200:203], a[252:255], v[24:27]
v_mfma_f32_16x16x32_bf16 v[12:15], v[204:207], a[248:251], v[12:15]
ds_read_b64_tr_b16 v[188:189], v211 offset:49152
ds_read_b64_tr_b16 v[190:191], v211 offset:53248
v_mfma_f32_16x16x32_bf16 v[28:31], v[204:207], a[252:255], v[28:31]
s_waitcnt lgkmcnt(8)
v_mfma_f32_16x16x32_bf16 v[32:35], v[160:163], a[248:251], v[32:35]
ds_read_b64_tr_b16 v[192:193], v208 offset:51200
ds_read_b64_tr_b16 v[194:195], v208 offset:55296
v_mfma_f32_16x16x32_bf16 v[48:51], v[160:163], a[252:255], v[48:51]
buffer_load_dwordx4 v233, s[40:43], s16 offen offset:1024 lds
v_mfma_f32_16x16x32_bf16 v[36:39], v[164:167], a[248:251], v[36:39]
ds_read_b64_tr_b16 v[196:197], v209 offset:51200
ds_read_b64_tr_b16 v[198:199], v209 offset:55296
v_mfma_f32_16x16x32_bf16 v[52:55], v[164:167], a[252:255], v[52:55]
v_mfma_f32_16x16x32_bf16 v[40:43], v[168:171], a[248:251], v[40:43]
ds_read_b64_tr_b16 v[200:201], v210 offset:51200
ds_read_b64_tr_b16 v[202:203], v210 offset:55296
v_mfma_f32_16x16x32_bf16 v[56:59], v[168:171], a[252:255], v[56:59]
buffer_load_dwordx4 v232, s[44:47], s16 offen offset:2048 lds
v_mfma_f32_16x16x32_bf16 v[44:47], v[172:175], a[248:251], v[44:47]
ds_read_b64_tr_b16 v[204:205], v211 offset:51200
ds_read_b64_tr_b16 v[206:207], v211 offset:55296
v_mfma_f32_16x16x32_bf16 v[60:63], v[172:175], a[252:255], v[60:63]
s_waitcnt lgkmcnt(8)
v_mfma_f32_16x16x32_bf16 v[64:67], v[176:179], a[0:3], 0
ds_read_b64_tr_b16 v[160:161], v208 offset:57344
ds_read_b64_tr_b16 v[162:163], v208 offset:61440
v_mfma_f32_16x16x32_bf16 v[80:83], v[176:179], a[4:7], 0
v_mfma_f32_16x16x32_bf16 v[68:71], v[180:183], a[0:3], 0
ds_read_b64_tr_b16 v[164:165], v209 offset:57344
ds_read_b64_tr_b16 v[166:167], v209 offset:61440
v_mfma_f32_16x16x32_bf16 v[84:87], v[180:183], a[4:7], 0
v_mfma_f32_16x16x32_bf16 v[72:75], v[184:187], a[0:3], 0
ds_read_b64_tr_b16 v[168:169], v210 offset:57344
ds_read_b64_tr_b16 v[170:171], v210 offset:61440
v_mfma_f32_16x16x32_bf16 v[88:91], v[184:187], a[4:7], 0
buffer_load_dwordx4 v233, s[44:47], s16 offen offset:3072 lds
LC12_%=:
s_cbranch_execz LC16_%=
v_mfma_f32_16x16x32_bf16 v[76:79], v[188:191], a[0:3], 0
ds_read_b64_tr_b16 v[172:173], v211 offset:57344
ds_read_b64_tr_b16 v[174:175], v211 offset:61440
v_mul_f32 v243, v0, v0
v_mfma_f32_16x16x32_bf16 v[92:95], v[188:191], a[4:7], 0
s_add_u32 m0, s77, 65536
s_waitcnt lgkmcnt(8)
v_mfma_f32_16x16x32_bf16 v[96:99], v[192:195], a[0:3], 0
ds_read_b64_tr_b16 v[176:177], v208 offset:59392
ds_read_b64_tr_b16 v[178:179], v208 offset:63488
v_mfma_f32_16x16x32_bf16 v[112:115], v[192:195], a[4:7], 0
v_fmaak_f32 v243, v243, v223, 0xc0135761
v_mul_f32 v243, v243, v0
v_mfma_f32_16x16x32_bf16 v[100:103], v[196:199], a[0:3], 0
ds_read_b64_tr_b16 v[180:181], v209 offset:59392
ds_read_b64_tr_b16 v[182:183], v209 offset:63488
v_mul_f32 v0, v0, v32
v_mfma_f32_16x16x32_bf16 v[116:119], v[196:199], a[4:7], 0
v_exp_f32 v243, v243
v_mfma_f32_16x16x32_bf16 v[104:107], v[200:203], a[0:3], 0
ds_read_b64_tr_b16 v[184:185], v210 offset:59392
ds_read_b64_tr_b16 v[186:187], v210 offset:63488
v_mfma_f32_16x16x32_bf16 v[120:123], v[200:203], a[4:7], 0
v_add_f32 v243, 1.0, v243
v_mul_f32 v244, v1, v1
v_mfma_f32_16x16x32_bf16 v[108:111], v[204:207], a[0:3], 0
ds_read_b64_tr_b16 v[188:189], v211 offset:59392
ds_read_b64_tr_b16 v[190:191], v211 offset:63488
v_fmaak_f32 v244, v244, v223, 0xc0135761
v_mfma_f32_16x16x32_bf16 v[124:127], v[204:207], a[4:7], 0
s_waitcnt vmcnt(20) lgkmcnt(8)
s_barrier
v_mfma_f32_16x16x32_bf16 v[64:67], v[160:163], a[8:11], v[64:67]
ds_read_b64_tr_b16 v[192:193], v212 offset:16384
ds_read_b64_tr_b16 v[194:195], v212 offset:20480
v_mfma_f32_16x16x32_bf16 v[80:83], v[160:163], a[12:15], v[80:83]
buffer_load_dwordx4 v232, s[40:43], s17 offen offset:0 lds
v_mfma_f32_16x16x32_bf16 v[68:71], v[164:167], a[8:11], v[68:71]
ds_read_b64_tr_b16 v[196:197], v213 offset:16384
ds_read_b64_tr_b16 v[198:199], v213 offset:20480
v_mul_f32 v244, v244, v1
v_mfma_f32_16x16x32_bf16 v[84:87], v[164:167], a[12:15], v[84:87]
v_lshlrev_b32 v132, 3, v231
v_sub_u32 v130, v220, v132
v_mfma_f32_16x16x32_bf16 v[72:75], v[168:171], a[8:11], v[72:75]
ds_read_b64_tr_b16 v[200:201], v214 offset:16384
ds_read_b64_tr_b16 v[202:203], v214 offset:20480
v_mfma_f32_16x16x32_bf16 v[88:91], v[168:171], a[12:15], v[88:91]
v_sub_u32 v131, v221, v132
v_mov_b32 v128, -1
v_mfma_f32_16x16x32_bf16 v[76:79], v[172:175], a[8:11], v[76:79]
ds_read_b64_tr_b16 v[204:205], v215 offset:16384
ds_read_b64_tr_b16 v[206:207], v215 offset:20480
v_mfma_f32_16x16x32_bf16 v[92:95], v[172:175], a[12:15], v[92:95]
s_add_u32 s85, s92, 4096
s_waitcnt lgkmcnt(8)
v_mfma_f32_16x16x32_bf16 v[96:99], v[176:179], a[8:11], v[96:99]
ds_read_b64_tr_b16 v[160:161], v212 offset:18432
ds_read_b64_tr_b16 v[162:163], v212 offset:22528
v_mul_f32 v1, v1, v33
v_mfma_f32_16x16x32_bf16 v[112:115], v[176:179], a[12:15], v[112:115]
buffer_load_dwordx4 v233, s[40:43], s17 offen offset:1024 lds
v_mfma_f32_16x16x32_bf16 v[100:103], v[180:183], a[8:11], v[100:103]
ds_read_b64_tr_b16 v[164:165], v213 offset:18432
ds_read_b64_tr_b16 v[166:167], v213 offset:22528
v_mfma_f32_16x16x32_bf16 v[116:119], v[180:183], a[12:15], v[116:119]
v_rcp_f32 v243, v243
v_mfma_f32_16x16x32_bf16 v[104:107], v[184:187], a[8:11], v[104:107]
ds_read_b64_tr_b16 v[168:169], v214 offset:18432
ds_read_b64_tr_b16 v[170:171], v214 offset:22528
v_mul_f32 v0, v0, v243
v_mfma_f32_16x16x32_bf16 v[120:123], v[184:187], a[12:15], v[120:123]
buffer_load_dwordx4 v232, s[44:47], s17 offen offset:2048 lds
v_mfma_f32_16x16x32_bf16 v[108:111], v[188:191], a[8:11], v[108:111]
ds_read_b64_tr_b16 v[172:173], v215 offset:18432
ds_read_b64_tr_b16 v[174:175], v215 offset:22528
v_mfma_f32_16x16x32_bf16 v[124:127], v[188:191], a[12:15], v[124:127]
v_add_u32 v232, 3670016, v232
s_waitcnt lgkmcnt(8)
v_mfma_f32_16x16x32_bf16 v[64:67], v[192:195], a[16:19], v[64:67]
ds_read_b64_tr_b16 v[176:177], v212 offset:24576
ds_read_b64_tr_b16 v[178:179], v212 offset:28672
v_mul_f32 v245, v2, v2
v_mfma_f32_16x16x32_bf16 v[80:83], v[192:195], a[20:23], v[80:83]
v_exp_f32 v244, v244
v_mfma_f32_16x16x32_bf16 v[68:71], v[196:199], a[16:19], v[68:71]
ds_read_b64_tr_b16 v[180:181], v213 offset:24576
ds_read_b64_tr_b16 v[182:183], v213 offset:28672
v_mfma_f32_16x16x32_bf16 v[84:87], v[196:199], a[20:23], v[84:87]
v_add_f32 v244, 1.0, v244
v_fmaak_f32 v245, v245, v223, 0xc0135761
v_mfma_f32_16x16x32_bf16 v[72:75], v[200:203], a[16:19], v[72:75]
ds_read_b64_tr_b16 v[184:185], v214 offset:24576
ds_read_b64_tr_b16 v[186:187], v214 offset:28672
v_mul_f32 v245, v245, v2
v_mfma_f32_16x16x32_bf16 v[88:91], v[200:203], a[20:23], v[88:91]
buffer_load_dwordx4 v233, s[44:47], s17 offen offset:3072 lds
v_mfma_f32_16x16x32_bf16 v[76:79], v[204:207], a[16:19], v[76:79]
ds_read_b64_tr_b16 v[188:189], v215 offset:24576
ds_read_b64_tr_b16 v[190:191], v215 offset:28672
v_mfma_f32_16x16x32_bf16 v[92:95], v[204:207], a[20:23], v[92:95]
v_add_u32 v233, 3670016, v233
s_waitcnt lgkmcnt(8)
v_mfma_f32_16x16x32_bf16 v[96:99], v[160:163], a[16:19], v[96:99]
ds_read_b64_tr_b16 v[192:193], v212 offset:26624
ds_read_b64_tr_b16 v[194:195], v212 offset:30720
v_mfma_f32_16x16x32_bf16 v[112:115], v[160:163], a[20:23], v[112:115]
v_rcp_f32 v244, v244
v_mfma_f32_16x16x32_bf16 v[100:103], v[164:167], a[16:19], v[100:103]
ds_read_b64_tr_b16 v[196:197], v213 offset:26624
ds_read_b64_tr_b16 v[198:199], v213 offset:30720
v_mul_f32 v1, v1, v244
v_mfma_f32_16x16x32_bf16 v[116:119], v[164:167], a[20:23], v[116:119]
v_exp_f32 v245, v245
v_mfma_f32_16x16x32_bf16 v[104:107], v[168:171], a[16:19], v[104:107]
ds_read_b64_tr_b16 v[200:201], v214 offset:26624
ds_read_b64_tr_b16 v[202:203], v214 offset:30720
v_mfma_f32_16x16x32_bf16 v[120:123], v[168:171], a[20:23], v[120:123]
s_add_u32 m0, s77, 49152
v_cvt_pk_bf16_f32 v32, v0, v1
v_mfma_f32_16x16x32_bf16 v[108:111], v[172:175], a[16:19], v[108:111]
ds_read_b64_tr_b16 v[204:205], v215 offset:26624
ds_read_b64_tr_b16 v[206:207], v215 offset:30720
v_mul_f32 v2, v2, v34
v_mfma_f32_16x16x32_bf16 v[124:127], v[172:175], a[20:23], v[124:127]
s_waitcnt vmcnt(20) lgkmcnt(8)
s_barrier
v_mfma_f32_16x16x32_bf16 v[64:67], v[176:179], a[24:27], v[64:67]
ds_read_b64_tr_b16 v[160:161], v208 offset:0
ds_read_b64_tr_b16 v[162:163], v208 offset:4096
v_mfma_f32_16x16x32_bf16 v[80:83], v[176:179], a[28:31], v[80:83]
buffer_load_dwordx4 v232, s[40:43], s10 offen offset:0 lds
v_mfma_f32_16x16x32_bf16 v[68:71], v[180:183], a[24:27], v[68:71]
ds_read_b64_tr_b16 v[164:165], v209 offset:0
ds_read_b64_tr_b16 v[166:167], v209 offset:4096
v_add_f32 v245, 1.0, v245
v_mfma_f32_16x16x32_bf16 v[84:87], v[180:183], a[28:31], v[84:87]
v_rcp_f32 v245, v245
v_mfma_f32_16x16x32_bf16 v[72:75], v[184:187], a[24:27], v[72:75]
ds_read_b64_tr_b16 v[168:169], v210 offset:0
ds_read_b64_tr_b16 v[170:171], v210 offset:4096
v_mfma_f32_16x16x32_bf16 v[88:91], v[184:187], a[28:31], v[88:91]
v_mul_f32 v2, v2, v245
v_mul_f32 v246, v3, v3
v_mfma_f32_16x16x32_bf16 v[76:79], v[188:191], a[24:27], v[76:79]
ds_read_b64_tr_b16 v[172:173], v211 offset:0
ds_read_b64_tr_b16 v[174:175], v211 offset:4096
v_fmaak_f32 v246, v246, v223, 0xc0135761
v_mfma_f32_16x16x32_bf16 v[92:95], v[188:191], a[28:31], v[92:95]
v_mul_f32 v246, v246, v3
s_waitcnt lgkmcnt(8)
v_mfma_f32_16x16x32_bf16 v[96:99], v[192:195], a[24:27], v[96:99]
ds_read_b64_tr_b16 v[176:177], v208 offset:2048
)"
R"(
ds_read_b64_tr_b16 v[178:179], v208 offset:6144
v_mfma_f32_16x16x32_bf16 v[112:115], v[192:195], a[28:31], v[112:115]
buffer_load_dwordx4 v233, s[40:43], s10 offen offset:1024 lds
v_mfma_f32_16x16x32_bf16 v[100:103], v[196:199], a[24:27], v[100:103]
ds_read_b64_tr_b16 v[180:181], v209 offset:2048
ds_read_b64_tr_b16 v[182:183], v209 offset:6144
v_mfma_f32_16x16x32_bf16 v[116:119], v[196:199], a[28:31], v[116:119]
buffer_store_dwordx2 v[128:129], v130, s[52:55], s85 offen offset:0 sc1
v_mfma_f32_16x16x32_bf16 v[104:107], v[200:203], a[24:27], v[104:107]
ds_read_b64_tr_b16 v[184:185], v210 offset:2048
ds_read_b64_tr_b16 v[186:187], v210 offset:6144
v_mul_f32 v3, v3, v35
v_mfma_f32_16x16x32_bf16 v[120:123], v[200:203], a[28:31], v[120:123]
buffer_load_dwordx4 v232, s[44:47], s10 offen offset:2048 lds
v_mfma_f32_16x16x32_bf16 v[108:111], v[204:207], a[24:27], v[108:111]
ds_read_b64_tr_b16 v[188:189], v211 offset:2048
ds_read_b64_tr_b16 v[190:191], v211 offset:6144
v_mfma_f32_16x16x32_bf16 v[124:127], v[204:207], a[28:31], v[124:127]
v_mul_f32 v247, v4, v4
s_waitcnt lgkmcnt(8)
v_mfma_f32_16x16x32_bf16 v[64:67], v[160:163], a[32:35], v[64:67]
ds_read_b64_tr_b16 v[192:193], v208 offset:8192
ds_read_b64_tr_b16 v[194:195], v208 offset:12288
v_fmaak_f32 v247, v247, v223, 0xc0135761
v_mfma_f32_16x16x32_bf16 v[80:83], v[160:163], a[36:39], v[80:83]
v_exp_f32 v246, v246
v_mfma_f32_16x16x32_bf16 v[68:71], v[164:167], a[32:35], v[68:71]
ds_read_b64_tr_b16 v[196:197], v209 offset:8192
ds_read_b64_tr_b16 v[198:199], v209 offset:12288
v_mfma_f32_16x16x32_bf16 v[84:87], v[164:167], a[36:39], v[84:87]
v_add_f32 v246, 1.0, v246
v_mul_f32 v247, v247, v4
v_mfma_f32_16x16x32_bf16 v[72:75], v[168:171], a[32:35], v[72:75]
ds_read_b64_tr_b16 v[200:201], v210 offset:8192
ds_read_b64_tr_b16 v[202:203], v210 offset:12288
v_mul_f32 v4, v4, v36
v_mfma_f32_16x16x32_bf16 v[88:91], v[168:171], a[36:39], v[88:91]
buffer_load_dwordx4 v233, s[44:47], s10 offen offset:3072 lds
v_mfma_f32_16x16x32_bf16 v[76:79], v[172:175], a[32:35], v[76:79]
ds_read_b64_tr_b16 v[204:205], v211 offset:8192
ds_read_b64_tr_b16 v[206:207], v211 offset:12288
v_mfma_f32_16x16x32_bf16 v[92:95], v[172:175], a[36:39], v[92:95]
s_add_u32 m0, s77, 81920
s_waitcnt lgkmcnt(8)
v_mfma_f32_16x16x32_bf16 v[96:99], v[176:179], a[32:35], v[96:99]
ds_read_b64_tr_b16 v[160:161], v208 offset:10240
ds_read_b64_tr_b16 v[162:163], v208 offset:14336
v_mul_f32 v248, v5, v5
v_mfma_f32_16x16x32_bf16 v[112:115], v[176:179], a[36:39], v[112:115]
v_rcp_f32 v246, v246
v_mfma_f32_16x16x32_bf16 v[100:103], v[180:183], a[32:35], v[100:103]
ds_read_b64_tr_b16 v[164:165], v209 offset:10240
ds_read_b64_tr_b16 v[166:167], v209 offset:14336
v_mfma_f32_16x16x32_bf16 v[116:119], v[180:183], a[36:39], v[116:119]
v_exp_f32 v247, v247
v_mfma_f32_16x16x32_bf16 v[104:107], v[184:187], a[32:35], v[104:107]
ds_read_b64_tr_b16 v[168:169], v210 offset:10240
ds_read_b64_tr_b16 v[170:171], v210 offset:14336
v_mfma_f32_16x16x32_bf16 v[120:123], v[184:187], a[36:39], v[120:123]
v_mul_f32 v3, v3, v246
v_cvt_pk_bf16_f32 v33, v2, v3
v_mfma_f32_16x16x32_bf16 v[108:111], v[188:191], a[32:35], v[108:111]
ds_read_b64_tr_b16 v[172:173], v211 offset:10240
ds_read_b64_tr_b16 v[174:175], v211 offset:14336
v_add_f32 v247, 1.0, v247
v_mfma_f32_16x16x32_bf16 v[124:127], v[188:191], a[36:39], v[124:127]
s_waitcnt vmcnt(21) lgkmcnt(8)
s_barrier
v_mfma_f32_16x16x32_bf16 v[64:67], v[192:195], a[40:43], v[64:67]
ds_read_b64_tr_b16 v[176:177], v212 offset:32768
ds_read_b64_tr_b16 v[178:179], v212 offset:36864
v_mfma_f32_16x16x32_bf16 v[80:83], v[192:195], a[44:47], v[80:83]
buffer_load_dwordx4 v232, s[40:43], s11 offen offset:0 lds
v_mfma_f32_16x16x32_bf16 v[68:71], v[196:199], a[40:43], v[68:71]
ds_read_b64_tr_b16 v[180:181], v213 offset:32768
ds_read_b64_tr_b16 v[182:183], v213 offset:36864
v_fmaak_f32 v248, v248, v223, 0xc0135761
v_mfma_f32_16x16x32_bf16 v[84:87], v[196:199], a[44:47], v[84:87]
v_rcp_f32 v247, v247
v_mfma_f32_16x16x32_bf16 v[72:75], v[200:203], a[40:43], v[72:75]
ds_read_b64_tr_b16 v[184:185], v214 offset:32768
ds_read_b64_tr_b16 v[186:187], v214 offset:36864
v_mfma_f32_16x16x32_bf16 v[88:91], v[200:203], a[44:47], v[88:91]
v_mul_f32 v4, v4, v247
v_mul_f32 v248, v248, v5
v_mfma_f32_16x16x32_bf16 v[76:79], v[204:207], a[40:43], v[76:79]
ds_read_b64_tr_b16 v[188:189], v215 offset:32768
ds_read_b64_tr_b16 v[190:191], v215 offset:36864
v_mul_f32 v5, v5, v37
v_mfma_f32_16x16x32_bf16 v[92:95], v[204:207], a[44:47], v[92:95]
v_mul_f32 v249, v6, v6
s_waitcnt lgkmcnt(8)
v_mfma_f32_16x16x32_bf16 v[96:99], v[160:163], a[40:43], v[96:99]
ds_read_b64_tr_b16 v[192:193], v212 offset:34816
ds_read_b64_tr_b16 v[194:195], v212 offset:38912
v_mfma_f32_16x16x32_bf16 v[112:115], v[160:163], a[44:47], v[112:115]
buffer_load_dwordx4 v233, s[40:43], s11 offen offset:1024 lds
v_mfma_f32_16x16x32_bf16 v[100:103], v[164:167], a[40:43], v[100:103]
ds_read_b64_tr_b16 v[196:197], v213 offset:34816
ds_read_b64_tr_b16 v[198:199], v213 offset:38912
v_fmaak_f32 v249, v249, v223, 0xc0135761
v_mfma_f32_16x16x32_bf16 v[116:119], v[164:167], a[44:47], v[116:119]
buffer_store_dwordx2 v[128:129], v130, s[52:55], s85 offen offset:512 sc1
v_mfma_f32_16x16x32_bf16 v[104:107], v[168:171], a[40:43], v[104:107]
ds_read_b64_tr_b16 v[200:201], v214 offset:34816
ds_read_b64_tr_b16 v[202:203], v214 offset:38912
v_mfma_f32_16x16x32_bf16 v[120:123], v[168:171], a[44:47], v[120:123]
buffer_load_dwordx4 v232, s[44:47], s11 offen offset:2048 lds
v_mfma_f32_16x16x32_bf16 v[108:111], v[172:175], a[40:43], v[108:111]
ds_read_b64_tr_b16 v[204:205], v215 offset:34816
ds_read_b64_tr_b16 v[206:207], v215 offset:38912
v_mfma_f32_16x16x32_bf16 v[124:127], v[172:175], a[44:47], v[124:127]
v_mul_f32 v249, v249, v6
s_waitcnt lgkmcnt(8)
v_mfma_f32_16x16x32_bf16 v[64:67], v[176:179], a[48:51], v[64:67]
ds_read_b64_tr_b16 v[160:161], v212 offset:40960
ds_read_b64_tr_b16 v[162:163], v212 offset:45056
v_mul_f32 v6, v6, v38
v_mfma_f32_16x16x32_bf16 v[80:83], v[176:179], a[52:55], v[80:83]
v_exp_f32 v248, v248
v_mfma_f32_16x16x32_bf16 v[68:71], v[180:183], a[48:51], v[68:71]
ds_read_b64_tr_b16 v[164:165], v213 offset:40960
ds_read_b64_tr_b16 v[166:167], v213 offset:45056
v_mfma_f32_16x16x32_bf16 v[84:87], v[180:183], a[52:55], v[84:87]
v_exp_f32 v249, v249
v_mfma_f32_16x16x32_bf16 v[72:75], v[184:187], a[48:51], v[72:75]
ds_read_b64_tr_b16 v[168:169], v214 offset:40960
ds_read_b64_tr_b16 v[170:171], v214 offset:45056
v_add_f32 v248, 1.0, v248
v_mfma_f32_16x16x32_bf16 v[88:91], v[184:187], a[52:55], v[88:91]
buffer_load_dwordx4 v233, s[44:47], s11 offen offset:3072 lds
v_mfma_f32_16x16x32_bf16 v[76:79], v[188:191], a[48:51], v[76:79]
ds_read_b64_tr_b16 v[172:173], v215 offset:40960
ds_read_b64_tr_b16 v[174:175], v215 offset:45056
v_mfma_f32_16x16x32_bf16 v[92:95], v[188:191], a[52:55], v[92:95]
s_add_u32 m0, s77, 0
s_waitcnt lgkmcnt(8)
v_mfma_f32_16x16x32_bf16 v[96:99], v[192:195], a[48:51], v[96:99]
ds_read_b64_tr_b16 v[176:177], v212 offset:43008
ds_read_b64_tr_b16 v[178:179], v212 offset:47104
v_add_f32 v249, 1.0, v249
v_mfma_f32_16x16x32_bf16 v[112:115], v[192:195], a[52:55], v[112:115]
v_rcp_f32 v248, v248
v_mfma_f32_16x16x32_bf16 v[100:103], v[196:199], a[48:51], v[100:103]
ds_read_b64_tr_b16 v[180:181], v213 offset:43008
ds_read_b64_tr_b16 v[182:183], v213 offset:47104
v_mfma_f32_16x16x32_bf16 v[116:119], v[196:199], a[52:55], v[116:119]
v_rcp_f32 v249, v249
v_mfma_f32_16x16x32_bf16 v[104:107], v[200:203], a[48:51], v[104:107]
ds_read_b64_tr_b16 v[184:185], v214 offset:43008
ds_read_b64_tr_b16 v[186:187], v214 offset:47104
v_mul_f32 v5, v5, v248
v_mfma_f32_16x16x32_bf16 v[120:123], v[200:203], a[52:55], v[120:123]
v_cvt_pk_bf16_f32 v34, v4, v5
v_mul_f32 v6, v6, v249
v_mfma_f32_16x16x32_bf16 v[108:111], v[204:207], a[48:51], v[108:111]
ds_read_b64_tr_b16 v[188:189], v215 offset:43008
ds_read_b64_tr_b16 v[190:191], v215 offset:47104
v_mfma_f32_16x16x32_bf16 v[124:127], v[204:207], a[52:55], v[124:127]
s_waitcnt vmcnt(22) lgkmcnt(8)
s_barrier
v_mfma_f32_16x16x32_bf16 v[64:67], v[160:163], a[56:59], v[64:67]
ds_read_b64_tr_b16 v[192:193], v208 offset:16384
ds_read_b64_tr_b16 v[194:195], v208 offset:20480
v_mfma_f32_16x16x32_bf16 v[80:83], v[160:163], a[60:63], v[80:83]
buffer_load_dwordx4 v232, s[40:43], s12 offen offset:0 lds
v_mfma_f32_16x16x32_bf16 v[68:71], v[164:167], a[56:59], v[68:71]
ds_read_b64_tr_b16 v[196:197], v209 offset:16384
ds_read_b64_tr_b16 v[198:199], v209 offset:20480
v_mul_f32 v250, v7, v7
v_mfma_f32_16x16x32_bf16 v[84:87], v[164:167], a[60:63], v[84:87]
v_fmaak_f32 v250, v250, v223, 0xc0135761
v_mul_f32 v250, v250, v7
v_mfma_f32_16x16x32_bf16 v[72:75], v[168:171], a[56:59], v[72:75]
ds_read_b64_tr_b16 v[200:201], v210 offset:16384
ds_read_b64_tr_b16 v[202:203], v210 offset:20480
v_mfma_f32_16x16x32_bf16 v[88:91], v[168:171], a[60:63], v[88:91]
v_exp_f32 v250, v250
v_mfma_f32_16x16x32_bf16 v[76:79], v[172:175], a[56:59], v[76:79]
ds_read_b64_tr_b16 v[204:205], v211 offset:16384
ds_read_b64_tr_b16 v[206:207], v211 offset:20480
v_mul_f32 v7, v7, v39
v_mfma_f32_16x16x32_bf16 v[92:95], v[172:175], a[60:63], v[92:95]
v_add_f32 v250, 1.0, v250
s_waitcnt lgkmcnt(8)
v_mfma_f32_16x16x32_bf16 v[96:99], v[176:179], a[56:59], v[96:99]
ds_read_b64_tr_b16 v[160:161], v208 offset:18432
ds_read_b64_tr_b16 v[162:163], v208 offset:22528
v_mfma_f32_16x16x32_bf16 v[112:115], v[176:179], a[60:63], v[112:115]
buffer_load_dwordx4 v233, s[40:43], s12 offen offset:1024 lds
v_mfma_f32_16x16x32_bf16 v[100:103], v[180:183], a[56:59], v[100:103]
ds_read_b64_tr_b16 v[164:165], v209 offset:18432
ds_read_b64_tr_b16 v[166:167], v209 offset:22528
v_mul_f32 v251, v8, v8
v_mfma_f32_16x16x32_bf16 v[116:119], v[180:183], a[60:63], v[116:119]
buffer_store_dwordx2 v[128:129], v131, s[52:55], s85 offen offset:0 sc1
v_mfma_f32_16x16x32_bf16 v[104:107], v[184:187], a[56:59], v[104:107]
ds_read_b64_tr_b16 v[168:169], v210 offset:18432
ds_read_b64_tr_b16 v[170:171], v210 offset:22528
v_mfma_f32_16x16x32_bf16 v[120:123], v[184:187], a[60:63], v[120:123]
buffer_load_dwordx4 v232, s[44:47], s12 offen offset:2048 lds
v_mfma_f32_16x16x32_bf16 v[108:111], v[188:191], a[56:59], v[108:111]
ds_read_b64_tr_b16 v[172:173], v211 offset:18432
ds_read_b64_tr_b16 v[174:175], v211 offset:22528
v_fmaak_f32 v251, v251, v223, 0xc0135761
v_mfma_f32_16x16x32_bf16 v[124:127], v[188:191], a[60:63], v[124:127]
v_mul_f32 v251, v251, v8
s_waitcnt lgkmcnt(8)
v_mfma_f32_16x16x32_bf16 v[64:67], v[192:195], a[64:67], v[64:67]
ds_read_b64_tr_b16 v[176:177], v208 offset:24576
ds_read_b64_tr_b16 v[178:179], v208 offset:28672
v_mfma_f32_16x16x32_bf16 v[80:83], v[192:195], a[68:71], v[80:83]
v_rcp_f32 v250, v250
v_mfma_f32_16x16x32_bf16 v[68:71], v[196:199], a[64:67], v[68:71]
ds_read_b64_tr_b16 v[180:181], v209 offset:24576
ds_read_b64_tr_b16 v[182:183], v209 offset:28672
v_mfma_f32_16x16x32_bf16 v[84:87], v[196:199], a[68:71], v[84:87]
v_exp_f32 v251, v251
v_mfma_f32_16x16x32_bf16 v[72:75], v[200:203], a[64:67], v[72:75]
ds_read_b64_tr_b16 v[184:185], v210 offset:24576
ds_read_b64_tr_b16 v[186:187], v210 offset:28672
v_mul_f32 v7, v7, v250
v_mfma_f32_16x16x32_bf16 v[88:91], v[200:203], a[68:71], v[88:91]
buffer_load_dwordx4 v233, s[44:47], s12 offen offset:3072 lds
v_mfma_f32_16x16x32_bf16 v[76:79], v[204:207], a[64:67], v[76:79]
ds_read_b64_tr_b16 v[188:189], v211 offset:24576
ds_read_b64_tr_b16 v[190:191], v211 offset:28672
v_mfma_f32_16x16x32_bf16 v[92:95], v[204:207], a[68:71], v[92:95]
s_add_u32 m0, s77, 98304
s_waitcnt lgkmcnt(8)
v_mfma_f32_16x16x32_bf16 v[96:99], v[160:163], a[64:67], v[96:99]
ds_read_b64_tr_b16 v[192:193], v208 offset:26624
ds_read_b64_tr_b16 v[194:195], v208 offset:30720
v_cvt_pk_bf16_f32 v35, v6, v7
v_mfma_f32_16x16x32_bf16 v[112:115], v[160:163], a[68:71], v[112:115]
buffer_store_dwordx4 v[32:35], v220, s[52:55], s92 offen offset:0 sc1
v_mfma_f32_16x16x32_bf16 v[100:103], v[164:167], a[64:67], v[100:103]
ds_read_b64_tr_b16 v[196:197], v209 offset:26624
ds_read_b64_tr_b16 v[198:199], v209 offset:30720
v_mfma_f32_16x16x32_bf16 v[116:119], v[164:167], a[68:71], v[116:119]
v_mul_f32 v8, v8, v40
v_add_f32 v251, 1.0, v251
v_mfma_f32_16x16x32_bf16 v[104:107], v[168:171], a[64:67], v[104:107]
ds_read_b64_tr_b16 v[200:201], v210 offset:26624
ds_read_b64_tr_b16 v[202:203], v210 offset:30720
v_mul_f32 v252, v9, v9
v_mfma_f32_16x16x32_bf16 v[120:123], v[168:171], a[68:71], v[120:123]
v_rcp_f32 v251, v251
v_mfma_f32_16x16x32_bf16 v[108:111], v[172:175], a[64:67], v[108:111]
ds_read_b64_tr_b16 v[204:205], v211 offset:26624
ds_read_b64_tr_b16 v[206:207], v211 offset:30720
v_mfma_f32_16x16x32_bf16 v[124:127], v[172:175], a[68:71], v[124:127]
s_waitcnt vmcnt(24) lgkmcnt(8)
s_barrier
v_mfma_f32_16x16x32_bf16 v[64:67], v[176:179], a[72:75], v[64:67]
ds_read_b64_tr_b16 v[160:161], v212 offset:49152
ds_read_b64_tr_b16 v[162:163], v212 offset:53248
v_mul_f32 v8, v8, v251
v_mfma_f32_16x16x32_bf16 v[80:83], v[176:179], a[76:79], v[80:83]
buffer_load_dwordx4 v232, s[40:43], s13 offen offset:0 lds
v_mfma_f32_16x16x32_bf16 v[68:71], v[180:183], a[72:75], v[68:71]
ds_read_b64_tr_b16 v[164:165], v213 offset:49152
ds_read_b64_tr_b16 v[166:167], v213 offset:53248
v_mfma_f32_16x16x32_bf16 v[84:87], v[180:183], a[76:79], v[84:87]
v_fmaak_f32 v252, v252, v223, 0xc0135761
v_mul_f32 v252, v252, v9
v_mfma_f32_16x16x32_bf16 v[72:75], v[184:187], a[72:75], v[72:75]
ds_read_b64_tr_b16 v[168:169], v214 offset:49152
ds_read_b64_tr_b16 v[170:171], v214 offset:53248
v_mfma_f32_16x16x32_bf16 v[88:91], v[184:187], a[76:79], v[88:91]
v_exp_f32 v252, v252
v_mfma_f32_16x16x32_bf16 v[76:79], v[188:191], a[72:75], v[76:79]
ds_read_b64_tr_b16 v[172:173], v215 offset:49152
ds_read_b64_tr_b16 v[174:175], v215 offset:53248
v_mul_f32 v9, v9, v41
v_mfma_f32_16x16x32_bf16 v[92:95], v[188:191], a[76:79], v[92:95]
v_add_f32 v252, 1.0, v252
s_waitcnt lgkmcnt(8)
v_mfma_f32_16x16x32_bf16 v[96:99], v[192:195], a[72:75], v[96:99]
ds_read_b64_tr_b16 v[176:177], v212 offset:51200
ds_read_b64_tr_b16 v[178:179], v212 offset:55296
v_mfma_f32_16x16x32_bf16 v[112:115], v[192:195], a[76:79], v[112:115]
buffer_load_dwordx4 v233, s[40:43], s13 offen offset:1024 lds
v_mfma_f32_16x16x32_bf16 v[100:103], v[196:199], a[72:75], v[100:103]
ds_read_b64_tr_b16 v[180:181], v213 offset:51200
ds_read_b64_tr_b16 v[182:183], v213 offset:55296
v_mul_f32 v253, v10, v10
v_mfma_f32_16x16x32_bf16 v[116:119], v[196:199], a[76:79], v[116:119]
buffer_store_dwordx2 v[128:129], v131, s[52:55], s85 offen offset:512 sc1
v_mfma_f32_16x16x32_bf16 v[104:107], v[200:203], a[72:75], v[104:107]
ds_read_b64_tr_b16 v[184:185], v214 offset:51200
ds_read_b64_tr_b16 v[186:187], v214 offset:55296
v_mfma_f32_16x16x32_bf16 v[120:123], v[200:203], a[76:79], v[120:123]
buffer_load_dwordx4 v232, s[44:47], s13 offen offset:2048 lds
v_mfma_f32_16x16x32_bf16 v[108:111], v[204:207], a[72:75], v[108:111]
ds_read_b64_tr_b16 v[188:189], v215 offset:51200
ds_read_b64_tr_b16 v[190:191], v215 offset:55296
v_fmaak_f32 v253, v253, v223, 0xc0135761
v_mfma_f32_16x16x32_bf16 v[124:127], v[204:207], a[76:79], v[124:127]
v_mul_f32 v253, v253, v10
s_waitcnt lgkmcnt(8)
v_mfma_f32_16x16x32_bf16 v[64:67], v[160:163], a[80:83], v[64:67]
ds_read_b64_tr_b16 v[192:193], v212 offset:57344
ds_read_b64_tr_b16 v[194:195], v212 offset:61440
v_mfma_f32_16x16x32_bf16 v[80:83], v[160:163], a[84:87], v[80:83]
v_rcp_f32 v252, v252
v_mfma_f32_16x16x32_bf16 v[68:71], v[164:167], a[80:83], v[68:71]
ds_read_b64_tr_b16 v[196:197], v213 offset:57344
ds_read_b64_tr_b16 v[198:199], v213 offset:61440
v_mul_f32 v9, v9, v252
v_mfma_f32_16x16x32_bf16 v[84:87], v[164:167], a[84:87], v[84:87]
v_exp_f32 v253, v253
v_mfma_f32_16x16x32_bf16 v[72:75], v[168:171], a[80:83], v[72:75]
ds_read_b64_tr_b16 v[200:201], v214 offset:57344
ds_read_b64_tr_b16 v[202:203], v214 offset:61440
v_mfma_f32_16x16x32_bf16 v[88:91], v[168:171], a[84:87], v[88:91]
buffer_load_dwordx4 v233, s[44:47], s13 offen offset:3072 lds
v_mfma_f32_16x16x32_bf16 v[76:79], v[172:175], a[80:83], v[76:79]
ds_read_b64_tr_b16 v[204:205], v215 offset:57344
ds_read_b64_tr_b16 v[206:207], v215 offset:61440
v_mfma_f32_16x16x32_bf16 v[92:95], v[172:175], a[84:87], v[92:95]
s_add_u32 m0, s77, 16384
s_waitcnt lgkmcnt(8)
v_mfma_f32_16x16x32_bf16 v[96:99], v[176:179], a[80:83], v[96:99]
ds_read_b64_tr_b16 v[160:161], v212 offset:59392
ds_read_b64_tr_b16 v[162:163], v212 offset:63488
v_cvt_pk_bf16_f32 v40, v8, v9
v_mfma_f32_16x16x32_bf16 v[112:115], v[176:179], a[84:87], v[112:115]
v_mul_f32 v10, v10, v42
v_add_f32 v253, 1.0, v253
v_mfma_f32_16x16x32_bf16 v[100:103], v[180:183], a[80:83], v[100:103]
ds_read_b64_tr_b16 v[164:165], v213 offset:59392
ds_read_b64_tr_b16 v[166:167], v213 offset:63488
v_mfma_f32_16x16x32_bf16 v[116:119], v[180:183], a[84:87], v[116:119]
v_rcp_f32 v253, v253
v_mfma_f32_16x16x32_bf16 v[104:107], v[184:187], a[80:83], v[104:107]
ds_read_b64_tr_b16 v[168:169], v214 offset:59392
ds_read_b64_tr_b16 v[170:171], v214 offset:63488
v_mul_f32 v10, v10, v253
v_mfma_f32_16x16x32_bf16 v[120:123], v[184:187], a[84:87], v[120:123]
v_mul_f32 v254, v11, v11
v_fmaak_f32 v254, v254, v223, 0xc0135761
v_mfma_f32_16x16x32_bf16 v[108:111], v[188:191], a[80:83], v[108:111]
ds_read_b64_tr_b16 v[172:173], v215 offset:59392
ds_read_b64_tr_b16 v[174:175], v215 offset:63488
v_mfma_f32_16x16x32_bf16 v[124:127], v[188:191], a[84:87], v[124:127]
s_waitcnt vmcnt(25) lgkmcnt(8)
s_barrier
v_mfma_f32_16x16x32_bf16 v[64:67], v[192:195], a[88:91], v[64:67]
ds_read_b64_tr_b16 v[176:177], v208 offset:32768
ds_read_b64_tr_b16 v[178:179], v208 offset:36864
v_mul_f32 v254, v254, v11
v_mfma_f32_16x16x32_bf16 v[80:83], v[192:195], a[92:95], v[80:83]
buffer_load_dwordx4 v232, s[40:43], s14 offen offset:0 lds
v_mfma_f32_16x16x32_bf16 v[68:71], v[196:199], a[88:91], v[68:71]
ds_read_b64_tr_b16 v[180:181], v209 offset:32768
ds_read_b64_tr_b16 v[182:183], v209 offset:36864
v_mfma_f32_16x16x32_bf16 v[84:87], v[196:199], a[92:95], v[84:87]
v_exp_f32 v254, v254
v_mfma_f32_16x16x32_bf16 v[72:75], v[200:203], a[88:91], v[72:75]
ds_read_b64_tr_b16 v[184:185], v210 offset:32768
ds_read_b64_tr_b16 v[186:187], v210 offset:36864
v_mul_f32 v11, v11, v43
v_mfma_f32_16x16x32_bf16 v[88:91], v[200:203], a[92:95], v[88:91]
v_add_f32 v254, 1.0, v254
v_mul_f32 v243, v12, v12
v_mfma_f32_16x16x32_bf16 v[76:79], v[204:207], a[88:91], v[76:79]
ds_read_b64_tr_b16 v[188:189], v211 offset:32768
ds_read_b64_tr_b16 v[190:191], v211 offset:36864
v_mfma_f32_16x16x32_bf16 v[92:95], v[204:207], a[92:95], v[92:95]
v_fmaak_f32 v243, v243, v223, 0xc0135761
s_waitcnt lgkmcnt(8)
v_mfma_f32_16x16x32_bf16 v[96:99], v[160:163], a[88:91], v[96:99]
ds_read_b64_tr_b16 v[192:193], v208 offset:34816
ds_read_b64_tr_b16 v[194:195], v208 offset:38912
v_mfma_f32_16x16x32_bf16 v[112:115], v[160:163], a[92:95], v[112:115]
buffer_load_dwordx4 v233, s[40:43], s14 offen offset:1024 lds
v_mfma_f32_16x16x32_bf16 v[100:103], v[164:167], a[88:91], v[100:103]
ds_read_b64_tr_b16 v[196:197], v209 offset:34816
ds_read_b64_tr_b16 v[198:199], v209 offset:38912
v_mul_f32 v243, v243, v12
v_mfma_f32_16x16x32_bf16 v[116:119], v[164:167], a[92:95], v[116:119]
buffer_store_dwordx2 v[128:129], v130, s[52:55], s85 offen offset:1024 sc1
v_mfma_f32_16x16x32_bf16 v[104:107], v[168:171], a[88:91], v[104:107]
ds_read_b64_tr_b16 v[200:201], v210 offset:34816
ds_read_b64_tr_b16 v[202:203], v210 offset:38912
v_mfma_f32_16x16x32_bf16 v[120:123], v[168:171], a[92:95], v[120:123]
buffer_load_dwordx4 v232, s[44:47], s14 offen offset:2048 lds
v_mfma_f32_16x16x32_bf16 v[108:111], v[172:175], a[88:91], v[108:111]
ds_read_b64_tr_b16 v[204:205], v211 offset:34816
ds_read_b64_tr_b16 v[206:207], v211 offset:38912
v_mul_f32 v12, v12, v44
v_mfma_f32_16x16x32_bf16 v[124:127], v[172:175], a[92:95], v[124:127]
v_mul_f32 v244, v13, v13
s_waitcnt lgkmcnt(8)
v_mfma_f32_16x16x32_bf16 v[64:67], v[176:179], a[96:99], v[64:67]
ds_read_b64_tr_b16 v[160:161], v208 offset:40960
ds_read_b64_tr_b16 v[162:163], v208 offset:45056
v_mfma_f32_16x16x32_bf16 v[80:83], v[176:179], a[100:103], v[80:83]
v_rcp_f32 v254, v254
)"
R"(
v_mfma_f32_16x16x32_bf16 v[68:71], v[180:183], a[96:99], v[68:71]
ds_read_b64_tr_b16 v[164:165], v209 offset:40960
ds_read_b64_tr_b16 v[166:167], v209 offset:45056
v_mul_f32 v11, v11, v254
v_mfma_f32_16x16x32_bf16 v[84:87], v[180:183], a[100:103], v[84:87]
v_exp_f32 v243, v243
v_mfma_f32_16x16x32_bf16 v[72:75], v[184:187], a[96:99], v[72:75]
ds_read_b64_tr_b16 v[168:169], v210 offset:40960
ds_read_b64_tr_b16 v[170:171], v210 offset:45056
v_mfma_f32_16x16x32_bf16 v[88:91], v[184:187], a[100:103], v[88:91]
buffer_load_dwordx4 v233, s[44:47], s14 offen offset:3072 lds
v_mfma_f32_16x16x32_bf16 v[76:79], v[188:191], a[96:99], v[76:79]
ds_read_b64_tr_b16 v[172:173], v211 offset:40960
ds_read_b64_tr_b16 v[174:175], v211 offset:45056
v_cvt_pk_bf16_f32 v41, v10, v11
v_mfma_f32_16x16x32_bf16 v[92:95], v[188:191], a[100:103], v[92:95]
s_add_u32 m0, s77, 114688
s_waitcnt lgkmcnt(8)
v_mfma_f32_16x16x32_bf16 v[96:99], v[192:195], a[96:99], v[96:99]
ds_read_b64_tr_b16 v[176:177], v208 offset:43008
ds_read_b64_tr_b16 v[178:179], v208 offset:47104
LC13_%=:
s_cbranch_execz LC17_%=
v_mfma_f32_16x16x32_bf16 v[112:115], v[192:195], a[100:103], v[112:115]
v_add_f32 v243, 1.0, v243
v_fmaak_f32 v244, v244, v223, 0xc0135761
v_mfma_f32_16x16x32_bf16 v[100:103], v[196:199], a[96:99], v[100:103]
ds_read_b64_tr_b16 v[180:181], v209 offset:43008
ds_read_b64_tr_b16 v[182:183], v209 offset:47104
v_mfma_f32_16x16x32_bf16 v[116:119], v[196:199], a[100:103], v[116:119]
v_rcp_f32 v243, v243
v_mfma_f32_16x16x32_bf16 v[104:107], v[200:203], a[96:99], v[104:107]
ds_read_b64_tr_b16 v[184:185], v210 offset:43008
ds_read_b64_tr_b16 v[186:187], v210 offset:47104
v_mul_f32 v12, v12, v243
v_mfma_f32_16x16x32_bf16 v[120:123], v[200:203], a[100:103], v[120:123]
v_mul_f32 v244, v244, v13
v_mul_f32 v13, v13, v45
v_mfma_f32_16x16x32_bf16 v[108:111], v[204:207], a[96:99], v[108:111]
ds_read_b64_tr_b16 v[188:189], v211 offset:43008
ds_read_b64_tr_b16 v[190:191], v211 offset:47104
v_mfma_f32_16x16x32_bf16 v[124:127], v[204:207], a[100:103], v[124:127]
s_waitcnt vmcnt(26) lgkmcnt(8)
s_barrier
v_mfma_f32_16x16x32_bf16 v[64:67], v[160:163], a[104:107], v[64:67]
ds_read_b64_tr_b16 v[192:193], v212 offset:0
ds_read_b64_tr_b16 v[194:195], v212 offset:4096
v_mul_f32 v245, v14, v14
v_mfma_f32_16x16x32_bf16 v[80:83], v[160:163], a[108:111], v[80:83]
buffer_load_dwordx4 v232, s[40:43], s15 offen offset:0 lds
v_mfma_f32_16x16x32_bf16 v[68:71], v[164:167], a[104:107], v[68:71]
ds_read_b64_tr_b16 v[196:197], v213 offset:0
ds_read_b64_tr_b16 v[198:199], v213 offset:4096
v_mfma_f32_16x16x32_bf16 v[84:87], v[164:167], a[108:111], v[84:87]
v_exp_f32 v244, v244
v_mfma_f32_16x16x32_bf16 v[72:75], v[168:171], a[104:107], v[72:75]
ds_read_b64_tr_b16 v[200:201], v214 offset:0
ds_read_b64_tr_b16 v[202:203], v214 offset:4096
v_add_f32 v244, 1.0, v244
v_mfma_f32_16x16x32_bf16 v[88:91], v[168:171], a[108:111], v[88:91]
v_rcp_f32 v244, v244
v_mfma_f32_16x16x32_bf16 v[76:79], v[172:175], a[104:107], v[76:79]
ds_read_b64_tr_b16 v[204:205], v215 offset:0
ds_read_b64_tr_b16 v[206:207], v215 offset:4096
v_mfma_f32_16x16x32_bf16 v[92:95], v[172:175], a[108:111], v[92:95]
v_mul_f32 v13, v13, v244
s_waitcnt lgkmcnt(8)
v_mfma_f32_16x16x32_bf16 v[96:99], v[176:179], a[104:107], v[96:99]
ds_read_b64_tr_b16 v[160:161], v212 offset:2048
ds_read_b64_tr_b16 v[162:163], v212 offset:6144
v_cvt_pk_bf16_f32 v42, v12, v13
v_mfma_f32_16x16x32_bf16 v[112:115], v[176:179], a[108:111], v[112:115]
buffer_load_dwordx4 v233, s[40:43], s15 offen offset:1024 lds
v_mfma_f32_16x16x32_bf16 v[100:103], v[180:183], a[104:107], v[100:103]
ds_read_b64_tr_b16 v[164:165], v213 offset:2048
ds_read_b64_tr_b16 v[166:167], v213 offset:6144
v_mfma_f32_16x16x32_bf16 v[116:119], v[180:183], a[108:111], v[116:119]
buffer_store_dwordx2 v[128:129], v130, s[52:55], s85 offen offset:1536 sc1
v_mfma_f32_16x16x32_bf16 v[104:107], v[184:187], a[104:107], v[104:107]
ds_read_b64_tr_b16 v[168:169], v214 offset:2048
ds_read_b64_tr_b16 v[170:171], v214 offset:6144
v_mfma_f32_16x16x32_bf16 v[120:123], v[184:187], a[108:111], v[120:123]
buffer_load_dwordx4 v232, s[44:47], s15 offen offset:2048 lds
v_mfma_f32_16x16x32_bf16 v[108:111], v[188:191], a[104:107], v[108:111]
ds_read_b64_tr_b16 v[172:173], v215 offset:2048
ds_read_b64_tr_b16 v[174:175], v215 offset:6144
v_fmaak_f32 v245, v245, v223, 0xc0135761
v_mfma_f32_16x16x32_bf16 v[124:127], v[188:191], a[108:111], v[124:127]
v_mul_f32 v245, v245, v14
s_waitcnt lgkmcnt(8)
v_mfma_f32_16x16x32_bf16 v[64:67], v[192:195], a[112:115], v[64:67]
ds_read_b64_tr_b16 v[176:177], v212 offset:8192
ds_read_b64_tr_b16 v[178:179], v212 offset:12288
v_mfma_f32_16x16x32_bf16 v[80:83], v[192:195], a[116:119], v[80:83]
v_exp_f32 v245, v245
v_mfma_f32_16x16x32_bf16 v[68:71], v[196:199], a[112:115], v[68:71]
ds_read_b64_tr_b16 v[180:181], v213 offset:8192
ds_read_b64_tr_b16 v[182:183], v213 offset:12288
v_mul_f32 v14, v14, v46
v_mfma_f32_16x16x32_bf16 v[84:87], v[196:199], a[116:119], v[84:87]
v_add_f32 v245, 1.0, v245
v_mul_f32 v246, v15, v15
v_mfma_f32_16x16x32_bf16 v[72:75], v[200:203], a[112:115], v[72:75]
ds_read_b64_tr_b16 v[184:185], v214 offset:8192
ds_read_b64_tr_b16 v[186:187], v214 offset:12288
v_mfma_f32_16x16x32_bf16 v[88:91], v[200:203], a[116:119], v[88:91]
buffer_load_dwordx4 v233, s[44:47], s15 offen offset:3072 lds
v_mfma_f32_16x16x32_bf16 v[76:79], v[204:207], a[112:115], v[76:79]
ds_read_b64_tr_b16 v[188:189], v215 offset:8192
ds_read_b64_tr_b16 v[190:191], v215 offset:12288
v_fmaak_f32 v246, v246, v223, 0xc0135761
v_mfma_f32_16x16x32_bf16 v[92:95], v[204:207], a[116:119], v[92:95]
s_add_u32 m0, s77, 32768
s_waitcnt lgkmcnt(8)
v_mfma_f32_16x16x32_bf16 v[96:99], v[160:163], a[112:115], v[96:99]
ds_read_b64_tr_b16 v[192:193], v212 offset:10240
ds_read_b64_tr_b16 v[194:195], v212 offset:14336
v_mfma_f32_16x16x32_bf16 v[112:115], v[160:163], a[116:119], v[112:115]
v_rcp_f32 v245, v245
v_mfma_f32_16x16x32_bf16 v[100:103], v[164:167], a[112:115], v[100:103]
ds_read_b64_tr_b16 v[196:197], v213 offset:10240
ds_read_b64_tr_b16 v[198:199], v213 offset:14336
v_mul_f32 v14, v14, v245
v_mfma_f32_16x16x32_bf16 v[116:119], v[164:167], a[116:119], v[116:119]
v_mul_f32 v246, v246, v15
v_mul_f32 v15, v15, v47
v_mfma_f32_16x16x32_bf16 v[104:107], v[168:171], a[112:115], v[104:107]
ds_read_b64_tr_b16 v[200:201], v214 offset:10240
ds_read_b64_tr_b16 v[202:203], v214 offset:14336
v_mfma_f32_16x16x32_bf16 v[120:123], v[168:171], a[116:119], v[120:123]
v_exp_f32 v246, v246
v_mfma_f32_16x16x32_bf16 v[108:111], v[172:175], a[112:115], v[108:111]
ds_read_b64_tr_b16 v[204:205], v215 offset:10240
ds_read_b64_tr_b16 v[206:207], v215 offset:14336
v_mfma_f32_16x16x32_bf16 v[124:127], v[172:175], a[116:119], v[124:127]
s_waitcnt vmcnt(26) lgkmcnt(8)
s_barrier
v_mfma_f32_16x16x32_bf16 v[64:67], v[176:179], a[120:123], v[64:67]
ds_read_b64_tr_b16 v[160:161], v208 offset:49152
ds_read_b64_tr_b16 v[162:163], v208 offset:53248
v_add_f32 v246, 1.0, v246
v_mfma_f32_16x16x32_bf16 v[80:83], v[176:179], a[124:127], v[80:83]
buffer_load_dwordx4 v232, s[40:43], s16 offen offset:0 lds
v_mfma_f32_16x16x32_bf16 v[68:71], v[180:183], a[120:123], v[68:71]
ds_read_b64_tr_b16 v[164:165], v209 offset:49152
ds_read_b64_tr_b16 v[166:167], v209 offset:53248
v_mfma_f32_16x16x32_bf16 v[84:87], v[180:183], a[124:127], v[84:87]
v_rcp_f32 v246, v246
v_mfma_f32_16x16x32_bf16 v[72:75], v[184:187], a[120:123], v[72:75]
ds_read_b64_tr_b16 v[168:169], v210 offset:49152
ds_read_b64_tr_b16 v[170:171], v210 offset:53248
v_mul_f32 v15, v15, v246
v_mfma_f32_16x16x32_bf16 v[88:91], v[184:187], a[124:127], v[88:91]
v_cvt_pk_bf16_f32 v43, v14, v15
v_mul_f32 v247, v16, v16
v_mfma_f32_16x16x32_bf16 v[76:79], v[188:191], a[120:123], v[76:79]
ds_read_b64_tr_b16 v[172:173], v211 offset:49152
ds_read_b64_tr_b16 v[174:175], v211 offset:53248
v_mfma_f32_16x16x32_bf16 v[92:95], v[188:191], a[124:127], v[92:95]
v_fmaak_f32 v247, v247, v223, 0xc0135761
s_waitcnt lgkmcnt(8)
v_mfma_f32_16x16x32_bf16 v[96:99], v[192:195], a[120:123], v[96:99]
ds_read_b64_tr_b16 v[176:177], v208 offset:51200
ds_read_b64_tr_b16 v[178:179], v208 offset:55296
v_mul_f32 v247, v247, v16
v_mfma_f32_16x16x32_bf16 v[112:115], v[192:195], a[124:127], v[112:115]
buffer_load_dwordx4 v233, s[40:43], s16 offen offset:1024 lds
v_mfma_f32_16x16x32_bf16 v[100:103], v[196:199], a[120:123], v[100:103]
ds_read_b64_tr_b16 v[180:181], v209 offset:51200
ds_read_b64_tr_b16 v[182:183], v209 offset:55296
v_mfma_f32_16x16x32_bf16 v[116:119], v[196:199], a[124:127], v[116:119]
buffer_store_dwordx2 v[128:129], v131, s[52:55], s85 offen offset:1024 sc1
v_mfma_f32_16x16x32_bf16 v[104:107], v[200:203], a[120:123], v[104:107]
ds_read_b64_tr_b16 v[184:185], v210 offset:51200
ds_read_b64_tr_b16 v[186:187], v210 offset:55296
v_mul_f32 v16, v16, v48
v_mfma_f32_16x16x32_bf16 v[120:123], v[200:203], a[124:127], v[120:123]
buffer_load_dwordx4 v232, s[44:47], s16 offen offset:2048 lds
v_mfma_f32_16x16x32_bf16 v[108:111], v[204:207], a[120:123], v[108:111]
ds_read_b64_tr_b16 v[188:189], v211 offset:51200
ds_read_b64_tr_b16 v[190:191], v211 offset:55296
v_mfma_f32_16x16x32_bf16 v[124:127], v[204:207], a[124:127], v[124:127]
v_mul_f32 v248, v17, v17
s_waitcnt lgkmcnt(8)
v_mfma_f32_16x16x32_bf16 v[64:67], v[160:163], a[128:131], v[64:67]
ds_read_b64_tr_b16 v[192:193], v208 offset:57344
ds_read_b64_tr_b16 v[194:195], v208 offset:61440
v_mfma_f32_16x16x32_bf16 v[80:83], v[160:163], a[132:135], v[80:83]
buffer_store_dwordx4 v[40:43], v220, s[52:55], s92 offen offset:1024 sc1
v_mfma_f32_16x16x32_bf16 v[68:71], v[164:167], a[128:131], v[68:71]
ds_read_b64_tr_b16 v[196:197], v209 offset:57344
ds_read_b64_tr_b16 v[198:199], v209 offset:61440
v_fmaak_f32 v248, v248, v223, 0xc0135761
v_mfma_f32_16x16x32_bf16 v[84:87], v[164:167], a[132:135], v[84:87]
v_exp_f32 v247, v247
v_mfma_f32_16x16x32_bf16 v[72:75], v[168:171], a[128:131], v[72:75]
ds_read_b64_tr_b16 v[200:201], v210 offset:57344
ds_read_b64_tr_b16 v[202:203], v210 offset:61440
v_mfma_f32_16x16x32_bf16 v[88:91], v[168:171], a[132:135], v[88:91]
buffer_load_dwordx4 v233, s[44:47], s16 offen offset:3072 lds
v_mfma_f32_16x16x32_bf16 v[76:79], v[172:175], a[128:131], v[76:79]
ds_read_b64_tr_b16 v[204:205], v211 offset:57344
ds_read_b64_tr_b16 v[206:207], v211 offset:61440
v_add_f32 v247, 1.0, v247
v_mfma_f32_16x16x32_bf16 v[92:95], v[172:175], a[132:135], v[92:95]
s_add_u32 m0, s77, 65536
s_waitcnt lgkmcnt(8)
v_mfma_f32_16x16x32_bf16 v[96:99], v[176:179], a[128:131], v[96:99]
ds_read_b64_tr_b16 v[160:161], v208 offset:59392
ds_read_b64_tr_b16 v[162:163], v208 offset:63488
v_mfma_f32_16x16x32_bf16 v[112:115], v[176:179], a[132:135], v[112:115]
v_rcp_f32 v247, v247
v_mfma_f32_16x16x32_bf16 v[100:103], v[180:183], a[128:131], v[100:103]
ds_read_b64_tr_b16 v[164:165], v209 offset:59392
ds_read_b64_tr_b16 v[166:167], v209 offset:63488
v_mul_f32 v16, v16, v247
v_mfma_f32_16x16x32_bf16 v[116:119], v[180:183], a[132:135], v[116:119]
v_mul_f32 v248, v248, v17
v_mul_f32 v17, v17, v49
v_mfma_f32_16x16x32_bf16 v[104:107], v[184:187], a[128:131], v[104:107]
ds_read_b64_tr_b16 v[168:169], v210 offset:59392
ds_read_b64_tr_b16 v[170:171], v210 offset:63488
v_mfma_f32_16x16x32_bf16 v[120:123], v[184:187], a[132:135], v[120:123]
v_exp_f32 v248, v248
v_mfma_f32_16x16x32_bf16 v[108:111], v[188:191], a[128:131], v[108:111]
ds_read_b64_tr_b16 v[172:173], v211 offset:59392
ds_read_b64_tr_b16 v[174:175], v211 offset:63488
v_add_f32 v248, 1.0, v248
v_mfma_f32_16x16x32_bf16 v[124:127], v[188:191], a[132:135], v[124:127]
s_waitcnt vmcnt(27) lgkmcnt(8)
s_barrier
v_mfma_f32_16x16x32_bf16 v[64:67], v[192:195], a[136:139], v[64:67]
ds_read_b64_tr_b16 v[176:177], v212 offset:16384
ds_read_b64_tr_b16 v[178:179], v212 offset:20480
v_mfma_f32_16x16x32_bf16 v[80:83], v[192:195], a[140:143], v[80:83]
buffer_load_dwordx4 v232, s[40:43], s17 offen offset:0 lds
v_mfma_f32_16x16x32_bf16 v[68:71], v[196:199], a[136:139], v[68:71]
ds_read_b64_tr_b16 v[180:181], v213 offset:16384
ds_read_b64_tr_b16 v[182:183], v213 offset:20480
v_mul_f32 v249, v18, v18
v_mfma_f32_16x16x32_bf16 v[84:87], v[196:199], a[140:143], v[84:87]
v_rcp_f32 v248, v248
v_mfma_f32_16x16x32_bf16 v[72:75], v[200:203], a[136:139], v[72:75]
ds_read_b64_tr_b16 v[184:185], v214 offset:16384
ds_read_b64_tr_b16 v[186:187], v214 offset:20480
v_mfma_f32_16x16x32_bf16 v[88:91], v[200:203], a[140:143], v[88:91]
v_mul_f32 v17, v17, v248
v_cvt_pk_bf16_f32 v48, v16, v17
v_mfma_f32_16x16x32_bf16 v[76:79], v[204:207], a[136:139], v[76:79]
ds_read_b64_tr_b16 v[188:189], v215 offset:16384
ds_read_b64_tr_b16 v[190:191], v215 offset:20480
v_mfma_f32_16x16x32_bf16 v[92:95], v[204:207], a[140:143], v[92:95]
v_fmaak_f32 v249, v249, v223, 0xc0135761
s_waitcnt lgkmcnt(8)
v_mfma_f32_16x16x32_bf16 v[96:99], v[160:163], a[136:139], v[96:99]
ds_read_b64_tr_b16 v[192:193], v212 offset:18432
ds_read_b64_tr_b16 v[194:195], v212 offset:22528
v_mul_f32 v249, v249, v18
v_mfma_f32_16x16x32_bf16 v[112:115], v[160:163], a[140:143], v[112:115]
buffer_load_dwordx4 v233, s[40:43], s17 offen offset:1024 lds
v_mfma_f32_16x16x32_bf16 v[100:103], v[164:167], a[136:139], v[100:103]
ds_read_b64_tr_b16 v[196:197], v213 offset:18432
ds_read_b64_tr_b16 v[198:199], v213 offset:22528
v_mfma_f32_16x16x32_bf16 v[116:119], v[164:167], a[140:143], v[116:119]
buffer_store_dwordx2 v[128:129], v131, s[52:55], s85 offen offset:1536 sc1
v_mfma_f32_16x16x32_bf16 v[104:107], v[168:171], a[136:139], v[104:107]
ds_read_b64_tr_b16 v[200:201], v214 offset:18432
ds_read_b64_tr_b16 v[202:203], v214 offset:22528
v_mul_f32 v18, v18, v50
v_mfma_f32_16x16x32_bf16 v[120:123], v[168:171], a[140:143], v[120:123]
buffer_load_dwordx4 v232, s[44:47], s17 offen offset:2048 lds
v_mfma_f32_16x16x32_bf16 v[108:111], v[172:175], a[136:139], v[108:111]
ds_read_b64_tr_b16 v[204:205], v215 offset:18432
ds_read_b64_tr_b16 v[206:207], v215 offset:22528
v_mfma_f32_16x16x32_bf16 v[124:127], v[172:175], a[140:143], v[124:127]
v_add_u32 v232, 4291297408, v232
s_waitcnt lgkmcnt(8)
v_mfma_f32_16x16x32_bf16 v[64:67], v[176:179], a[144:147], v[64:67]
ds_read_b64_tr_b16 v[160:161], v212 offset:24576
ds_read_b64_tr_b16 v[162:163], v212 offset:28672
v_mul_f32 v250, v19, v19
v_mfma_f32_16x16x32_bf16 v[80:83], v[176:179], a[148:151], v[80:83]
v_exp_f32 v249, v249
v_mfma_f32_16x16x32_bf16 v[68:71], v[180:183], a[144:147], v[68:71]
ds_read_b64_tr_b16 v[164:165], v213 offset:24576
ds_read_b64_tr_b16 v[166:167], v213 offset:28672
v_mfma_f32_16x16x32_bf16 v[84:87], v[180:183], a[148:151], v[84:87]
v_add_f32 v249, 1.0, v249
v_fmaak_f32 v250, v250, v223, 0xc0135761
v_mfma_f32_16x16x32_bf16 v[72:75], v[184:187], a[144:147], v[72:75]
ds_read_b64_tr_b16 v[168:169], v214 offset:24576
ds_read_b64_tr_b16 v[170:171], v214 offset:28672
v_mul_f32 v250, v250, v19
v_mfma_f32_16x16x32_bf16 v[88:91], v[184:187], a[148:151], v[88:91]
buffer_load_dwordx4 v233, s[44:47], s17 offen offset:3072 lds
v_mfma_f32_16x16x32_bf16 v[76:79], v[188:191], a[144:147], v[76:79]
ds_read_b64_tr_b16 v[172:173], v215 offset:24576
ds_read_b64_tr_b16 v[174:175], v215 offset:28672
v_mfma_f32_16x16x32_bf16 v[92:95], v[188:191], a[148:151], v[92:95]
v_add_u32 v233, 4291297408, v233
s_waitcnt lgkmcnt(8)
v_mfma_f32_16x16x32_bf16 v[96:99], v[192:195], a[144:147], v[96:99]
ds_read_b64_tr_b16 v[176:177], v212 offset:26624
ds_read_b64_tr_b16 v[178:179], v212 offset:30720
v_mfma_f32_16x16x32_bf16 v[112:115], v[192:195], a[148:151], v[112:115]
v_rcp_f32 v249, v249
v_mfma_f32_16x16x32_bf16 v[100:103], v[196:199], a[144:147], v[100:103]
ds_read_b64_tr_b16 v[180:181], v213 offset:26624
ds_read_b64_tr_b16 v[182:183], v213 offset:30720
v_mul_f32 v18, v18, v249
v_mfma_f32_16x16x32_bf16 v[116:119], v[196:199], a[148:151], v[116:119]
v_exp_f32 v250, v250
v_mfma_f32_16x16x32_bf16 v[104:107], v[200:203], a[144:147], v[104:107]
ds_read_b64_tr_b16 v[184:185], v214 offset:26624
ds_read_b64_tr_b16 v[186:187], v214 offset:30720
v_mfma_f32_16x16x32_bf16 v[120:123], v[200:203], a[148:151], v[120:123]
s_add_u32 m0, s77, 49152
v_mul_f32 v19, v19, v51
v_mfma_f32_16x16x32_bf16 v[108:111], v[204:207], a[144:147], v[108:111]
ds_read_b64_tr_b16 v[188:189], v215 offset:26624
ds_read_b64_tr_b16 v[190:191], v215 offset:30720
v_add_f32 v250, 1.0, v250
v_mfma_f32_16x16x32_bf16 v[124:127], v[204:207], a[148:151], v[124:127]
s_waitcnt vmcnt(27) lgkmcnt(8)
s_barrier
v_mfma_f32_16x16x32_bf16 v[64:67], v[160:163], a[152:155], v[64:67]
ds_read_b64_tr_b16 v[192:193], v208 offset:0
ds_read_b64_tr_b16 v[194:195], v208 offset:4096
v_mfma_f32_16x16x32_bf16 v[80:83], v[160:163], a[156:159], v[80:83]
buffer_load_dwordx4 v232, s[40:43], s10 offen offset:0 lds
v_mfma_f32_16x16x32_bf16 v[68:71], v[164:167], a[152:155], v[68:71]
ds_read_b64_tr_b16 v[196:197], v209 offset:0
ds_read_b64_tr_b16 v[198:199], v209 offset:4096
v_mul_f32 v251, v20, v20
v_mfma_f32_16x16x32_bf16 v[84:87], v[164:167], a[156:159], v[84:87]
v_rcp_f32 v250, v250
v_mfma_f32_16x16x32_bf16 v[72:75], v[168:171], a[152:155], v[72:75]
ds_read_b64_tr_b16 v[200:201], v210 offset:0
ds_read_b64_tr_b16 v[202:203], v210 offset:4096
v_mfma_f32_16x16x32_bf16 v[88:91], v[168:171], a[156:159], v[88:91]
v_mul_f32 v19, v19, v250
v_cvt_pk_bf16_f32 v49, v18, v19
v_mfma_f32_16x16x32_bf16 v[76:79], v[172:175], a[152:155], v[76:79]
ds_read_b64_tr_b16 v[204:205], v211 offset:0
ds_read_b64_tr_b16 v[206:207], v211 offset:4096
v_fmaak_f32 v251, v251, v223, 0xc0135761
v_mfma_f32_16x16x32_bf16 v[92:95], v[172:175], a[156:159], v[92:95]
v_mul_f32 v251, v251, v20
s_waitcnt lgkmcnt(8)
v_mfma_f32_16x16x32_bf16 v[96:99], v[176:179], a[152:155], v[96:99]
ds_read_b64_tr_b16 v[160:161], v208 offset:2048
ds_read_b64_tr_b16 v[162:163], v208 offset:6144
v_mfma_f32_16x16x32_bf16 v[112:115], v[176:179], a[156:159], v[112:115]
buffer_load_dwordx4 v233, s[40:43], s10 offen offset:1024 lds
v_mfma_f32_16x16x32_bf16 v[100:103], v[180:183], a[152:155], v[100:103]
ds_read_b64_tr_b16 v[164:165], v209 offset:2048
ds_read_b64_tr_b16 v[166:167], v209 offset:6144
v_mfma_f32_16x16x32_bf16 v[116:119], v[180:183], a[156:159], v[116:119]
buffer_store_dwordx2 v[128:129], v130, s[52:55], s90 offen offset:0 sc1
v_mfma_f32_16x16x32_bf16 v[104:107], v[184:187], a[152:155], v[104:107]
ds_read_b64_tr_b16 v[168:169], v210 offset:2048
ds_read_b64_tr_b16 v[170:171], v210 offset:6144
v_mul_f32 v20, v20, v52
v_mfma_f32_16x16x32_bf16 v[120:123], v[184:187], a[156:159], v[120:123]
buffer_load_dwordx4 v232, s[44:47], s10 offen offset:2048 lds
v_mfma_f32_16x16x32_bf16 v[108:111], v[188:191], a[152:155], v[108:111]
ds_read_b64_tr_b16 v[172:173], v211 offset:2048
ds_read_b64_tr_b16 v[174:175], v211 offset:6144
v_mfma_f32_16x16x32_bf16 v[124:127], v[188:191], a[156:159], v[124:127]
v_mul_f32 v252, v21, v21
s_waitcnt lgkmcnt(8)
v_mfma_f32_16x16x32_bf16 v[64:67], v[192:195], a[160:163], v[64:67]
ds_read_b64_tr_b16 v[176:177], v208 offset:8192
ds_read_b64_tr_b16 v[178:179], v208 offset:12288
v_fmaak_f32 v252, v252, v223, 0xc0135761
v_mfma_f32_16x16x32_bf16 v[80:83], v[192:195], a[164:167], v[80:83]
v_exp_f32 v251, v251
v_mfma_f32_16x16x32_bf16 v[68:71], v[196:199], a[160:163], v[68:71]
ds_read_b64_tr_b16 v[180:181], v209 offset:8192
ds_read_b64_tr_b16 v[182:183], v209 offset:12288
v_mfma_f32_16x16x32_bf16 v[84:87], v[196:199], a[164:167], v[84:87]
v_add_f32 v251, 1.0, v251
v_mul_f32 v252, v252, v21
v_mfma_f32_16x16x32_bf16 v[72:75], v[200:203], a[160:163], v[72:75]
ds_read_b64_tr_b16 v[184:185], v210 offset:8192
ds_read_b64_tr_b16 v[186:187], v210 offset:12288
v_mul_f32 v21, v21, v53
v_mfma_f32_16x16x32_bf16 v[88:91], v[200:203], a[164:167], v[88:91]
buffer_load_dwordx4 v233, s[44:47], s10 offen offset:3072 lds
v_mfma_f32_16x16x32_bf16 v[76:79], v[204:207], a[160:163], v[76:79]
ds_read_b64_tr_b16 v[188:189], v211 offset:8192
ds_read_b64_tr_b16 v[190:191], v211 offset:12288
v_mfma_f32_16x16x32_bf16 v[92:95], v[204:207], a[164:167], v[92:95]
s_add_u32 m0, s77, 81920
s_waitcnt lgkmcnt(8)
v_mfma_f32_16x16x32_bf16 v[96:99], v[160:163], a[160:163], v[96:99]
ds_read_b64_tr_b16 v[192:193], v208 offset:10240
ds_read_b64_tr_b16 v[194:195], v208 offset:14336
v_mul_f32 v253, v22, v22
v_mfma_f32_16x16x32_bf16 v[112:115], v[160:163], a[164:167], v[112:115]
)"
R"(
v_rcp_f32 v251, v251
v_mfma_f32_16x16x32_bf16 v[100:103], v[164:167], a[160:163], v[100:103]
ds_read_b64_tr_b16 v[196:197], v209 offset:10240
ds_read_b64_tr_b16 v[198:199], v209 offset:14336
v_mfma_f32_16x16x32_bf16 v[116:119], v[164:167], a[164:167], v[116:119]
v_exp_f32 v252, v252
v_mfma_f32_16x16x32_bf16 v[104:107], v[168:171], a[160:163], v[104:107]
ds_read_b64_tr_b16 v[200:201], v210 offset:10240
ds_read_b64_tr_b16 v[202:203], v210 offset:14336
v_mfma_f32_16x16x32_bf16 v[120:123], v[168:171], a[164:167], v[120:123]
v_mul_f32 v20, v20, v251
v_add_f32 v252, 1.0, v252
v_mfma_f32_16x16x32_bf16 v[108:111], v[172:175], a[160:163], v[108:111]
ds_read_b64_tr_b16 v[204:205], v211 offset:10240
ds_read_b64_tr_b16 v[206:207], v211 offset:14336
v_fmaak_f32 v253, v253, v223, 0xc0135761
v_mfma_f32_16x16x32_bf16 v[124:127], v[172:175], a[164:167], v[124:127]
s_waitcnt vmcnt(26) lgkmcnt(8)
s_barrier
v_mfma_f32_16x16x32_bf16 v[64:67], v[176:179], a[168:171], v[64:67]
ds_read_b64_tr_b16 v[160:161], v212 offset:32768
ds_read_b64_tr_b16 v[162:163], v212 offset:36864
v_mfma_f32_16x16x32_bf16 v[80:83], v[176:179], a[172:175], v[80:83]
buffer_load_dwordx4 v232, s[40:43], s11 offen offset:0 lds
v_mfma_f32_16x16x32_bf16 v[68:71], v[180:183], a[168:171], v[68:71]
ds_read_b64_tr_b16 v[164:165], v213 offset:32768
ds_read_b64_tr_b16 v[166:167], v213 offset:36864
v_mul_f32 v253, v253, v22
v_mfma_f32_16x16x32_bf16 v[84:87], v[180:183], a[172:175], v[84:87]
v_rcp_f32 v252, v252
v_mfma_f32_16x16x32_bf16 v[72:75], v[184:187], a[168:171], v[72:75]
ds_read_b64_tr_b16 v[168:169], v214 offset:32768
ds_read_b64_tr_b16 v[170:171], v214 offset:36864
v_mfma_f32_16x16x32_bf16 v[88:91], v[184:187], a[172:175], v[88:91]
v_exp_f32 v253, v253
v_mfma_f32_16x16x32_bf16 v[76:79], v[188:191], a[168:171], v[76:79]
ds_read_b64_tr_b16 v[172:173], v215 offset:32768
ds_read_b64_tr_b16 v[174:175], v215 offset:36864
v_mul_f32 v21, v21, v252
v_mfma_f32_16x16x32_bf16 v[92:95], v[188:191], a[172:175], v[92:95]
v_cvt_pk_bf16_f32 v50, v20, v21
s_waitcnt lgkmcnt(8)
v_mfma_f32_16x16x32_bf16 v[96:99], v[192:195], a[168:171], v[96:99]
ds_read_b64_tr_b16 v[176:177], v212 offset:34816
ds_read_b64_tr_b16 v[178:179], v212 offset:38912
v_mfma_f32_16x16x32_bf16 v[112:115], v[192:195], a[172:175], v[112:115]
buffer_load_dwordx4 v233, s[40:43], s11 offen offset:1024 lds
v_mfma_f32_16x16x32_bf16 v[100:103], v[196:199], a[168:171], v[100:103]
ds_read_b64_tr_b16 v[180:181], v213 offset:34816
ds_read_b64_tr_b16 v[182:183], v213 offset:38912
v_mul_f32 v22, v22, v54
v_mfma_f32_16x16x32_bf16 v[116:119], v[196:199], a[172:175], v[116:119]
buffer_store_dwordx2 v[128:129], v130, s[52:55], s90 offen offset:512 sc1
v_mfma_f32_16x16x32_bf16 v[104:107], v[200:203], a[168:171], v[104:107]
ds_read_b64_tr_b16 v[184:185], v214 offset:34816
ds_read_b64_tr_b16 v[186:187], v214 offset:38912
v_mfma_f32_16x16x32_bf16 v[120:123], v[200:203], a[172:175], v[120:123]
buffer_load_dwordx4 v232, s[44:47], s11 offen offset:2048 lds
v_mfma_f32_16x16x32_bf16 v[108:111], v[204:207], a[168:171], v[108:111]
ds_read_b64_tr_b16 v[188:189], v215 offset:34816
ds_read_b64_tr_b16 v[190:191], v215 offset:38912
v_mfma_f32_16x16x32_bf16 v[124:127], v[204:207], a[172:175], v[124:127]
v_add_f32 v253, 1.0, v253
s_waitcnt lgkmcnt(8)
v_mfma_f32_16x16x32_bf16 v[64:67], v[160:163], a[176:179], v[64:67]
ds_read_b64_tr_b16 v[192:193], v212 offset:40960
ds_read_b64_tr_b16 v[194:195], v212 offset:45056
v_mul_f32 v254, v23, v23
v_mfma_f32_16x16x32_bf16 v[80:83], v[160:163], a[180:183], v[80:83]
v_rcp_f32 v253, v253
v_mfma_f32_16x16x32_bf16 v[68:71], v[164:167], a[176:179], v[68:71]
ds_read_b64_tr_b16 v[196:197], v213 offset:40960
ds_read_b64_tr_b16 v[198:199], v213 offset:45056
v_mfma_f32_16x16x32_bf16 v[84:87], v[164:167], a[180:183], v[84:87]
v_mul_f32 v22, v22, v253
v_fmaak_f32 v254, v254, v223, 0xc0135761
v_mfma_f32_16x16x32_bf16 v[72:75], v[168:171], a[176:179], v[72:75]
ds_read_b64_tr_b16 v[200:201], v214 offset:40960
ds_read_b64_tr_b16 v[202:203], v214 offset:45056
v_mul_f32 v254, v254, v23
v_mfma_f32_16x16x32_bf16 v[88:91], v[168:171], a[180:183], v[88:91]
buffer_load_dwordx4 v233, s[44:47], s11 offen offset:3072 lds
v_mfma_f32_16x16x32_bf16 v[76:79], v[172:175], a[176:179], v[76:79]
ds_read_b64_tr_b16 v[204:205], v215 offset:40960
ds_read_b64_tr_b16 v[206:207], v215 offset:45056
v_mfma_f32_16x16x32_bf16 v[92:95], v[172:175], a[180:183], v[92:95]
s_add_u32 m0, s77, 0
s_waitcnt lgkmcnt(8)
v_mfma_f32_16x16x32_bf16 v[96:99], v[176:179], a[176:179], v[96:99]
ds_read_b64_tr_b16 v[160:161], v212 offset:43008
ds_read_b64_tr_b16 v[162:163], v212 offset:47104
v_mul_f32 v23, v23, v55
v_mfma_f32_16x16x32_bf16 v[112:115], v[176:179], a[180:183], v[112:115]
v_exp_f32 v254, v254
v_mfma_f32_16x16x32_bf16 v[100:103], v[180:183], a[176:179], v[100:103]
ds_read_b64_tr_b16 v[164:165], v213 offset:43008
ds_read_b64_tr_b16 v[166:167], v213 offset:47104
v_mfma_f32_16x16x32_bf16 v[116:119], v[180:183], a[180:183], v[116:119]
v_add_f32 v254, 1.0, v254
v_mul_f32 v243, v24, v24
v_mfma_f32_16x16x32_bf16 v[104:107], v[184:187], a[176:179], v[104:107]
ds_read_b64_tr_b16 v[168:169], v214 offset:43008
ds_read_b64_tr_b16 v[170:171], v214 offset:47104
v_fmaak_f32 v243, v243, v223, 0xc0135761
v_mfma_f32_16x16x32_bf16 v[120:123], v[184:187], a[180:183], v[120:123]
v_rcp_f32 v254, v254
v_mfma_f32_16x16x32_bf16 v[108:111], v[188:191], a[176:179], v[108:111]
ds_read_b64_tr_b16 v[172:173], v215 offset:43008
ds_read_b64_tr_b16 v[174:175], v215 offset:47104
v_mfma_f32_16x16x32_bf16 v[124:127], v[188:191], a[180:183], v[124:127]
s_waitcnt vmcnt(26) lgkmcnt(8)
s_barrier
v_mfma_f32_16x16x32_bf16 v[64:67], v[192:195], a[184:187], v[64:67]
ds_read_b64_tr_b16 v[176:177], v208 offset:16384
ds_read_b64_tr_b16 v[178:179], v208 offset:20480
v_mfma_f32_16x16x32_bf16 v[80:83], v[192:195], a[188:191], v[80:83]
buffer_load_dwordx4 v232, s[40:43], s12 offen offset:0 lds
v_mfma_f32_16x16x32_bf16 v[68:71], v[196:199], a[184:187], v[68:71]
ds_read_b64_tr_b16 v[180:181], v209 offset:16384
ds_read_b64_tr_b16 v[182:183], v209 offset:20480
v_mul_f32 v23, v23, v254
v_mfma_f32_16x16x32_bf16 v[84:87], v[196:199], a[188:191], v[84:87]
v_cvt_pk_bf16_f32 v51, v22, v23
v_mul_f32 v243, v243, v24
v_mfma_f32_16x16x32_bf16 v[72:75], v[200:203], a[184:187], v[72:75]
ds_read_b64_tr_b16 v[184:185], v210 offset:16384
ds_read_b64_tr_b16 v[186:187], v210 offset:20480
v_mfma_f32_16x16x32_bf16 v[88:91], v[200:203], a[188:191], v[88:91]
buffer_store_dwordx4 v[48:51], v221, s[52:55], s92 offen offset:0 sc1
v_mfma_f32_16x16x32_bf16 v[76:79], v[204:207], a[184:187], v[76:79]
ds_read_b64_tr_b16 v[188:189], v211 offset:16384
ds_read_b64_tr_b16 v[190:191], v211 offset:20480
v_mul_f32 v24, v24, v56
v_mfma_f32_16x16x32_bf16 v[92:95], v[204:207], a[188:191], v[92:95]
v_mul_f32 v244, v25, v25
s_waitcnt lgkmcnt(8)
v_mfma_f32_16x16x32_bf16 v[96:99], v[160:163], a[184:187], v[96:99]
ds_read_b64_tr_b16 v[192:193], v208 offset:18432
ds_read_b64_tr_b16 v[194:195], v208 offset:22528
v_mfma_f32_16x16x32_bf16 v[112:115], v[160:163], a[188:191], v[112:115]
buffer_load_dwordx4 v233, s[40:43], s12 offen offset:1024 lds
v_mfma_f32_16x16x32_bf16 v[100:103], v[164:167], a[184:187], v[100:103]
ds_read_b64_tr_b16 v[196:197], v209 offset:18432
ds_read_b64_tr_b16 v[198:199], v209 offset:22528
v_fmaak_f32 v244, v244, v223, 0xc0135761
v_mfma_f32_16x16x32_bf16 v[116:119], v[164:167], a[188:191], v[116:119]
buffer_store_dwordx2 v[128:129], v131, s[52:55], s90 offen offset:0 sc1
v_mfma_f32_16x16x32_bf16 v[104:107], v[168:171], a[184:187], v[104:107]
ds_read_b64_tr_b16 v[200:201], v210 offset:18432
ds_read_b64_tr_b16 v[202:203], v210 offset:22528
v_mfma_f32_16x16x32_bf16 v[120:123], v[168:171], a[188:191], v[120:123]
buffer_load_dwordx4 v232, s[44:47], s12 offen offset:2048 lds
v_mfma_f32_16x16x32_bf16 v[108:111], v[172:175], a[184:187], v[108:111]
ds_read_b64_tr_b16 v[204:205], v211 offset:18432
ds_read_b64_tr_b16 v[206:207], v211 offset:22528
v_mul_f32 v244, v244, v25
v_mfma_f32_16x16x32_bf16 v[124:127], v[172:175], a[188:191], v[124:127]
v_mul_f32 v25, v25, v57
s_waitcnt lgkmcnt(8)
v_mfma_f32_16x16x32_bf16 v[64:67], v[176:179], a[192:195], v[64:67]
ds_read_b64_tr_b16 v[160:161], v208 offset:24576
ds_read_b64_tr_b16 v[162:163], v208 offset:28672
v_mfma_f32_16x16x32_bf16 v[80:83], v[176:179], a[196:199], v[80:83]
v_exp_f32 v243, v243
v_mfma_f32_16x16x32_bf16 v[68:71], v[180:183], a[192:195], v[68:71]
ds_read_b64_tr_b16 v[164:165], v209 offset:24576
ds_read_b64_tr_b16 v[166:167], v209 offset:28672
v_mfma_f32_16x16x32_bf16 v[84:87], v[180:183], a[196:199], v[84:87]
v_exp_f32 v244, v244
v_mfma_f32_16x16x32_bf16 v[72:75], v[184:187], a[192:195], v[72:75]
ds_read_b64_tr_b16 v[168:169], v210 offset:24576
ds_read_b64_tr_b16 v[170:171], v210 offset:28672
v_add_f32 v243, 1.0, v243
v_mfma_f32_16x16x32_bf16 v[88:91], v[184:187], a[196:199], v[88:91]
buffer_load_dwordx4 v233, s[44:47], s12 offen offset:3072 lds
v_mfma_f32_16x16x32_bf16 v[76:79], v[188:191], a[192:195], v[76:79]
ds_read_b64_tr_b16 v[172:173], v211 offset:24576
ds_read_b64_tr_b16 v[174:175], v211 offset:28672
v_mfma_f32_16x16x32_bf16 v[92:95], v[188:191], a[196:199], v[92:95]
s_add_u32 m0, s77, 98304
s_waitcnt lgkmcnt(8)
v_mfma_f32_16x16x32_bf16 v[96:99], v[192:195], a[192:195], v[96:99]
ds_read_b64_tr_b16 v[176:177], v208 offset:26624
ds_read_b64_tr_b16 v[178:179], v208 offset:30720
v_add_f32 v244, 1.0, v244
v_mfma_f32_16x16x32_bf16 v[112:115], v[192:195], a[196:199], v[112:115]
v_rcp_f32 v243, v243
v_mfma_f32_16x16x32_bf16 v[100:103], v[196:199], a[192:195], v[100:103]
ds_read_b64_tr_b16 v[180:181], v209 offset:26624
ds_read_b64_tr_b16 v[182:183], v209 offset:30720
v_mfma_f32_16x16x32_bf16 v[116:119], v[196:199], a[196:199], v[116:119]
v_rcp_f32 v244, v244
v_mfma_f32_16x16x32_bf16 v[104:107], v[200:203], a[192:195], v[104:107]
ds_read_b64_tr_b16 v[184:185], v210 offset:26624
ds_read_b64_tr_b16 v[186:187], v210 offset:30720
LC14_%=:
s_cbranch_execz LC18_%=
v_mul_f32 v24, v24, v243
v_mfma_f32_16x16x32_bf16 v[120:123], v[200:203], a[196:199], v[120:123]
v_mul_f32 v25, v25, v244
v_cvt_pk_bf16_f32 v56, v24, v25
v_mfma_f32_16x16x32_bf16 v[108:111], v[204:207], a[192:195], v[108:111]
ds_read_b64_tr_b16 v[188:189], v211 offset:26624
ds_read_b64_tr_b16 v[190:191], v211 offset:30720
v_mfma_f32_16x16x32_bf16 v[124:127], v[204:207], a[196:199], v[124:127]
s_waitcnt vmcnt(27) lgkmcnt(8)
s_barrier
v_mfma_f32_16x16x32_bf16 v[64:67], v[160:163], a[200:203], v[64:67]
ds_read_b64_tr_b16 v[192:193], v212 offset:49152
ds_read_b64_tr_b16 v[194:195], v212 offset:53248
v_mul_f32 v245, v26, v26
v_mfma_f32_16x16x32_bf16 v[80:83], v[160:163], a[204:207], v[80:83]
buffer_load_dwordx4 v232, s[40:43], s13 offen offset:0 lds
v_mfma_f32_16x16x32_bf16 v[68:71], v[164:167], a[200:203], v[68:71]
ds_read_b64_tr_b16 v[196:197], v213 offset:49152
ds_read_b64_tr_b16 v[198:199], v213 offset:53248
v_mfma_f32_16x16x32_bf16 v[84:87], v[164:167], a[204:207], v[84:87]
v_fmaak_f32 v245, v245, v223, 0xc0135761
v_mul_f32 v245, v245, v26
v_mfma_f32_16x16x32_bf16 v[72:75], v[168:171], a[200:203], v[72:75]
ds_read_b64_tr_b16 v[200:201], v214 offset:49152
ds_read_b64_tr_b16 v[202:203], v214 offset:53248
v_mfma_f32_16x16x32_bf16 v[88:91], v[168:171], a[204:207], v[88:91]
v_exp_f32 v245, v245
v_mfma_f32_16x16x32_bf16 v[76:79], v[172:175], a[200:203], v[76:79]
ds_read_b64_tr_b16 v[204:205], v215 offset:49152
ds_read_b64_tr_b16 v[206:207], v215 offset:53248
v_mul_f32 v26, v26, v58
v_mfma_f32_16x16x32_bf16 v[92:95], v[172:175], a[204:207], v[92:95]
v_add_f32 v245, 1.0, v245
s_waitcnt lgkmcnt(8)
v_mfma_f32_16x16x32_bf16 v[96:99], v[176:179], a[200:203], v[96:99]
ds_read_b64_tr_b16 v[160:161], v212 offset:51200
ds_read_b64_tr_b16 v[162:163], v212 offset:55296
v_mfma_f32_16x16x32_bf16 v[112:115], v[176:179], a[204:207], v[112:115]
buffer_load_dwordx4 v233, s[40:43], s13 offen offset:1024 lds
v_mfma_f32_16x16x32_bf16 v[100:103], v[180:183], a[200:203], v[100:103]
ds_read_b64_tr_b16 v[164:165], v213 offset:51200
ds_read_b64_tr_b16 v[166:167], v213 offset:55296
v_mul_f32 v246, v27, v27
v_mfma_f32_16x16x32_bf16 v[116:119], v[180:183], a[204:207], v[116:119]
buffer_store_dwordx2 v[128:129], v131, s[52:55], s90 offen offset:512 sc1
v_mfma_f32_16x16x32_bf16 v[104:107], v[184:187], a[200:203], v[104:107]
ds_read_b64_tr_b16 v[168:169], v214 offset:51200
ds_read_b64_tr_b16 v[170:171], v214 offset:55296
v_mfma_f32_16x16x32_bf16 v[120:123], v[184:187], a[204:207], v[120:123]
buffer_load_dwordx4 v232, s[44:47], s13 offen offset:2048 lds
v_mfma_f32_16x16x32_bf16 v[108:111], v[188:191], a[200:203], v[108:111]
ds_read_b64_tr_b16 v[172:173], v215 offset:51200
ds_read_b64_tr_b16 v[174:175], v215 offset:55296
v_fmaak_f32 v246, v246, v223, 0xc0135761
v_mfma_f32_16x16x32_bf16 v[124:127], v[188:191], a[204:207], v[124:127]
s_bfe_u32 s86, s83, 65547
s_waitcnt lgkmcnt(8)
v_mfma_f32_16x16x32_bf16 v[64:67], v[192:195], a[208:211], v[64:67]
ds_read_b64_tr_b16 v[176:177], v212 offset:57344
ds_read_b64_tr_b16 v[178:179], v212 offset:61440
v_mfma_f32_16x16x32_bf16 v[80:83], v[192:195], a[212:215], v[80:83]
s_lshl_b32 s86, s86, 1
s_xor_b32 s85, s72, s86
v_mfma_f32_16x16x32_bf16 v[68:71], v[196:199], a[208:211], v[68:71]
ds_read_b64_tr_b16 v[180:181], v213 offset:57344
ds_read_b64_tr_b16 v[182:183], v213 offset:61440
v_mul_f32 v246, v246, v27
v_mfma_f32_16x16x32_bf16 v[84:87], v[196:199], a[212:215], v[84:87]
s_mul_i32 s85, s85, 229376
s_mul_i32 s86, s74, 448
v_mfma_f32_16x16x32_bf16 v[72:75], v[200:203], a[208:211], v[72:75]
ds_read_b64_tr_b16 v[184:185], v214 offset:57344
ds_read_b64_tr_b16 v[186:187], v214 offset:61440
v_mfma_f32_16x16x32_bf16 v[88:91], v[200:203], a[212:215], v[88:91]
buffer_load_dwordx4 v233, s[44:47], s13 offen offset:3072 lds
v_mfma_f32_16x16x32_bf16 v[76:79], v[204:207], a[208:211], v[76:79]
ds_read_b64_tr_b16 v[188:189], v215 offset:57344
ds_read_b64_tr_b16 v[190:191], v215 offset:61440
v_mfma_f32_16x16x32_bf16 v[92:95], v[204:207], a[212:215], v[92:95]
s_add_u32 m0, s77, 16384
s_waitcnt lgkmcnt(8)
v_mfma_f32_16x16x32_bf16 v[96:99], v[160:163], a[208:211], v[96:99]
ds_read_b64_tr_b16 v[192:193], v212 offset:59392
ds_read_b64_tr_b16 v[194:195], v212 offset:63488
v_mul_f32 v27, v27, v59
v_mfma_f32_16x16x32_bf16 v[112:115], v[160:163], a[212:215], v[112:115]
s_add_u32 s85, s85, s86
v_lshrrev_b32 v236, 3, v231
v_mfma_f32_16x16x32_bf16 v[100:103], v[164:167], a[208:211], v[100:103]
ds_read_b64_tr_b16 v[196:197], v213 offset:59392
ds_read_b64_tr_b16 v[198:199], v213 offset:63488
v_mfma_f32_16x16x32_bf16 v[116:119], v[164:167], a[212:215], v[116:119]
s_and_b32 s86, s74, 1
s_cselect_b32 s86, 0, 384
v_mfma_f32_16x16x32_bf16 v[104:107], v[168:171], a[208:211], v[104:107]
ds_read_b64_tr_b16 v[200:201], v214 offset:59392
ds_read_b64_tr_b16 v[202:203], v214 offset:63488
v_mul_f32 v247, v28, v28
v_mfma_f32_16x16x32_bf16 v[120:123], v[168:171], a[212:215], v[120:123]
s_add_u32 s85, s85, s86
v_and_b32 v237, 2, v236
v_mfma_f32_16x16x32_bf16 v[108:111], v[172:175], a[208:211], v[108:111]
ds_read_b64_tr_b16 v[204:205], v215 offset:59392
ds_read_b64_tr_b16 v[206:207], v215 offset:63488
v_mfma_f32_16x16x32_bf16 v[124:127], v[172:175], a[212:215], v[124:127]
s_waitcnt vmcnt(26) lgkmcnt(8)
s_barrier
v_mfma_f32_16x16x32_bf16 v[64:67], v[176:179], a[216:219], v[64:67]
ds_read_b64_tr_b16 v[160:161], v208 offset:32768
ds_read_b64_tr_b16 v[162:163], v208 offset:36864
v_fmaak_f32 v247, v247, v223, 0xc0135761
v_mfma_f32_16x16x32_bf16 v[80:83], v[176:179], a[220:223], v[80:83]
buffer_load_dwordx4 v232, s[40:43], s14 offen offset:0 lds
v_mfma_f32_16x16x32_bf16 v[68:71], v[180:183], a[216:219], v[68:71]
ds_read_b64_tr_b16 v[164:165], v209 offset:32768
ds_read_b64_tr_b16 v[166:167], v209 offset:36864
v_mfma_f32_16x16x32_bf16 v[84:87], v[180:183], a[220:223], v[84:87]
v_xor_b32 v237, v237, v231
v_and_b32 v237, 3, v237
v_mfma_f32_16x16x32_bf16 v[72:75], v[184:187], a[216:219], v[72:75]
ds_read_b64_tr_b16 v[168:169], v210 offset:32768
ds_read_b64_tr_b16 v[170:171], v210 offset:36864
v_mul_f32 v247, v247, v28
v_mfma_f32_16x16x32_bf16 v[88:91], v[184:187], a[220:223], v[88:91]
v_lshrrev_b32 v236, 2, v231
v_mul_u32_u24 v236, 7168, v236
v_mfma_f32_16x16x32_bf16 v[76:79], v[188:191], a[216:219], v[76:79]
ds_read_b64_tr_b16 v[172:173], v211 offset:32768
ds_read_b64_tr_b16 v[174:175], v211 offset:36864
v_mfma_f32_16x16x32_bf16 v[92:95], v[188:191], a[220:223], v[92:95]
v_lshl_add_u32 v236, v237, 4, v236
s_waitcnt lgkmcnt(8)
v_mfma_f32_16x16x32_bf16 v[96:99], v[192:195], a[216:219], v[96:99]
ds_read_b64_tr_b16 v[176:177], v208 offset:34816
ds_read_b64_tr_b16 v[178:179], v208 offset:38912
v_mfma_f32_16x16x32_bf16 v[112:115], v[192:195], a[220:223], v[112:115]
buffer_load_dwordx4 v233, s[40:43], s14 offen offset:1024 lds
v_mfma_f32_16x16x32_bf16 v[100:103], v[196:199], a[216:219], v[100:103]
ds_read_b64_tr_b16 v[180:181], v209 offset:34816
ds_read_b64_tr_b16 v[182:183], v209 offset:38912
v_mul_f32 v28, v28, v60
v_mfma_f32_16x16x32_bf16 v[116:119], v[196:199], a[220:223], v[116:119]
v_add_u32 v228, s85, v236
v_add_u32 v229, 113664, v228
v_mfma_f32_16x16x32_bf16 v[104:107], v[200:203], a[216:219], v[104:107]
ds_read_b64_tr_b16 v[184:185], v210 offset:34816
ds_read_b64_tr_b16 v[186:187], v210 offset:38912
v_mfma_f32_16x16x32_bf16 v[120:123], v[200:203], a[220:223], v[120:123]
buffer_load_dwordx4 v232, s[44:47], s14 offen offset:2048 lds
v_mfma_f32_16x16x32_bf16 v[108:111], v[204:207], a[216:219], v[108:111]
ds_read_b64_tr_b16 v[188:189], v211 offset:34816
ds_read_b64_tr_b16 v[190:191], v211 offset:38912
v_mul_f32 v248, v29, v29
v_mfma_f32_16x16x32_bf16 v[124:127], v[204:207], a[220:223], v[124:127]
v_fmaak_f32 v248, v248, v223, 0xc0135761
s_waitcnt lgkmcnt(8)
v_mfma_f32_16x16x32_bf16 v[64:67], v[160:163], a[224:227], v[64:67]
ds_read_b64_tr_b16 v[192:193], v208 offset:40960
ds_read_b64_tr_b16 v[194:195], v208 offset:45056
v_mfma_f32_16x16x32_bf16 v[80:83], v[160:163], a[228:231], v[80:83]
v_rcp_f32 v245, v245
v_mfma_f32_16x16x32_bf16 v[68:71], v[164:167], a[224:227], v[68:71]
ds_read_b64_tr_b16 v[196:197], v209 offset:40960
ds_read_b64_tr_b16 v[198:199], v209 offset:45056
v_mul_f32 v26, v26, v245
v_mfma_f32_16x16x32_bf16 v[84:87], v[164:167], a[228:231], v[84:87]
v_exp_f32 v246, v246
v_mfma_f32_16x16x32_bf16 v[72:75], v[168:171], a[224:227], v[72:75]
ds_read_b64_tr_b16 v[200:201], v210 offset:40960
ds_read_b64_tr_b16 v[202:203], v210 offset:45056
v_mfma_f32_16x16x32_bf16 v[88:91], v[168:171], a[228:231], v[88:91]
buffer_load_dwordx4 v233, s[44:47], s14 offen offset:3072 lds
v_mfma_f32_16x16x32_bf16 v[76:79], v[172:175], a[224:227], v[76:79]
ds_read_b64_tr_b16 v[204:205], v211 offset:40960
ds_read_b64_tr_b16 v[206:207], v211 offset:45056
v_add_f32 v246, 1.0, v246
v_mfma_f32_16x16x32_bf16 v[92:95], v[172:175], a[228:231], v[92:95]
s_add_u32 m0, s77, 114688
s_waitcnt lgkmcnt(8)
v_mfma_f32_16x16x32_bf16 v[96:99], v[176:179], a[224:227], v[96:99]
ds_read_b64_tr_b16 v[160:161], v208 offset:43008
ds_read_b64_tr_b16 v[162:163], v208 offset:47104
v_mfma_f32_16x16x32_bf16 v[112:115], v[176:179], a[228:231], v[112:115]
v_rcp_f32 v246, v246
v_mfma_f32_16x16x32_bf16 v[100:103], v[180:183], a[224:227], v[100:103]
ds_read_b64_tr_b16 v[164:165], v209 offset:43008
ds_read_b64_tr_b16 v[166:167], v209 offset:47104
v_mfma_f32_16x16x32_bf16 v[116:119], v[180:183], a[228:231], v[116:119]
v_exp_f32 v247, v247
v_mfma_f32_16x16x32_bf16 v[104:107], v[184:187], a[224:227], v[104:107]
ds_read_b64_tr_b16 v[168:169], v210 offset:43008
ds_read_b64_tr_b16 v[170:171], v210 offset:47104
v_mul_f32 v27, v27, v246
v_mfma_f32_16x16x32_bf16 v[120:123], v[184:187], a[228:231], v[120:123]
v_cvt_pk_bf16_f32 v57, v26, v27
v_add_f32 v247, 1.0, v247
v_mfma_f32_16x16x32_bf16 v[108:111], v[188:191], a[224:227], v[108:111]
ds_read_b64_tr_b16 v[172:173], v211 offset:43008
ds_read_b64_tr_b16 v[174:175], v211 offset:47104
v_mfma_f32_16x16x32_bf16 v[124:127], v[188:191], a[228:231], v[124:127]
s_waitcnt vmcnt(25) lgkmcnt(8)
s_barrier
v_mfma_f32_16x16x32_bf16 v[64:67], v[192:195], a[232:235], v[64:67]
)"
R"(
ds_read_b64_tr_b16 v[176:177], v212 offset:0
ds_read_b64_tr_b16 v[178:179], v212 offset:4096
v_mul_f32 v248, v248, v29
v_mfma_f32_16x16x32_bf16 v[80:83], v[192:195], a[236:239], v[80:83]
buffer_load_dwordx4 v232, s[40:43], s15 offen offset:0 lds
v_mfma_f32_16x16x32_bf16 v[68:71], v[196:199], a[232:235], v[68:71]
ds_read_b64_tr_b16 v[180:181], v213 offset:0
ds_read_b64_tr_b16 v[182:183], v213 offset:4096
v_mfma_f32_16x16x32_bf16 v[84:87], v[196:199], a[236:239], v[84:87]
v_rcp_f32 v247, v247
v_mfma_f32_16x16x32_bf16 v[72:75], v[200:203], a[232:235], v[72:75]
ds_read_b64_tr_b16 v[184:185], v214 offset:0
ds_read_b64_tr_b16 v[186:187], v214 offset:4096
v_mul_f32 v28, v28, v247
v_mfma_f32_16x16x32_bf16 v[88:91], v[200:203], a[236:239], v[88:91]
v_exp_f32 v248, v248
v_mfma_f32_16x16x32_bf16 v[76:79], v[204:207], a[232:235], v[76:79]
ds_read_b64_tr_b16 v[188:189], v215 offset:0
ds_read_b64_tr_b16 v[190:191], v215 offset:4096
v_mfma_f32_16x16x32_bf16 v[92:95], v[204:207], a[236:239], v[92:95]
v_mul_f32 v29, v29, v61
s_waitcnt lgkmcnt(8)
v_mfma_f32_16x16x32_bf16 v[96:99], v[160:163], a[232:235], v[96:99]
ds_read_b64_tr_b16 v[192:193], v212 offset:2048
ds_read_b64_tr_b16 v[194:195], v212 offset:6144
v_add_f32 v248, 1.0, v248
v_mfma_f32_16x16x32_bf16 v[112:115], v[160:163], a[236:239], v[112:115]
buffer_load_dwordx4 v233, s[40:43], s15 offen offset:1024 lds
v_mfma_f32_16x16x32_bf16 v[100:103], v[164:167], a[232:235], v[100:103]
ds_read_b64_tr_b16 v[196:197], v213 offset:2048
ds_read_b64_tr_b16 v[198:199], v213 offset:6144
v_mfma_f32_16x16x32_bf16 v[116:119], v[164:167], a[236:239], v[116:119]
v_rcp_f32 v248, v248
v_mfma_f32_16x16x32_bf16 v[104:107], v[168:171], a[232:235], v[104:107]
ds_read_b64_tr_b16 v[200:201], v214 offset:2048
ds_read_b64_tr_b16 v[202:203], v214 offset:6144
v_mfma_f32_16x16x32_bf16 v[120:123], v[168:171], a[236:239], v[120:123]
buffer_load_dwordx4 v232, s[44:47], s15 offen offset:2048 lds
v_mfma_f32_16x16x32_bf16 v[108:111], v[172:175], a[232:235], v[108:111]
ds_read_b64_tr_b16 v[204:205], v215 offset:2048
ds_read_b64_tr_b16 v[206:207], v215 offset:6144
v_mul_f32 v29, v29, v248
v_mfma_f32_16x16x32_bf16 v[124:127], v[172:175], a[236:239], v[124:127]
v_cvt_pk_bf16_f32 v58, v28, v29
s_waitcnt lgkmcnt(8)
v_mfma_f32_16x16x32_bf16 v[64:67], v[176:179], a[240:243], v[64:67]
ds_read_b64_tr_b16 v[160:161], v212 offset:8192
ds_read_b64_tr_b16 v[162:163], v212 offset:12288
v_mfma_f32_16x16x32_bf16 v[80:83], v[176:179], a[244:247], v[80:83]
v_mul_f32 v249, v30, v30
v_fmaak_f32 v249, v249, v223, 0xc0135761
v_mfma_f32_16x16x32_bf16 v[68:71], v[180:183], a[240:243], v[68:71]
ds_read_b64_tr_b16 v[164:165], v213 offset:8192
ds_read_b64_tr_b16 v[166:167], v213 offset:12288
v_mul_f32 v249, v249, v30
v_mfma_f32_16x16x32_bf16 v[84:87], v[180:183], a[244:247], v[84:87]
v_exp_f32 v249, v249
v_mfma_f32_16x16x32_bf16 v[72:75], v[184:187], a[240:243], v[72:75]
ds_read_b64_tr_b16 v[168:169], v214 offset:8192
ds_read_b64_tr_b16 v[170:171], v214 offset:12288
v_mfma_f32_16x16x32_bf16 v[88:91], v[184:187], a[244:247], v[88:91]
buffer_load_dwordx4 v233, s[44:47], s15 offen offset:3072 lds
v_mfma_f32_16x16x32_bf16 v[76:79], v[188:191], a[240:243], v[76:79]
ds_read_b64_tr_b16 v[172:173], v215 offset:8192
ds_read_b64_tr_b16 v[174:175], v215 offset:12288
v_mul_f32 v30, v30, v62
v_mfma_f32_16x16x32_bf16 v[92:95], v[188:191], a[244:247], v[92:95]
s_add_u32 m0, s77, 32768
s_waitcnt lgkmcnt(8)
v_mfma_f32_16x16x32_bf16 v[96:99], v[192:195], a[240:243], v[96:99]
ds_read_b64_tr_b16 v[176:177], v212 offset:10240
ds_read_b64_tr_b16 v[178:179], v212 offset:14336
v_mfma_f32_16x16x32_bf16 v[112:115], v[192:195], a[244:247], v[112:115]
v_add_f32 v249, 1.0, v249
v_mul_f32 v250, v31, v31
v_mfma_f32_16x16x32_bf16 v[100:103], v[196:199], a[240:243], v[100:103]
ds_read_b64_tr_b16 v[180:181], v213 offset:10240
ds_read_b64_tr_b16 v[182:183], v213 offset:14336
v_fmaak_f32 v250, v250, v223, 0xc0135761
v_mfma_f32_16x16x32_bf16 v[116:119], v[196:199], a[244:247], v[116:119]
v_rcp_f32 v249, v249
v_mfma_f32_16x16x32_bf16 v[104:107], v[200:203], a[240:243], v[104:107]
ds_read_b64_tr_b16 v[184:185], v214 offset:10240
ds_read_b64_tr_b16 v[186:187], v214 offset:14336
v_mfma_f32_16x16x32_bf16 v[120:123], v[200:203], a[244:247], v[120:123]
v_mul_f32 v30, v30, v249
v_mul_f32 v250, v250, v31
v_mfma_f32_16x16x32_bf16 v[108:111], v[204:207], a[240:243], v[108:111]
ds_read_b64_tr_b16 v[188:189], v215 offset:10240
ds_read_b64_tr_b16 v[190:191], v215 offset:14336
v_mfma_f32_16x16x32_bf16 v[124:127], v[204:207], a[244:247], v[124:127]
s_waitcnt vmcnt(24) lgkmcnt(8)
s_barrier
v_mfma_f32_16x16x32_bf16 v[64:67], v[160:163], a[248:251], v[64:67]
ds_read_b64_tr_b16 v[192:193], v208 offset:49152
ds_read_b64_tr_b16 v[194:195], v208 offset:53248
v_mul_f32 v31, v31, v63
v_mfma_f32_16x16x32_bf16 v[80:83], v[160:163], a[252:255], v[80:83]
buffer_load_dwordx4 v232, s[40:43], s16 offen offset:0 lds
v_mfma_f32_16x16x32_bf16 v[68:71], v[164:167], a[248:251], v[68:71]
ds_read_b64_tr_b16 v[196:197], v209 offset:49152
ds_read_b64_tr_b16 v[198:199], v209 offset:53248
v_mfma_f32_16x16x32_bf16 v[84:87], v[164:167], a[252:255], v[84:87]
v_exp_f32 v250, v250
v_mfma_f32_16x16x32_bf16 v[72:75], v[168:171], a[248:251], v[72:75]
ds_read_b64_tr_b16 v[200:201], v210 offset:49152
ds_read_b64_tr_b16 v[202:203], v210 offset:53248
v_add_f32 v250, 1.0, v250
v_mfma_f32_16x16x32_bf16 v[88:91], v[168:171], a[252:255], v[88:91]
v_rcp_f32 v250, v250
v_mfma_f32_16x16x32_bf16 v[76:79], v[172:175], a[248:251], v[76:79]
ds_read_b64_tr_b16 v[204:205], v211 offset:49152
ds_read_b64_tr_b16 v[206:207], v211 offset:53248
v_mfma_f32_16x16x32_bf16 v[92:95], v[172:175], a[252:255], v[92:95]
v_mul_f32 v31, v31, v250
s_waitcnt lgkmcnt(8)
v_mfma_f32_16x16x32_bf16 v[96:99], v[176:179], a[248:251], v[96:99]
ds_read_b64_tr_b16 v[160:161], v208 offset:51200
ds_read_b64_tr_b16 v[162:163], v208 offset:55296
v_cvt_pk_bf16_f32 v59, v30, v31
v_mfma_f32_16x16x32_bf16 v[112:115], v[176:179], a[252:255], v[112:115]
buffer_load_dwordx4 v233, s[40:43], s16 offen offset:1024 lds
v_mfma_f32_16x16x32_bf16 v[100:103], v[180:183], a[248:251], v[100:103]
ds_read_b64_tr_b16 v[164:165], v209 offset:51200
ds_read_b64_tr_b16 v[166:167], v209 offset:55296
v_mfma_f32_16x16x32_bf16 v[116:119], v[180:183], a[252:255], v[116:119]
buffer_store_dwordx4 v[56:59], v221, s[52:55], s92 offen offset:1024 sc1
v_mfma_f32_16x16x32_bf16 v[104:107], v[184:187], a[248:251], v[104:107]
ds_read_b64_tr_b16 v[168:169], v210 offset:51200
ds_read_b64_tr_b16 v[170:171], v210 offset:55296
v_mfma_f32_16x16x32_bf16 v[120:123], v[184:187], a[252:255], v[120:123]
buffer_load_dwordx4 v232, s[44:47], s16 offen offset:2048 lds
v_mfma_f32_16x16x32_bf16 v[108:111], v[188:191], a[248:251], v[108:111]
ds_read_b64_tr_b16 v[172:173], v211 offset:51200
ds_read_b64_tr_b16 v[174:175], v211 offset:55296
v_mfma_f32_16x16x32_bf16 v[124:127], v[188:191], a[252:255], v[124:127]
s_waitcnt lgkmcnt(8)
v_mfma_f32_16x16x32_bf16 v[0:3], v[192:195], a[0:3], 0
ds_read_b64_tr_b16 v[176:177], v208 offset:57344
ds_read_b64_tr_b16 v[178:179], v208 offset:61440
v_mfma_f32_16x16x32_bf16 v[16:19], v[192:195], a[4:7], 0
v_mfma_f32_16x16x32_bf16 v[4:7], v[196:199], a[0:3], 0
ds_read_b64_tr_b16 v[180:181], v209 offset:57344
ds_read_b64_tr_b16 v[182:183], v209 offset:61440
v_mfma_f32_16x16x32_bf16 v[20:23], v[196:199], a[4:7], 0
v_mfma_f32_16x16x32_bf16 v[8:11], v[200:203], a[0:3], 0
ds_read_b64_tr_b16 v[184:185], v210 offset:57344
ds_read_b64_tr_b16 v[186:187], v210 offset:61440
v_mfma_f32_16x16x32_bf16 v[24:27], v[200:203], a[4:7], 0
buffer_load_dwordx4 v233, s[44:47], s16 offen offset:3072 lds
v_mfma_f32_16x16x32_bf16 v[12:15], v[204:207], a[0:3], 0
ds_read_b64_tr_b16 v[188:189], v211 offset:57344
ds_read_b64_tr_b16 v[190:191], v211 offset:61440
v_mul_f32 v243, v64, v64
v_mfma_f32_16x16x32_bf16 v[28:31], v[204:207], a[4:7], 0
s_add_u32 m0, s77, 65536
s_waitcnt lgkmcnt(8)
v_mfma_f32_16x16x32_bf16 v[32:35], v[160:163], a[0:3], 0
ds_read_b64_tr_b16 v[192:193], v208 offset:59392
ds_read_b64_tr_b16 v[194:195], v208 offset:63488
v_mfma_f32_16x16x32_bf16 v[48:51], v[160:163], a[4:7], 0
v_fmaak_f32 v243, v243, v223, 0xc0135761
v_mul_f32 v243, v243, v64
v_mfma_f32_16x16x32_bf16 v[36:39], v[164:167], a[0:3], 0
ds_read_b64_tr_b16 v[196:197], v209 offset:59392
ds_read_b64_tr_b16 v[198:199], v209 offset:63488
v_mfma_f32_16x16x32_bf16 v[52:55], v[164:167], a[4:7], 0
v_exp_f32 v243, v243
v_mfma_f32_16x16x32_bf16 v[40:43], v[168:171], a[0:3], 0
ds_read_b64_tr_b16 v[200:201], v210 offset:59392
ds_read_b64_tr_b16 v[202:203], v210 offset:63488
v_mul_f32 v64, v64, v96
v_mfma_f32_16x16x32_bf16 v[56:59], v[168:171], a[4:7], 0
v_add_f32 v243, 1.0, v243
v_mul_f32 v244, v65, v65
v_mfma_f32_16x16x32_bf16 v[44:47], v[172:175], a[0:3], 0
ds_read_b64_tr_b16 v[204:205], v211 offset:59392
ds_read_b64_tr_b16 v[206:207], v211 offset:63488
v_mfma_f32_16x16x32_bf16 v[60:63], v[172:175], a[4:7], 0
s_waitcnt vmcnt(24) lgkmcnt(8)
s_barrier
v_mfma_f32_16x16x32_bf16 v[0:3], v[176:179], a[8:11], v[0:3]
ds_read_b64_tr_b16 v[160:161], v212 offset:16384
ds_read_b64_tr_b16 v[162:163], v212 offset:20480
v_mfma_f32_16x16x32_bf16 v[16:19], v[176:179], a[12:15], v[16:19]
buffer_load_dwordx4 v232, s[40:43], s17 offen offset:0 lds
v_mfma_f32_16x16x32_bf16 v[4:7], v[180:183], a[8:11], v[4:7]
ds_read_b64_tr_b16 v[164:165], v213 offset:16384
ds_read_b64_tr_b16 v[166:167], v213 offset:20480
v_mfma_f32_16x16x32_bf16 v[20:23], v[180:183], a[12:15], v[20:23]
v_rcp_f32 v243, v243
v_mfma_f32_16x16x32_bf16 v[8:11], v[184:187], a[8:11], v[8:11]
ds_read_b64_tr_b16 v[168:169], v214 offset:16384
ds_read_b64_tr_b16 v[170:171], v214 offset:20480
v_mul_f32 v64, v64, v243
v_mfma_f32_16x16x32_bf16 v[24:27], v[184:187], a[12:15], v[24:27]
v_fmaak_f32 v244, v244, v223, 0xc0135761
v_mul_f32 v244, v244, v65
v_mfma_f32_16x16x32_bf16 v[12:15], v[188:191], a[8:11], v[12:15]
ds_read_b64_tr_b16 v[172:173], v215 offset:16384
ds_read_b64_tr_b16 v[174:175], v215 offset:20480
v_mfma_f32_16x16x32_bf16 v[28:31], v[188:191], a[12:15], v[28:31]
v_mul_f32 v65, v65, v97
s_waitcnt lgkmcnt(8)
v_mfma_f32_16x16x32_bf16 v[32:35], v[192:195], a[8:11], v[32:35]
ds_read_b64_tr_b16 v[176:177], v212 offset:18432
ds_read_b64_tr_b16 v[178:179], v212 offset:22528
v_mfma_f32_16x16x32_bf16 v[48:51], v[192:195], a[12:15], v[48:51]
buffer_load_dwordx4 v233, s[40:43], s17 offen offset:1024 lds
v_mfma_f32_16x16x32_bf16 v[36:39], v[196:199], a[8:11], v[36:39]
ds_read_b64_tr_b16 v[180:181], v213 offset:18432
ds_read_b64_tr_b16 v[182:183], v213 offset:22528
v_mul_f32 v245, v66, v66
v_mfma_f32_16x16x32_bf16 v[52:55], v[196:199], a[12:15], v[52:55]
v_exp_f32 v244, v244
v_mfma_f32_16x16x32_bf16 v[40:43], v[200:203], a[8:11], v[40:43]
ds_read_b64_tr_b16 v[184:185], v214 offset:18432
ds_read_b64_tr_b16 v[186:187], v214 offset:22528
v_mfma_f32_16x16x32_bf16 v[56:59], v[200:203], a[12:15], v[56:59]
buffer_load_dwordx4 v232, s[44:47], s17 offen offset:2048 lds
v_mfma_f32_16x16x32_bf16 v[44:47], v[204:207], a[8:11], v[44:47]
ds_read_b64_tr_b16 v[188:189], v215 offset:18432
ds_read_b64_tr_b16 v[190:191], v215 offset:22528
v_mfma_f32_16x16x32_bf16 v[60:63], v[204:207], a[12:15], v[60:63]
v_add_u32 v232, 3670016, v232
s_waitcnt lgkmcnt(8)
v_mfma_f32_16x16x32_bf16 v[0:3], v[160:163], a[16:19], v[0:3]
ds_read_b64_tr_b16 v[192:193], v212 offset:24576
ds_read_b64_tr_b16 v[194:195], v212 offset:28672
v_add_f32 v244, 1.0, v244
v_mfma_f32_16x16x32_bf16 v[16:19], v[160:163], a[20:23], v[16:19]
v_rcp_f32 v244, v244
v_mfma_f32_16x16x32_bf16 v[4:7], v[164:167], a[16:19], v[4:7]
ds_read_b64_tr_b16 v[196:197], v213 offset:24576
ds_read_b64_tr_b16 v[198:199], v213 offset:28672
v_mfma_f32_16x16x32_bf16 v[20:23], v[164:167], a[20:23], v[20:23]
v_mul_f32 v65, v65, v244
v_cvt_pk_bf16_f32 v96, v64, v65
v_mfma_f32_16x16x32_bf16 v[8:11], v[168:171], a[16:19], v[8:11]
ds_read_b64_tr_b16 v[200:201], v214 offset:24576
ds_read_b64_tr_b16 v[202:203], v214 offset:28672
v_mfma_f32_16x16x32_bf16 v[24:27], v[168:171], a[20:23], v[24:27]
buffer_load_dwordx4 v233, s[44:47], s17 offen offset:3072 lds
v_mfma_f32_16x16x32_bf16 v[12:15], v[172:175], a[16:19], v[12:15]
ds_read_b64_tr_b16 v[204:205], v215 offset:24576
ds_read_b64_tr_b16 v[206:207], v215 offset:28672
v_fmaak_f32 v245, v245, v223, 0xc0135761
v_mfma_f32_16x16x32_bf16 v[28:31], v[172:175], a[20:23], v[28:31]
v_add_u32 v233, 3670016, v233
s_waitcnt lgkmcnt(8)
v_mfma_f32_16x16x32_bf16 v[32:35], v[176:179], a[16:19], v[32:35]
ds_read_b64_tr_b16 v[160:161], v212 offset:26624
ds_read_b64_tr_b16 v[162:163], v212 offset:30720
v_mfma_f32_16x16x32_bf16 v[48:51], v[176:179], a[20:23], v[48:51]
v_mul_f32 v245, v245, v66
v_mul_f32 v66, v66, v98
v_mfma_f32_16x16x32_bf16 v[36:39], v[180:183], a[16:19], v[36:39]
ds_read_b64_tr_b16 v[164:165], v213 offset:26624
ds_read_b64_tr_b16 v[166:167], v213 offset:30720
v_mfma_f32_16x16x32_bf16 v[52:55], v[180:183], a[20:23], v[52:55]
v_exp_f32 v245, v245
v_mfma_f32_16x16x32_bf16 v[40:43], v[184:187], a[16:19], v[40:43]
ds_read_b64_tr_b16 v[168:169], v214 offset:26624
ds_read_b64_tr_b16 v[170:171], v214 offset:30720
v_add_f32 v245, 1.0, v245
v_mfma_f32_16x16x32_bf16 v[56:59], v[184:187], a[20:23], v[56:59]
s_add_u32 m0, s77, 49152
v_mul_f32 v246, v67, v67
v_mfma_f32_16x16x32_bf16 v[44:47], v[188:191], a[16:19], v[44:47]
ds_read_b64_tr_b16 v[172:173], v215 offset:26624
ds_read_b64_tr_b16 v[174:175], v215 offset:30720
v_mfma_f32_16x16x32_bf16 v[60:63], v[188:191], a[20:23], v[60:63]
s_waitcnt vmcnt(22) lgkmcnt(8)
s_barrier
v_mfma_f32_16x16x32_bf16 v[0:3], v[192:195], a[24:27], v[0:3]
ds_read_b64_tr_b16 v[176:177], v208 offset:0
ds_read_b64_tr_b16 v[178:179], v208 offset:4096
v_mfma_f32_16x16x32_bf16 v[16:19], v[192:195], a[28:31], v[16:19]
buffer_load_dwordx4 v232, s[40:43], s10 offen offset:0 lds
v_mfma_f32_16x16x32_bf16 v[4:7], v[196:199], a[24:27], v[4:7]
ds_read_b64_tr_b16 v[180:181], v209 offset:0
ds_read_b64_tr_b16 v[182:183], v209 offset:4096
v_mfma_f32_16x16x32_bf16 v[20:23], v[196:199], a[28:31], v[20:23]
v_rcp_f32 v245, v245
v_mfma_f32_16x16x32_bf16 v[8:11], v[200:203], a[24:27], v[8:11]
ds_read_b64_tr_b16 v[184:185], v210 offset:0
ds_read_b64_tr_b16 v[186:187], v210 offset:4096
v_mul_f32 v66, v66, v245
v_mfma_f32_16x16x32_bf16 v[24:27], v[200:203], a[28:31], v[24:27]
v_fmaak_f32 v246, v246, v223, 0xc0135761
v_mul_f32 v246, v246, v67
v_mfma_f32_16x16x32_bf16 v[12:15], v[204:207], a[24:27], v[12:15]
ds_read_b64_tr_b16 v[188:189], v211 offset:0
ds_read_b64_tr_b16 v[190:191], v211 offset:4096
v_mfma_f32_16x16x32_bf16 v[28:31], v[204:207], a[28:31], v[28:31]
v_mul_f32 v67, v67, v99
s_waitcnt lgkmcnt(8)
v_mfma_f32_16x16x32_bf16 v[32:35], v[160:163], a[24:27], v[32:35]
ds_read_b64_tr_b16 v[192:193], v208 offset:2048
ds_read_b64_tr_b16 v[194:195], v208 offset:6144
v_mfma_f32_16x16x32_bf16 v[48:51], v[160:163], a[28:31], v[48:51]
buffer_load_dwordx4 v233, s[40:43], s10 offen offset:1024 lds
v_mfma_f32_16x16x32_bf16 v[36:39], v[164:167], a[24:27], v[36:39]
ds_read_b64_tr_b16 v[196:197], v209 offset:2048
ds_read_b64_tr_b16 v[198:199], v209 offset:6144
v_mul_f32 v247, v68, v68
v_mfma_f32_16x16x32_bf16 v[52:55], v[164:167], a[28:31], v[52:55]
v_exp_f32 v246, v246
v_mfma_f32_16x16x32_bf16 v[40:43], v[168:171], a[24:27], v[40:43]
ds_read_b64_tr_b16 v[200:201], v210 offset:2048
ds_read_b64_tr_b16 v[202:203], v210 offset:6144
v_mfma_f32_16x16x32_bf16 v[56:59], v[168:171], a[28:31], v[56:59]
buffer_load_dwordx4 v232, s[44:47], s10 offen offset:2048 lds
v_mfma_f32_16x16x32_bf16 v[44:47], v[172:175], a[24:27], v[44:47]
ds_read_b64_tr_b16 v[204:205], v211 offset:2048
ds_read_b64_tr_b16 v[206:207], v211 offset:6144
v_mfma_f32_16x16x32_bf16 v[60:63], v[172:175], a[28:31], v[60:63]
v_add_f32 v246, 1.0, v246
s_waitcnt lgkmcnt(8)
v_mfma_f32_16x16x32_bf16 v[0:3], v[176:179], a[32:35], v[0:3]
ds_read_b64_tr_b16 v[160:161], v208 offset:8192
ds_read_b64_tr_b16 v[162:163], v208 offset:12288
v_fmaak_f32 v247, v247, v223, 0xc0135761
v_mfma_f32_16x16x32_bf16 v[16:19], v[176:179], a[36:39], v[16:19]
v_rcp_f32 v246, v246
v_mfma_f32_16x16x32_bf16 v[4:7], v[180:183], a[32:35], v[4:7]
ds_read_b64_tr_b16 v[164:165], v209 offset:8192
ds_read_b64_tr_b16 v[166:167], v209 offset:12288
v_mfma_f32_16x16x32_bf16 v[20:23], v[180:183], a[36:39], v[20:23]
v_mul_f32 v67, v67, v246
v_cvt_pk_bf16_f32 v97, v66, v67
v_mfma_f32_16x16x32_bf16 v[8:11], v[184:187], a[32:35], v[8:11]
ds_read_b64_tr_b16 v[168:169], v210 offset:8192
ds_read_b64_tr_b16 v[170:171], v210 offset:12288
v_mfma_f32_16x16x32_bf16 v[24:27], v[184:187], a[36:39], v[24:27]
buffer_load_dwordx4 v233, s[44:47], s10 offen offset:3072 lds
v_mfma_f32_16x16x32_bf16 v[12:15], v[188:191], a[32:35], v[12:15]
ds_read_b64_tr_b16 v[172:173], v211 offset:8192
ds_read_b64_tr_b16 v[174:175], v211 offset:12288
v_mul_f32 v247, v247, v68
v_mfma_f32_16x16x32_bf16 v[28:31], v[188:191], a[36:39], v[28:31]
s_add_u32 m0, s77, 81920
s_waitcnt lgkmcnt(8)
v_mfma_f32_16x16x32_bf16 v[32:35], v[192:195], a[32:35], v[32:35]
ds_read_b64_tr_b16 v[176:177], v208 offset:10240
ds_read_b64_tr_b16 v[178:179], v208 offset:14336
v_mfma_f32_16x16x32_bf16 v[48:51], v[192:195], a[36:39], v[48:51]
v_exp_f32 v247, v247
v_mfma_f32_16x16x32_bf16 v[36:39], v[196:199], a[32:35], v[36:39]
ds_read_b64_tr_b16 v[180:181], v209 offset:10240
ds_read_b64_tr_b16 v[182:183], v209 offset:14336
v_mfma_f32_16x16x32_bf16 v[52:55], v[196:199], a[36:39], v[52:55]
v_mul_f32 v68, v68, v100
v_add_f32 v247, 1.0, v247
v_mfma_f32_16x16x32_bf16 v[40:43], v[200:203], a[32:35], v[40:43]
ds_read_b64_tr_b16 v[184:185], v210 offset:10240
ds_read_b64_tr_b16 v[186:187], v210 offset:14336
v_mul_f32 v248, v69, v69
v_mfma_f32_16x16x32_bf16 v[56:59], v[200:203], a[36:39], v[56:59]
v_rcp_f32 v247, v247
v_mfma_f32_16x16x32_bf16 v[44:47], v[204:207], a[32:35], v[44:47]
ds_read_b64_tr_b16 v[188:189], v211 offset:10240
ds_read_b64_tr_b16 v[190:191], v211 offset:14336
v_mfma_f32_16x16x32_bf16 v[60:63], v[204:207], a[36:39], v[60:63]
s_waitcnt vmcnt(21) lgkmcnt(8)
s_barrier
v_mfma_f32_16x16x32_bf16 v[0:3], v[160:163], a[40:43], v[0:3]
ds_read_b64_tr_b16 v[192:193], v212 offset:32768
ds_read_b64_tr_b16 v[194:195], v212 offset:36864
v_mfma_f32_16x16x32_bf16 v[16:19], v[160:163], a[44:47], v[16:19]
LC15_%=:
s_cbranch_execz LC19_%=
buffer_load_dwordx4 v232, s[40:43], s11 offen offset:0 lds
v_mfma_f32_16x16x32_bf16 v[4:7], v[164:167], a[40:43], v[4:7]
ds_read_b64_tr_b16 v[196:197], v213 offset:32768
ds_read_b64_tr_b16 v[198:199], v213 offset:36864
v_mfma_f32_16x16x32_bf16 v[20:23], v[164:167], a[44:47], v[20:23]
v_mul_f32 v68, v68, v247
v_fmaak_f32 v248, v248, v223, 0xc0135761
v_mfma_f32_16x16x32_bf16 v[8:11], v[168:171], a[40:43], v[8:11]
ds_read_b64_tr_b16 v[200:201], v214 offset:32768
ds_read_b64_tr_b16 v[202:203], v214 offset:36864
v_mul_f32 v248, v248, v69
v_mfma_f32_16x16x32_bf16 v[24:27], v[168:171], a[44:47], v[24:27]
v_exp_f32 v248, v248
v_mfma_f32_16x16x32_bf16 v[12:15], v[172:175], a[40:43], v[12:15]
ds_read_b64_tr_b16 v[204:205], v215 offset:32768
ds_read_b64_tr_b16 v[206:207], v215 offset:36864
v_mfma_f32_16x16x32_bf16 v[28:31], v[172:175], a[44:47], v[28:31]
v_mul_f32 v69, v69, v101
s_waitcnt lgkmcnt(8)
v_mfma_f32_16x16x32_bf16 v[32:35], v[176:179], a[40:43], v[32:35]
ds_read_b64_tr_b16 v[160:161], v212 offset:34816
ds_read_b64_tr_b16 v[162:163], v212 offset:38912
v_mfma_f32_16x16x32_bf16 v[48:51], v[176:179], a[44:47], v[48:51]
buffer_load_dwordx4 v233, s[40:43], s11 offen offset:1024 lds
v_mfma_f32_16x16x32_bf16 v[36:39], v[180:183], a[40:43], v[36:39]
ds_read_b64_tr_b16 v[164:165], v213 offset:34816
ds_read_b64_tr_b16 v[166:167], v213 offset:38912
v_add_f32 v248, 1.0, v248
)"
R"(
v_mfma_f32_16x16x32_bf16 v[52:55], v[180:183], a[44:47], v[52:55]
v_rcp_f32 v248, v248
v_mfma_f32_16x16x32_bf16 v[40:43], v[184:187], a[40:43], v[40:43]
ds_read_b64_tr_b16 v[168:169], v214 offset:34816
ds_read_b64_tr_b16 v[170:171], v214 offset:38912
v_mfma_f32_16x16x32_bf16 v[56:59], v[184:187], a[44:47], v[56:59]
buffer_load_dwordx4 v232, s[44:47], s11 offen offset:2048 lds
v_mfma_f32_16x16x32_bf16 v[44:47], v[188:191], a[40:43], v[44:47]
ds_read_b64_tr_b16 v[172:173], v215 offset:34816
ds_read_b64_tr_b16 v[174:175], v215 offset:38912
v_mfma_f32_16x16x32_bf16 v[60:63], v[188:191], a[44:47], v[60:63]
v_mul_f32 v69, v69, v248
s_waitcnt lgkmcnt(8)
v_mfma_f32_16x16x32_bf16 v[0:3], v[192:195], a[48:51], v[0:3]
ds_read_b64_tr_b16 v[176:177], v212 offset:40960
ds_read_b64_tr_b16 v[178:179], v212 offset:45056
v_cvt_pk_bf16_f32 v98, v68, v69
v_mfma_f32_16x16x32_bf16 v[16:19], v[192:195], a[52:55], v[16:19]
v_mul_f32 v249, v70, v70
v_fmaak_f32 v249, v249, v223, 0xc0135761
v_mfma_f32_16x16x32_bf16 v[4:7], v[196:199], a[48:51], v[4:7]
ds_read_b64_tr_b16 v[180:181], v213 offset:40960
ds_read_b64_tr_b16 v[182:183], v213 offset:45056
v_mfma_f32_16x16x32_bf16 v[20:23], v[196:199], a[52:55], v[20:23]
v_mul_f32 v249, v249, v70
v_mul_f32 v70, v70, v102
v_mfma_f32_16x16x32_bf16 v[8:11], v[200:203], a[48:51], v[8:11]
ds_read_b64_tr_b16 v[184:185], v214 offset:40960
ds_read_b64_tr_b16 v[186:187], v214 offset:45056
v_mfma_f32_16x16x32_bf16 v[24:27], v[200:203], a[52:55], v[24:27]
buffer_load_dwordx4 v233, s[44:47], s11 offen offset:3072 lds
v_mfma_f32_16x16x32_bf16 v[12:15], v[204:207], a[48:51], v[12:15]
ds_read_b64_tr_b16 v[188:189], v215 offset:40960
ds_read_b64_tr_b16 v[190:191], v215 offset:45056
v_mul_f32 v250, v71, v71
v_mfma_f32_16x16x32_bf16 v[28:31], v[204:207], a[52:55], v[28:31]
s_add_u32 m0, s77, 0
s_waitcnt lgkmcnt(8)
v_mfma_f32_16x16x32_bf16 v[32:35], v[160:163], a[48:51], v[32:35]
ds_read_b64_tr_b16 v[192:193], v212 offset:43008
ds_read_b64_tr_b16 v[194:195], v212 offset:47104
v_mfma_f32_16x16x32_bf16 v[48:51], v[160:163], a[52:55], v[48:51]
v_exp_f32 v249, v249
v_mfma_f32_16x16x32_bf16 v[36:39], v[164:167], a[48:51], v[36:39]
ds_read_b64_tr_b16 v[196:197], v213 offset:43008
ds_read_b64_tr_b16 v[198:199], v213 offset:47104
v_mfma_f32_16x16x32_bf16 v[52:55], v[164:167], a[52:55], v[52:55]
v_add_f32 v249, 1.0, v249
v_fmaak_f32 v250, v250, v223, 0xc0135761
v_mfma_f32_16x16x32_bf16 v[40:43], v[168:171], a[48:51], v[40:43]
ds_read_b64_tr_b16 v[200:201], v214 offset:43008
ds_read_b64_tr_b16 v[202:203], v214 offset:47104
v_mfma_f32_16x16x32_bf16 v[56:59], v[168:171], a[52:55], v[56:59]
v_rcp_f32 v249, v249
v_mfma_f32_16x16x32_bf16 v[44:47], v[172:175], a[48:51], v[44:47]
ds_read_b64_tr_b16 v[204:205], v215 offset:43008
ds_read_b64_tr_b16 v[206:207], v215 offset:47104
v_mul_f32 v70, v70, v249
v_mfma_f32_16x16x32_bf16 v[60:63], v[172:175], a[52:55], v[60:63]
s_waitcnt vmcnt(21) lgkmcnt(8)
s_barrier
v_mfma_f32_16x16x32_bf16 v[0:3], v[176:179], a[56:59], v[0:3]
ds_read_b64_tr_b16 v[160:161], v208 offset:16384
ds_read_b64_tr_b16 v[162:163], v208 offset:20480
v_mfma_f32_16x16x32_bf16 v[16:19], v[176:179], a[60:63], v[16:19]
buffer_load_dwordx4 v232, s[40:43], s12 offen offset:0 lds
v_mfma_f32_16x16x32_bf16 v[4:7], v[180:183], a[56:59], v[4:7]
ds_read_b64_tr_b16 v[164:165], v209 offset:16384
ds_read_b64_tr_b16 v[166:167], v209 offset:20480
v_mfma_f32_16x16x32_bf16 v[20:23], v[180:183], a[60:63], v[20:23]
v_mul_f32 v250, v250, v71
v_mul_f32 v71, v71, v103
v_mfma_f32_16x16x32_bf16 v[8:11], v[184:187], a[56:59], v[8:11]
ds_read_b64_tr_b16 v[168:169], v210 offset:16384
ds_read_b64_tr_b16 v[170:171], v210 offset:20480
v_mul_f32 v251, v72, v72
v_mfma_f32_16x16x32_bf16 v[24:27], v[184:187], a[60:63], v[24:27]
v_exp_f32 v250, v250
v_mfma_f32_16x16x32_bf16 v[12:15], v[188:191], a[56:59], v[12:15]
ds_read_b64_tr_b16 v[172:173], v211 offset:16384
ds_read_b64_tr_b16 v[174:175], v211 offset:20480
v_mfma_f32_16x16x32_bf16 v[28:31], v[188:191], a[60:63], v[28:31]
v_add_f32 v250, 1.0, v250
s_waitcnt lgkmcnt(8)
v_mfma_f32_16x16x32_bf16 v[32:35], v[192:195], a[56:59], v[32:35]
ds_read_b64_tr_b16 v[176:177], v208 offset:18432
ds_read_b64_tr_b16 v[178:179], v208 offset:22528
v_mfma_f32_16x16x32_bf16 v[48:51], v[192:195], a[60:63], v[48:51]
buffer_load_dwordx4 v233, s[40:43], s12 offen offset:1024 lds
v_mfma_f32_16x16x32_bf16 v[36:39], v[196:199], a[56:59], v[36:39]
ds_read_b64_tr_b16 v[180:181], v209 offset:18432
ds_read_b64_tr_b16 v[182:183], v209 offset:22528
v_fmaak_f32 v251, v251, v223, 0xc0135761
v_mfma_f32_16x16x32_bf16 v[52:55], v[196:199], a[60:63], v[52:55]
v_rcp_f32 v250, v250
v_mfma_f32_16x16x32_bf16 v[40:43], v[200:203], a[56:59], v[40:43]
ds_read_b64_tr_b16 v[184:185], v210 offset:18432
ds_read_b64_tr_b16 v[186:187], v210 offset:22528
v_mfma_f32_16x16x32_bf16 v[56:59], v[200:203], a[60:63], v[56:59]
buffer_load_dwordx4 v232, s[44:47], s12 offen offset:2048 lds
v_mfma_f32_16x16x32_bf16 v[44:47], v[204:207], a[56:59], v[44:47]
ds_read_b64_tr_b16 v[188:189], v211 offset:18432
ds_read_b64_tr_b16 v[190:191], v211 offset:22528
v_mfma_f32_16x16x32_bf16 v[60:63], v[204:207], a[60:63], v[60:63]
v_mul_f32 v71, v71, v250
s_waitcnt lgkmcnt(8)
v_mfma_f32_16x16x32_bf16 v[0:3], v[160:163], a[64:67], v[0:3]
ds_read_b64_tr_b16 v[192:193], v208 offset:24576
ds_read_b64_tr_b16 v[194:195], v208 offset:28672
v_cvt_pk_bf16_f32 v99, v70, v71
v_mfma_f32_16x16x32_bf16 v[16:19], v[160:163], a[68:71], v[16:19]
buffer_store_dwordx4 v[96:99], v220, s[52:55], s92 offen offset:2048 sc1
v_mfma_f32_16x16x32_bf16 v[4:7], v[164:167], a[64:67], v[4:7]
ds_read_b64_tr_b16 v[196:197], v209 offset:24576
ds_read_b64_tr_b16 v[198:199], v209 offset:28672
v_mfma_f32_16x16x32_bf16 v[20:23], v[164:167], a[68:71], v[20:23]
v_mul_f32 v251, v251, v72
v_mul_f32 v72, v72, v104
v_mfma_f32_16x16x32_bf16 v[8:11], v[168:171], a[64:67], v[8:11]
ds_read_b64_tr_b16 v[200:201], v210 offset:24576
ds_read_b64_tr_b16 v[202:203], v210 offset:28672
v_mfma_f32_16x16x32_bf16 v[24:27], v[168:171], a[68:71], v[24:27]
buffer_load_dwordx4 v233, s[44:47], s12 offen offset:3072 lds
v_mfma_f32_16x16x32_bf16 v[12:15], v[172:175], a[64:67], v[12:15]
ds_read_b64_tr_b16 v[204:205], v211 offset:24576
ds_read_b64_tr_b16 v[206:207], v211 offset:28672
v_mul_f32 v252, v73, v73
v_mfma_f32_16x16x32_bf16 v[28:31], v[172:175], a[68:71], v[28:31]
s_add_u32 m0, s77, 98304
s_waitcnt lgkmcnt(8)
v_mfma_f32_16x16x32_bf16 v[32:35], v[176:179], a[64:67], v[32:35]
ds_read_b64_tr_b16 v[160:161], v208 offset:26624
ds_read_b64_tr_b16 v[162:163], v208 offset:30720
v_mfma_f32_16x16x32_bf16 v[48:51], v[176:179], a[68:71], v[48:51]
v_exp_f32 v251, v251
v_mfma_f32_16x16x32_bf16 v[36:39], v[180:183], a[64:67], v[36:39]
ds_read_b64_tr_b16 v[164:165], v209 offset:26624
ds_read_b64_tr_b16 v[166:167], v209 offset:30720
v_mfma_f32_16x16x32_bf16 v[52:55], v[180:183], a[68:71], v[52:55]
v_add_f32 v251, 1.0, v251
v_fmaak_f32 v252, v252, v223, 0xc0135761
v_mfma_f32_16x16x32_bf16 v[40:43], v[184:187], a[64:67], v[40:43]
ds_read_b64_tr_b16 v[168:169], v210 offset:26624
ds_read_b64_tr_b16 v[170:171], v210 offset:30720
v_mfma_f32_16x16x32_bf16 v[56:59], v[184:187], a[68:71], v[56:59]
v_rcp_f32 v251, v251
v_mfma_f32_16x16x32_bf16 v[44:47], v[188:191], a[64:67], v[44:47]
ds_read_b64_tr_b16 v[172:173], v211 offset:26624
ds_read_b64_tr_b16 v[174:175], v211 offset:30720
v_mul_f32 v72, v72, v251
v_mfma_f32_16x16x32_bf16 v[60:63], v[188:191], a[68:71], v[60:63]
s_waitcnt vmcnt(22) lgkmcnt(8)
s_barrier
v_mfma_f32_16x16x32_bf16 v[0:3], v[192:195], a[72:75], v[0:3]
ds_read_b64_tr_b16 v[176:177], v212 offset:49152
ds_read_b64_tr_b16 v[178:179], v212 offset:53248
v_mfma_f32_16x16x32_bf16 v[16:19], v[192:195], a[76:79], v[16:19]
buffer_load_dwordx4 v232, s[40:43], s13 offen offset:0 lds
v_mfma_f32_16x16x32_bf16 v[4:7], v[196:199], a[72:75], v[4:7]
ds_read_b64_tr_b16 v[180:181], v213 offset:49152
ds_read_b64_tr_b16 v[182:183], v213 offset:53248
v_mfma_f32_16x16x32_bf16 v[20:23], v[196:199], a[76:79], v[20:23]
v_mul_f32 v252, v252, v73
v_mul_f32 v73, v73, v105
v_mfma_f32_16x16x32_bf16 v[8:11], v[200:203], a[72:75], v[8:11]
ds_read_b64_tr_b16 v[184:185], v214 offset:49152
ds_read_b64_tr_b16 v[186:187], v214 offset:53248
v_mul_f32 v253, v74, v74
v_mfma_f32_16x16x32_bf16 v[24:27], v[200:203], a[76:79], v[24:27]
v_exp_f32 v252, v252
v_mfma_f32_16x16x32_bf16 v[12:15], v[204:207], a[72:75], v[12:15]
ds_read_b64_tr_b16 v[188:189], v215 offset:49152
ds_read_b64_tr_b16 v[190:191], v215 offset:53248
v_mfma_f32_16x16x32_bf16 v[28:31], v[204:207], a[76:79], v[28:31]
v_add_f32 v252, 1.0, v252
s_waitcnt lgkmcnt(8)
v_mfma_f32_16x16x32_bf16 v[32:35], v[160:163], a[72:75], v[32:35]
ds_read_b64_tr_b16 v[192:193], v212 offset:51200
ds_read_b64_tr_b16 v[194:195], v212 offset:55296
v_mfma_f32_16x16x32_bf16 v[48:51], v[160:163], a[76:79], v[48:51]
buffer_load_dwordx4 v233, s[40:43], s13 offen offset:1024 lds
v_mfma_f32_16x16x32_bf16 v[36:39], v[164:167], a[72:75], v[36:39]
ds_read_b64_tr_b16 v[196:197], v213 offset:51200
ds_read_b64_tr_b16 v[198:199], v213 offset:55296
v_fmaak_f32 v253, v253, v223, 0xc0135761
v_mfma_f32_16x16x32_bf16 v[52:55], v[164:167], a[76:79], v[52:55]
v_rcp_f32 v252, v252
v_mfma_f32_16x16x32_bf16 v[40:43], v[168:171], a[72:75], v[40:43]
ds_read_b64_tr_b16 v[200:201], v214 offset:51200
ds_read_b64_tr_b16 v[202:203], v214 offset:55296
v_mfma_f32_16x16x32_bf16 v[56:59], v[168:171], a[76:79], v[56:59]
buffer_load_dwordx4 v232, s[44:47], s13 offen offset:2048 lds
v_mfma_f32_16x16x32_bf16 v[44:47], v[172:175], a[72:75], v[44:47]
ds_read_b64_tr_b16 v[204:205], v215 offset:51200
ds_read_b64_tr_b16 v[206:207], v215 offset:55296
v_mfma_f32_16x16x32_bf16 v[60:63], v[172:175], a[76:79], v[60:63]
v_mul_f32 v73, v73, v252
s_waitcnt lgkmcnt(8)
v_mfma_f32_16x16x32_bf16 v[0:3], v[176:179], a[80:83], v[0:3]
ds_read_b64_tr_b16 v[160:161], v212 offset:57344
ds_read_b64_tr_b16 v[162:163], v212 offset:61440
v_cvt_pk_bf16_f32 v104, v72, v73
v_mfma_f32_16x16x32_bf16 v[16:19], v[176:179], a[84:87], v[16:19]
v_mul_f32 v253, v253, v74
v_mul_f32 v74, v74, v106
v_mfma_f32_16x16x32_bf16 v[4:7], v[180:183], a[80:83], v[4:7]
ds_read_b64_tr_b16 v[164:165], v213 offset:57344
ds_read_b64_tr_b16 v[166:167], v213 offset:61440
v_mfma_f32_16x16x32_bf16 v[20:23], v[180:183], a[84:87], v[20:23]
v_exp_f32 v253, v253
v_mfma_f32_16x16x32_bf16 v[8:11], v[184:187], a[80:83], v[8:11]
ds_read_b64_tr_b16 v[168:169], v214 offset:57344
ds_read_b64_tr_b16 v[170:171], v214 offset:61440
v_mfma_f32_16x16x32_bf16 v[24:27], v[184:187], a[84:87], v[24:27]
buffer_load_dwordx4 v233, s[44:47], s13 offen offset:3072 lds
v_mfma_f32_16x16x32_bf16 v[12:15], v[188:191], a[80:83], v[12:15]
ds_read_b64_tr_b16 v[172:173], v215 offset:57344
ds_read_b64_tr_b16 v[174:175], v215 offset:61440
v_add_f32 v253, 1.0, v253
v_mfma_f32_16x16x32_bf16 v[28:31], v[188:191], a[84:87], v[28:31]
v_mul_f32 v254, v75, v75
s_waitcnt lgkmcnt(8)
v_mfma_f32_16x16x32_bf16 v[32:35], v[192:195], a[80:83], v[32:35]
ds_read_b64_tr_b16 v[176:177], v212 offset:59392
ds_read_b64_tr_b16 v[178:179], v212 offset:63488
v_mfma_f32_16x16x32_bf16 v[48:51], v[192:195], a[84:87], v[48:51]
v_rcp_f32 v253, v253
v_mfma_f32_16x16x32_bf16 v[36:39], v[196:199], a[80:83], v[36:39]
ds_read_b64_tr_b16 v[180:181], v213 offset:59392
ds_read_b64_tr_b16 v[182:183], v213 offset:63488
v_mfma_f32_16x16x32_bf16 v[52:55], v[196:199], a[84:87], v[52:55]
v_mul_f32 v74, v74, v253
v_fmaak_f32 v254, v254, v223, 0xc0135761
v_mfma_f32_16x16x32_bf16 v[40:43], v[200:203], a[80:83], v[40:43]
ds_read_b64_tr_b16 v[184:185], v214 offset:59392
ds_read_b64_tr_b16 v[186:187], v214 offset:63488
v_mfma_f32_16x16x32_bf16 v[56:59], v[200:203], a[84:87], v[56:59]
v_mul_f32 v254, v254, v75
v_mul_f32 v75, v75, v107
v_mfma_f32_16x16x32_bf16 v[44:47], v[204:207], a[80:83], v[44:47]
ds_read_b64_tr_b16 v[188:189], v215 offset:59392
ds_read_b64_tr_b16 v[190:191], v215 offset:63488
v_mul_f32 v243, v76, v76
v_mfma_f32_16x16x32_bf16 v[60:63], v[204:207], a[84:87], v[60:63]
s_waitcnt vmcnt(21) lgkmcnt(8)
s_barrier
v_mfma_f32_16x16x32_bf16 v[0:3], v[160:163], a[88:91], v[0:3]
ds_read_b64_tr_b16 v[192:193], v208 offset:32768
ds_read_b64_tr_b16 v[194:195], v208 offset:36864
v_mfma_f32_16x16x32_bf16 v[16:19], v[160:163], a[92:95], v[16:19]
s_mov_b64 exec, 1
buffer_store_dword v234, off, s[52:55], s79 sc1
s_mov_b64 exec, -1
v_mfma_f32_16x16x32_bf16 v[4:7], v[164:167], a[88:91], v[4:7]
ds_read_b64_tr_b16 v[196:197], v209 offset:32768
ds_read_b64_tr_b16 v[198:199], v209 offset:36864
v_mfma_f32_16x16x32_bf16 v[20:23], v[164:167], a[92:95], v[20:23]
s_add_u32 s79, s79, 2048
v_fmaak_f32 v243, v243, v223, 0xc0135761
v_mfma_f32_16x16x32_bf16 v[8:11], v[168:171], a[88:91], v[8:11]
ds_read_b64_tr_b16 v[200:201], v210 offset:32768
ds_read_b64_tr_b16 v[202:203], v210 offset:36864
v_mul_f32 v243, v243, v76
v_mfma_f32_16x16x32_bf16 v[24:27], v[168:171], a[92:95], v[24:27]
v_exp_f32 v254, v254
v_mfma_f32_16x16x32_bf16 v[12:15], v[172:175], a[88:91], v[12:15]
ds_read_b64_tr_b16 v[204:205], v211 offset:32768
ds_read_b64_tr_b16 v[206:207], v211 offset:36864
v_mfma_f32_16x16x32_bf16 v[28:31], v[172:175], a[92:95], v[28:31]
s_add_u32 m0, s77, 16384
s_waitcnt lgkmcnt(8)
v_mfma_f32_16x16x32_bf16 v[32:35], v[176:179], a[88:91], v[32:35]
ds_read_b64_tr_b16 v[160:161], v208 offset:34816
ds_read_b64_tr_b16 v[162:163], v208 offset:38912
v_mfma_f32_16x16x32_bf16 v[48:51], v[176:179], a[92:95], v[48:51]
buffer_load_dwordx4 v232, s[40:43], s14 offen offset:0 lds
v_mfma_f32_16x16x32_bf16 v[36:39], v[180:183], a[88:91], v[36:39]
ds_read_b64_tr_b16 v[164:165], v209 offset:34816
ds_read_b64_tr_b16 v[166:167], v209 offset:38912
v_add_f32 v254, 1.0, v254
v_mfma_f32_16x16x32_bf16 v[52:55], v[180:183], a[92:95], v[52:55]
v_rcp_f32 v254, v254
v_mfma_f32_16x16x32_bf16 v[40:43], v[184:187], a[88:91], v[40:43]
ds_read_b64_tr_b16 v[168:169], v210 offset:34816
ds_read_b64_tr_b16 v[170:171], v210 offset:38912
v_mfma_f32_16x16x32_bf16 v[56:59], v[184:187], a[92:95], v[56:59]
buffer_load_dwordx4 v233, s[40:43], s14 offen offset:1024 lds
v_mfma_f32_16x16x32_bf16 v[44:47], v[188:191], a[88:91], v[44:47]
ds_read_b64_tr_b16 v[172:173], v211 offset:34816
ds_read_b64_tr_b16 v[174:175], v211 offset:38912
v_mfma_f32_16x16x32_bf16 v[60:63], v[188:191], a[92:95], v[60:63]
v_mul_f32 v75, v75, v254
s_waitcnt lgkmcnt(8)
v_mfma_f32_16x16x32_bf16 v[0:3], v[192:195], a[96:99], v[0:3]
ds_read_b64_tr_b16 v[176:177], v208 offset:40960
ds_read_b64_tr_b16 v[178:179], v208 offset:45056
v_cvt_pk_bf16_f32 v105, v74, v75
v_mfma_f32_16x16x32_bf16 v[16:19], v[192:195], a[100:103], v[16:19]
v_exp_f32 v243, v243
v_mfma_f32_16x16x32_bf16 v[4:7], v[196:199], a[96:99], v[4:7]
ds_read_b64_tr_b16 v[180:181], v209 offset:40960
ds_read_b64_tr_b16 v[182:183], v209 offset:45056
v_mfma_f32_16x16x32_bf16 v[20:23], v[196:199], a[100:103], v[20:23]
v_mul_f32 v76, v76, v108
v_add_f32 v243, 1.0, v243
v_mfma_f32_16x16x32_bf16 v[8:11], v[200:203], a[96:99], v[8:11]
ds_read_b64_tr_b16 v[184:185], v210 offset:40960
ds_read_b64_tr_b16 v[186:187], v210 offset:45056
v_mfma_f32_16x16x32_bf16 v[24:27], v[200:203], a[100:103], v[24:27]
buffer_load_dwordx4 v232, s[44:47], s14 offen offset:2048 lds
v_mfma_f32_16x16x32_bf16 v[12:15], v[204:207], a[96:99], v[12:15]
ds_read_b64_tr_b16 v[188:189], v211 offset:40960
ds_read_b64_tr_b16 v[190:191], v211 offset:45056
v_mfma_f32_16x16x32_bf16 v[28:31], v[204:207], a[100:103], v[28:31]
v_mul_f32 v244, v77, v77
s_waitcnt lgkmcnt(8)
v_mfma_f32_16x16x32_bf16 v[32:35], v[160:163], a[96:99], v[32:35]
ds_read_b64_tr_b16 v[192:193], v208 offset:43008
ds_read_b64_tr_b16 v[194:195], v208 offset:47104
v_fmaak_f32 v244, v244, v223, 0xc0135761
v_mfma_f32_16x16x32_bf16 v[48:51], v[160:163], a[100:103], v[48:51]
v_rcp_f32 v243, v243
v_mfma_f32_16x16x32_bf16 v[36:39], v[164:167], a[96:99], v[36:39]
ds_read_b64_tr_b16 v[196:197], v209 offset:43008
ds_read_b64_tr_b16 v[198:199], v209 offset:47104
v_mfma_f32_16x16x32_bf16 v[52:55], v[164:167], a[100:103], v[52:55]
buffer_load_dwordx4 v233, s[44:47], s14 offen offset:3072 lds
v_mfma_f32_16x16x32_bf16 v[40:43], v[168:171], a[96:99], v[40:43]
ds_read_b64_tr_b16 v[200:201], v210 offset:43008
ds_read_b64_tr_b16 v[202:203], v210 offset:47104
v_mfma_f32_16x16x32_bf16 v[56:59], v[168:171], a[100:103], v[56:59]
s_add_u32 m0, s77, 114688
v_mul_f32 v76, v76, v243
v_mfma_f32_16x16x32_bf16 v[44:47], v[172:175], a[96:99], v[44:47]
ds_read_b64_tr_b16 v[204:205], v211 offset:43008
ds_read_b64_tr_b16 v[206:207], v211 offset:47104
v_mul_f32 v244, v244, v77
v_mfma_f32_16x16x32_bf16 v[60:63], v[172:175], a[100:103], v[60:63]
s_waitcnt vmcnt(22) lgkmcnt(8)
s_barrier
v_mfma_f32_16x16x32_bf16 v[0:3], v[176:179], a[104:107], v[0:3]
ds_read_b64_tr_b16 v[160:161], v212 offset:0
ds_read_b64_tr_b16 v[162:163], v212 offset:4096
v_mfma_f32_16x16x32_bf16 v[16:19], v[176:179], a[108:111], v[16:19]
buffer_load_dwordx4 v232, s[40:43], s15 offen offset:0 lds
v_mfma_f32_16x16x32_bf16 v[4:7], v[180:183], a[104:107], v[4:7]
ds_read_b64_tr_b16 v[164:165], v213 offset:0
ds_read_b64_tr_b16 v[166:167], v213 offset:4096
v_mfma_f32_16x16x32_bf16 v[20:23], v[180:183], a[108:111], v[20:23]
v_exp_f32 v244, v244
v_mfma_f32_16x16x32_bf16 v[8:11], v[184:187], a[104:107], v[8:11]
ds_read_b64_tr_b16 v[168:169], v214 offset:0
ds_read_b64_tr_b16 v[170:171], v214 offset:4096
v_mul_f32 v77, v77, v109
v_mfma_f32_16x16x32_bf16 v[24:27], v[184:187], a[108:111], v[24:27]
v_add_f32 v244, 1.0, v244
v_mul_f32 v245, v78, v78
v_mfma_f32_16x16x32_bf16 v[12:15], v[188:191], a[104:107], v[12:15]
ds_read_b64_tr_b16 v[172:173], v215 offset:0
ds_read_b64_tr_b16 v[174:175], v215 offset:4096
v_mfma_f32_16x16x32_bf16 v[28:31], v[188:191], a[108:111], v[28:31]
v_fmaak_f32 v245, v245, v223, 0xc0135761
s_waitcnt lgkmcnt(8)
v_mfma_f32_16x16x32_bf16 v[32:35], v[192:195], a[104:107], v[32:35]
ds_read_b64_tr_b16 v[176:177], v212 offset:2048
ds_read_b64_tr_b16 v[178:179], v212 offset:6144
v_mfma_f32_16x16x32_bf16 v[48:51], v[192:195], a[108:111], v[48:51]
buffer_load_dwordx4 v233, s[40:43], s15 offen offset:1024 lds
v_mfma_f32_16x16x32_bf16 v[36:39], v[196:199], a[104:107], v[36:39]
ds_read_b64_tr_b16 v[180:181], v213 offset:2048
ds_read_b64_tr_b16 v[182:183], v213 offset:6144
v_mul_f32 v245, v245, v78
v_mfma_f32_16x16x32_bf16 v[52:55], v[196:199], a[108:111], v[52:55]
v_rcp_f32 v244, v244
v_mfma_f32_16x16x32_bf16 v[40:43], v[200:203], a[104:107], v[40:43]
ds_read_b64_tr_b16 v[184:185], v214 offset:2048
ds_read_b64_tr_b16 v[186:187], v214 offset:6144
v_mfma_f32_16x16x32_bf16 v[56:59], v[200:203], a[108:111], v[56:59]
buffer_load_dwordx4 v232, s[44:47], s15 offen offset:2048 lds
v_mfma_f32_16x16x32_bf16 v[44:47], v[204:207], a[104:107], v[44:47]
ds_read_b64_tr_b16 v[188:189], v215 offset:2048
ds_read_b64_tr_b16 v[190:191], v215 offset:6144
v_mfma_f32_16x16x32_bf16 v[60:63], v[204:207], a[108:111], v[60:63]
v_mul_f32 v77, v77, v244
s_waitcnt lgkmcnt(8)
v_mfma_f32_16x16x32_bf16 v[0:3], v[160:163], a[112:115], v[0:3]
ds_read_b64_tr_b16 v[192:193], v212 offset:8192
ds_read_b64_tr_b16 v[194:195], v212 offset:12288
v_cvt_pk_bf16_f32 v106, v76, v77
v_mfma_f32_16x16x32_bf16 v[16:19], v[160:163], a[116:119], v[16:19]
v_exp_f32 v245, v245
v_mfma_f32_16x16x32_bf16 v[4:7], v[164:167], a[112:115], v[4:7]
ds_read_b64_tr_b16 v[196:197], v213 offset:8192
ds_read_b64_tr_b16 v[198:199], v213 offset:12288
v_mfma_f32_16x16x32_bf16 v[20:23], v[164:167], a[116:119], v[20:23]
v_mul_f32 v78, v78, v110
v_add_f32 v245, 1.0, v245
v_mfma_f32_16x16x32_bf16 v[8:11], v[168:171], a[112:115], v[8:11]
ds_read_b64_tr_b16 v[200:201], v214 offset:8192
ds_read_b64_tr_b16 v[202:203], v214 offset:12288
)"
R"(
v_mfma_f32_16x16x32_bf16 v[24:27], v[168:171], a[116:119], v[24:27]
buffer_load_dwordx4 v233, s[44:47], s15 offen offset:3072 lds
v_mfma_f32_16x16x32_bf16 v[12:15], v[172:175], a[112:115], v[12:15]
ds_read_b64_tr_b16 v[204:205], v215 offset:8192
ds_read_b64_tr_b16 v[206:207], v215 offset:12288
v_mfma_f32_16x16x32_bf16 v[28:31], v[172:175], a[116:119], v[28:31]
s_add_u32 m0, s77, 32768
s_waitcnt lgkmcnt(8)
v_mfma_f32_16x16x32_bf16 v[32:35], v[176:179], a[112:115], v[32:35]
ds_read_b64_tr_b16 v[160:161], v212 offset:10240
ds_read_b64_tr_b16 v[162:163], v212 offset:14336
v_mul_f32 v246, v79, v79
v_mfma_f32_16x16x32_bf16 v[48:51], v[176:179], a[116:119], v[48:51]
v_rcp_f32 v245, v245
v_mfma_f32_16x16x32_bf16 v[36:39], v[180:183], a[112:115], v[36:39]
ds_read_b64_tr_b16 v[164:165], v213 offset:10240
ds_read_b64_tr_b16 v[166:167], v213 offset:14336
v_mfma_f32_16x16x32_bf16 v[52:55], v[180:183], a[116:119], v[52:55]
v_mul_f32 v78, v78, v245
v_fmaak_f32 v246, v246, v223, 0xc0135761
v_mfma_f32_16x16x32_bf16 v[40:43], v[184:187], a[112:115], v[40:43]
ds_read_b64_tr_b16 v[168:169], v214 offset:10240
ds_read_b64_tr_b16 v[170:171], v214 offset:14336
v_mfma_f32_16x16x32_bf16 v[56:59], v[184:187], a[116:119], v[56:59]
v_mul_f32 v246, v246, v79
v_mul_f32 v79, v79, v111
v_mfma_f32_16x16x32_bf16 v[44:47], v[188:191], a[112:115], v[44:47]
ds_read_b64_tr_b16 v[172:173], v215 offset:10240
ds_read_b64_tr_b16 v[174:175], v215 offset:14336
v_mul_f32 v247, v80, v80
v_mfma_f32_16x16x32_bf16 v[60:63], v[188:191], a[116:119], v[60:63]
s_waitcnt vmcnt(22) lgkmcnt(8)
s_barrier
v_mfma_f32_16x16x32_bf16 v[0:3], v[192:195], a[120:123], v[0:3]
ds_read_b64_tr_b16 v[176:177], v208 offset:49152
ds_read_b64_tr_b16 v[178:179], v208 offset:53248
v_mfma_f32_16x16x32_bf16 v[16:19], v[192:195], a[124:127], v[16:19]
buffer_load_dwordx4 v232, s[40:43], s16 offen offset:0 lds
v_mfma_f32_16x16x32_bf16 v[4:7], v[196:199], a[120:123], v[4:7]
ds_read_b64_tr_b16 v[180:181], v209 offset:49152
ds_read_b64_tr_b16 v[182:183], v209 offset:53248
v_mfma_f32_16x16x32_bf16 v[20:23], v[196:199], a[124:127], v[20:23]
v_exp_f32 v246, v246
v_mfma_f32_16x16x32_bf16 v[8:11], v[200:203], a[120:123], v[8:11]
ds_read_b64_tr_b16 v[184:185], v210 offset:49152
ds_read_b64_tr_b16 v[186:187], v210 offset:53248
v_add_f32 v246, 1.0, v246
v_mfma_f32_16x16x32_bf16 v[24:27], v[200:203], a[124:127], v[24:27]
v_rcp_f32 v246, v246
v_mfma_f32_16x16x32_bf16 v[12:15], v[204:207], a[120:123], v[12:15]
ds_read_b64_tr_b16 v[188:189], v211 offset:49152
ds_read_b64_tr_b16 v[190:191], v211 offset:53248
v_mfma_f32_16x16x32_bf16 v[28:31], v[204:207], a[124:127], v[28:31]
v_mul_f32 v79, v79, v246
s_waitcnt lgkmcnt(8)
v_mfma_f32_16x16x32_bf16 v[32:35], v[160:163], a[120:123], v[32:35]
ds_read_b64_tr_b16 v[192:193], v208 offset:51200
ds_read_b64_tr_b16 v[194:195], v208 offset:55296
v_mfma_f32_16x16x32_bf16 v[48:51], v[160:163], a[124:127], v[48:51]
buffer_load_dwordx4 v233, s[40:43], s16 offen offset:1024 lds
v_mfma_f32_16x16x32_bf16 v[36:39], v[164:167], a[120:123], v[36:39]
ds_read_b64_tr_b16 v[196:197], v209 offset:51200
ds_read_b64_tr_b16 v[198:199], v209 offset:55296
v_cvt_pk_bf16_f32 v107, v78, v79
v_mfma_f32_16x16x32_bf16 v[52:55], v[164:167], a[124:127], v[52:55]
buffer_store_dwordx4 v[104:107], v220, s[52:55], s92 offen offset:3072 sc1
v_mfma_f32_16x16x32_bf16 v[40:43], v[168:171], a[120:123], v[40:43]
ds_read_b64_tr_b16 v[200:201], v210 offset:51200
ds_read_b64_tr_b16 v[202:203], v210 offset:55296
v_mfma_f32_16x16x32_bf16 v[56:59], v[168:171], a[124:127], v[56:59]
buffer_load_dwordx4 v232, s[44:47], s16 offen offset:2048 lds
v_mfma_f32_16x16x32_bf16 v[44:47], v[172:175], a[120:123], v[44:47]
ds_read_b64_tr_b16 v[204:205], v211 offset:51200
ds_read_b64_tr_b16 v[206:207], v211 offset:55296
v_mfma_f32_16x16x32_bf16 v[60:63], v[172:175], a[124:127], v[60:63]
v_fmaak_f32 v247, v247, v223, 0xc0135761
s_waitcnt lgkmcnt(8)
v_mfma_f32_16x16x32_bf16 v[0:3], v[176:179], a[128:131], v[0:3]
ds_read_b64_tr_b16 v[160:161], v208 offset:57344
ds_read_b64_tr_b16 v[162:163], v208 offset:61440
v_mfma_f32_16x16x32_bf16 v[16:19], v[176:179], a[132:135], v[16:19]
v_mul_f32 v247, v247, v80
v_mul_f32 v80, v80, v112
v_mfma_f32_16x16x32_bf16 v[4:7], v[180:183], a[128:131], v[4:7]
ds_read_b64_tr_b16 v[164:165], v209 offset:57344
ds_read_b64_tr_b16 v[166:167], v209 offset:61440
v_mul_f32 v248, v81, v81
v_mfma_f32_16x16x32_bf16 v[20:23], v[180:183], a[132:135], v[20:23]
v_exp_f32 v247, v247
v_mfma_f32_16x16x32_bf16 v[8:11], v[184:187], a[128:131], v[8:11]
ds_read_b64_tr_b16 v[168:169], v210 offset:57344
ds_read_b64_tr_b16 v[170:171], v210 offset:61440
v_mfma_f32_16x16x32_bf16 v[24:27], v[184:187], a[132:135], v[24:27]
buffer_load_dwordx4 v233, s[44:47], s16 offen offset:3072 lds
v_mfma_f32_16x16x32_bf16 v[12:15], v[188:191], a[128:131], v[12:15]
ds_read_b64_tr_b16 v[172:173], v211 offset:57344
ds_read_b64_tr_b16 v[174:175], v211 offset:61440
v_mfma_f32_16x16x32_bf16 v[28:31], v[188:191], a[132:135], v[28:31]
s_add_u32 m0, s77, 65536
s_waitcnt lgkmcnt(8)
v_mfma_f32_16x16x32_bf16 v[32:35], v[192:195], a[128:131], v[32:35]
ds_read_b64_tr_b16 v[176:177], v208 offset:59392
ds_read_b64_tr_b16 v[178:179], v208 offset:63488
v_add_f32 v247, 1.0, v247
v_mfma_f32_16x16x32_bf16 v[48:51], v[192:195], a[132:135], v[48:51]
v_rcp_f32 v247, v247
v_mfma_f32_16x16x32_bf16 v[36:39], v[196:199], a[128:131], v[36:39]
ds_read_b64_tr_b16 v[180:181], v209 offset:59392
ds_read_b64_tr_b16 v[182:183], v209 offset:63488
v_mfma_f32_16x16x32_bf16 v[52:55], v[196:199], a[132:135], v[52:55]
v_mul_f32 v80, v80, v247
v_fmaak_f32 v248, v248, v223, 0xc0135761
v_mfma_f32_16x16x32_bf16 v[40:43], v[200:203], a[128:131], v[40:43]
ds_read_b64_tr_b16 v[184:185], v210 offset:59392
ds_read_b64_tr_b16 v[186:187], v210 offset:63488
v_mfma_f32_16x16x32_bf16 v[56:59], v[200:203], a[132:135], v[56:59]
v_mul_f32 v248, v248, v81
v_mul_f32 v81, v81, v113
v_mfma_f32_16x16x32_bf16 v[44:47], v[204:207], a[128:131], v[44:47]
ds_read_b64_tr_b16 v[188:189], v211 offset:59392
ds_read_b64_tr_b16 v[190:191], v211 offset:63488
v_mul_f32 v249, v82, v82
v_mfma_f32_16x16x32_bf16 v[60:63], v[204:207], a[132:135], v[60:63]
s_waitcnt vmcnt(23) lgkmcnt(8)
s_barrier
v_mfma_f32_16x16x32_bf16 v[0:3], v[160:163], a[136:139], v[0:3]
ds_read_b64_tr_b16 v[192:193], v212 offset:16384
ds_read_b64_tr_b16 v[194:195], v212 offset:20480
v_mfma_f32_16x16x32_bf16 v[16:19], v[160:163], a[140:143], v[16:19]
buffer_load_dwordx4 v232, s[40:43], s17 offen offset:0 lds
v_mfma_f32_16x16x32_bf16 v[4:7], v[164:167], a[136:139], v[4:7]
ds_read_b64_tr_b16 v[196:197], v213 offset:16384
ds_read_b64_tr_b16 v[198:199], v213 offset:20480
v_mfma_f32_16x16x32_bf16 v[20:23], v[164:167], a[140:143], v[20:23]
v_exp_f32 v248, v248
v_mfma_f32_16x16x32_bf16 v[8:11], v[168:171], a[136:139], v[8:11]
ds_read_b64_tr_b16 v[200:201], v214 offset:16384
ds_read_b64_tr_b16 v[202:203], v214 offset:20480
v_add_f32 v248, 1.0, v248
v_mfma_f32_16x16x32_bf16 v[24:27], v[168:171], a[140:143], v[24:27]
v_rcp_f32 v248, v248
v_mfma_f32_16x16x32_bf16 v[12:15], v[172:175], a[136:139], v[12:15]
ds_read_b64_tr_b16 v[204:205], v215 offset:16384
ds_read_b64_tr_b16 v[206:207], v215 offset:20480
v_mfma_f32_16x16x32_bf16 v[28:31], v[172:175], a[140:143], v[28:31]
LC16_%=:
s_cbranch_execz LC20_%=
v_mul_f32 v81, v81, v248
s_waitcnt lgkmcnt(8)
v_mfma_f32_16x16x32_bf16 v[32:35], v[176:179], a[136:139], v[32:35]
ds_read_b64_tr_b16 v[160:161], v212 offset:18432
ds_read_b64_tr_b16 v[162:163], v212 offset:22528
v_mfma_f32_16x16x32_bf16 v[48:51], v[176:179], a[140:143], v[48:51]
buffer_load_dwordx4 v233, s[40:43], s17 offen offset:1024 lds
v_mfma_f32_16x16x32_bf16 v[36:39], v[180:183], a[136:139], v[36:39]
ds_read_b64_tr_b16 v[164:165], v213 offset:18432
ds_read_b64_tr_b16 v[166:167], v213 offset:22528
v_cvt_pk_bf16_f32 v112, v80, v81
v_mfma_f32_16x16x32_bf16 v[52:55], v[180:183], a[140:143], v[52:55]
v_fmaak_f32 v249, v249, v223, 0xc0135761
v_mul_f32 v249, v249, v82
v_mfma_f32_16x16x32_bf16 v[40:43], v[184:187], a[136:139], v[40:43]
ds_read_b64_tr_b16 v[168:169], v214 offset:18432
ds_read_b64_tr_b16 v[170:171], v214 offset:22528
v_mfma_f32_16x16x32_bf16 v[56:59], v[184:187], a[140:143], v[56:59]
buffer_load_dwordx4 v232, s[44:47], s17 offen offset:2048 lds
v_mfma_f32_16x16x32_bf16 v[44:47], v[188:191], a[136:139], v[44:47]
ds_read_b64_tr_b16 v[172:173], v215 offset:18432
ds_read_b64_tr_b16 v[174:175], v215 offset:22528
v_mfma_f32_16x16x32_bf16 v[60:63], v[188:191], a[140:143], v[60:63]
s_min_u32 s10, s10, s11
s_waitcnt lgkmcnt(8)
v_mfma_f32_16x16x32_bf16 v[0:3], v[192:195], a[144:147], v[0:3]
ds_read_b64_tr_b16 v[176:177], v212 offset:24576
ds_read_b64_tr_b16 v[178:179], v212 offset:28672
v_mfma_f32_16x16x32_bf16 v[16:19], v[192:195], a[148:151], v[16:19]
v_exp_f32 v249, v249
v_mfma_f32_16x16x32_bf16 v[4:7], v[196:199], a[144:147], v[4:7]
ds_read_b64_tr_b16 v[180:181], v213 offset:24576
ds_read_b64_tr_b16 v[182:183], v213 offset:28672
v_mul_f32 v82, v82, v114
v_mfma_f32_16x16x32_bf16 v[20:23], v[196:199], a[148:151], v[20:23]
v_add_f32 v249, 1.0, v249
v_mul_f32 v250, v83, v83
v_mfma_f32_16x16x32_bf16 v[8:11], v[200:203], a[144:147], v[8:11]
ds_read_b64_tr_b16 v[184:185], v214 offset:24576
ds_read_b64_tr_b16 v[186:187], v214 offset:28672
v_mfma_f32_16x16x32_bf16 v[24:27], v[200:203], a[148:151], v[24:27]
buffer_load_dwordx4 v233, s[44:47], s17 offen offset:3072 lds
v_mfma_f32_16x16x32_bf16 v[12:15], v[204:207], a[144:147], v[12:15]
ds_read_b64_tr_b16 v[188:189], v215 offset:24576
ds_read_b64_tr_b16 v[190:191], v215 offset:28672
v_mfma_f32_16x16x32_bf16 v[28:31], v[204:207], a[148:151], v[28:31]
s_add_u32 m0, s77, 49152
s_waitcnt lgkmcnt(8)
v_mfma_f32_16x16x32_bf16 v[32:35], v[160:163], a[144:147], v[32:35]
ds_read_b64_tr_b16 v[192:193], v212 offset:26624
ds_read_b64_tr_b16 v[194:195], v212 offset:30720
v_fmaak_f32 v250, v250, v223, 0xc0135761
v_mfma_f32_16x16x32_bf16 v[48:51], v[160:163], a[148:151], v[48:51]
v_rcp_f32 v249, v249
v_mfma_f32_16x16x32_bf16 v[36:39], v[164:167], a[144:147], v[36:39]
ds_read_b64_tr_b16 v[196:197], v213 offset:26624
ds_read_b64_tr_b16 v[198:199], v213 offset:30720
v_mfma_f32_16x16x32_bf16 v[52:55], v[164:167], a[148:151], v[52:55]
v_mul_f32 v82, v82, v249
v_mul_f32 v250, v250, v83
v_mfma_f32_16x16x32_bf16 v[40:43], v[168:171], a[144:147], v[40:43]
ds_read_b64_tr_b16 v[200:201], v214 offset:26624
ds_read_b64_tr_b16 v[202:203], v214 offset:30720
v_mfma_f32_16x16x32_bf16 v[56:59], v[168:171], a[148:151], v[56:59]
v_exp_f32 v250, v250
v_mfma_f32_16x16x32_bf16 v[44:47], v[172:175], a[144:147], v[44:47]
ds_read_b64_tr_b16 v[204:205], v215 offset:26624
ds_read_b64_tr_b16 v[206:207], v215 offset:30720
v_mul_f32 v83, v83, v115
v_mfma_f32_16x16x32_bf16 v[60:63], v[172:175], a[148:151], v[60:63]
s_waitcnt vmcnt(22) lgkmcnt(8)
s_barrier
v_mfma_f32_16x16x32_bf16 v[0:3], v[176:179], a[152:155], v[0:3]
ds_read_b64_tr_b16 v[160:161], v208 offset:0
ds_read_b64_tr_b16 v[162:163], v208 offset:4096
v_mfma_f32_16x16x32_bf16 v[16:19], v[176:179], a[156:159], v[16:19]
buffer_load_dwordx4 v228, s[40:43], s10 offen offset:0 lds
v_mfma_f32_16x16x32_bf16 v[4:7], v[180:183], a[152:155], v[4:7]
ds_read_b64_tr_b16 v[164:165], v209 offset:0
ds_read_b64_tr_b16 v[166:167], v209 offset:4096
v_mfma_f32_16x16x32_bf16 v[20:23], v[180:183], a[156:159], v[20:23]
s_mul_i32 s85, s75, 57344
v_add_u32 v232, s85, v220
v_mfma_f32_16x16x32_bf16 v[8:11], v[184:187], a[152:155], v[8:11]
ds_read_b64_tr_b16 v[168:169], v210 offset:0
ds_read_b64_tr_b16 v[170:171], v210 offset:4096
v_add_f32 v250, 1.0, v250
v_mfma_f32_16x16x32_bf16 v[24:27], v[184:187], a[156:159], v[24:27]
v_add_u32 v233, s85, v221
v_mul_f32 v251, v84, v84
v_mfma_f32_16x16x32_bf16 v[12:15], v[188:191], a[152:155], v[12:15]
ds_read_b64_tr_b16 v[172:173], v211 offset:0
ds_read_b64_tr_b16 v[174:175], v211 offset:4096
v_mfma_f32_16x16x32_bf16 v[28:31], v[188:191], a[156:159], v[28:31]
v_fmaak_f32 v251, v251, v223, 0xc0135761
s_waitcnt lgkmcnt(8)
v_mfma_f32_16x16x32_bf16 v[32:35], v[192:195], a[152:155], v[32:35]
ds_read_b64_tr_b16 v[176:177], v208 offset:2048
ds_read_b64_tr_b16 v[178:179], v208 offset:6144
v_mfma_f32_16x16x32_bf16 v[48:51], v[192:195], a[156:159], v[48:51]
buffer_load_dwordx4 v229, s[40:43], s10 offen offset:1024 lds
v_mfma_f32_16x16x32_bf16 v[36:39], v[196:199], a[152:155], v[36:39]
ds_read_b64_tr_b16 v[180:181], v209 offset:2048
ds_read_b64_tr_b16 v[182:183], v209 offset:6144
v_mul_f32 v251, v251, v84
v_mfma_f32_16x16x32_bf16 v[52:55], v[196:199], a[156:159], v[52:55]
v_rcp_f32 v250, v250
v_mfma_f32_16x16x32_bf16 v[40:43], v[200:203], a[152:155], v[40:43]
ds_read_b64_tr_b16 v[184:185], v210 offset:2048
ds_read_b64_tr_b16 v[186:187], v210 offset:6144
v_mfma_f32_16x16x32_bf16 v[56:59], v[200:203], a[156:159], v[56:59]
buffer_load_dwordx4 v228, s[44:47], s10 offen offset:2048 lds
v_mfma_f32_16x16x32_bf16 v[44:47], v[204:207], a[152:155], v[44:47]
ds_read_b64_tr_b16 v[188:189], v211 offset:2048
ds_read_b64_tr_b16 v[190:191], v211 offset:6144
v_mfma_f32_16x16x32_bf16 v[60:63], v[204:207], a[156:159], v[60:63]
s_min_u32 s12, s12, s13
s_waitcnt lgkmcnt(8)
v_mfma_f32_16x16x32_bf16 v[0:3], v[160:163], a[160:163], v[0:3]
ds_read_b64_tr_b16 v[192:193], v208 offset:8192
ds_read_b64_tr_b16 v[194:195], v208 offset:12288
v_mfma_f32_16x16x32_bf16 v[16:19], v[160:163], a[164:167], v[16:19]
v_exp_f32 v251, v251
v_mfma_f32_16x16x32_bf16 v[4:7], v[164:167], a[160:163], v[4:7]
ds_read_b64_tr_b16 v[196:197], v209 offset:8192
ds_read_b64_tr_b16 v[198:199], v209 offset:12288
v_mul_f32 v83, v83, v250
v_mfma_f32_16x16x32_bf16 v[20:23], v[164:167], a[164:167], v[20:23]
v_cvt_pk_bf16_f32 v113, v82, v83
v_mul_f32 v84, v84, v116
v_mfma_f32_16x16x32_bf16 v[8:11], v[168:171], a[160:163], v[8:11]
ds_read_b64_tr_b16 v[200:201], v210 offset:8192
ds_read_b64_tr_b16 v[202:203], v210 offset:12288
v_mfma_f32_16x16x32_bf16 v[24:27], v[168:171], a[164:167], v[24:27]
buffer_load_dwordx4 v229, s[44:47], s10 offen offset:3072 lds
v_mfma_f32_16x16x32_bf16 v[12:15], v[172:175], a[160:163], v[12:15]
ds_read_b64_tr_b16 v[204:205], v211 offset:8192
ds_read_b64_tr_b16 v[206:207], v211 offset:12288
v_mfma_f32_16x16x32_bf16 v[28:31], v[172:175], a[164:167], v[28:31]
s_add_u32 m0, s77, 81920
s_waitcnt lgkmcnt(8)
v_mfma_f32_16x16x32_bf16 v[32:35], v[176:179], a[160:163], v[32:35]
ds_read_b64_tr_b16 v[160:161], v208 offset:10240
ds_read_b64_tr_b16 v[162:163], v208 offset:14336
v_add_f32 v251, 1.0, v251
v_mfma_f32_16x16x32_bf16 v[48:51], v[176:179], a[164:167], v[48:51]
v_rcp_f32 v251, v251
v_mfma_f32_16x16x32_bf16 v[36:39], v[180:183], a[160:163], v[36:39]
ds_read_b64_tr_b16 v[164:165], v209 offset:10240
ds_read_b64_tr_b16 v[166:167], v209 offset:14336
v_mfma_f32_16x16x32_bf16 v[52:55], v[180:183], a[164:167], v[52:55]
v_mul_f32 v84, v84, v251
v_mul_f32 v252, v85, v85
v_mfma_f32_16x16x32_bf16 v[40:43], v[184:187], a[160:163], v[40:43]
ds_read_b64_tr_b16 v[168:169], v210 offset:10240
ds_read_b64_tr_b16 v[170:171], v210 offset:14336
v_mfma_f32_16x16x32_bf16 v[56:59], v[184:187], a[164:167], v[56:59]
v_fmaak_f32 v252, v252, v223, 0xc0135761
v_mul_f32 v252, v252, v85
v_mfma_f32_16x16x32_bf16 v[44:47], v[188:191], a[160:163], v[44:47]
ds_read_b64_tr_b16 v[172:173], v211 offset:10240
ds_read_b64_tr_b16 v[174:175], v211 offset:14336
v_mul_f32 v85, v85, v117
v_mfma_f32_16x16x32_bf16 v[60:63], v[188:191], a[164:167], v[60:63]
s_waitcnt vmcnt(22) lgkmcnt(8)
s_barrier
v_mfma_f32_16x16x32_bf16 v[0:3], v[192:195], a[168:171], v[0:3]
ds_read_b64_tr_b16 v[176:177], v212 offset:32768
ds_read_b64_tr_b16 v[178:179], v212 offset:36864
v_mfma_f32_16x16x32_bf16 v[16:19], v[192:195], a[172:175], v[16:19]
buffer_load_dwordx4 v228, s[40:43], s12 offen offset:0 lds
v_mfma_f32_16x16x32_bf16 v[4:7], v[196:199], a[168:171], v[4:7]
ds_read_b64_tr_b16 v[180:181], v213 offset:32768
ds_read_b64_tr_b16 v[182:183], v213 offset:36864
v_mfma_f32_16x16x32_bf16 v[20:23], v[196:199], a[172:175], v[20:23]
v_exp_f32 v252, v252
v_mfma_f32_16x16x32_bf16 v[8:11], v[200:203], a[168:171], v[8:11]
ds_read_b64_tr_b16 v[184:185], v214 offset:32768
ds_read_b64_tr_b16 v[186:187], v214 offset:36864
v_add_f32 v252, 1.0, v252
v_mfma_f32_16x16x32_bf16 v[24:27], v[200:203], a[172:175], v[24:27]
v_rcp_f32 v252, v252
v_mfma_f32_16x16x32_bf16 v[12:15], v[204:207], a[168:171], v[12:15]
ds_read_b64_tr_b16 v[188:189], v215 offset:32768
ds_read_b64_tr_b16 v[190:191], v215 offset:36864
v_mfma_f32_16x16x32_bf16 v[28:31], v[204:207], a[172:175], v[28:31]
v_mul_f32 v85, v85, v252
s_waitcnt lgkmcnt(8)
v_mfma_f32_16x16x32_bf16 v[32:35], v[160:163], a[168:171], v[32:35]
ds_read_b64_tr_b16 v[192:193], v212 offset:34816
ds_read_b64_tr_b16 v[194:195], v212 offset:38912
v_mfma_f32_16x16x32_bf16 v[48:51], v[160:163], a[172:175], v[48:51]
buffer_load_dwordx4 v229, s[40:43], s12 offen offset:1024 lds
v_mfma_f32_16x16x32_bf16 v[36:39], v[164:167], a[168:171], v[36:39]
ds_read_b64_tr_b16 v[196:197], v213 offset:34816
ds_read_b64_tr_b16 v[198:199], v213 offset:38912
v_mfma_f32_16x16x32_bf16 v[52:55], v[164:167], a[172:175], v[52:55]
v_cvt_pk_bf16_f32 v114, v84, v85
v_mul_f32 v253, v86, v86
v_mfma_f32_16x16x32_bf16 v[40:43], v[168:171], a[168:171], v[40:43]
ds_read_b64_tr_b16 v[200:201], v214 offset:34816
ds_read_b64_tr_b16 v[202:203], v214 offset:38912
v_fmaak_f32 v253, v253, v223, 0xc0135761
v_mfma_f32_16x16x32_bf16 v[56:59], v[168:171], a[172:175], v[56:59]
buffer_load_dwordx4 v228, s[44:47], s12 offen offset:2048 lds
v_mfma_f32_16x16x32_bf16 v[44:47], v[172:175], a[168:171], v[44:47]
ds_read_b64_tr_b16 v[204:205], v215 offset:34816
ds_read_b64_tr_b16 v[206:207], v215 offset:38912
v_mfma_f32_16x16x32_bf16 v[60:63], v[172:175], a[172:175], v[60:63]
s_min_u32 s14, s14, s15
s_waitcnt lgkmcnt(8)
v_mfma_f32_16x16x32_bf16 v[0:3], v[176:179], a[176:179], v[0:3]
ds_read_b64_tr_b16 v[160:161], v212 offset:40960
ds_read_b64_tr_b16 v[162:163], v212 offset:45056
v_mfma_f32_16x16x32_bf16 v[16:19], v[176:179], a[180:183], v[16:19]
v_mul_f32 v253, v253, v86
v_mul_f32 v86, v86, v118
v_mfma_f32_16x16x32_bf16 v[4:7], v[180:183], a[176:179], v[4:7]
ds_read_b64_tr_b16 v[164:165], v213 offset:40960
ds_read_b64_tr_b16 v[166:167], v213 offset:45056
v_mul_f32 v254, v87, v87
v_mfma_f32_16x16x32_bf16 v[20:23], v[180:183], a[180:183], v[20:23]
v_exp_f32 v253, v253
v_mfma_f32_16x16x32_bf16 v[8:11], v[184:187], a[176:179], v[8:11]
ds_read_b64_tr_b16 v[168:169], v214 offset:40960
ds_read_b64_tr_b16 v[170:171], v214 offset:45056
v_mfma_f32_16x16x32_bf16 v[24:27], v[184:187], a[180:183], v[24:27]
buffer_load_dwordx4 v229, s[44:47], s12 offen offset:3072 lds
v_mfma_f32_16x16x32_bf16 v[12:15], v[188:191], a[176:179], v[12:15]
ds_read_b64_tr_b16 v[172:173], v215 offset:40960
ds_read_b64_tr_b16 v[174:175], v215 offset:45056
v_mfma_f32_16x16x32_bf16 v[28:31], v[188:191], a[180:183], v[28:31]
s_add_u32 m0, s77, 0
s_waitcnt lgkmcnt(8)
v_mfma_f32_16x16x32_bf16 v[32:35], v[192:195], a[176:179], v[32:35]
ds_read_b64_tr_b16 v[176:177], v212 offset:43008
ds_read_b64_tr_b16 v[178:179], v212 offset:47104
v_add_f32 v253, 1.0, v253
v_mfma_f32_16x16x32_bf16 v[48:51], v[192:195], a[180:183], v[48:51]
v_rcp_f32 v253, v253
v_mfma_f32_16x16x32_bf16 v[36:39], v[196:199], a[176:179], v[36:39]
ds_read_b64_tr_b16 v[180:181], v213 offset:43008
ds_read_b64_tr_b16 v[182:183], v213 offset:47104
v_mfma_f32_16x16x32_bf16 v[52:55], v[196:199], a[180:183], v[52:55]
v_mul_f32 v86, v86, v253
v_fmaak_f32 v254, v254, v223, 0xc0135761
v_mfma_f32_16x16x32_bf16 v[40:43], v[200:203], a[176:179], v[40:43]
ds_read_b64_tr_b16 v[184:185], v214 offset:43008
ds_read_b64_tr_b16 v[186:187], v214 offset:47104
v_mfma_f32_16x16x32_bf16 v[56:59], v[200:203], a[180:183], v[56:59]
v_mul_f32 v254, v254, v87
v_mul_f32 v87, v87, v119
v_mfma_f32_16x16x32_bf16 v[44:47], v[204:207], a[176:179], v[44:47]
)"
R"(
ds_read_b64_tr_b16 v[188:189], v215 offset:43008
ds_read_b64_tr_b16 v[190:191], v215 offset:47104
v_mul_f32 v243, v88, v88
v_mfma_f32_16x16x32_bf16 v[60:63], v[204:207], a[180:183], v[60:63]
s_waitcnt vmcnt(21) lgkmcnt(8)
s_barrier
v_mfma_f32_16x16x32_bf16 v[0:3], v[160:163], a[184:187], v[0:3]
ds_read_b64_tr_b16 v[192:193], v208 offset:16384
ds_read_b64_tr_b16 v[194:195], v208 offset:20480
v_mfma_f32_16x16x32_bf16 v[16:19], v[160:163], a[188:191], v[16:19]
v_exp_f32 v254, v254
v_mfma_f32_16x16x32_bf16 v[4:7], v[164:167], a[184:187], v[4:7]
ds_read_b64_tr_b16 v[196:197], v209 offset:16384
ds_read_b64_tr_b16 v[198:199], v209 offset:20480
v_mfma_f32_16x16x32_bf16 v[20:23], v[164:167], a[188:191], v[20:23]
buffer_load_dwordx4 v228, s[40:43], s14 offen offset:0 lds
v_mfma_f32_16x16x32_bf16 v[8:11], v[168:171], a[184:187], v[8:11]
ds_read_b64_tr_b16 v[200:201], v210 offset:16384
ds_read_b64_tr_b16 v[202:203], v210 offset:20480
v_add_f32 v254, 1.0, v254
v_mfma_f32_16x16x32_bf16 v[24:27], v[168:171], a[188:191], v[24:27]
v_rcp_f32 v254, v254
v_mfma_f32_16x16x32_bf16 v[12:15], v[172:175], a[184:187], v[12:15]
ds_read_b64_tr_b16 v[204:205], v211 offset:16384
ds_read_b64_tr_b16 v[206:207], v211 offset:20480
v_mfma_f32_16x16x32_bf16 v[28:31], v[172:175], a[188:191], v[28:31]
v_mul_f32 v87, v87, v254
s_waitcnt lgkmcnt(8)
v_mfma_f32_16x16x32_bf16 v[32:35], v[176:179], a[184:187], v[32:35]
ds_read_b64_tr_b16 v[160:161], v208 offset:18432
ds_read_b64_tr_b16 v[162:163], v208 offset:22528
v_mfma_f32_16x16x32_bf16 v[48:51], v[176:179], a[188:191], v[48:51]
v_cvt_pk_bf16_f32 v115, v86, v87
v_fmaak_f32 v243, v243, v223, 0xc0135761
v_mfma_f32_16x16x32_bf16 v[36:39], v[180:183], a[184:187], v[36:39]
ds_read_b64_tr_b16 v[164:165], v209 offset:18432
ds_read_b64_tr_b16 v[166:167], v209 offset:22528
v_mfma_f32_16x16x32_bf16 v[52:55], v[180:183], a[188:191], v[52:55]
buffer_load_dwordx4 v229, s[40:43], s14 offen offset:1024 lds
v_mfma_f32_16x16x32_bf16 v[40:43], v[184:187], a[184:187], v[40:43]
ds_read_b64_tr_b16 v[168:169], v210 offset:18432
ds_read_b64_tr_b16 v[170:171], v210 offset:22528
v_mul_f32 v243, v243, v88
v_mfma_f32_16x16x32_bf16 v[56:59], v[184:187], a[188:191], v[56:59]
buffer_store_dwordx4 v[112:115], v221, s[52:55], s92 offen offset:2048 sc1
v_mfma_f32_16x16x32_bf16 v[44:47], v[188:191], a[184:187], v[44:47]
ds_read_b64_tr_b16 v[172:173], v211 offset:18432
ds_read_b64_tr_b16 v[174:175], v211 offset:22528
v_mfma_f32_16x16x32_bf16 v[60:63], v[188:191], a[188:191], v[60:63]
s_min_u32 s16, s16, s17
s_waitcnt lgkmcnt(8)
v_mfma_f32_16x16x32_bf16 v[0:3], v[192:195], a[192:195], v[0:3]
ds_read_b64_tr_b16 v[176:177], v208 offset:24576
ds_read_b64_tr_b16 v[178:179], v208 offset:28672
v_mfma_f32_16x16x32_bf16 v[16:19], v[192:195], a[196:199], v[16:19]
buffer_load_dword v238, v222, s[52:55], 0 offen offset:0 sc1
v_mfma_f32_16x16x32_bf16 v[4:7], v[196:199], a[192:195], v[4:7]
ds_read_b64_tr_b16 v[180:181], v209 offset:24576
ds_read_b64_tr_b16 v[182:183], v209 offset:28672
v_mul_f32 v88, v88, v120
v_mfma_f32_16x16x32_bf16 v[20:23], v[196:199], a[196:199], v[20:23]
buffer_load_dwordx4 v228, s[44:47], s14 offen offset:2048 lds
v_mfma_f32_16x16x32_bf16 v[8:11], v[200:203], a[192:195], v[8:11]
ds_read_b64_tr_b16 v[184:185], v210 offset:24576
ds_read_b64_tr_b16 v[186:187], v210 offset:28672
v_mfma_f32_16x16x32_bf16 v[24:27], v[200:203], a[196:199], v[24:27]
v_exp_f32 v243, v243
v_mfma_f32_16x16x32_bf16 v[12:15], v[204:207], a[192:195], v[12:15]
ds_read_b64_tr_b16 v[188:189], v211 offset:24576
ds_read_b64_tr_b16 v[190:191], v211 offset:28672
v_mfma_f32_16x16x32_bf16 v[28:31], v[204:207], a[196:199], v[28:31]
v_add_f32 v243, 1.0, v243
s_waitcnt lgkmcnt(8)
v_mfma_f32_16x16x32_bf16 v[32:35], v[160:163], a[192:195], v[32:35]
ds_read_b64_tr_b16 v[192:193], v208 offset:26624
ds_read_b64_tr_b16 v[194:195], v208 offset:30720
v_mul_f32 v244, v89, v89
v_mfma_f32_16x16x32_bf16 v[48:51], v[160:163], a[196:199], v[48:51]
v_rcp_f32 v243, v243
v_mfma_f32_16x16x32_bf16 v[36:39], v[164:167], a[192:195], v[36:39]
ds_read_b64_tr_b16 v[196:197], v209 offset:26624
ds_read_b64_tr_b16 v[198:199], v209 offset:30720
v_mfma_f32_16x16x32_bf16 v[52:55], v[164:167], a[196:199], v[52:55]
buffer_load_dwordx4 v229, s[44:47], s14 offen offset:3072 lds
v_mfma_f32_16x16x32_bf16 v[40:43], v[168:171], a[192:195], v[40:43]
ds_read_b64_tr_b16 v[200:201], v210 offset:26624
ds_read_b64_tr_b16 v[202:203], v210 offset:30720
v_mfma_f32_16x16x32_bf16 v[56:59], v[168:171], a[196:199], v[56:59]
s_add_u32 m0, s77, 98304
v_mul_f32 v88, v88, v243
v_mfma_f32_16x16x32_bf16 v[44:47], v[172:175], a[192:195], v[44:47]
ds_read_b64_tr_b16 v[204:205], v211 offset:26624
ds_read_b64_tr_b16 v[206:207], v211 offset:30720
v_fmaak_f32 v244, v244, v223, 0xc0135761
v_mfma_f32_16x16x32_bf16 v[60:63], v[172:175], a[196:199], v[60:63]
s_waitcnt vmcnt(23) lgkmcnt(8)
s_barrier
v_mfma_f32_16x16x32_bf16 v[0:3], v[176:179], a[200:203], v[0:3]
ds_read_b64_tr_b16 v[160:161], v212 offset:49152
ds_read_b64_tr_b16 v[162:163], v212 offset:53248
v_mfma_f32_16x16x32_bf16 v[16:19], v[176:179], a[204:207], v[16:19]
buffer_load_dwordx4 v228, s[40:43], s16 offen offset:0 lds
v_mfma_f32_16x16x32_bf16 v[4:7], v[180:183], a[200:203], v[4:7]
ds_read_b64_tr_b16 v[164:165], v213 offset:49152
ds_read_b64_tr_b16 v[166:167], v213 offset:53248
v_mfma_f32_16x16x32_bf16 v[20:23], v[180:183], a[204:207], v[20:23]
v_mul_f32 v244, v244, v89
v_mul_f32 v89, v89, v121
v_mfma_f32_16x16x32_bf16 v[8:11], v[184:187], a[200:203], v[8:11]
ds_read_b64_tr_b16 v[168:169], v214 offset:49152
ds_read_b64_tr_b16 v[170:171], v214 offset:53248
v_mul_f32 v245, v90, v90
v_mfma_f32_16x16x32_bf16 v[24:27], v[184:187], a[204:207], v[24:27]
v_exp_f32 v244, v244
v_mfma_f32_16x16x32_bf16 v[12:15], v[188:191], a[200:203], v[12:15]
ds_read_b64_tr_b16 v[172:173], v215 offset:49152
ds_read_b64_tr_b16 v[174:175], v215 offset:53248
v_mfma_f32_16x16x32_bf16 v[28:31], v[188:191], a[204:207], v[28:31]
v_add_f32 v244, 1.0, v244
s_waitcnt lgkmcnt(8)
v_mfma_f32_16x16x32_bf16 v[32:35], v[192:195], a[200:203], v[32:35]
ds_read_b64_tr_b16 v[176:177], v212 offset:51200
ds_read_b64_tr_b16 v[178:179], v212 offset:55296
v_mfma_f32_16x16x32_bf16 v[48:51], v[192:195], a[204:207], v[48:51]
buffer_load_dwordx4 v229, s[40:43], s16 offen offset:1024 lds
v_mfma_f32_16x16x32_bf16 v[36:39], v[196:199], a[200:203], v[36:39]
ds_read_b64_tr_b16 v[180:181], v213 offset:51200
ds_read_b64_tr_b16 v[182:183], v213 offset:55296
v_mfma_f32_16x16x32_bf16 v[52:55], v[196:199], a[204:207], v[52:55]
v_rcp_f32 v244, v244
v_mfma_f32_16x16x32_bf16 v[40:43], v[200:203], a[200:203], v[40:43]
ds_read_b64_tr_b16 v[184:185], v214 offset:51200
ds_read_b64_tr_b16 v[186:187], v214 offset:55296
v_mul_f32 v89, v89, v244
v_mfma_f32_16x16x32_bf16 v[56:59], v[200:203], a[204:207], v[56:59]
buffer_load_dwordx4 v228, s[44:47], s16 offen offset:2048 lds
v_mfma_f32_16x16x32_bf16 v[44:47], v[204:207], a[200:203], v[44:47]
ds_read_b64_tr_b16 v[188:189], v215 offset:51200
ds_read_b64_tr_b16 v[190:191], v215 offset:55296
v_mfma_f32_16x16x32_bf16 v[60:63], v[204:207], a[204:207], v[60:63]
v_add_u32 v228, 3670016, v228
s_waitcnt lgkmcnt(8)
v_mfma_f32_16x16x32_bf16 v[0:3], v[160:163], a[208:211], v[0:3]
ds_read_b64_tr_b16 v[192:193], v212 offset:57344
ds_read_b64_tr_b16 v[194:195], v212 offset:61440
v_mfma_f32_16x16x32_bf16 v[16:19], v[160:163], a[212:215], v[16:19]
v_cvt_pk_bf16_f32 v120, v88, v89
v_fmaak_f32 v245, v245, v223, 0xc0135761
v_mfma_f32_16x16x32_bf16 v[4:7], v[164:167], a[208:211], v[4:7]
ds_read_b64_tr_b16 v[196:197], v213 offset:57344
ds_read_b64_tr_b16 v[198:199], v213 offset:61440
v_mul_f32 v245, v245, v90
v_mfma_f32_16x16x32_bf16 v[20:23], v[164:167], a[212:215], v[20:23]
v_exp_f32 v245, v245
v_mfma_f32_16x16x32_bf16 v[8:11], v[168:171], a[208:211], v[8:11]
ds_read_b64_tr_b16 v[200:201], v214 offset:57344
ds_read_b64_tr_b16 v[202:203], v214 offset:61440
v_mfma_f32_16x16x32_bf16 v[24:27], v[168:171], a[212:215], v[24:27]
buffer_load_dwordx4 v229, s[44:47], s16 offen offset:3072 lds
v_mfma_f32_16x16x32_bf16 v[12:15], v[172:175], a[208:211], v[12:15]
ds_read_b64_tr_b16 v[204:205], v215 offset:57344
ds_read_b64_tr_b16 v[206:207], v215 offset:61440
v_mfma_f32_16x16x32_bf16 v[28:31], v[172:175], a[212:215], v[28:31]
v_add_u32 v229, 3670016, v229
s_waitcnt lgkmcnt(8)
v_mfma_f32_16x16x32_bf16 v[32:35], v[176:179], a[208:211], v[32:35]
ds_read_b64_tr_b16 v[160:161], v212 offset:59392
ds_read_b64_tr_b16 v[162:163], v212 offset:63488
v_mul_f32 v90, v90, v122
v_mfma_f32_16x16x32_bf16 v[48:51], v[176:179], a[212:215], v[48:51]
v_add_f32 v245, 1.0, v245
v_mul_f32 v246, v91, v91
v_mfma_f32_16x16x32_bf16 v[36:39], v[180:183], a[208:211], v[36:39]
ds_read_b64_tr_b16 v[164:165], v213 offset:59392
ds_read_b64_tr_b16 v[166:167], v213 offset:63488
v_mfma_f32_16x16x32_bf16 v[52:55], v[180:183], a[212:215], v[52:55]
v_rcp_f32 v245, v245
v_mfma_f32_16x16x32_bf16 v[40:43], v[184:187], a[208:211], v[40:43]
ds_read_b64_tr_b16 v[168:169], v214 offset:59392
ds_read_b64_tr_b16 v[170:171], v214 offset:63488
v_mfma_f32_16x16x32_bf16 v[56:59], v[184:187], a[212:215], v[56:59]
s_add_u32 m0, s77, 16384
v_mul_f32 v90, v90, v245
v_mfma_f32_16x16x32_bf16 v[44:47], v[188:191], a[208:211], v[44:47]
ds_read_b64_tr_b16 v[172:173], v215 offset:59392
ds_read_b64_tr_b16 v[174:175], v215 offset:63488
v_fmaak_f32 v246, v246, v223, 0xc0135761
v_mfma_f32_16x16x32_bf16 v[60:63], v[188:191], a[212:215], v[60:63]
s_waitcnt vmcnt(22) lgkmcnt(8)
s_barrier
v_mfma_f32_16x16x32_bf16 v[0:3], v[192:195], a[216:219], v[0:3]
ds_read_b64_tr_b16 v[176:177], v208 offset:32768
ds_read_b64_tr_b16 v[178:179], v208 offset:36864
v_mfma_f32_16x16x32_bf16 v[16:19], v[192:195], a[220:223], v[16:19]
buffer_load_dwordx4 v228, s[40:43], s10 offen offset:0 lds
v_mfma_f32_16x16x32_bf16 v[4:7], v[196:199], a[216:219], v[4:7]
ds_read_b64_tr_b16 v[180:181], v209 offset:32768
ds_read_b64_tr_b16 v[182:183], v209 offset:36864
v_mfma_f32_16x16x32_bf16 v[20:23], v[196:199], a[220:223], v[20:23]
v_mul_f32 v246, v246, v91
v_mul_f32 v91, v91, v123
v_mfma_f32_16x16x32_bf16 v[8:11], v[200:203], a[216:219], v[8:11]
ds_read_b64_tr_b16 v[184:185], v210 offset:32768
ds_read_b64_tr_b16 v[186:187], v210 offset:36864
v_mfma_f32_16x16x32_bf16 v[24:27], v[200:203], a[220:223], v[24:27]
v_exp_f32 v246, v246
v_mfma_f32_16x16x32_bf16 v[12:15], v[204:207], a[216:219], v[12:15]
ds_read_b64_tr_b16 v[188:189], v211 offset:32768
ds_read_b64_tr_b16 v[190:191], v211 offset:36864
v_add_f32 v246, 1.0, v246
v_mfma_f32_16x16x32_bf16 v[28:31], v[204:207], a[220:223], v[28:31]
v_mul_f32 v247, v92, v92
s_waitcnt lgkmcnt(8)
v_mfma_f32_16x16x32_bf16 v[32:35], v[160:163], a[216:219], v[32:35]
ds_read_b64_tr_b16 v[192:193], v208 offset:34816
ds_read_b64_tr_b16 v[194:195], v208 offset:38912
v_mfma_f32_16x16x32_bf16 v[48:51], v[160:163], a[220:223], v[48:51]
buffer_load_dwordx4 v229, s[40:43], s10 offen offset:1024 lds
v_mfma_f32_16x16x32_bf16 v[36:39], v[164:167], a[216:219], v[36:39]
ds_read_b64_tr_b16 v[196:197], v209 offset:34816
ds_read_b64_tr_b16 v[198:199], v209 offset:38912
v_mfma_f32_16x16x32_bf16 v[52:55], v[164:167], a[220:223], v[52:55]
v_rcp_f32 v246, v246
v_mfma_f32_16x16x32_bf16 v[40:43], v[168:171], a[216:219], v[40:43]
ds_read_b64_tr_b16 v[200:201], v210 offset:34816
ds_read_b64_tr_b16 v[202:203], v210 offset:38912
v_mul_f32 v91, v91, v246
v_mfma_f32_16x16x32_bf16 v[56:59], v[168:171], a[220:223], v[56:59]
buffer_load_dwordx4 v228, s[44:47], s10 offen offset:2048 lds
v_mfma_f32_16x16x32_bf16 v[44:47], v[172:175], a[216:219], v[44:47]
ds_read_b64_tr_b16 v[204:205], v211 offset:34816
ds_read_b64_tr_b16 v[206:207], v211 offset:38912
v_mfma_f32_16x16x32_bf16 v[60:63], v[172:175], a[220:223], v[60:63]
v_cvt_pk_bf16_f32 v121, v90, v91
s_waitcnt lgkmcnt(8)
v_mfma_f32_16x16x32_bf16 v[0:3], v[176:179], a[224:227], v[0:3]
ds_read_b64_tr_b16 v[160:161], v208 offset:40960
ds_read_b64_tr_b16 v[162:163], v208 offset:45056
v_mfma_f32_16x16x32_bf16 v[16:19], v[176:179], a[228:231], v[16:19]
v_fmaak_f32 v247, v247, v223, 0xc0135761
v_mul_f32 v247, v247, v92
v_mfma_f32_16x16x32_bf16 v[4:7], v[180:183], a[224:227], v[4:7]
ds_read_b64_tr_b16 v[164:165], v209 offset:40960
ds_read_b64_tr_b16 v[166:167], v209 offset:45056
v_mul_f32 v92, v92, v124
v_mfma_f32_16x16x32_bf16 v[20:23], v[180:183], a[228:231], v[20:23]
v_exp_f32 v247, v247
v_mfma_f32_16x16x32_bf16 v[8:11], v[184:187], a[224:227], v[8:11]
ds_read_b64_tr_b16 v[168:169], v210 offset:40960
ds_read_b64_tr_b16 v[170:171], v210 offset:45056
v_mfma_f32_16x16x32_bf16 v[24:27], v[184:187], a[228:231], v[24:27]
buffer_load_dwordx4 v229, s[44:47], s10 offen offset:3072 lds
v_mfma_f32_16x16x32_bf16 v[12:15], v[188:191], a[224:227], v[12:15]
ds_read_b64_tr_b16 v[172:173], v211 offset:40960
ds_read_b64_tr_b16 v[174:175], v211 offset:45056
v_mfma_f32_16x16x32_bf16 v[28:31], v[188:191], a[228:231], v[28:31]
s_add_u32 m0, s77, 114688
s_waitcnt lgkmcnt(8)
v_mfma_f32_16x16x32_bf16 v[32:35], v[192:195], a[224:227], v[32:35]
ds_read_b64_tr_b16 v[176:177], v208 offset:43008
ds_read_b64_tr_b16 v[178:179], v208 offset:47104
v_add_f32 v247, 1.0, v247
v_mfma_f32_16x16x32_bf16 v[48:51], v[192:195], a[228:231], v[48:51]
v_rcp_f32 v247, v247
v_mfma_f32_16x16x32_bf16 v[36:39], v[196:199], a[224:227], v[36:39]
ds_read_b64_tr_b16 v[180:181], v209 offset:43008
ds_read_b64_tr_b16 v[182:183], v209 offset:47104
v_mfma_f32_16x16x32_bf16 v[52:55], v[196:199], a[228:231], v[52:55]
v_mul_f32 v92, v92, v247
v_mul_f32 v248, v93, v93
v_mfma_f32_16x16x32_bf16 v[40:43], v[200:203], a[224:227], v[40:43]
ds_read_b64_tr_b16 v[184:185], v210 offset:43008
ds_read_b64_tr_b16 v[186:187], v210 offset:47104
v_mfma_f32_16x16x32_bf16 v[56:59], v[200:203], a[228:231], v[56:59]
v_fmaak_f32 v248, v248, v223, 0xc0135761
v_mul_f32 v248, v248, v93
v_mfma_f32_16x16x32_bf16 v[44:47], v[204:207], a[224:227], v[44:47]
ds_read_b64_tr_b16 v[188:189], v211 offset:43008
ds_read_b64_tr_b16 v[190:191], v211 offset:47104
v_mul_f32 v93, v93, v125
v_mfma_f32_16x16x32_bf16 v[60:63], v[204:207], a[228:231], v[60:63]
s_waitcnt vmcnt(22) lgkmcnt(8)
s_barrier
v_mfma_f32_16x16x32_bf16 v[0:3], v[160:163], a[232:235], v[0:3]
ds_read_b64_tr_b16 v[192:193], v212 offset:0
ds_read_b64_tr_b16 v[194:195], v212 offset:4096
v_mfma_f32_16x16x32_bf16 v[16:19], v[160:163], a[236:239], v[16:19]
buffer_load_dwordx4 v228, s[40:43], s12 offen offset:0 lds
v_mfma_f32_16x16x32_bf16 v[4:7], v[164:167], a[232:235], v[4:7]
ds_read_b64_tr_b16 v[196:197], v213 offset:0
ds_read_b64_tr_b16 v[198:199], v213 offset:4096
v_mfma_f32_16x16x32_bf16 v[20:23], v[164:167], a[236:239], v[20:23]
v_exp_f32 v248, v248
v_mfma_f32_16x16x32_bf16 v[8:11], v[168:171], a[232:235], v[8:11]
ds_read_b64_tr_b16 v[200:201], v214 offset:0
ds_read_b64_tr_b16 v[202:203], v214 offset:4096
v_mfma_f32_16x16x32_bf16 v[24:27], v[168:171], a[236:239], v[24:27]
v_add_f32 v248, 1.0, v248
v_mul_f32 v249, v94, v94
v_mfma_f32_16x16x32_bf16 v[12:15], v[172:175], a[232:235], v[12:15]
ds_read_b64_tr_b16 v[204:205], v215 offset:0
ds_read_b64_tr_b16 v[206:207], v215 offset:4096
v_fmaak_f32 v249, v249, v223, 0xc0135761
v_mfma_f32_16x16x32_bf16 v[28:31], v[172:175], a[236:239], v[28:31]
v_mul_f32 v249, v249, v94
s_waitcnt lgkmcnt(8)
v_mfma_f32_16x16x32_bf16 v[32:35], v[176:179], a[232:235], v[32:35]
ds_read_b64_tr_b16 v[160:161], v212 offset:2048
ds_read_b64_tr_b16 v[162:163], v212 offset:6144
v_mfma_f32_16x16x32_bf16 v[48:51], v[176:179], a[236:239], v[48:51]
buffer_load_dwordx4 v229, s[40:43], s12 offen offset:1024 lds
v_mfma_f32_16x16x32_bf16 v[36:39], v[180:183], a[232:235], v[36:39]
ds_read_b64_tr_b16 v[164:165], v213 offset:2048
ds_read_b64_tr_b16 v[166:167], v213 offset:6144
v_mfma_f32_16x16x32_bf16 v[52:55], v[180:183], a[236:239], v[52:55]
v_rcp_f32 v248, v248
v_mfma_f32_16x16x32_bf16 v[40:43], v[184:187], a[232:235], v[40:43]
ds_read_b64_tr_b16 v[168:169], v214 offset:2048
ds_read_b64_tr_b16 v[170:171], v214 offset:6144
v_mul_f32 v93, v93, v248
v_mfma_f32_16x16x32_bf16 v[56:59], v[184:187], a[236:239], v[56:59]
LC17_%=:
s_cbranch_execz LC21_%=
buffer_load_dwordx4 v228, s[44:47], s12 offen offset:2048 lds
v_mfma_f32_16x16x32_bf16 v[44:47], v[188:191], a[232:235], v[44:47]
ds_read_b64_tr_b16 v[172:173], v215 offset:2048
ds_read_b64_tr_b16 v[174:175], v215 offset:6144
v_mfma_f32_16x16x32_bf16 v[60:63], v[188:191], a[236:239], v[60:63]
v_cvt_pk_bf16_f32 v122, v92, v93
s_waitcnt lgkmcnt(8)
v_mfma_f32_16x16x32_bf16 v[0:3], v[192:195], a[240:243], v[0:3]
ds_read_b64_tr_b16 v[176:177], v212 offset:8192
ds_read_b64_tr_b16 v[178:179], v212 offset:12288
v_mfma_f32_16x16x32_bf16 v[16:19], v[192:195], a[244:247], v[16:19]
v_exp_f32 v249, v249
v_mfma_f32_16x16x32_bf16 v[4:7], v[196:199], a[240:243], v[4:7]
ds_read_b64_tr_b16 v[180:181], v213 offset:8192
ds_read_b64_tr_b16 v[182:183], v213 offset:12288
v_mul_f32 v94, v94, v126
v_mfma_f32_16x16x32_bf16 v[20:23], v[196:199], a[244:247], v[20:23]
v_add_f32 v249, 1.0, v249
v_mul_f32 v250, v95, v95
v_mfma_f32_16x16x32_bf16 v[8:11], v[200:203], a[240:243], v[8:11]
ds_read_b64_tr_b16 v[184:185], v214 offset:8192
ds_read_b64_tr_b16 v[186:187], v214 offset:12288
v_mfma_f32_16x16x32_bf16 v[24:27], v[200:203], a[244:247], v[24:27]
buffer_load_dwordx4 v229, s[44:47], s12 offen offset:3072 lds
v_mfma_f32_16x16x32_bf16 v[12:15], v[204:207], a[240:243], v[12:15]
ds_read_b64_tr_b16 v[188:189], v215 offset:8192
ds_read_b64_tr_b16 v[190:191], v215 offset:12288
v_mfma_f32_16x16x32_bf16 v[28:31], v[204:207], a[244:247], v[28:31]
s_add_u32 m0, s77, 32768
s_waitcnt lgkmcnt(8)
v_mfma_f32_16x16x32_bf16 v[32:35], v[160:163], a[240:243], v[32:35]
ds_read_b64_tr_b16 v[192:193], v212 offset:10240
ds_read_b64_tr_b16 v[194:195], v212 offset:14336
v_fmaak_f32 v250, v250, v223, 0xc0135761
v_mfma_f32_16x16x32_bf16 v[48:51], v[160:163], a[244:247], v[48:51]
v_rcp_f32 v249, v249
v_mfma_f32_16x16x32_bf16 v[36:39], v[164:167], a[240:243], v[36:39]
ds_read_b64_tr_b16 v[196:197], v213 offset:10240
ds_read_b64_tr_b16 v[198:199], v213 offset:14336
v_mfma_f32_16x16x32_bf16 v[52:55], v[164:167], a[244:247], v[52:55]
v_mul_f32 v94, v94, v249
v_mul_f32 v250, v250, v95
v_mfma_f32_16x16x32_bf16 v[40:43], v[168:171], a[240:243], v[40:43]
ds_read_b64_tr_b16 v[200:201], v214 offset:10240
ds_read_b64_tr_b16 v[202:203], v214 offset:14336
v_mfma_f32_16x16x32_bf16 v[56:59], v[168:171], a[244:247], v[56:59]
v_exp_f32 v250, v250
v_mfma_f32_16x16x32_bf16 v[44:47], v[172:175], a[240:243], v[44:47]
ds_read_b64_tr_b16 v[204:205], v215 offset:10240
ds_read_b64_tr_b16 v[206:207], v215 offset:14336
v_mul_f32 v95, v95, v127
v_mfma_f32_16x16x32_bf16 v[60:63], v[172:175], a[244:247], v[60:63]
s_waitcnt vmcnt(22) lgkmcnt(8)
s_barrier
v_mfma_f32_16x16x32_bf16 v[0:3], v[176:179], a[248:251], v[0:3]
ds_read_b64_tr_b16 v[160:161], v216 offset:49152
ds_read_b64_tr_b16 v[162:163], v216 offset:50176
v_mfma_f32_16x16x32_bf16 v[16:19], v[176:179], a[252:255], v[16:19]
buffer_load_dwordx4 v228, s[40:43], s14 offen offset:0 lds
v_mfma_f32_16x16x32_bf16 v[4:7], v[180:183], a[248:251], v[4:7]
ds_read_b64_tr_b16 v[164:165], v217 offset:49152
ds_read_b64_tr_b16 v[166:167], v217 offset:50176
v_mfma_f32_16x16x32_bf16 v[20:23], v[180:183], a[252:255], v[20:23]
v_add_f32 v250, 1.0, v250
v_mfma_f32_16x16x32_bf16 v[8:11], v[184:187], a[248:251], v[8:11]
ds_read_b64_tr_b16 v[168:169], v216 offset:53248
ds_read_b64_tr_b16 v[170:171], v216 offset:54272
v_mfma_f32_16x16x32_bf16 v[24:27], v[184:187], a[252:255], v[24:27]
v_rcp_f32 v250, v250
v_mfma_f32_16x16x32_bf16 v[12:15], v[188:191], a[248:251], v[12:15]
ds_read_b64_tr_b16 v[172:173], v217 offset:53248
ds_read_b64_tr_b16 v[174:175], v217 offset:54272
v_mul_f32 v95, v95, v250
v_mfma_f32_16x16x32_bf16 v[28:31], v[188:191], a[252:255], v[28:31]
v_cvt_pk_bf16_f32 v123, v94, v95
s_waitcnt lgkmcnt(8)
v_mfma_f32_16x16x32_bf16 v[32:35], v[192:195], a[248:251], v[32:35]
)"
R"(
ds_read_b64_tr_b16 v[176:177], v216 offset:57344
ds_read_b64_tr_b16 v[178:179], v216 offset:58368
v_mfma_f32_16x16x32_bf16 v[48:51], v[192:195], a[252:255], v[48:51]
buffer_load_dwordx4 v229, s[40:43], s14 offen offset:1024 lds
v_mfma_f32_16x16x32_bf16 v[36:39], v[196:199], a[248:251], v[36:39]
ds_read_b64_tr_b16 v[180:181], v217 offset:57344
ds_read_b64_tr_b16 v[182:183], v217 offset:58368
v_mfma_f32_16x16x32_bf16 v[52:55], v[196:199], a[252:255], v[52:55]
buffer_store_dwordx4 v[120:123], v221, s[52:55], s92 offen offset:3072 sc1
v_mfma_f32_16x16x32_bf16 v[40:43], v[200:203], a[248:251], v[40:43]
ds_read_b64_tr_b16 v[184:185], v216 offset:61440
ds_read_b64_tr_b16 v[186:187], v216 offset:62464
s_add_u32 s92, s92, 4096
v_mfma_f32_16x16x32_bf16 v[56:59], v[200:203], a[252:255], v[56:59]
buffer_load_dwordx4 v228, s[44:47], s14 offen offset:2048 lds
v_mfma_f32_16x16x32_bf16 v[44:47], v[204:207], a[248:251], v[44:47]
ds_read_b64_tr_b16 v[188:189], v217 offset:61440
ds_read_b64_tr_b16 v[190:191], v217 offset:62464
v_mfma_f32_16x16x32_bf16 v[60:63], v[204:207], a[252:255], v[60:63]
s_waitcnt lgkmcnt(8)
v_mfma_f32_16x16x32_bf16 v[128:131], v[160:163], a[0:3], 0
ds_read_b64_tr_b16 v[192:193], v216 offset:51200
ds_read_b64_tr_b16 v[194:195], v216 offset:52224
v_mfma_f32_16x16x32_bf16 v[136:139], v[160:163], a[4:7], 0
v_mfma_f32_16x16x32_bf16 v[132:135], v[164:167], a[0:3], 0
ds_read_b64_tr_b16 v[196:197], v217 offset:51200
ds_read_b64_tr_b16 v[198:199], v217 offset:52224
v_mfma_f32_16x16x32_bf16 v[140:143], v[164:167], a[4:7], 0
v_mfma_f32_16x16x32_bf16 v[128:131], v[168:171], a[8:11], v[128:131]
ds_read_b64_tr_b16 v[200:201], v216 offset:55296
ds_read_b64_tr_b16 v[202:203], v216 offset:56320
v_mfma_f32_16x16x32_bf16 v[136:139], v[168:171], a[12:15], v[136:139]
buffer_load_dwordx4 v229, s[44:47], s14 offen offset:3072 lds
v_mul_f32 v243, v0, v0
v_mfma_f32_16x16x32_bf16 v[132:135], v[172:175], a[8:11], v[132:135]
ds_read_b64_tr_b16 v[204:205], v217 offset:55296
ds_read_b64_tr_b16 v[206:207], v217 offset:56320
v_mfma_f32_16x16x32_bf16 v[140:143], v[172:175], a[12:15], v[140:143]
s_add_u32 m0, s77, 65536
v_fmaak_f32 v243, v243, v223, 0xc0135761
s_waitcnt lgkmcnt(8)
v_mfma_f32_16x16x32_bf16 v[128:131], v[176:179], a[16:19], v[128:131]
ds_read_b64_tr_b16 v[160:161], v216 offset:59392
ds_read_b64_tr_b16 v[162:163], v216 offset:60416
v_mfma_f32_16x16x32_bf16 v[136:139], v[176:179], a[20:23], v[136:139]
v_mul_f32 v243, v243, v0
v_mul_f32 v0, v0, v32
v_mfma_f32_16x16x32_bf16 v[132:135], v[180:183], a[16:19], v[132:135]
ds_read_b64_tr_b16 v[164:165], v217 offset:59392
ds_read_b64_tr_b16 v[166:167], v217 offset:60416
v_mfma_f32_16x16x32_bf16 v[140:143], v[180:183], a[20:23], v[140:143]
v_exp_f32 v243, v243
v_mfma_f32_16x16x32_bf16 v[128:131], v[184:187], a[24:27], v[128:131]
ds_read_b64_tr_b16 v[168:169], v216 offset:63488
ds_read_b64_tr_b16 v[170:171], v216 offset:64512
v_mfma_f32_16x16x32_bf16 v[136:139], v[184:187], a[28:31], v[136:139]
v_add_f32 v243, 1.0, v243
v_mul_f32 v244, v1, v1
v_mfma_f32_16x16x32_bf16 v[132:135], v[188:191], a[24:27], v[132:135]
ds_read_b64_tr_b16 v[172:173], v217 offset:63488
ds_read_b64_tr_b16 v[174:175], v217 offset:64512
v_mfma_f32_16x16x32_bf16 v[140:143], v[188:191], a[28:31], v[140:143]
s_waitcnt vmcnt(23) lgkmcnt(8)
s_barrier
v_mfma_f32_16x16x32_bf16 v[144:147], v[192:195], a[0:3], 0
ds_read_b64_tr_b16 v[176:177], v218 offset:16384
ds_read_b64_tr_b16 v[178:179], v218 offset:17408
v_mfma_f32_16x16x32_bf16 v[152:155], v[192:195], a[4:7], 0
buffer_load_dwordx4 v228, s[40:43], s16 offen offset:0 lds
v_mfma_f32_16x16x32_bf16 v[148:151], v[196:199], a[0:3], 0
ds_read_b64_tr_b16 v[180:181], v219 offset:16384
ds_read_b64_tr_b16 v[182:183], v219 offset:17408
v_mfma_f32_16x16x32_bf16 v[156:159], v[196:199], a[4:7], 0
v_rcp_f32 v243, v243
v_mfma_f32_16x16x32_bf16 v[144:147], v[200:203], a[8:11], v[144:147]
ds_read_b64_tr_b16 v[184:185], v218 offset:20480
ds_read_b64_tr_b16 v[186:187], v218 offset:21504
v_mfma_f32_16x16x32_bf16 v[152:155], v[200:203], a[12:15], v[152:155]
v_mul_f32 v0, v0, v243
v_fmaak_f32 v244, v244, v223, 0xc0135761
v_mfma_f32_16x16x32_bf16 v[148:151], v[204:207], a[8:11], v[148:151]
ds_read_b64_tr_b16 v[188:189], v219 offset:20480
ds_read_b64_tr_b16 v[190:191], v219 offset:21504
v_mfma_f32_16x16x32_bf16 v[156:159], v[204:207], a[12:15], v[156:159]
v_mul_f32 v244, v244, v1
s_waitcnt lgkmcnt(8)
v_mfma_f32_16x16x32_bf16 v[144:147], v[160:163], a[16:19], v[144:147]
ds_read_b64_tr_b16 v[192:193], v218 offset:24576
ds_read_b64_tr_b16 v[194:195], v218 offset:25600
v_mfma_f32_16x16x32_bf16 v[152:155], v[160:163], a[20:23], v[152:155]
buffer_load_dwordx4 v229, s[40:43], s16 offen offset:1024 lds
v_mfma_f32_16x16x32_bf16 v[148:151], v[164:167], a[16:19], v[148:151]
ds_read_b64_tr_b16 v[196:197], v219 offset:24576
ds_read_b64_tr_b16 v[198:199], v219 offset:25600
v_mfma_f32_16x16x32_bf16 v[156:159], v[164:167], a[20:23], v[156:159]
v_exp_f32 v244, v244
v_mfma_f32_16x16x32_bf16 v[144:147], v[168:171], a[24:27], v[144:147]
ds_read_b64_tr_b16 v[200:201], v218 offset:28672
ds_read_b64_tr_b16 v[202:203], v218 offset:29696
v_mul_f32 v1, v1, v33
v_mfma_f32_16x16x32_bf16 v[152:155], v[168:171], a[28:31], v[152:155]
buffer_load_dwordx4 v228, s[44:47], s16 offen offset:2048 lds
v_mfma_f32_16x16x32_bf16 v[148:151], v[172:175], a[24:27], v[148:151]
ds_read_b64_tr_b16 v[204:205], v219 offset:28672
ds_read_b64_tr_b16 v[206:207], v219 offset:29696
v_add_f32 v244, 1.0, v244
v_mfma_f32_16x16x32_bf16 v[156:159], v[172:175], a[28:31], v[156:159]
v_mul_f32 v245, v2, v2
s_waitcnt lgkmcnt(8)
v_mfma_f32_16x16x32_bf16 v[128:131], v[176:179], a[32:35], v[128:131]
ds_read_b64_tr_b16 v[160:161], v218 offset:18432
ds_read_b64_tr_b16 v[162:163], v218 offset:19456
v_mfma_f32_16x16x32_bf16 v[136:139], v[176:179], a[36:39], v[136:139]
v_rcp_f32 v244, v244
v_mfma_f32_16x16x32_bf16 v[132:135], v[180:183], a[32:35], v[132:135]
ds_read_b64_tr_b16 v[164:165], v219 offset:18432
ds_read_b64_tr_b16 v[166:167], v219 offset:19456
v_mfma_f32_16x16x32_bf16 v[140:143], v[180:183], a[36:39], v[140:143]
v_mul_f32 v1, v1, v244
v_cvt_pk_bf16_f32 v32, v0, v1
v_mfma_f32_16x16x32_bf16 v[128:131], v[184:187], a[40:43], v[128:131]
ds_read_b64_tr_b16 v[168:169], v218 offset:22528
ds_read_b64_tr_b16 v[170:171], v218 offset:23552
v_mfma_f32_16x16x32_bf16 v[136:139], v[184:187], a[44:47], v[136:139]
buffer_load_dwordx4 v229, s[44:47], s16 offen offset:3072 lds
v_mfma_f32_16x16x32_bf16 v[132:135], v[188:191], a[40:43], v[132:135]
ds_read_b64_tr_b16 v[172:173], v219 offset:22528
ds_read_b64_tr_b16 v[174:175], v219 offset:23552
v_mfma_f32_16x16x32_bf16 v[140:143], v[188:191], a[44:47], v[140:143]
s_add_u32 m0, s77, 49152
s_waitcnt lgkmcnt(8)
v_mfma_f32_16x16x32_bf16 v[128:131], v[192:195], a[48:51], v[128:131]
ds_read_b64_tr_b16 v[176:177], v218 offset:26624
ds_read_b64_tr_b16 v[178:179], v218 offset:27648
v_fmaak_f32 v245, v245, v223, 0xc0135761
v_mfma_f32_16x16x32_bf16 v[136:139], v[192:195], a[52:55], v[136:139]
v_mul_f32 v245, v245, v2
v_mul_f32 v2, v2, v34
v_mfma_f32_16x16x32_bf16 v[132:135], v[196:199], a[48:51], v[132:135]
ds_read_b64_tr_b16 v[180:181], v219 offset:26624
ds_read_b64_tr_b16 v[182:183], v219 offset:27648
v_mfma_f32_16x16x32_bf16 v[140:143], v[196:199], a[52:55], v[140:143]
v_exp_f32 v245, v245
v_mfma_f32_16x16x32_bf16 v[128:131], v[200:203], a[56:59], v[128:131]
ds_read_b64_tr_b16 v[184:185], v218 offset:30720
ds_read_b64_tr_b16 v[186:187], v218 offset:31744
v_mfma_f32_16x16x32_bf16 v[136:139], v[200:203], a[60:63], v[136:139]
v_add_f32 v245, 1.0, v245
v_mul_f32 v246, v3, v3
v_mfma_f32_16x16x32_bf16 v[132:135], v[204:207], a[56:59], v[132:135]
ds_read_b64_tr_b16 v[188:189], v219 offset:30720
ds_read_b64_tr_b16 v[190:191], v219 offset:31744
v_mfma_f32_16x16x32_bf16 v[140:143], v[204:207], a[60:63], v[140:143]
s_waitcnt vmcnt(21) lgkmcnt(8)
s_barrier
v_mfma_f32_16x16x32_bf16 v[144:147], v[160:163], a[32:35], v[144:147]
ds_read_b64_tr_b16 v[192:193], v216 offset:0
ds_read_b64_tr_b16 v[194:195], v216 offset:1024
v_mfma_f32_16x16x32_bf16 v[152:155], v[160:163], a[36:39], v[152:155]
v_rcp_f32 v245, v245
v_mfma_f32_16x16x32_bf16 v[148:151], v[164:167], a[32:35], v[148:151]
ds_read_b64_tr_b16 v[196:197], v217 offset:0
ds_read_b64_tr_b16 v[198:199], v217 offset:1024
v_mfma_f32_16x16x32_bf16 v[156:159], v[164:167], a[36:39], v[156:159]
buffer_load_dwordx4 v224, s[48:51], s18 offen offset:0 lds
v_mfma_f32_16x16x32_bf16 v[144:147], v[168:171], a[40:43], v[144:147]
ds_read_b64_tr_b16 v[200:201], v216 offset:4096
ds_read_b64_tr_b16 v[202:203], v216 offset:5120
v_mfma_f32_16x16x32_bf16 v[152:155], v[168:171], a[44:47], v[152:155]
v_mul_f32 v2, v2, v245
v_fmaak_f32 v246, v246, v223, 0xc0135761
v_mfma_f32_16x16x32_bf16 v[148:151], v[172:175], a[40:43], v[148:151]
ds_read_b64_tr_b16 v[204:205], v217 offset:4096
ds_read_b64_tr_b16 v[206:207], v217 offset:5120
v_mfma_f32_16x16x32_bf16 v[156:159], v[172:175], a[44:47], v[156:159]
v_mul_f32 v246, v246, v3
s_waitcnt lgkmcnt(8)
v_mfma_f32_16x16x32_bf16 v[144:147], v[176:179], a[48:51], v[144:147]
ds_read_b64_tr_b16 v[160:161], v216 offset:8192
ds_read_b64_tr_b16 v[162:163], v216 offset:9216
v_mfma_f32_16x16x32_bf16 v[152:155], v[176:179], a[52:55], v[152:155]
v_exp_f32 v246, v246
v_mfma_f32_16x16x32_bf16 v[148:151], v[180:183], a[48:51], v[148:151]
ds_read_b64_tr_b16 v[164:165], v217 offset:8192
ds_read_b64_tr_b16 v[166:167], v217 offset:9216
v_mfma_f32_16x16x32_bf16 v[156:159], v[180:183], a[52:55], v[156:159]
buffer_load_dwordx4 v225, s[48:51], s18 offen offset:1024 lds
v_mfma_f32_16x16x32_bf16 v[144:147], v[184:187], a[56:59], v[144:147]
ds_read_b64_tr_b16 v[168:169], v216 offset:12288
ds_read_b64_tr_b16 v[170:171], v216 offset:13312
v_mfma_f32_16x16x32_bf16 v[152:155], v[184:187], a[60:63], v[152:155]
v_mul_f32 v3, v3, v35
v_add_f32 v246, 1.0, v246
v_mfma_f32_16x16x32_bf16 v[148:151], v[188:191], a[56:59], v[148:151]
ds_read_b64_tr_b16 v[172:173], v217 offset:12288
ds_read_b64_tr_b16 v[174:175], v217 offset:13312
v_mfma_f32_16x16x32_bf16 v[156:159], v[188:191], a[60:63], v[156:159]
v_mul_f32 v247, v4, v4
s_waitcnt lgkmcnt(8)
v_mfma_f32_16x16x32_bf16 v[128:131], v[192:195], a[64:67], v[128:131]
ds_read_b64_tr_b16 v[176:177], v216 offset:2048
ds_read_b64_tr_b16 v[178:179], v216 offset:3072
v_mfma_f32_16x16x32_bf16 v[136:139], v[192:195], a[68:71], v[136:139]
buffer_load_dwordx4 v226, s[48:51], s18 offen offset:2048 lds
v_mfma_f32_16x16x32_bf16 v[132:135], v[196:199], a[64:67], v[132:135]
ds_read_b64_tr_b16 v[180:181], v217 offset:2048
ds_read_b64_tr_b16 v[182:183], v217 offset:3072
v_mfma_f32_16x16x32_bf16 v[140:143], v[196:199], a[68:71], v[140:143]
v_rcp_f32 v246, v246
v_mfma_f32_16x16x32_bf16 v[128:131], v[200:203], a[72:75], v[128:131]
ds_read_b64_tr_b16 v[184:185], v216 offset:6144
ds_read_b64_tr_b16 v[186:187], v216 offset:7168
v_mfma_f32_16x16x32_bf16 v[136:139], v[200:203], a[76:79], v[136:139]
v_mul_f32 v3, v3, v246
v_cvt_pk_bf16_f32 v33, v2, v3
v_mfma_f32_16x16x32_bf16 v[132:135], v[204:207], a[72:75], v[132:135]
ds_read_b64_tr_b16 v[188:189], v217 offset:6144
ds_read_b64_tr_b16 v[190:191], v217 offset:7168
v_mfma_f32_16x16x32_bf16 v[140:143], v[204:207], a[76:79], v[140:143]
v_fmaak_f32 v247, v247, v223, 0xc0135761
s_waitcnt lgkmcnt(8)
v_mfma_f32_16x16x32_bf16 v[128:131], v[160:163], a[80:83], v[128:131]
ds_read_b64_tr_b16 v[192:193], v216 offset:10240
ds_read_b64_tr_b16 v[194:195], v216 offset:11264
v_mfma_f32_16x16x32_bf16 v[136:139], v[160:163], a[84:87], v[136:139]
v_cmp_ne_u32 vcc, s78, v238
s_nop 0
s_cbranch_vccz LRa0_%=
LSa0_%=:
s_sub_u32 s84, s84, 1
s_cbranch_scc1 LZa0_%=
buffer_load_dword v238, v222, s[52:55], 0 offen offset:0 sc1
s_waitcnt vmcnt(0)
v_cmp_ne_u32 vcc, s78, v238
s_nop 0
s_cbranch_vccnz LSa0_%=
LZa0_%=:
s_max_i32 s84, s84, 0
LRa0_%=:
v_mfma_f32_16x16x32_bf16 v[132:135], v[164:167], a[80:83], v[132:135]
ds_read_b64_tr_b16 v[196:197], v217 offset:10240
ds_read_b64_tr_b16 v[198:199], v217 offset:11264
v_mfma_f32_16x16x32_bf16 v[140:143], v[164:167], a[84:87], v[140:143]
buffer_load_dwordx4 v227, s[48:51], s18 offen offset:3072 lds
v_mul_f32 v247, v247, v4
v_mfma_f32_16x16x32_bf16 v[128:131], v[168:171], a[88:91], v[128:131]
ds_read_b64_tr_b16 v[200:201], v216 offset:14336
ds_read_b64_tr_b16 v[202:203], v216 offset:15360
v_mfma_f32_16x16x32_bf16 v[136:139], v[168:171], a[92:95], v[136:139]
v_exp_f32 v247, v247
v_mfma_f32_16x16x32_bf16 v[132:135], v[172:175], a[88:91], v[132:135]
ds_read_b64_tr_b16 v[204:205], v217 offset:14336
ds_read_b64_tr_b16 v[206:207], v217 offset:15360
v_mul_f32 v4, v4, v36
v_mfma_f32_16x16x32_bf16 v[140:143], v[172:175], a[92:95], v[140:143]
s_waitcnt vmcnt(21) lgkmcnt(8)
s_barrier
v_mfma_f32_16x16x32_bf16 v[144:147], v[176:179], a[64:67], v[144:147]
ds_read_b64_tr_b16 v[160:161], v218 offset:32768
ds_read_b64_tr_b16 v[162:163], v218 offset:33792
v_add_f32 v247, 1.0, v247
v_mfma_f32_16x16x32_bf16 v[152:155], v[176:179], a[68:71], v[152:155]
s_waitcnt vmcnt(10)
s_mov_b64 exec, 1
buffer_store_dword v234, off, s[52:55], s79 sc1
s_mov_b64 exec, -1
v_mfma_f32_16x16x32_bf16 v[148:151], v[180:183], a[64:67], v[148:151]
ds_read_b64_tr_b16 v[164:165], v219 offset:32768
ds_read_b64_tr_b16 v[166:167], v219 offset:33792
v_rcp_f32 v247, v247
v_mfma_f32_16x16x32_bf16 v[156:159], v[180:183], a[68:71], v[156:159]
s_add_u32 s79, s79, 2048
v_mul_f32 v4, v4, v247
v_mfma_f32_16x16x32_bf16 v[144:147], v[184:187], a[72:75], v[144:147]
ds_read_b64_tr_b16 v[168:169], v218 offset:36864
ds_read_b64_tr_b16 v[170:171], v218 offset:37888
v_mfma_f32_16x16x32_bf16 v[152:155], v[184:187], a[76:79], v[152:155]
buffer_load_dwordx4 a[0:3], v232, s[52:55], s94 offen offset:0 sc1
v_mfma_f32_16x16x32_bf16 v[148:151], v[188:191], a[72:75], v[148:151]
ds_read_b64_tr_b16 v[172:173], v219 offset:36864
ds_read_b64_tr_b16 v[174:175], v219 offset:37888
v_mul_f32 v248, v5, v5
v_mfma_f32_16x16x32_bf16 v[156:159], v[188:191], a[76:79], v[156:159]
s_add_u32 m0, s77, 81920
s_waitcnt lgkmcnt(8)
v_mfma_f32_16x16x32_bf16 v[144:147], v[192:195], a[80:83], v[144:147]
ds_read_b64_tr_b16 v[176:177], v218 offset:40960
ds_read_b64_tr_b16 v[178:179], v218 offset:41984
v_fmaak_f32 v248, v248, v223, 0xc0135761
v_mfma_f32_16x16x32_bf16 v[152:155], v[192:195], a[84:87], v[152:155]
buffer_load_dwordx4 v224, s[48:51], s19 offen offset:0 lds
v_mfma_f32_16x16x32_bf16 v[148:151], v[196:199], a[80:83], v[148:151]
ds_read_b64_tr_b16 v[180:181], v219 offset:40960
ds_read_b64_tr_b16 v[182:183], v219 offset:41984
v_mul_f32 v248, v248, v5
v_mfma_f32_16x16x32_bf16 v[156:159], v[196:199], a[84:87], v[156:159]
buffer_load_dwordx4 a[4:7], v233, s[52:55], s94 offen offset:0 sc1
v_mfma_f32_16x16x32_bf16 v[144:147], v[200:203], a[88:91], v[144:147]
ds_read_b64_tr_b16 v[184:185], v218 offset:45056
ds_read_b64_tr_b16 v[186:187], v218 offset:46080
v_exp_f32 v248, v248
v_mfma_f32_16x16x32_bf16 v[152:155], v[200:203], a[92:95], v[152:155]
buffer_load_dwordx4 v225, s[48:51], s19 offen offset:1024 lds
v_mfma_f32_16x16x32_bf16 v[148:151], v[204:207], a[88:91], v[148:151]
ds_read_b64_tr_b16 v[188:189], v219 offset:45056
ds_read_b64_tr_b16 v[190:191], v219 offset:46080
v_mul_f32 v5, v5, v37
v_mfma_f32_16x16x32_bf16 v[156:159], v[204:207], a[92:95], v[156:159]
v_add_f32 v248, 1.0, v248
s_waitcnt lgkmcnt(8)
v_mfma_f32_16x16x32_bf16 v[128:131], v[160:163], a[96:99], v[128:131]
ds_read_b64_tr_b16 v[192:193], v218 offset:34816
ds_read_b64_tr_b16 v[194:195], v218 offset:35840
v_mfma_f32_16x16x32_bf16 v[136:139], v[160:163], a[100:103], v[136:139]
buffer_load_dwordx4 a[8:11], v232, s[52:55], s94 offen offset:1024 sc1
v_mfma_f32_16x16x32_bf16 v[132:135], v[164:167], a[96:99], v[132:135]
ds_read_b64_tr_b16 v[196:197], v219 offset:34816
ds_read_b64_tr_b16 v[198:199], v219 offset:35840
v_rcp_f32 v248, v248
v_mfma_f32_16x16x32_bf16 v[140:143], v[164:167], a[100:103], v[140:143]
v_mul_f32 v5, v5, v248
v_cvt_pk_bf16_f32 v34, v4, v5
v_mfma_f32_16x16x32_bf16 v[128:131], v[168:171], a[104:107], v[128:131]
ds_read_b64_tr_b16 v[200:201], v218 offset:38912
ds_read_b64_tr_b16 v[202:203], v218 offset:39936
v_mfma_f32_16x16x32_bf16 v[136:139], v[168:171], a[108:111], v[136:139]
buffer_load_dwordx4 v226, s[48:51], s19 offen offset:2048 lds
v_mfma_f32_16x16x32_bf16 v[132:135], v[172:175], a[104:107], v[132:135]
ds_read_b64_tr_b16 v[204:205], v219 offset:38912
ds_read_b64_tr_b16 v[206:207], v219 offset:39936
v_mfma_f32_16x16x32_bf16 v[140:143], v[172:175], a[108:111], v[140:143]
v_mul_f32 v249, v6, v6
s_waitcnt lgkmcnt(8)
v_mfma_f32_16x16x32_bf16 v[128:131], v[176:179], a[112:115], v[128:131]
ds_read_b64_tr_b16 v[160:161], v218 offset:43008
ds_read_b64_tr_b16 v[162:163], v218 offset:44032
v_mfma_f32_16x16x32_bf16 v[136:139], v[176:179], a[116:119], v[136:139]
buffer_load_dwordx4 a[12:15], v233, s[52:55], s94 offen offset:1024 sc1
v_fmaak_f32 v249, v249, v223, 0xc0135761
v_mfma_f32_16x16x32_bf16 v[132:135], v[180:183], a[112:115], v[132:135]
ds_read_b64_tr_b16 v[164:165], v219 offset:43008
ds_read_b64_tr_b16 v[166:167], v219 offset:44032
v_mfma_f32_16x16x32_bf16 v[140:143], v[180:183], a[116:119], v[140:143]
buffer_load_dwordx4 v227, s[48:51], s19 offen offset:3072 lds
v_mul_f32 v249, v249, v6
v_mfma_f32_16x16x32_bf16 v[128:131], v[184:187], a[120:123], v[128:131]
ds_read_b64_tr_b16 v[168:169], v218 offset:47104
ds_read_b64_tr_b16 v[170:171], v218 offset:48128
v_mfma_f32_16x16x32_bf16 v[136:139], v[184:187], a[124:127], v[136:139]
s_add_u32 m0, s77, 0
v_mul_f32 v6, v6, v38
v_mfma_f32_16x16x32_bf16 v[132:135], v[188:191], a[120:123], v[132:135]
ds_read_b64_tr_b16 v[172:173], v219 offset:47104
ds_read_b64_tr_b16 v[174:175], v219 offset:48128
v_mfma_f32_16x16x32_bf16 v[140:143], v[188:191], a[124:127], v[140:143]
v_exp_f32 v249, v249
s_waitcnt lgkmcnt(8)
s_barrier
v_mfma_f32_16x16x32_bf16 v[144:147], v[192:195], a[96:99], v[144:147]
ds_read_b64_tr_b16 v[176:177], v216 offset:16384
ds_read_b64_tr_b16 v[178:179], v216 offset:17408
v_mfma_f32_16x16x32_bf16 v[152:155], v[192:195], a[100:103], v[152:155]
buffer_load_dwordx4 v224, s[48:51], s20 offen offset:0 lds
v_add_f32 v249, 1.0, v249
v_mfma_f32_16x16x32_bf16 v[148:151], v[196:199], a[96:99], v[148:151]
ds_read_b64_tr_b16 v[180:181], v217 offset:16384
ds_read_b64_tr_b16 v[182:183], v217 offset:17408
v_mfma_f32_16x16x32_bf16 v[156:159], v[196:199], a[100:103], v[156:159]
buffer_load_dwordx4 a[16:19], v232, s[52:55], s95 offen offset:0 sc1
v_rcp_f32 v249, v249
v_mfma_f32_16x16x32_bf16 v[144:147], v[200:203], a[104:107], v[144:147]
ds_read_b64_tr_b16 v[184:185], v216 offset:20480
ds_read_b64_tr_b16 v[186:187], v216 offset:21504
v_mfma_f32_16x16x32_bf16 v[152:155], v[200:203], a[108:111], v[152:155]
v_mul_f32 v6, v6, v249
v_mul_f32 v250, v7, v7
v_mfma_f32_16x16x32_bf16 v[148:151], v[204:207], a[104:107], v[148:151]
ds_read_b64_tr_b16 v[188:189], v217 offset:20480
ds_read_b64_tr_b16 v[190:191], v217 offset:21504
v_mfma_f32_16x16x32_bf16 v[156:159], v[204:207], a[108:111], v[156:159]
v_fmaak_f32 v250, v250, v223, 0xc0135761
s_waitcnt lgkmcnt(8)
v_mfma_f32_16x16x32_bf16 v[144:147], v[160:163], a[112:115], v[144:147]
ds_read_b64_tr_b16 v[192:193], v216 offset:24576
ds_read_b64_tr_b16 v[194:195], v216 offset:25600
v_mfma_f32_16x16x32_bf16 v[152:155], v[160:163], a[116:119], v[152:155]
buffer_load_dwordx4 v225, s[48:51], s20 offen offset:1024 lds
v_mfma_f32_16x16x32_bf16 v[148:151], v[164:167], a[112:115], v[148:151]
ds_read_b64_tr_b16 v[196:197], v217 offset:24576
ds_read_b64_tr_b16 v[198:199], v217 offset:25600
v_mfma_f32_16x16x32_bf16 v[156:159], v[164:167], a[116:119], v[156:159]
buffer_load_dwordx4 a[20:23], v233, s[52:55], s95 offen offset:0 sc1
v_mul_f32 v250, v250, v7
v_mfma_f32_16x16x32_bf16 v[144:147], v[168:171], a[120:123], v[144:147]
ds_read_b64_tr_b16 v[200:201], v216 offset:28672
ds_read_b64_tr_b16 v[202:203], v216 offset:29696
v_mfma_f32_16x16x32_bf16 v[152:155], v[168:171], a[124:127], v[152:155]
buffer_load_dwordx4 v226, s[48:51], s20 offen offset:2048 lds
v_exp_f32 v250, v250
v_mfma_f32_16x16x32_bf16 v[148:151], v[172:175], a[120:123], v[148:151]
)"
R"(
ds_read_b64_tr_b16 v[204:205], v217 offset:28672
ds_read_b64_tr_b16 v[206:207], v217 offset:29696
v_mul_f32 v7, v7, v39
v_mfma_f32_16x16x32_bf16 v[156:159], v[172:175], a[124:127], v[156:159]
v_add_f32 v250, 1.0, v250
s_waitcnt lgkmcnt(8)
v_mfma_f32_16x16x32_bf16 v[128:131], v[176:179], a[128:131], v[128:131]
ds_read_b64_tr_b16 v[160:161], v216 offset:18432
ds_read_b64_tr_b16 v[162:163], v216 offset:19456
v_mfma_f32_16x16x32_bf16 v[136:139], v[176:179], a[132:135], v[136:139]
buffer_load_dwordx4 a[24:27], v232, s[52:55], s95 offen offset:1024 sc1
v_mfma_f32_16x16x32_bf16 v[132:135], v[180:183], a[128:131], v[132:135]
ds_read_b64_tr_b16 v[164:165], v217 offset:18432
ds_read_b64_tr_b16 v[166:167], v217 offset:19456
v_rcp_f32 v250, v250
v_mfma_f32_16x16x32_bf16 v[140:143], v[180:183], a[132:135], v[140:143]
v_mul_f32 v7, v7, v250
v_cvt_pk_bf16_f32 v35, v6, v7
v_mfma_f32_16x16x32_bf16 v[128:131], v[184:187], a[136:139], v[128:131]
ds_read_b64_tr_b16 v[168:169], v216 offset:22528
ds_read_b64_tr_b16 v[170:171], v216 offset:23552
v_mfma_f32_16x16x32_bf16 v[136:139], v[184:187], a[140:143], v[136:139]
buffer_load_dwordx4 v227, s[48:51], s20 offen offset:3072 lds
v_mfma_f32_16x16x32_bf16 v[132:135], v[188:191], a[136:139], v[132:135]
ds_read_b64_tr_b16 v[172:173], v217 offset:22528
ds_read_b64_tr_b16 v[174:175], v217 offset:23552
v_mfma_f32_16x16x32_bf16 v[140:143], v[188:191], a[140:143], v[140:143]
s_add_u32 m0, s77, 98304
s_waitcnt lgkmcnt(8)
v_mfma_f32_16x16x32_bf16 v[128:131], v[192:195], a[144:147], v[128:131]
ds_read_b64_tr_b16 v[176:177], v216 offset:26624
ds_read_b64_tr_b16 v[178:179], v216 offset:27648
v_min_u32 v32, -2, v32
v_mfma_f32_16x16x32_bf16 v[136:139], v[192:195], a[148:151], v[136:139]
buffer_load_dwordx4 a[28:31], v233, s[52:55], s95 offen offset:1024 sc1
v_mfma_f32_16x16x32_bf16 v[132:135], v[196:199], a[144:147], v[132:135]
ds_read_b64_tr_b16 v[180:181], v217 offset:26624
ds_read_b64_tr_b16 v[182:183], v217 offset:27648
buffer_store_dwordx4 v[32:35], v220, s[52:55], s92 offen offset:0 sc1
v_mfma_f32_16x16x32_bf16 v[140:143], v[196:199], a[148:151], v[140:143]
v_mul_f32 v251, v8, v8
v_fmaak_f32 v251, v251, v223, 0xc0135761
v_mfma_f32_16x16x32_bf16 v[128:131], v[200:203], a[152:155], v[128:131]
ds_read_b64_tr_b16 v[184:185], v216 offset:30720
ds_read_b64_tr_b16 v[186:187], v216 offset:31744
v_mfma_f32_16x16x32_bf16 v[136:139], v[200:203], a[156:159], v[136:139]
v_mul_f32 v251, v251, v8
v_mul_f32 v8, v8, v40
v_mfma_f32_16x16x32_bf16 v[132:135], v[204:207], a[152:155], v[132:135]
ds_read_b64_tr_b16 v[188:189], v217 offset:30720
ds_read_b64_tr_b16 v[190:191], v217 offset:31744
v_mfma_f32_16x16x32_bf16 v[140:143], v[204:207], a[156:159], v[140:143]
s_waitcnt lgkmcnt(8)
s_barrier
v_mfma_f32_16x16x32_bf16 v[144:147], v[160:163], a[128:131], v[144:147]
ds_read_b64_tr_b16 v[192:193], v218 offset:49152
ds_read_b64_tr_b16 v[194:195], v218 offset:50176
v_mfma_f32_16x16x32_bf16 v[152:155], v[160:163], a[132:135], v[152:155]
buffer_load_dwordx4 v224, s[48:51], s21 offen offset:0 lds
v_mfma_f32_16x16x32_bf16 v[148:151], v[164:167], a[128:131], v[148:151]
ds_read_b64_tr_b16 v[196:197], v219 offset:49152
ds_read_b64_tr_b16 v[198:199], v219 offset:50176
v_mfma_f32_16x16x32_bf16 v[156:159], v[164:167], a[132:135], v[156:159]
buffer_load_dwordx4 a[32:35], v232, s[52:55], s96 offen offset:0 sc1
v_mfma_f32_16x16x32_bf16 v[144:147], v[168:171], a[136:139], v[144:147]
ds_read_b64_tr_b16 v[200:201], v218 offset:53248
ds_read_b64_tr_b16 v[202:203], v218 offset:54272
v_exp_f32 v251, v251
v_mfma_f32_16x16x32_bf16 v[152:155], v[168:171], a[140:143], v[152:155]
v_add_f32 v251, 1.0, v251
v_mul_f32 v252, v9, v9
v_mfma_f32_16x16x32_bf16 v[148:151], v[172:175], a[136:139], v[148:151]
ds_read_b64_tr_b16 v[204:205], v219 offset:53248
ds_read_b64_tr_b16 v[206:207], v219 offset:54272
v_mfma_f32_16x16x32_bf16 v[156:159], v[172:175], a[140:143], v[156:159]
v_fmaak_f32 v252, v252, v223, 0xc0135761
s_waitcnt lgkmcnt(8)
v_mfma_f32_16x16x32_bf16 v[144:147], v[176:179], a[144:147], v[144:147]
ds_read_b64_tr_b16 v[160:161], v218 offset:57344
ds_read_b64_tr_b16 v[162:163], v218 offset:58368
v_mfma_f32_16x16x32_bf16 v[152:155], v[176:179], a[148:151], v[152:155]
buffer_load_dwordx4 v225, s[48:51], s21 offen offset:1024 lds
v_mfma_f32_16x16x32_bf16 v[148:151], v[180:183], a[144:147], v[148:151]
ds_read_b64_tr_b16 v[164:165], v219 offset:57344
ds_read_b64_tr_b16 v[166:167], v219 offset:58368
v_mfma_f32_16x16x32_bf16 v[156:159], v[180:183], a[148:151], v[156:159]
buffer_load_dwordx4 a[36:39], v233, s[52:55], s96 offen offset:0 sc1
v_rcp_f32 v251, v251
v_mfma_f32_16x16x32_bf16 v[144:147], v[184:187], a[152:155], v[144:147]
ds_read_b64_tr_b16 v[168:169], v218 offset:61440
ds_read_b64_tr_b16 v[170:171], v218 offset:62464
v_mfma_f32_16x16x32_bf16 v[152:155], v[184:187], a[156:159], v[152:155]
buffer_load_dwordx4 v226, s[48:51], s21 offen offset:2048 lds
v_mul_f32 v8, v8, v251
v_mfma_f32_16x16x32_bf16 v[148:151], v[188:191], a[152:155], v[148:151]
ds_read_b64_tr_b16 v[172:173], v219 offset:61440
ds_read_b64_tr_b16 v[174:175], v219 offset:62464
v_mfma_f32_16x16x32_bf16 v[156:159], v[188:191], a[156:159], v[156:159]
LC18_%=:
s_cbranch_execz LC22_%=
v_mul_f32 v252, v252, v9
s_waitcnt lgkmcnt(8)
v_mfma_f32_16x16x32_bf16 v[128:131], v[192:195], a[160:163], v[128:131]
ds_read_b64_tr_b16 v[176:177], v218 offset:51200
ds_read_b64_tr_b16 v[178:179], v218 offset:52224
v_mfma_f32_16x16x32_bf16 v[136:139], v[192:195], a[164:167], v[136:139]
buffer_load_dwordx4 a[40:43], v232, s[52:55], s96 offen offset:1024 sc1
v_exp_f32 v252, v252
v_mfma_f32_16x16x32_bf16 v[132:135], v[196:199], a[160:163], v[132:135]
ds_read_b64_tr_b16 v[180:181], v219 offset:51200
ds_read_b64_tr_b16 v[182:183], v219 offset:52224
v_mfma_f32_16x16x32_bf16 v[140:143], v[196:199], a[164:167], v[140:143]
v_mul_f32 v9, v9, v41
v_add_f32 v252, 1.0, v252
v_mfma_f32_16x16x32_bf16 v[128:131], v[200:203], a[168:171], v[128:131]
ds_read_b64_tr_b16 v[184:185], v218 offset:55296
ds_read_b64_tr_b16 v[186:187], v218 offset:56320
v_mfma_f32_16x16x32_bf16 v[136:139], v[200:203], a[172:175], v[136:139]
buffer_load_dwordx4 v227, s[48:51], s21 offen offset:3072 lds
v_mfma_f32_16x16x32_bf16 v[132:135], v[204:207], a[168:171], v[132:135]
ds_read_b64_tr_b16 v[188:189], v219 offset:55296
ds_read_b64_tr_b16 v[190:191], v219 offset:56320
v_mfma_f32_16x16x32_bf16 v[140:143], v[204:207], a[172:175], v[140:143]
s_add_u32 m0, s77, 16384
v_rcp_f32 v252, v252
s_waitcnt lgkmcnt(8)
v_mfma_f32_16x16x32_bf16 v[128:131], v[160:163], a[176:179], v[128:131]
ds_read_b64_tr_b16 v[192:193], v218 offset:59392
ds_read_b64_tr_b16 v[194:195], v218 offset:60416
v_mfma_f32_16x16x32_bf16 v[136:139], v[160:163], a[180:183], v[136:139]
buffer_load_dwordx4 a[44:47], v233, s[52:55], s96 offen offset:1024 sc1
v_mul_f32 v9, v9, v252
v_mfma_f32_16x16x32_bf16 v[132:135], v[164:167], a[176:179], v[132:135]
ds_read_b64_tr_b16 v[196:197], v219 offset:59392
ds_read_b64_tr_b16 v[198:199], v219 offset:60416
v_mfma_f32_16x16x32_bf16 v[140:143], v[164:167], a[180:183], v[140:143]
v_cvt_pk_bf16_f32 v40, v8, v9
v_mul_f32 v253, v10, v10
v_mfma_f32_16x16x32_bf16 v[128:131], v[168:171], a[184:187], v[128:131]
ds_read_b64_tr_b16 v[200:201], v218 offset:63488
ds_read_b64_tr_b16 v[202:203], v218 offset:64512
v_mfma_f32_16x16x32_bf16 v[136:139], v[168:171], a[188:191], v[136:139]
v_fmaak_f32 v253, v253, v223, 0xc0135761
v_mul_f32 v253, v253, v10
v_mfma_f32_16x16x32_bf16 v[132:135], v[172:175], a[184:187], v[132:135]
ds_read_b64_tr_b16 v[204:205], v219 offset:63488
ds_read_b64_tr_b16 v[206:207], v219 offset:64512
v_mfma_f32_16x16x32_bf16 v[140:143], v[172:175], a[188:191], v[140:143]
s_waitcnt vmcnt(34) lgkmcnt(8)
s_barrier
v_mfma_f32_16x16x32_bf16 v[144:147], v[176:179], a[160:163], v[144:147]
ds_read_b64_tr_b16 v[160:161], v216 offset:32768
ds_read_b64_tr_b16 v[162:163], v216 offset:33792
v_mfma_f32_16x16x32_bf16 v[152:155], v[176:179], a[164:167], v[152:155]
buffer_load_dwordx4 v224, s[48:51], s22 offen offset:0 lds
v_mfma_f32_16x16x32_bf16 v[148:151], v[180:183], a[160:163], v[148:151]
ds_read_b64_tr_b16 v[164:165], v217 offset:32768
ds_read_b64_tr_b16 v[166:167], v217 offset:33792
v_exp_f32 v253, v253
v_mfma_f32_16x16x32_bf16 v[156:159], v[180:183], a[164:167], v[156:159]
buffer_load_dwordx4 a[48:51], v232, s[52:55], s97 offen offset:0 sc1
v_mfma_f32_16x16x32_bf16 v[144:147], v[184:187], a[168:171], v[144:147]
ds_read_b64_tr_b16 v[168:169], v216 offset:36864
ds_read_b64_tr_b16 v[170:171], v216 offset:37888
v_mul_f32 v10, v10, v42
v_mfma_f32_16x16x32_bf16 v[152:155], v[184:187], a[172:175], v[152:155]
v_add_f32 v253, 1.0, v253
v_mul_f32 v254, v11, v11
v_mfma_f32_16x16x32_bf16 v[148:151], v[188:191], a[168:171], v[148:151]
ds_read_b64_tr_b16 v[172:173], v217 offset:36864
ds_read_b64_tr_b16 v[174:175], v217 offset:37888
v_mfma_f32_16x16x32_bf16 v[156:159], v[188:191], a[172:175], v[156:159]
v_fmaak_f32 v254, v254, v223, 0xc0135761
s_waitcnt lgkmcnt(8)
v_mfma_f32_16x16x32_bf16 v[144:147], v[192:195], a[176:179], v[144:147]
ds_read_b64_tr_b16 v[176:177], v216 offset:40960
ds_read_b64_tr_b16 v[178:179], v216 offset:41984
v_mfma_f32_16x16x32_bf16 v[152:155], v[192:195], a[180:183], v[152:155]
buffer_load_dwordx4 v225, s[48:51], s22 offen offset:1024 lds
v_mfma_f32_16x16x32_bf16 v[148:151], v[196:199], a[176:179], v[148:151]
ds_read_b64_tr_b16 v[180:181], v217 offset:40960
ds_read_b64_tr_b16 v[182:183], v217 offset:41984
v_mfma_f32_16x16x32_bf16 v[156:159], v[196:199], a[180:183], v[156:159]
buffer_load_dwordx4 a[52:55], v233, s[52:55], s97 offen offset:0 sc1
v_mfma_f32_16x16x32_bf16 v[144:147], v[200:203], a[184:187], v[144:147]
ds_read_b64_tr_b16 v[184:185], v216 offset:45056
ds_read_b64_tr_b16 v[186:187], v216 offset:46080
v_rcp_f32 v253, v253
v_mfma_f32_16x16x32_bf16 v[152:155], v[200:203], a[188:191], v[152:155]
buffer_load_dwordx4 v226, s[48:51], s22 offen offset:2048 lds
v_mfma_f32_16x16x32_bf16 v[148:151], v[204:207], a[184:187], v[148:151]
ds_read_b64_tr_b16 v[188:189], v217 offset:45056
ds_read_b64_tr_b16 v[190:191], v217 offset:46080
v_mul_f32 v10, v10, v253
v_mfma_f32_16x16x32_bf16 v[156:159], v[204:207], a[188:191], v[156:159]
v_mul_f32 v254, v254, v11
s_waitcnt lgkmcnt(8)
v_mfma_f32_16x16x32_bf16 v[128:131], v[160:163], a[192:195], v[128:131]
ds_read_b64_tr_b16 v[192:193], v216 offset:34816
ds_read_b64_tr_b16 v[194:195], v216 offset:35840
v_mfma_f32_16x16x32_bf16 v[136:139], v[160:163], a[196:199], v[136:139]
buffer_load_dwordx4 a[56:59], v232, s[52:55], s97 offen offset:1024 sc1
v_mfma_f32_16x16x32_bf16 v[132:135], v[164:167], a[192:195], v[132:135]
ds_read_b64_tr_b16 v[196:197], v217 offset:34816
ds_read_b64_tr_b16 v[198:199], v217 offset:35840
v_exp_f32 v254, v254
v_mfma_f32_16x16x32_bf16 v[140:143], v[164:167], a[196:199], v[140:143]
v_mul_f32 v11, v11, v43
v_add_f32 v254, 1.0, v254
v_mfma_f32_16x16x32_bf16 v[128:131], v[168:171], a[200:203], v[128:131]
ds_read_b64_tr_b16 v[200:201], v216 offset:38912
ds_read_b64_tr_b16 v[202:203], v216 offset:39936
v_mfma_f32_16x16x32_bf16 v[136:139], v[168:171], a[204:207], v[136:139]
buffer_load_dwordx4 v227, s[48:51], s22 offen offset:3072 lds
v_mfma_f32_16x16x32_bf16 v[132:135], v[172:175], a[200:203], v[132:135]
ds_read_b64_tr_b16 v[204:205], v217 offset:38912
ds_read_b64_tr_b16 v[206:207], v217 offset:39936
v_mfma_f32_16x16x32_bf16 v[140:143], v[172:175], a[204:207], v[140:143]
s_add_u32 m0, s77, 114688
s_waitcnt lgkmcnt(8)
v_mfma_f32_16x16x32_bf16 v[128:131], v[176:179], a[208:211], v[128:131]
ds_read_b64_tr_b16 v[160:161], v216 offset:43008
ds_read_b64_tr_b16 v[162:163], v216 offset:44032
v_rcp_f32 v254, v254
v_mfma_f32_16x16x32_bf16 v[136:139], v[176:179], a[212:215], v[136:139]
buffer_load_dwordx4 a[60:63], v233, s[52:55], s97 offen offset:1024 sc1
v_mul_f32 v11, v11, v254
v_mfma_f32_16x16x32_bf16 v[132:135], v[180:183], a[208:211], v[132:135]
ds_read_b64_tr_b16 v[164:165], v217 offset:43008
ds_read_b64_tr_b16 v[166:167], v217 offset:44032
v_mfma_f32_16x16x32_bf16 v[140:143], v[180:183], a[212:215], v[140:143]
v_cvt_pk_bf16_f32 v41, v10, v11
v_mul_f32 v243, v12, v12
v_mfma_f32_16x16x32_bf16 v[128:131], v[184:187], a[216:219], v[128:131]
ds_read_b64_tr_b16 v[168:169], v216 offset:47104
ds_read_b64_tr_b16 v[170:171], v216 offset:48128
v_mfma_f32_16x16x32_bf16 v[136:139], v[184:187], a[220:223], v[136:139]
v_fmaak_f32 v243, v243, v223, 0xc0135761
v_mul_f32 v243, v243, v12
v_mfma_f32_16x16x32_bf16 v[132:135], v[188:191], a[216:219], v[132:135]
ds_read_b64_tr_b16 v[172:173], v217 offset:47104
ds_read_b64_tr_b16 v[174:175], v217 offset:48128
v_mfma_f32_16x16x32_bf16 v[140:143], v[188:191], a[220:223], v[140:143]
s_waitcnt vmcnt(38) lgkmcnt(8)
s_barrier
v_mfma_f32_16x16x32_bf16 v[144:147], v[192:195], a[192:195], v[144:147]
ds_read_b64_tr_b16 v[176:177], v218 offset:0
ds_read_b64_tr_b16 v[178:179], v218 offset:1024
v_mfma_f32_16x16x32_bf16 v[152:155], v[192:195], a[196:199], v[152:155]
buffer_load_dwordx4 v224, s[48:51], s23 offen offset:0 lds
v_mfma_f32_16x16x32_bf16 v[148:151], v[196:199], a[192:195], v[148:151]
ds_read_b64_tr_b16 v[180:181], v219 offset:0
ds_read_b64_tr_b16 v[182:183], v219 offset:1024
v_mfma_f32_16x16x32_bf16 v[156:159], v[196:199], a[196:199], v[156:159]
buffer_load_dwordx4 a[64:67], v232, s[52:55], s98 offen offset:0 sc1
v_exp_f32 v243, v243
v_mfma_f32_16x16x32_bf16 v[144:147], v[200:203], a[200:203], v[144:147]
ds_read_b64_tr_b16 v[184:185], v218 offset:4096
ds_read_b64_tr_b16 v[186:187], v218 offset:5120
v_mfma_f32_16x16x32_bf16 v[152:155], v[200:203], a[204:207], v[152:155]
v_mul_f32 v12, v12, v44
v_add_f32 v243, 1.0, v243
v_mfma_f32_16x16x32_bf16 v[148:151], v[204:207], a[200:203], v[148:151]
ds_read_b64_tr_b16 v[188:189], v219 offset:4096
ds_read_b64_tr_b16 v[190:191], v219 offset:5120
v_mfma_f32_16x16x32_bf16 v[156:159], v[204:207], a[204:207], v[156:159]
v_mul_f32 v244, v13, v13
s_waitcnt lgkmcnt(8)
v_mfma_f32_16x16x32_bf16 v[144:147], v[160:163], a[208:211], v[144:147]
ds_read_b64_tr_b16 v[192:193], v218 offset:8192
ds_read_b64_tr_b16 v[194:195], v218 offset:9216
v_mfma_f32_16x16x32_bf16 v[152:155], v[160:163], a[212:215], v[152:155]
buffer_load_dwordx4 v225, s[48:51], s23 offen offset:1024 lds
v_mfma_f32_16x16x32_bf16 v[148:151], v[164:167], a[208:211], v[148:151]
ds_read_b64_tr_b16 v[196:197], v219 offset:8192
ds_read_b64_tr_b16 v[198:199], v219 offset:9216
v_mfma_f32_16x16x32_bf16 v[156:159], v[164:167], a[212:215], v[156:159]
buffer_load_dwordx4 a[68:71], v233, s[52:55], s98 offen offset:0 sc1
v_rcp_f32 v243, v243
v_mfma_f32_16x16x32_bf16 v[144:147], v[168:171], a[216:219], v[144:147]
ds_read_b64_tr_b16 v[200:201], v218 offset:12288
ds_read_b64_tr_b16 v[202:203], v218 offset:13312
v_mfma_f32_16x16x32_bf16 v[152:155], v[168:171], a[220:223], v[152:155]
buffer_load_dwordx4 v226, s[48:51], s23 offen offset:2048 lds
v_mul_f32 v12, v12, v243
v_mfma_f32_16x16x32_bf16 v[148:151], v[172:175], a[216:219], v[148:151]
ds_read_b64_tr_b16 v[204:205], v219 offset:12288
ds_read_b64_tr_b16 v[206:207], v219 offset:13312
v_mfma_f32_16x16x32_bf16 v[156:159], v[172:175], a[220:223], v[156:159]
v_fmaak_f32 v244, v244, v223, 0xc0135761
s_waitcnt lgkmcnt(8)
v_mfma_f32_16x16x32_bf16 v[128:131], v[176:179], a[224:227], v[128:131]
ds_read_b64_tr_b16 v[160:161], v218 offset:2048
ds_read_b64_tr_b16 v[162:163], v218 offset:3072
v_mul_f32 v244, v244, v13
v_mfma_f32_16x16x32_bf16 v[136:139], v[176:179], a[228:231], v[136:139]
buffer_load_dwordx4 a[72:75], v232, s[52:55], s98 offen offset:1024 sc1
v_mfma_f32_16x16x32_bf16 v[132:135], v[180:183], a[224:227], v[132:135]
ds_read_b64_tr_b16 v[164:165], v219 offset:2048
ds_read_b64_tr_b16 v[166:167], v219 offset:3072
v_exp_f32 v244, v244
v_mfma_f32_16x16x32_bf16 v[140:143], v[180:183], a[228:231], v[140:143]
v_mul_f32 v13, v13, v45
v_add_f32 v244, 1.0, v244
v_mfma_f32_16x16x32_bf16 v[128:131], v[184:187], a[232:235], v[128:131]
ds_read_b64_tr_b16 v[168:169], v218 offset:6144
ds_read_b64_tr_b16 v[170:171], v218 offset:7168
v_mfma_f32_16x16x32_bf16 v[136:139], v[184:187], a[236:239], v[136:139]
buffer_load_dwordx4 v227, s[48:51], s23 offen offset:3072 lds
v_mfma_f32_16x16x32_bf16 v[132:135], v[188:191], a[232:235], v[132:135]
ds_read_b64_tr_b16 v[172:173], v219 offset:6144
ds_read_b64_tr_b16 v[174:175], v219 offset:7168
v_mfma_f32_16x16x32_bf16 v[140:143], v[188:191], a[236:239], v[140:143]
s_add_u32 m0, s77, 32768
s_waitcnt lgkmcnt(8)
v_mfma_f32_16x16x32_bf16 v[128:131], v[192:195], a[240:243], v[128:131]
ds_read_b64_tr_b16 v[176:177], v218 offset:10240
ds_read_b64_tr_b16 v[178:179], v218 offset:11264
v_rcp_f32 v244, v244
v_mfma_f32_16x16x32_bf16 v[136:139], v[192:195], a[244:247], v[136:139]
buffer_load_dwordx4 a[76:79], v233, s[52:55], s98 offen offset:1024 sc1
v_mfma_f32_16x16x32_bf16 v[132:135], v[196:199], a[240:243], v[132:135]
ds_read_b64_tr_b16 v[180:181], v219 offset:10240
ds_read_b64_tr_b16 v[182:183], v219 offset:11264
v_mul_f32 v13, v13, v244
v_mfma_f32_16x16x32_bf16 v[140:143], v[196:199], a[244:247], v[140:143]
v_cvt_pk_bf16_f32 v42, v12, v13
v_mul_f32 v245, v14, v14
v_mfma_f32_16x16x32_bf16 v[128:131], v[200:203], a[248:251], v[128:131]
ds_read_b64_tr_b16 v[184:185], v218 offset:14336
ds_read_b64_tr_b16 v[186:187], v218 offset:15360
v_mfma_f32_16x16x32_bf16 v[136:139], v[200:203], a[252:255], v[136:139]
v_fmaak_f32 v245, v245, v223, 0xc0135761
v_mul_f32 v245, v245, v14
v_mfma_f32_16x16x32_bf16 v[132:135], v[204:207], a[248:251], v[132:135]
ds_read_b64_tr_b16 v[188:189], v219 offset:14336
ds_read_b64_tr_b16 v[190:191], v219 offset:15360
v_mfma_f32_16x16x32_bf16 v[140:143], v[204:207], a[252:255], v[140:143]
s_waitcnt vmcnt(34) lgkmcnt(8)
s_barrier
v_mfma_f32_16x16x32_bf16 v[144:147], v[160:163], a[224:227], v[144:147]
ds_read_b64_tr_b16 v[192:193], v208 offset:49152
ds_read_b64_tr_b16 v[194:195], v208 offset:53248
v_mfma_f32_16x16x32_bf16 v[152:155], v[160:163], a[228:231], v[152:155]
buffer_load_dwordx4 a[80:83], v232, s[52:55], s99 offen offset:0 sc1
v_mfma_f32_16x16x32_bf16 v[148:151], v[164:167], a[224:227], v[148:151]
ds_read_b64_tr_b16 v[196:197], v209 offset:49152
ds_read_b64_tr_b16 v[198:199], v209 offset:53248
v_mfma_f32_16x16x32_bf16 v[156:159], v[164:167], a[228:231], v[156:159]
buffer_load_dwordx4 v224, s[48:51], s24 offen offset:0 lds
v_mfma_f32_16x16x32_bf16 v[144:147], v[168:171], a[232:235], v[144:147]
ds_read_b64_tr_b16 v[200:201], v210 offset:49152
ds_read_b64_tr_b16 v[202:203], v210 offset:53248
v_exp_f32 v245, v245
v_mfma_f32_16x16x32_bf16 v[152:155], v[168:171], a[236:239], v[152:155]
v_mul_f32 v14, v14, v46
v_add_f32 v245, 1.0, v245
v_mfma_f32_16x16x32_bf16 v[148:151], v[172:175], a[232:235], v[148:151]
ds_read_b64_tr_b16 v[204:205], v211 offset:49152
ds_read_b64_tr_b16 v[206:207], v211 offset:53248
v_mfma_f32_16x16x32_bf16 v[156:159], v[172:175], a[236:239], v[156:159]
v_mul_f32 v246, v15, v15
s_waitcnt lgkmcnt(8)
v_mfma_f32_16x16x32_bf16 v[144:147], v[176:179], a[240:243], v[144:147]
ds_read_b64_tr_b16 v[160:161], v208 offset:51200
ds_read_b64_tr_b16 v[162:163], v208 offset:55296
v_mfma_f32_16x16x32_bf16 v[152:155], v[176:179], a[244:247], v[152:155]
buffer_load_dwordx4 a[84:87], v233, s[52:55], s99 offen offset:0 sc1
v_mfma_f32_16x16x32_bf16 v[148:151], v[180:183], a[240:243], v[148:151]
ds_read_b64_tr_b16 v[164:165], v209 offset:51200
ds_read_b64_tr_b16 v[166:167], v209 offset:55296
v_mfma_f32_16x16x32_bf16 v[156:159], v[180:183], a[244:247], v[156:159]
buffer_load_dwordx4 v225, s[48:51], s24 offen offset:1024 lds
v_rcp_f32 v245, v245
v_mfma_f32_16x16x32_bf16 v[144:147], v[184:187], a[248:251], v[144:147]
ds_read_b64_tr_b16 v[168:169], v210 offset:51200
ds_read_b64_tr_b16 v[170:171], v210 offset:55296
v_mfma_f32_16x16x32_bf16 v[152:155], v[184:187], a[252:255], v[152:155]
v_mul_f32 v14, v14, v245
v_fmaak_f32 v246, v246, v223, 0xc0135761
v_mfma_f32_16x16x32_bf16 v[148:151], v[188:191], a[248:251], v[148:151]
ds_read_b64_tr_b16 v[172:173], v211 offset:51200
ds_read_b64_tr_b16 v[174:175], v211 offset:55296
v_mfma_f32_16x16x32_bf16 v[156:159], v[188:191], a[252:255], v[156:159]
v_mul_f32 v246, v246, v15
s_waitcnt lgkmcnt(8)
v_mfma_f32_16x16x32_bf16 v[64:67], v[192:195], a[0:3], 0
ds_read_b64_tr_b16 v[176:177], v208 offset:57344
ds_read_b64_tr_b16 v[178:179], v208 offset:61440
v_mfma_f32_16x16x32_bf16 v[80:83], v[192:195], a[4:7], 0
buffer_load_dword v238, v222, s[52:55], 0 offen offset:2048 sc1
v_mfma_f32_16x16x32_bf16 v[68:71], v[196:199], a[0:3], 0
ds_read_b64_tr_b16 v[180:181], v209 offset:57344
ds_read_b64_tr_b16 v[182:183], v209 offset:61440
v_mfma_f32_16x16x32_bf16 v[84:87], v[196:199], a[4:7], 0
buffer_load_dwordx4 v226, s[48:51], s24 offen offset:2048 lds
v_exp_f32 v246, v246
v_mfma_f32_16x16x32_bf16 v[72:75], v[200:203], a[0:3], 0
ds_read_b64_tr_b16 v[184:185], v210 offset:57344
ds_read_b64_tr_b16 v[186:187], v210 offset:61440
)"
R"(
v_mfma_f32_16x16x32_bf16 v[88:91], v[200:203], a[4:7], 0
buffer_load_dwordx4 a[88:91], v232, s[52:55], s99 offen offset:1024 sc1
v_mul_f32 v15, v15, v47
v_mfma_f32_16x16x32_bf16 v[76:79], v[204:207], a[0:3], 0
ds_read_b64_tr_b16 v[188:189], v211 offset:57344
ds_read_b64_tr_b16 v[190:191], v211 offset:61440
v_mfma_f32_16x16x32_bf16 v[92:95], v[204:207], a[4:7], 0
v_add_f32 v246, 1.0, v246
s_waitcnt lgkmcnt(8)
v_mfma_f32_16x16x32_bf16 v[96:99], v[160:163], a[0:3], 0
ds_read_b64_tr_b16 v[192:193], v208 offset:59392
ds_read_b64_tr_b16 v[194:195], v208 offset:63488
v_mfma_f32_16x16x32_bf16 v[112:115], v[160:163], a[4:7], 0
buffer_load_dwordx4 a[92:95], v233, s[52:55], s99 offen offset:1024 sc1
v_rcp_f32 v246, v246
v_mfma_f32_16x16x32_bf16 v[100:103], v[164:167], a[0:3], 0
ds_read_b64_tr_b16 v[196:197], v209 offset:59392
ds_read_b64_tr_b16 v[198:199], v209 offset:63488
v_mfma_f32_16x16x32_bf16 v[116:119], v[164:167], a[4:7], 0
buffer_load_dwordx4 v227, s[48:51], s24 offen offset:3072 lds
v_mul_f32 v15, v15, v246
v_mfma_f32_16x16x32_bf16 v[104:107], v[168:171], a[0:3], 0
ds_read_b64_tr_b16 v[200:201], v210 offset:59392
ds_read_b64_tr_b16 v[202:203], v210 offset:63488
v_mfma_f32_16x16x32_bf16 v[120:123], v[168:171], a[4:7], 0
s_add_u32 m0, s77, 65536
v_cvt_pk_bf16_f32 v43, v14, v15
v_mfma_f32_16x16x32_bf16 v[108:111], v[172:175], a[0:3], 0
ds_read_b64_tr_b16 v[204:205], v211 offset:59392
ds_read_b64_tr_b16 v[206:207], v211 offset:63488
v_mfma_f32_16x16x32_bf16 v[124:127], v[172:175], a[4:7], 0
v_min_u32 v40, -2, v40
s_waitcnt vmcnt(34) lgkmcnt(8)
s_barrier
v_mfma_f32_16x16x32_bf16 v[64:67], v[176:179], a[8:11], v[64:67]
ds_read_b64_tr_b16 v[160:161], v212 offset:16384
ds_read_b64_tr_b16 v[162:163], v212 offset:20480
v_mfma_f32_16x16x32_bf16 v[80:83], v[176:179], a[12:15], v[80:83]
buffer_load_dwordx4 v224, s[48:51], s25 offen offset:0 lds
buffer_store_dwordx4 v[40:43], v220, s[52:55], s92 offen offset:1024 sc1
v_mfma_f32_16x16x32_bf16 v[68:71], v[180:183], a[8:11], v[68:71]
ds_read_b64_tr_b16 v[164:165], v213 offset:16384
ds_read_b64_tr_b16 v[166:167], v213 offset:20480
v_mul_f32 v247, v16, v16
v_mfma_f32_16x16x32_bf16 v[84:87], v[180:183], a[12:15], v[84:87]
buffer_load_dwordx4 a[96:99], v232, s[52:55], s100 offen offset:0 sc1
v_mfma_f32_16x16x32_bf16 v[72:75], v[184:187], a[8:11], v[72:75]
ds_read_b64_tr_b16 v[168:169], v214 offset:16384
ds_read_b64_tr_b16 v[170:171], v214 offset:20480
v_fmaak_f32 v247, v247, v223, 0xc0135761
v_mfma_f32_16x16x32_bf16 v[88:91], v[184:187], a[12:15], v[88:91]
v_mul_f32 v247, v247, v16
v_mul_f32 v16, v16, v48
v_mfma_f32_16x16x32_bf16 v[76:79], v[188:191], a[8:11], v[76:79]
ds_read_b64_tr_b16 v[172:173], v215 offset:16384
ds_read_b64_tr_b16 v[174:175], v215 offset:20480
v_mfma_f32_16x16x32_bf16 v[92:95], v[188:191], a[12:15], v[92:95]
v_add_u32 v224, 131072, v224
s_waitcnt lgkmcnt(8)
v_mfma_f32_16x16x32_bf16 v[96:99], v[192:195], a[8:11], v[96:99]
ds_read_b64_tr_b16 v[176:177], v212 offset:18432
ds_read_b64_tr_b16 v[178:179], v212 offset:22528
v_mfma_f32_16x16x32_bf16 v[112:115], v[192:195], a[12:15], v[112:115]
buffer_load_dwordx4 v225, s[48:51], s25 offen offset:1024 lds
v_mfma_f32_16x16x32_bf16 v[100:103], v[196:199], a[8:11], v[100:103]
ds_read_b64_tr_b16 v[180:181], v213 offset:18432
ds_read_b64_tr_b16 v[182:183], v213 offset:22528
v_exp_f32 v247, v247
v_mfma_f32_16x16x32_bf16 v[116:119], v[196:199], a[12:15], v[116:119]
buffer_load_dwordx4 a[100:103], v233, s[52:55], s100 offen offset:0 sc1
v_mfma_f32_16x16x32_bf16 v[104:107], v[200:203], a[8:11], v[104:107]
ds_read_b64_tr_b16 v[184:185], v214 offset:18432
ds_read_b64_tr_b16 v[186:187], v214 offset:22528
v_add_f32 v247, 1.0, v247
v_mfma_f32_16x16x32_bf16 v[120:123], v[200:203], a[12:15], v[120:123]
buffer_load_dwordx4 v226, s[48:51], s25 offen offset:2048 lds
v_mfma_f32_16x16x32_bf16 v[108:111], v[204:207], a[8:11], v[108:111]
ds_read_b64_tr_b16 v[188:189], v215 offset:18432
ds_read_b64_tr_b16 v[190:191], v215 offset:22528
v_rcp_f32 v247, v247
v_mfma_f32_16x16x32_bf16 v[124:127], v[204:207], a[12:15], v[124:127]
v_add_u32 v225, 131072, v225
s_waitcnt lgkmcnt(8)
v_mfma_f32_16x16x32_bf16 v[64:67], v[160:163], a[16:19], v[64:67]
ds_read_b64_tr_b16 v[192:193], v212 offset:24576
ds_read_b64_tr_b16 v[194:195], v212 offset:28672
v_mul_f32 v16, v16, v247
v_mfma_f32_16x16x32_bf16 v[80:83], v[160:163], a[20:23], v[80:83]
buffer_load_dwordx4 a[104:107], v232, s[52:55], s100 offen offset:1024 sc1
v_mfma_f32_16x16x32_bf16 v[68:71], v[164:167], a[16:19], v[68:71]
ds_read_b64_tr_b16 v[196:197], v213 offset:24576
ds_read_b64_tr_b16 v[198:199], v213 offset:28672
v_mul_f32 v248, v17, v17
v_mfma_f32_16x16x32_bf16 v[84:87], v[164:167], a[20:23], v[84:87]
v_fmaak_f32 v248, v248, v223, 0xc0135761
v_mul_f32 v248, v248, v17
v_mfma_f32_16x16x32_bf16 v[72:75], v[168:171], a[16:19], v[72:75]
ds_read_b64_tr_b16 v[200:201], v214 offset:24576
ds_read_b64_tr_b16 v[202:203], v214 offset:28672
v_mfma_f32_16x16x32_bf16 v[88:91], v[168:171], a[20:23], v[88:91]
buffer_load_dwordx4 v227, s[48:51], s25 offen offset:3072 lds
v_mfma_f32_16x16x32_bf16 v[76:79], v[172:175], a[16:19], v[76:79]
ds_read_b64_tr_b16 v[204:205], v215 offset:24576
ds_read_b64_tr_b16 v[206:207], v215 offset:28672
v_mfma_f32_16x16x32_bf16 v[92:95], v[172:175], a[20:23], v[92:95]
s_add_u32 m0, s77, 49152
s_waitcnt lgkmcnt(8)
v_mfma_f32_16x16x32_bf16 v[96:99], v[176:179], a[16:19], v[96:99]
ds_read_b64_tr_b16 v[160:161], v212 offset:26624
ds_read_b64_tr_b16 v[162:163], v212 offset:30720
v_exp_f32 v248, v248
v_mfma_f32_16x16x32_bf16 v[112:115], v[176:179], a[20:23], v[112:115]
buffer_load_dwordx4 a[108:111], v233, s[52:55], s100 offen offset:1024 sc1
v_mfma_f32_16x16x32_bf16 v[100:103], v[180:183], a[16:19], v[100:103]
ds_read_b64_tr_b16 v[164:165], v213 offset:26624
ds_read_b64_tr_b16 v[166:167], v213 offset:30720
v_mul_f32 v17, v17, v49
v_mfma_f32_16x16x32_bf16 v[116:119], v[180:183], a[20:23], v[116:119]
v_add_f32 v248, 1.0, v248
v_mul_f32 v249, v18, v18
v_mfma_f32_16x16x32_bf16 v[104:107], v[184:187], a[16:19], v[104:107]
ds_read_b64_tr_b16 v[168:169], v214 offset:26624
ds_read_b64_tr_b16 v[170:171], v214 offset:30720
v_mfma_f32_16x16x32_bf16 v[120:123], v[184:187], a[20:23], v[120:123]
v_rcp_f32 v248, v248
v_mfma_f32_16x16x32_bf16 v[108:111], v[188:191], a[16:19], v[108:111]
ds_read_b64_tr_b16 v[172:173], v215 offset:26624
ds_read_b64_tr_b16 v[174:175], v215 offset:30720
v_mfma_f32_16x16x32_bf16 v[124:127], v[188:191], a[20:23], v[124:127]
s_waitcnt vmcnt(34) lgkmcnt(8)
s_barrier
v_mfma_f32_16x16x32_bf16 v[64:67], v[192:195], a[24:27], v[64:67]
ds_read_b64_tr_b16 v[176:177], v208 offset:0
ds_read_b64_tr_b16 v[178:179], v208 offset:4096
v_mfma_f32_16x16x32_bf16 v[80:83], v[192:195], a[28:31], v[80:83]
buffer_load_dwordx4 a[112:115], v232, s[52:55], s101 offen offset:0 sc1
v_mul_f32 v17, v17, v248
v_mfma_f32_16x16x32_bf16 v[68:71], v[196:199], a[24:27], v[68:71]
ds_read_b64_tr_b16 v[180:181], v209 offset:0
ds_read_b64_tr_b16 v[182:183], v209 offset:4096
v_mfma_f32_16x16x32_bf16 v[84:87], v[196:199], a[28:31], v[84:87]
buffer_load_dwordx4 v224, s[48:51], s18 offen offset:0 lds
v_cvt_pk_bf16_f32 v48, v16, v17
v_mfma_f32_16x16x32_bf16 v[72:75], v[200:203], a[24:27], v[72:75]
ds_read_b64_tr_b16 v[184:185], v210 offset:0
ds_read_b64_tr_b16 v[186:187], v210 offset:4096
v_mfma_f32_16x16x32_bf16 v[88:91], v[200:203], a[28:31], v[88:91]
v_fmaak_f32 v249, v249, v223, 0xc0135761
v_mul_f32 v249, v249, v18
v_mfma_f32_16x16x32_bf16 v[76:79], v[204:207], a[24:27], v[76:79]
ds_read_b64_tr_b16 v[188:189], v211 offset:0
ds_read_b64_tr_b16 v[190:191], v211 offset:4096
v_mfma_f32_16x16x32_bf16 v[92:95], v[204:207], a[28:31], v[92:95]
v_add_u32 v226, 131072, v226
s_waitcnt lgkmcnt(8)
v_mfma_f32_16x16x32_bf16 v[96:99], v[160:163], a[24:27], v[96:99]
ds_read_b64_tr_b16 v[192:193], v208 offset:2048
ds_read_b64_tr_b16 v[194:195], v208 offset:6144
v_mfma_f32_16x16x32_bf16 v[112:115], v[160:163], a[28:31], v[112:115]
buffer_load_dwordx4 a[116:119], v233, s[52:55], s101 offen offset:0 sc1
v_exp_f32 v249, v249
v_mfma_f32_16x16x32_bf16 v[100:103], v[164:167], a[24:27], v[100:103]
ds_read_b64_tr_b16 v[196:197], v209 offset:2048
ds_read_b64_tr_b16 v[198:199], v209 offset:6144
v_mfma_f32_16x16x32_bf16 v[116:119], v[164:167], a[28:31], v[116:119]
buffer_load_dwordx4 v225, s[48:51], s18 offen offset:1024 lds
v_mul_f32 v18, v18, v50
v_mfma_f32_16x16x32_bf16 v[104:107], v[168:171], a[24:27], v[104:107]
ds_read_b64_tr_b16 v[200:201], v210 offset:2048
ds_read_b64_tr_b16 v[202:203], v210 offset:6144
v_mfma_f32_16x16x32_bf16 v[120:123], v[168:171], a[28:31], v[120:123]
v_add_f32 v249, 1.0, v249
v_mul_f32 v250, v19, v19
v_mfma_f32_16x16x32_bf16 v[108:111], v[172:175], a[24:27], v[108:111]
ds_read_b64_tr_b16 v[204:205], v211 offset:2048
ds_read_b64_tr_b16 v[206:207], v211 offset:6144
v_mfma_f32_16x16x32_bf16 v[124:127], v[172:175], a[28:31], v[124:127]
v_add_u32 v227, 131072, v227
s_waitcnt lgkmcnt(8)
v_mfma_f32_16x16x32_bf16 v[64:67], v[176:179], a[32:35], v[64:67]
ds_read_b64_tr_b16 v[160:161], v208 offset:8192
ds_read_b64_tr_b16 v[162:163], v208 offset:12288
v_mfma_f32_16x16x32_bf16 v[80:83], v[176:179], a[36:39], v[80:83]
buffer_load_dwordx4 v226, s[48:51], s18 offen offset:2048 lds
v_rcp_f32 v249, v249
v_mfma_f32_16x16x32_bf16 v[68:71], v[180:183], a[32:35], v[68:71]
ds_read_b64_tr_b16 v[164:165], v209 offset:8192
ds_read_b64_tr_b16 v[166:167], v209 offset:12288
v_mfma_f32_16x16x32_bf16 v[84:87], v[180:183], a[36:39], v[84:87]
buffer_load_dwordx4 a[120:123], v232, s[52:55], s101 offen offset:1024 sc1
v_mul_f32 v18, v18, v249
v_mfma_f32_16x16x32_bf16 v[72:75], v[184:187], a[32:35], v[72:75]
ds_read_b64_tr_b16 v[168:169], v210 offset:8192
ds_read_b64_tr_b16 v[170:171], v210 offset:12288
v_fmaak_f32 v250, v250, v223, 0xc0135761
v_mfma_f32_16x16x32_bf16 v[88:91], v[184:187], a[36:39], v[88:91]
v_mul_f32 v250, v250, v19
v_mul_f32 v19, v19, v51
v_mfma_f32_16x16x32_bf16 v[76:79], v[188:191], a[32:35], v[76:79]
ds_read_b64_tr_b16 v[172:173], v211 offset:8192
ds_read_b64_tr_b16 v[174:175], v211 offset:12288
v_mfma_f32_16x16x32_bf16 v[92:95], v[188:191], a[36:39], v[92:95]
v_add_u32 v232, 2048, v232
s_waitcnt lgkmcnt(8)
v_mfma_f32_16x16x32_bf16 v[96:99], v[192:195], a[32:35], v[96:99]
ds_read_b64_tr_b16 v[176:177], v208 offset:10240
ds_read_b64_tr_b16 v[178:179], v208 offset:14336
v_mfma_f32_16x16x32_bf16 v[112:115], v[192:195], a[36:39], v[112:115]
s_waitcnt vmcnt(19)
v_cmp_ne_u32 vcc, s78, v238
s_nop 0
s_cbranch_vccz LRa1_%=
LSa1_%=:
s_sub_u32 s84, s84, 1
s_cbranch_scc1 LZa1_%=
buffer_load_dword v238, v222, s[52:55], 0 offen offset:2048 sc1
s_waitcnt vmcnt(0)
v_cmp_ne_u32 vcc, s78, v238
s_nop 0
s_cbranch_vccnz LSa1_%=
LZa1_%=:
s_max_i32 s84, s84, 0
LRa1_%=:
v_mfma_f32_16x16x32_bf16 v[100:103], v[196:199], a[32:35], v[100:103]
ds_read_b64_tr_b16 v[180:181], v209 offset:10240
ds_read_b64_tr_b16 v[182:183], v209 offset:14336
v_exp_f32 v250, v250
v_mfma_f32_16x16x32_bf16 v[116:119], v[196:199], a[36:39], v[116:119]
buffer_load_dwordx4 v227, s[48:51], s18 offen offset:3072 lds
v_mfma_f32_16x16x32_bf16 v[104:107], v[200:203], a[32:35], v[104:107]
ds_read_b64_tr_b16 v[184:185], v210 offset:10240
ds_read_b64_tr_b16 v[186:187], v210 offset:14336
v_add_f32 v250, 1.0, v250
v_mfma_f32_16x16x32_bf16 v[120:123], v[200:203], a[36:39], v[120:123]
buffer_load_dwordx4 a[124:127], v233, s[52:55], s101 offen offset:1024 sc1
v_mfma_f32_16x16x32_bf16 v[108:111], v[204:207], a[32:35], v[108:111]
ds_read_b64_tr_b16 v[188:189], v211 offset:10240
ds_read_b64_tr_b16 v[190:191], v211 offset:14336
v_rcp_f32 v250, v250
v_mfma_f32_16x16x32_bf16 v[124:127], v[204:207], a[36:39], v[124:127]
s_waitcnt lgkmcnt(8)
s_barrier
v_mfma_f32_16x16x32_bf16 v[64:67], v[160:163], a[40:43], v[64:67]
ds_read_b64_tr_b16 v[192:193], v212 offset:32768
ds_read_b64_tr_b16 v[194:195], v212 offset:36864
s_add_u32 m0, s77, 81920
v_mul_f32 v19, v19, v250
v_mfma_f32_16x16x32_bf16 v[80:83], v[160:163], a[44:47], v[80:83]
buffer_load_dwordx4 v224, s[48:51], s19 offen offset:0 lds
v_mfma_f32_16x16x32_bf16 v[68:71], v[164:167], a[40:43], v[68:71]
ds_read_b64_tr_b16 v[196:197], v213 offset:32768
ds_read_b64_tr_b16 v[198:199], v213 offset:36864
v_add_u32 v233, 2048, v233
v_cvt_pk_bf16_f32 v49, v18, v19
v_mfma_f32_16x16x32_bf16 v[84:87], v[164:167], a[44:47], v[84:87]
buffer_load_dwordx4 a[128:131], v232, s[52:55], s94 offen offset:0 sc1
v_mfma_f32_16x16x32_bf16 v[72:75], v[168:171], a[40:43], v[72:75]
ds_read_b64_tr_b16 v[200:201], v214 offset:32768
ds_read_b64_tr_b16 v[202:203], v214 offset:36864
v_mul_f32 v251, v20, v20
v_mfma_f32_16x16x32_bf16 v[88:91], v[168:171], a[44:47], v[88:91]
v_fmaak_f32 v251, v251, v223, 0xc0135761
v_mul_f32 v251, v251, v20
v_mfma_f32_16x16x32_bf16 v[76:79], v[172:175], a[40:43], v[76:79]
ds_read_b64_tr_b16 v[204:205], v215 offset:32768
ds_read_b64_tr_b16 v[206:207], v215 offset:36864
v_mfma_f32_16x16x32_bf16 v[92:95], v[172:175], a[44:47], v[92:95]
v_mul_f32 v20, v20, v52
s_waitcnt lgkmcnt(8)
v_mfma_f32_16x16x32_bf16 v[96:99], v[176:179], a[40:43], v[96:99]
ds_read_b64_tr_b16 v[160:161], v212 offset:34816
ds_read_b64_tr_b16 v[162:163], v212 offset:38912
v_mfma_f32_16x16x32_bf16 v[112:115], v[176:179], a[44:47], v[112:115]
buffer_load_dwordx4 v225, s[48:51], s19 offen offset:1024 lds
v_mfma_f32_16x16x32_bf16 v[100:103], v[180:183], a[40:43], v[100:103]
LC19_%=:
s_cbranch_execz LC23_%=
ds_read_b64_tr_b16 v[164:165], v213 offset:34816
ds_read_b64_tr_b16 v[166:167], v213 offset:38912
v_mfma_f32_16x16x32_bf16 v[116:119], v[180:183], a[44:47], v[116:119]
buffer_load_dwordx4 a[132:135], v233, s[52:55], s94 offen offset:0 sc1
v_exp_f32 v251, v251
v_mfma_f32_16x16x32_bf16 v[104:107], v[184:187], a[40:43], v[104:107]
ds_read_b64_tr_b16 v[168:169], v214 offset:34816
ds_read_b64_tr_b16 v[170:171], v214 offset:38912
v_mfma_f32_16x16x32_bf16 v[120:123], v[184:187], a[44:47], v[120:123]
buffer_load_dwordx4 v226, s[48:51], s19 offen offset:2048 lds
v_add_f32 v251, 1.0, v251
v_mfma_f32_16x16x32_bf16 v[108:111], v[188:191], a[40:43], v[108:111]
ds_read_b64_tr_b16 v[172:173], v215 offset:34816
ds_read_b64_tr_b16 v[174:175], v215 offset:38912
v_mfma_f32_16x16x32_bf16 v[124:127], v[188:191], a[44:47], v[124:127]
v_mul_f32 v252, v21, v21
s_waitcnt lgkmcnt(8)
v_mfma_f32_16x16x32_bf16 v[64:67], v[192:195], a[48:51], v[64:67]
ds_read_b64_tr_b16 v[176:177], v212 offset:40960
ds_read_b64_tr_b16 v[178:179], v212 offset:45056
v_mfma_f32_16x16x32_bf16 v[80:83], v[192:195], a[52:55], v[80:83]
buffer_load_dwordx4 a[136:139], v232, s[52:55], s94 offen offset:1024 sc1
v_rcp_f32 v251, v251
v_mfma_f32_16x16x32_bf16 v[68:71], v[196:199], a[48:51], v[68:71]
ds_read_b64_tr_b16 v[180:181], v213 offset:40960
ds_read_b64_tr_b16 v[182:183], v213 offset:45056
v_mfma_f32_16x16x32_bf16 v[84:87], v[196:199], a[52:55], v[84:87]
v_mul_f32 v20, v20, v251
v_fmaak_f32 v252, v252, v223, 0xc0135761
v_mfma_f32_16x16x32_bf16 v[72:75], v[200:203], a[48:51], v[72:75]
ds_read_b64_tr_b16 v[184:185], v214 offset:40960
ds_read_b64_tr_b16 v[186:187], v214 offset:45056
v_mfma_f32_16x16x32_bf16 v[88:91], v[200:203], a[52:55], v[88:91]
buffer_load_dwordx4 v227, s[48:51], s19 offen offset:3072 lds
v_mfma_f32_16x16x32_bf16 v[76:79], v[204:207], a[48:51], v[76:79]
ds_read_b64_tr_b16 v[188:189], v215 offset:40960
ds_read_b64_tr_b16 v[190:191], v215 offset:45056
v_mfma_f32_16x16x32_bf16 v[92:95], v[204:207], a[52:55], v[92:95]
s_add_u32 m0, s77, 0
v_mul_f32 v252, v252, v21
s_waitcnt lgkmcnt(8)
v_mfma_f32_16x16x32_bf16 v[96:99], v[160:163], a[48:51], v[96:99]
ds_read_b64_tr_b16 v[192:193], v212 offset:43008
ds_read_b64_tr_b16 v[194:195], v212 offset:47104
v_mfma_f32_16x16x32_bf16 v[112:115], v[160:163], a[52:55], v[112:115]
buffer_load_dwordx4 a[140:143], v233, s[52:55], s94 offen offset:1024 sc1
v_exp_f32 v252, v252
v_mfma_f32_16x16x32_bf16 v[100:103], v[164:167], a[48:51], v[100:103]
ds_read_b64_tr_b16 v[196:197], v213 offset:43008
ds_read_b64_tr_b16 v[198:199], v213 offset:47104
v_mfma_f32_16x16x32_bf16 v[116:119], v[164:167], a[52:55], v[116:119]
v_mul_f32 v21, v21, v53
v_add_f32 v252, 1.0, v252
v_mfma_f32_16x16x32_bf16 v[104:107], v[168:171], a[48:51], v[104:107]
ds_read_b64_tr_b16 v[200:201], v214 offset:43008
ds_read_b64_tr_b16 v[202:203], v214 offset:47104
v_mfma_f32_16x16x32_bf16 v[120:123], v[168:171], a[52:55], v[120:123]
v_rcp_f32 v252, v252
v_mfma_f32_16x16x32_bf16 v[108:111], v[172:175], a[48:51], v[108:111]
ds_read_b64_tr_b16 v[204:205], v215 offset:43008
ds_read_b64_tr_b16 v[206:207], v215 offset:47104
v_mfma_f32_16x16x32_bf16 v[124:127], v[172:175], a[52:55], v[124:127]
s_waitcnt lgkmcnt(8)
s_barrier
v_mfma_f32_16x16x32_bf16 v[64:67], v[176:179], a[56:59], v[64:67]
ds_read_b64_tr_b16 v[160:161], v208 offset:16384
ds_read_b64_tr_b16 v[162:163], v208 offset:20480
v_mfma_f32_16x16x32_bf16 v[80:83], v[176:179], a[60:63], v[80:83]
buffer_load_dwordx4 v224, s[48:51], s20 offen offset:0 lds
v_mul_f32 v21, v21, v252
v_mfma_f32_16x16x32_bf16 v[68:71], v[180:183], a[56:59], v[68:71]
ds_read_b64_tr_b16 v[164:165], v209 offset:16384
ds_read_b64_tr_b16 v[166:167], v209 offset:20480
v_mfma_f32_16x16x32_bf16 v[84:87], v[180:183], a[60:63], v[84:87]
buffer_load_dwordx4 a[144:147], v232, s[52:55], s95 offen offset:0 sc1
v_cvt_pk_bf16_f32 v50, v20, v21
v_mfma_f32_16x16x32_bf16 v[72:75], v[184:187], a[56:59], v[72:75]
ds_read_b64_tr_b16 v[168:169], v210 offset:16384
ds_read_b64_tr_b16 v[170:171], v210 offset:20480
v_mul_f32 v253, v22, v22
v_mfma_f32_16x16x32_bf16 v[88:91], v[184:187], a[60:63], v[88:91]
v_fmaak_f32 v253, v253, v223, 0xc0135761
v_mul_f32 v253, v253, v22
v_mfma_f32_16x16x32_bf16 v[76:79], v[188:191], a[56:59], v[76:79]
ds_read_b64_tr_b16 v[172:173], v211 offset:16384
ds_read_b64_tr_b16 v[174:175], v211 offset:20480
v_mfma_f32_16x16x32_bf16 v[92:95], v[188:191], a[60:63], v[92:95]
v_mul_f32 v22, v22, v54
s_waitcnt lgkmcnt(8)
v_mfma_f32_16x16x32_bf16 v[96:99], v[192:195], a[56:59], v[96:99]
ds_read_b64_tr_b16 v[176:177], v208 offset:18432
ds_read_b64_tr_b16 v[178:179], v208 offset:22528
v_mfma_f32_16x16x32_bf16 v[112:115], v[192:195], a[60:63], v[112:115]
buffer_load_dwordx4 v225, s[48:51], s20 offen offset:1024 lds
v_mfma_f32_16x16x32_bf16 v[100:103], v[196:199], a[56:59], v[100:103]
ds_read_b64_tr_b16 v[180:181], v209 offset:18432
ds_read_b64_tr_b16 v[182:183], v209 offset:22528
v_mfma_f32_16x16x32_bf16 v[116:119], v[196:199], a[60:63], v[116:119]
buffer_load_dwordx4 a[148:151], v233, s[52:55], s95 offen offset:0 sc1
v_mfma_f32_16x16x32_bf16 v[104:107], v[200:203], a[56:59], v[104:107]
ds_read_b64_tr_b16 v[184:185], v210 offset:18432
ds_read_b64_tr_b16 v[186:187], v210 offset:22528
v_exp_f32 v253, v253
v_mfma_f32_16x16x32_bf16 v[120:123], v[200:203], a[60:63], v[120:123]
buffer_load_dwordx4 v226, s[48:51], s20 offen offset:2048 lds
v_mfma_f32_16x16x32_bf16 v[108:111], v[204:207], a[56:59], v[108:111]
ds_read_b64_tr_b16 v[188:189], v211 offset:18432
ds_read_b64_tr_b16 v[190:191], v211 offset:22528
v_add_f32 v253, 1.0, v253
v_mfma_f32_16x16x32_bf16 v[124:127], v[204:207], a[60:63], v[124:127]
v_mul_f32 v254, v23, v23
s_waitcnt lgkmcnt(8)
v_mfma_f32_16x16x32_bf16 v[64:67], v[160:163], a[64:67], v[64:67]
ds_read_b64_tr_b16 v[192:193], v208 offset:24576
ds_read_b64_tr_b16 v[194:195], v208 offset:28672
v_mfma_f32_16x16x32_bf16 v[80:83], v[160:163], a[68:71], v[80:83]
buffer_load_dwordx4 a[152:155], v232, s[52:55], s95 offen offset:1024 sc1
v_mfma_f32_16x16x32_bf16 v[68:71], v[164:167], a[64:67], v[68:71]
ds_read_b64_tr_b16 v[196:197], v209 offset:24576
ds_read_b64_tr_b16 v[198:199], v209 offset:28672
v_rcp_f32 v253, v253
v_mfma_f32_16x16x32_bf16 v[84:87], v[164:167], a[68:71], v[84:87]
v_mul_f32 v22, v22, v253
)"
R"(
v_fmaak_f32 v254, v254, v223, 0xc0135761
v_mfma_f32_16x16x32_bf16 v[72:75], v[168:171], a[64:67], v[72:75]
ds_read_b64_tr_b16 v[200:201], v210 offset:24576
ds_read_b64_tr_b16 v[202:203], v210 offset:28672
v_mfma_f32_16x16x32_bf16 v[88:91], v[168:171], a[68:71], v[88:91]
buffer_load_dwordx4 v227, s[48:51], s20 offen offset:3072 lds
v_mfma_f32_16x16x32_bf16 v[76:79], v[172:175], a[64:67], v[76:79]
ds_read_b64_tr_b16 v[204:205], v211 offset:24576
ds_read_b64_tr_b16 v[206:207], v211 offset:28672
v_mfma_f32_16x16x32_bf16 v[92:95], v[172:175], a[68:71], v[92:95]
s_add_u32 m0, s77, 98304
s_waitcnt lgkmcnt(8)
v_mfma_f32_16x16x32_bf16 v[96:99], v[176:179], a[64:67], v[96:99]
ds_read_b64_tr_b16 v[160:161], v208 offset:26624
ds_read_b64_tr_b16 v[162:163], v208 offset:30720
v_mul_f32 v254, v254, v23
v_mfma_f32_16x16x32_bf16 v[112:115], v[176:179], a[68:71], v[112:115]
buffer_load_dwordx4 a[156:159], v233, s[52:55], s95 offen offset:1024 sc1
v_mfma_f32_16x16x32_bf16 v[100:103], v[180:183], a[64:67], v[100:103]
ds_read_b64_tr_b16 v[164:165], v209 offset:26624
ds_read_b64_tr_b16 v[166:167], v209 offset:30720
v_exp_f32 v254, v254
v_mfma_f32_16x16x32_bf16 v[116:119], v[180:183], a[68:71], v[116:119]
v_mul_f32 v23, v23, v55
v_add_f32 v254, 1.0, v254
v_mfma_f32_16x16x32_bf16 v[104:107], v[184:187], a[64:67], v[104:107]
ds_read_b64_tr_b16 v[168:169], v210 offset:26624
ds_read_b64_tr_b16 v[170:171], v210 offset:30720
v_mfma_f32_16x16x32_bf16 v[120:123], v[184:187], a[68:71], v[120:123]
v_rcp_f32 v254, v254
v_mfma_f32_16x16x32_bf16 v[108:111], v[188:191], a[64:67], v[108:111]
ds_read_b64_tr_b16 v[172:173], v211 offset:26624
ds_read_b64_tr_b16 v[174:175], v211 offset:30720
v_mfma_f32_16x16x32_bf16 v[124:127], v[188:191], a[68:71], v[124:127]
s_waitcnt vmcnt(34) lgkmcnt(8)
s_barrier
v_mfma_f32_16x16x32_bf16 v[64:67], v[192:195], a[72:75], v[64:67]
ds_read_b64_tr_b16 v[176:177], v212 offset:49152
ds_read_b64_tr_b16 v[178:179], v212 offset:53248
v_mfma_f32_16x16x32_bf16 v[80:83], v[192:195], a[76:79], v[80:83]
buffer_load_dwordx4 v224, s[48:51], s21 offen offset:0 lds
v_mul_f32 v23, v23, v254
v_mfma_f32_16x16x32_bf16 v[68:71], v[196:199], a[72:75], v[68:71]
ds_read_b64_tr_b16 v[180:181], v213 offset:49152
ds_read_b64_tr_b16 v[182:183], v213 offset:53248
v_mfma_f32_16x16x32_bf16 v[84:87], v[196:199], a[76:79], v[84:87]
buffer_load_dwordx4 a[160:163], v232, s[52:55], s96 offen offset:0 sc1
v_cvt_pk_bf16_f32 v51, v22, v23
v_mfma_f32_16x16x32_bf16 v[72:75], v[200:203], a[72:75], v[72:75]
ds_read_b64_tr_b16 v[184:185], v214 offset:49152
ds_read_b64_tr_b16 v[186:187], v214 offset:53248
v_mfma_f32_16x16x32_bf16 v[88:91], v[200:203], a[76:79], v[88:91]
v_min_u32 v48, -2, v48
v_mul_f32 v243, v24, v24
v_mfma_f32_16x16x32_bf16 v[76:79], v[204:207], a[72:75], v[76:79]
ds_read_b64_tr_b16 v[188:189], v215 offset:49152
ds_read_b64_tr_b16 v[190:191], v215 offset:53248
v_mfma_f32_16x16x32_bf16 v[92:95], v[204:207], a[76:79], v[92:95]
v_fmaak_f32 v243, v243, v223, 0xc0135761
s_waitcnt lgkmcnt(8)
v_mfma_f32_16x16x32_bf16 v[96:99], v[160:163], a[72:75], v[96:99]
ds_read_b64_tr_b16 v[192:193], v212 offset:51200
ds_read_b64_tr_b16 v[194:195], v212 offset:55296
v_mfma_f32_16x16x32_bf16 v[112:115], v[160:163], a[76:79], v[112:115]
buffer_load_dwordx4 v225, s[48:51], s21 offen offset:1024 lds
v_mfma_f32_16x16x32_bf16 v[100:103], v[164:167], a[72:75], v[100:103]
ds_read_b64_tr_b16 v[196:197], v213 offset:51200
ds_read_b64_tr_b16 v[198:199], v213 offset:55296
v_mfma_f32_16x16x32_bf16 v[116:119], v[164:167], a[76:79], v[116:119]
buffer_load_dwordx4 a[164:167], v233, s[52:55], s96 offen offset:0 sc1
buffer_store_dwordx4 v[48:51], v221, s[52:55], s92 offen offset:0 sc1
v_mfma_f32_16x16x32_bf16 v[104:107], v[168:171], a[72:75], v[104:107]
ds_read_b64_tr_b16 v[200:201], v214 offset:51200
ds_read_b64_tr_b16 v[202:203], v214 offset:55296
v_mfma_f32_16x16x32_bf16 v[120:123], v[168:171], a[76:79], v[120:123]
buffer_load_dwordx4 v226, s[48:51], s21 offen offset:2048 lds
v_mul_f32 v243, v243, v24
v_mfma_f32_16x16x32_bf16 v[108:111], v[172:175], a[72:75], v[108:111]
ds_read_b64_tr_b16 v[204:205], v215 offset:51200
ds_read_b64_tr_b16 v[206:207], v215 offset:55296
v_mfma_f32_16x16x32_bf16 v[124:127], v[172:175], a[76:79], v[124:127]
v_mul_f32 v24, v24, v56
s_waitcnt lgkmcnt(8)
v_mfma_f32_16x16x32_bf16 v[64:67], v[176:179], a[80:83], v[64:67]
ds_read_b64_tr_b16 v[160:161], v212 offset:57344
ds_read_b64_tr_b16 v[162:163], v212 offset:61440
v_mfma_f32_16x16x32_bf16 v[80:83], v[176:179], a[84:87], v[80:83]
buffer_load_dwordx4 a[168:171], v232, s[52:55], s96 offen offset:1024 sc1
v_exp_f32 v243, v243
v_mfma_f32_16x16x32_bf16 v[68:71], v[180:183], a[80:83], v[68:71]
ds_read_b64_tr_b16 v[164:165], v213 offset:57344
ds_read_b64_tr_b16 v[166:167], v213 offset:61440
v_mfma_f32_16x16x32_bf16 v[84:87], v[180:183], a[84:87], v[84:87]
v_add_f32 v243, 1.0, v243
v_mul_f32 v244, v25, v25
v_mfma_f32_16x16x32_bf16 v[72:75], v[184:187], a[80:83], v[72:75]
ds_read_b64_tr_b16 v[168:169], v214 offset:57344
ds_read_b64_tr_b16 v[170:171], v214 offset:61440
v_mfma_f32_16x16x32_bf16 v[88:91], v[184:187], a[84:87], v[88:91]
buffer_load_dwordx4 v227, s[48:51], s21 offen offset:3072 lds
v_mfma_f32_16x16x32_bf16 v[76:79], v[188:191], a[80:83], v[76:79]
ds_read_b64_tr_b16 v[172:173], v215 offset:57344
ds_read_b64_tr_b16 v[174:175], v215 offset:61440
v_rcp_f32 v243, v243
v_mfma_f32_16x16x32_bf16 v[92:95], v[188:191], a[84:87], v[92:95]
s_add_u32 m0, s77, 16384
s_waitcnt lgkmcnt(8)
v_mfma_f32_16x16x32_bf16 v[96:99], v[192:195], a[80:83], v[96:99]
ds_read_b64_tr_b16 v[176:177], v212 offset:59392
ds_read_b64_tr_b16 v[178:179], v212 offset:63488
v_mul_f32 v24, v24, v243
v_mfma_f32_16x16x32_bf16 v[112:115], v[192:195], a[84:87], v[112:115]
buffer_load_dwordx4 a[172:175], v233, s[52:55], s96 offen offset:1024 sc1
v_mfma_f32_16x16x32_bf16 v[100:103], v[196:199], a[80:83], v[100:103]
ds_read_b64_tr_b16 v[180:181], v213 offset:59392
ds_read_b64_tr_b16 v[182:183], v213 offset:63488
v_fmaak_f32 v244, v244, v223, 0xc0135761
v_mfma_f32_16x16x32_bf16 v[116:119], v[196:199], a[84:87], v[116:119]
v_mul_f32 v244, v244, v25
v_mul_f32 v25, v25, v57
v_mfma_f32_16x16x32_bf16 v[104:107], v[200:203], a[80:83], v[104:107]
ds_read_b64_tr_b16 v[184:185], v214 offset:59392
ds_read_b64_tr_b16 v[186:187], v214 offset:63488
v_mfma_f32_16x16x32_bf16 v[120:123], v[200:203], a[84:87], v[120:123]
v_exp_f32 v244, v244
v_mfma_f32_16x16x32_bf16 v[108:111], v[204:207], a[80:83], v[108:111]
ds_read_b64_tr_b16 v[188:189], v215 offset:59392
ds_read_b64_tr_b16 v[190:191], v215 offset:63488
v_mfma_f32_16x16x32_bf16 v[124:127], v[204:207], a[84:87], v[124:127]
s_waitcnt vmcnt(33) lgkmcnt(8)
s_barrier
v_mfma_f32_16x16x32_bf16 v[64:67], v[160:163], a[88:91], v[64:67]
ds_read_b64_tr_b16 v[192:193], v208 offset:32768
ds_read_b64_tr_b16 v[194:195], v208 offset:36864
v_mfma_f32_16x16x32_bf16 v[80:83], v[160:163], a[92:95], v[80:83]
buffer_load_dwordx4 v224, s[48:51], s22 offen offset:0 lds
v_mfma_f32_16x16x32_bf16 v[68:71], v[164:167], a[88:91], v[68:71]
ds_read_b64_tr_b16 v[196:197], v209 offset:32768
ds_read_b64_tr_b16 v[198:199], v209 offset:36864
v_add_f32 v244, 1.0, v244
v_mfma_f32_16x16x32_bf16 v[84:87], v[164:167], a[92:95], v[84:87]
buffer_load_dwordx4 a[176:179], v232, s[52:55], s97 offen offset:0 sc1
v_mfma_f32_16x16x32_bf16 v[72:75], v[168:171], a[88:91], v[72:75]
ds_read_b64_tr_b16 v[200:201], v210 offset:32768
ds_read_b64_tr_b16 v[202:203], v210 offset:36864
v_rcp_f32 v244, v244
v_mfma_f32_16x16x32_bf16 v[88:91], v[168:171], a[92:95], v[88:91]
v_mul_f32 v25, v25, v244
v_cvt_pk_bf16_f32 v56, v24, v25
v_mfma_f32_16x16x32_bf16 v[76:79], v[172:175], a[88:91], v[76:79]
ds_read_b64_tr_b16 v[204:205], v211 offset:32768
ds_read_b64_tr_b16 v[206:207], v211 offset:36864
v_mfma_f32_16x16x32_bf16 v[92:95], v[172:175], a[92:95], v[92:95]
v_mul_f32 v245, v26, v26
s_waitcnt lgkmcnt(8)
v_mfma_f32_16x16x32_bf16 v[96:99], v[176:179], a[88:91], v[96:99]
ds_read_b64_tr_b16 v[160:161], v208 offset:34816
ds_read_b64_tr_b16 v[162:163], v208 offset:38912
v_mfma_f32_16x16x32_bf16 v[112:115], v[176:179], a[92:95], v[112:115]
buffer_load_dwordx4 v225, s[48:51], s22 offen offset:1024 lds
v_mfma_f32_16x16x32_bf16 v[100:103], v[180:183], a[88:91], v[100:103]
ds_read_b64_tr_b16 v[164:165], v209 offset:34816
ds_read_b64_tr_b16 v[166:167], v209 offset:38912
v_mfma_f32_16x16x32_bf16 v[116:119], v[180:183], a[92:95], v[116:119]
buffer_load_dwordx4 a[180:183], v233, s[52:55], s97 offen offset:0 sc1
v_mfma_f32_16x16x32_bf16 v[104:107], v[184:187], a[88:91], v[104:107]
ds_read_b64_tr_b16 v[168:169], v210 offset:34816
ds_read_b64_tr_b16 v[170:171], v210 offset:38912
v_fmaak_f32 v245, v245, v223, 0xc0135761
v_mfma_f32_16x16x32_bf16 v[120:123], v[184:187], a[92:95], v[120:123]
buffer_load_dwordx4 v226, s[48:51], s22 offen offset:2048 lds
v_mul_f32 v245, v245, v26
v_mfma_f32_16x16x32_bf16 v[108:111], v[188:191], a[88:91], v[108:111]
ds_read_b64_tr_b16 v[172:173], v211 offset:34816
ds_read_b64_tr_b16 v[174:175], v211 offset:38912
v_mfma_f32_16x16x32_bf16 v[124:127], v[188:191], a[92:95], v[124:127]
v_mul_f32 v26, v26, v58
s_waitcnt lgkmcnt(8)
v_mfma_f32_16x16x32_bf16 v[64:67], v[192:195], a[96:99], v[64:67]
ds_read_b64_tr_b16 v[176:177], v208 offset:40960
ds_read_b64_tr_b16 v[178:179], v208 offset:45056
v_mfma_f32_16x16x32_bf16 v[80:83], v[192:195], a[100:103], v[80:83]
buffer_load_dwordx4 a[184:187], v232, s[52:55], s97 offen offset:1024 sc1
v_exp_f32 v245, v245
v_mfma_f32_16x16x32_bf16 v[68:71], v[196:199], a[96:99], v[68:71]
ds_read_b64_tr_b16 v[180:181], v209 offset:40960
ds_read_b64_tr_b16 v[182:183], v209 offset:45056
v_mfma_f32_16x16x32_bf16 v[84:87], v[196:199], a[100:103], v[84:87]
v_add_f32 v245, 1.0, v245
v_mul_f32 v246, v27, v27
v_mfma_f32_16x16x32_bf16 v[72:75], v[200:203], a[96:99], v[72:75]
ds_read_b64_tr_b16 v[184:185], v210 offset:40960
ds_read_b64_tr_b16 v[186:187], v210 offset:45056
v_mfma_f32_16x16x32_bf16 v[88:91], v[200:203], a[100:103], v[88:91]
buffer_load_dwordx4 v227, s[48:51], s22 offen offset:3072 lds
v_mfma_f32_16x16x32_bf16 v[76:79], v[204:207], a[96:99], v[76:79]
ds_read_b64_tr_b16 v[188:189], v211 offset:40960
ds_read_b64_tr_b16 v[190:191], v211 offset:45056
v_mfma_f32_16x16x32_bf16 v[92:95], v[204:207], a[100:103], v[92:95]
s_add_u32 m0, s77, 114688
v_rcp_f32 v245, v245
s_waitcnt lgkmcnt(8)
v_mfma_f32_16x16x32_bf16 v[96:99], v[160:163], a[96:99], v[96:99]
ds_read_b64_tr_b16 v[192:193], v208 offset:43008
ds_read_b64_tr_b16 v[194:195], v208 offset:47104
v_mfma_f32_16x16x32_bf16 v[112:115], v[160:163], a[100:103], v[112:115]
buffer_load_dwordx4 a[188:191], v233, s[52:55], s97 offen offset:1024 sc1
v_mul_f32 v26, v26, v245
v_mfma_f32_16x16x32_bf16 v[100:103], v[164:167], a[96:99], v[100:103]
ds_read_b64_tr_b16 v[196:197], v209 offset:43008
ds_read_b64_tr_b16 v[198:199], v209 offset:47104
v_mfma_f32_16x16x32_bf16 v[116:119], v[164:167], a[100:103], v[116:119]
v_fmaak_f32 v246, v246, v223, 0xc0135761
v_mul_f32 v246, v246, v27
v_mfma_f32_16x16x32_bf16 v[104:107], v[168:171], a[96:99], v[104:107]
ds_read_b64_tr_b16 v[200:201], v210 offset:43008
ds_read_b64_tr_b16 v[202:203], v210 offset:47104
v_mfma_f32_16x16x32_bf16 v[120:123], v[168:171], a[100:103], v[120:123]
v_exp_f32 v246, v246
v_mfma_f32_16x16x32_bf16 v[108:111], v[172:175], a[96:99], v[108:111]
ds_read_b64_tr_b16 v[204:205], v211 offset:43008
ds_read_b64_tr_b16 v[206:207], v211 offset:47104
v_mfma_f32_16x16x32_bf16 v[124:127], v[172:175], a[100:103], v[124:127]
s_waitcnt vmcnt(33) lgkmcnt(8)
s_barrier
v_mfma_f32_16x16x32_bf16 v[64:67], v[176:179], a[104:107], v[64:67]
ds_read_b64_tr_b16 v[160:161], v212 offset:0
ds_read_b64_tr_b16 v[162:163], v212 offset:4096
v_mfma_f32_16x16x32_bf16 v[80:83], v[176:179], a[108:111], v[80:83]
buffer_load_dwordx4 v224, s[48:51], s23 offen offset:0 lds
v_mul_f32 v27, v27, v59
v_mfma_f32_16x16x32_bf16 v[68:71], v[180:183], a[104:107], v[68:71]
ds_read_b64_tr_b16 v[164:165], v213 offset:0
ds_read_b64_tr_b16 v[166:167], v213 offset:4096
v_mfma_f32_16x16x32_bf16 v[84:87], v[180:183], a[108:111], v[84:87]
buffer_load_dwordx4 a[192:195], v232, s[52:55], s98 offen offset:0 sc1
v_add_f32 v246, 1.0, v246
v_mfma_f32_16x16x32_bf16 v[72:75], v[184:187], a[104:107], v[72:75]
ds_read_b64_tr_b16 v[168:169], v214 offset:0
ds_read_b64_tr_b16 v[170:171], v214 offset:4096
v_mfma_f32_16x16x32_bf16 v[88:91], v[184:187], a[108:111], v[88:91]
v_rcp_f32 v246, v246
v_mfma_f32_16x16x32_bf16 v[76:79], v[188:191], a[104:107], v[76:79]
ds_read_b64_tr_b16 v[172:173], v215 offset:0
ds_read_b64_tr_b16 v[174:175], v215 offset:4096
v_mul_f32 v27, v27, v246
v_mfma_f32_16x16x32_bf16 v[92:95], v[188:191], a[108:111], v[92:95]
v_cvt_pk_bf16_f32 v57, v26, v27
s_waitcnt lgkmcnt(8)
v_mfma_f32_16x16x32_bf16 v[96:99], v[192:195], a[104:107], v[96:99]
ds_read_b64_tr_b16 v[176:177], v212 offset:2048
ds_read_b64_tr_b16 v[178:179], v212 offset:6144
v_mfma_f32_16x16x32_bf16 v[112:115], v[192:195], a[108:111], v[112:115]
buffer_load_dwordx4 v225, s[48:51], s23 offen offset:1024 lds
v_mfma_f32_16x16x32_bf16 v[100:103], v[196:199], a[104:107], v[100:103]
ds_read_b64_tr_b16 v[180:181], v213 offset:2048
ds_read_b64_tr_b16 v[182:183], v213 offset:6144
v_mul_f32 v247, v28, v28
v_mfma_f32_16x16x32_bf16 v[116:119], v[196:199], a[108:111], v[116:119]
buffer_load_dwordx4 a[196:199], v233, s[52:55], s98 offen offset:0 sc1
v_mfma_f32_16x16x32_bf16 v[104:107], v[200:203], a[104:107], v[104:107]
ds_read_b64_tr_b16 v[184:185], v214 offset:2048
ds_read_b64_tr_b16 v[186:187], v214 offset:6144
v_fmaak_f32 v247, v247, v223, 0xc0135761
v_mfma_f32_16x16x32_bf16 v[120:123], v[200:203], a[108:111], v[120:123]
buffer_load_dwordx4 v226, s[48:51], s23 offen offset:2048 lds
v_mfma_f32_16x16x32_bf16 v[108:111], v[204:207], a[104:107], v[108:111]
ds_read_b64_tr_b16 v[188:189], v215 offset:2048
ds_read_b64_tr_b16 v[190:191], v215 offset:6144
v_mul_f32 v247, v247, v28
v_mfma_f32_16x16x32_bf16 v[124:127], v[204:207], a[108:111], v[124:127]
v_mul_f32 v28, v28, v60
s_waitcnt lgkmcnt(8)
v_mfma_f32_16x16x32_bf16 v[64:67], v[160:163], a[112:115], v[64:67]
ds_read_b64_tr_b16 v[192:193], v212 offset:8192
ds_read_b64_tr_b16 v[194:195], v212 offset:12288
v_mfma_f32_16x16x32_bf16 v[80:83], v[160:163], a[116:119], v[80:83]
buffer_load_dwordx4 a[200:203], v232, s[52:55], s98 offen offset:1024 sc1
v_mfma_f32_16x16x32_bf16 v[68:71], v[164:167], a[112:115], v[68:71]
ds_read_b64_tr_b16 v[196:197], v213 offset:8192
ds_read_b64_tr_b16 v[198:199], v213 offset:12288
v_exp_f32 v247, v247
v_mfma_f32_16x16x32_bf16 v[84:87], v[164:167], a[116:119], v[84:87]
v_add_f32 v247, 1.0, v247
v_mul_f32 v248, v29, v29
v_mfma_f32_16x16x32_bf16 v[72:75], v[168:171], a[112:115], v[72:75]
ds_read_b64_tr_b16 v[200:201], v214 offset:8192
ds_read_b64_tr_b16 v[202:203], v214 offset:12288
v_mfma_f32_16x16x32_bf16 v[88:91], v[168:171], a[116:119], v[88:91]
buffer_load_dwordx4 v227, s[48:51], s23 offen offset:3072 lds
v_mfma_f32_16x16x32_bf16 v[76:79], v[172:175], a[112:115], v[76:79]
ds_read_b64_tr_b16 v[204:205], v215 offset:8192
ds_read_b64_tr_b16 v[206:207], v215 offset:12288
v_mfma_f32_16x16x32_bf16 v[92:95], v[172:175], a[116:119], v[92:95]
s_add_u32 m0, s77, 32768
s_waitcnt lgkmcnt(8)
v_mfma_f32_16x16x32_bf16 v[96:99], v[176:179], a[112:115], v[96:99]
ds_read_b64_tr_b16 v[160:161], v212 offset:10240
ds_read_b64_tr_b16 v[162:163], v212 offset:14336
v_rcp_f32 v247, v247
v_mfma_f32_16x16x32_bf16 v[112:115], v[176:179], a[116:119], v[112:115]
buffer_load_dwordx4 a[204:207], v233, s[52:55], s98 offen offset:1024 sc1
v_mfma_f32_16x16x32_bf16 v[100:103], v[180:183], a[112:115], v[100:103]
ds_read_b64_tr_b16 v[164:165], v213 offset:10240
ds_read_b64_tr_b16 v[166:167], v213 offset:14336
v_mul_f32 v28, v28, v247
v_mfma_f32_16x16x32_bf16 v[116:119], v[180:183], a[116:119], v[116:119]
v_fmaak_f32 v248, v248, v223, 0xc0135761
v_mul_f32 v248, v248, v29
v_mfma_f32_16x16x32_bf16 v[104:107], v[184:187], a[112:115], v[104:107]
ds_read_b64_tr_b16 v[168:169], v214 offset:10240
ds_read_b64_tr_b16 v[170:171], v214 offset:14336
v_mfma_f32_16x16x32_bf16 v[120:123], v[184:187], a[116:119], v[120:123]
v_exp_f32 v248, v248
v_mfma_f32_16x16x32_bf16 v[108:111], v[188:191], a[112:115], v[108:111]
ds_read_b64_tr_b16 v[172:173], v215 offset:10240
ds_read_b64_tr_b16 v[174:175], v215 offset:14336
v_mfma_f32_16x16x32_bf16 v[124:127], v[188:191], a[116:119], v[124:127]
s_waitcnt vmcnt(33) lgkmcnt(8)
s_barrier
v_mfma_f32_16x16x32_bf16 v[64:67], v[192:195], a[120:123], v[64:67]
ds_read_b64_tr_b16 v[176:177], v208 offset:49152
ds_read_b64_tr_b16 v[178:179], v208 offset:53248
v_mfma_f32_16x16x32_bf16 v[80:83], v[192:195], a[124:127], v[80:83]
buffer_load_dwordx4 v224, s[48:51], s24 offen offset:0 lds
v_mul_f32 v29, v29, v61
v_mfma_f32_16x16x32_bf16 v[68:71], v[196:199], a[120:123], v[68:71]
ds_read_b64_tr_b16 v[180:181], v209 offset:49152
ds_read_b64_tr_b16 v[182:183], v209 offset:53248
v_mfma_f32_16x16x32_bf16 v[84:87], v[196:199], a[124:127], v[84:87]
buffer_load_dwordx4 a[208:211], v232, s[52:55], s99 offen offset:0 sc1
v_add_f32 v248, 1.0, v248
v_mfma_f32_16x16x32_bf16 v[72:75], v[200:203], a[120:123], v[72:75]
ds_read_b64_tr_b16 v[184:185], v210 offset:49152
ds_read_b64_tr_b16 v[186:187], v210 offset:53248
v_mfma_f32_16x16x32_bf16 v[88:91], v[200:203], a[124:127], v[88:91]
v_rcp_f32 v248, v248
v_mfma_f32_16x16x32_bf16 v[76:79], v[204:207], a[120:123], v[76:79]
ds_read_b64_tr_b16 v[188:189], v211 offset:49152
ds_read_b64_tr_b16 v[190:191], v211 offset:53248
v_mfma_f32_16x16x32_bf16 v[92:95], v[204:207], a[124:127], v[92:95]
v_mul_f32 v29, v29, v248
s_waitcnt lgkmcnt(8)
v_mfma_f32_16x16x32_bf16 v[96:99], v[160:163], a[120:123], v[96:99]
ds_read_b64_tr_b16 v[192:193], v208 offset:51200
ds_read_b64_tr_b16 v[194:195], v208 offset:55296
v_mfma_f32_16x16x32_bf16 v[112:115], v[160:163], a[124:127], v[112:115]
buffer_load_dwordx4 v225, s[48:51], s24 offen offset:1024 lds
v_cvt_pk_bf16_f32 v58, v28, v29
v_mfma_f32_16x16x32_bf16 v[100:103], v[164:167], a[120:123], v[100:103]
ds_read_b64_tr_b16 v[196:197], v209 offset:51200
ds_read_b64_tr_b16 v[198:199], v209 offset:55296
v_mfma_f32_16x16x32_bf16 v[116:119], v[164:167], a[124:127], v[116:119]
buffer_load_dwordx4 a[212:215], v233, s[52:55], s99 offen offset:0 sc1
v_mul_f32 v249, v30, v30
v_mfma_f32_16x16x32_bf16 v[104:107], v[168:171], a[120:123], v[104:107]
ds_read_b64_tr_b16 v[200:201], v210 offset:51200
ds_read_b64_tr_b16 v[202:203], v210 offset:55296
v_mfma_f32_16x16x32_bf16 v[120:123], v[168:171], a[124:127], v[120:123]
buffer_load_dwordx4 v226, s[48:51], s24 offen offset:2048 lds
v_fmaak_f32 v249, v249, v223, 0xc0135761
v_mfma_f32_16x16x32_bf16 v[108:111], v[172:175], a[120:123], v[108:111]
ds_read_b64_tr_b16 v[204:205], v211 offset:51200
ds_read_b64_tr_b16 v[206:207], v211 offset:55296
v_mfma_f32_16x16x32_bf16 v[124:127], v[172:175], a[124:127], v[124:127]
v_mul_f32 v249, v249, v30
s_waitcnt lgkmcnt(8)
v_mfma_f32_16x16x32_bf16 v[64:67], v[176:179], a[128:131], v[64:67]
ds_read_b64_tr_b16 v[160:161], v208 offset:57344
ds_read_b64_tr_b16 v[162:163], v208 offset:61440
v_mfma_f32_16x16x32_bf16 v[80:83], v[176:179], a[132:135], v[80:83]
buffer_load_dwordx4 a[216:219], v232, s[52:55], s99 offen offset:1024 sc1
v_exp_f32 v249, v249
v_mfma_f32_16x16x32_bf16 v[68:71], v[180:183], a[128:131], v[68:71]
ds_read_b64_tr_b16 v[164:165], v209 offset:57344
ds_read_b64_tr_b16 v[166:167], v209 offset:61440
v_mfma_f32_16x16x32_bf16 v[84:87], v[180:183], a[132:135], v[84:87]
v_mul_f32 v30, v30, v62
v_add_f32 v249, 1.0, v249
v_mfma_f32_16x16x32_bf16 v[72:75], v[184:187], a[128:131], v[72:75]
ds_read_b64_tr_b16 v[168:169], v210 offset:57344
ds_read_b64_tr_b16 v[170:171], v210 offset:61440
v_mfma_f32_16x16x32_bf16 v[88:91], v[184:187], a[132:135], v[88:91]
buffer_load_dwordx4 v227, s[48:51], s24 offen offset:3072 lds
v_mfma_f32_16x16x32_bf16 v[76:79], v[188:191], a[128:131], v[76:79]
ds_read_b64_tr_b16 v[172:173], v211 offset:57344
ds_read_b64_tr_b16 v[174:175], v211 offset:61440
v_mfma_f32_16x16x32_bf16 v[92:95], v[188:191], a[132:135], v[92:95]
s_add_u32 m0, s77, 65536
v_rcp_f32 v249, v249
s_waitcnt lgkmcnt(8)
v_mfma_f32_16x16x32_bf16 v[96:99], v[192:195], a[128:131], v[96:99]
ds_read_b64_tr_b16 v[176:177], v208 offset:59392
ds_read_b64_tr_b16 v[178:179], v208 offset:63488
v_mul_f32 v30, v30, v249
v_mfma_f32_16x16x32_bf16 v[112:115], v[192:195], a[132:135], v[112:115]
buffer_load_dwordx4 a[220:223], v233, s[52:55], s99 offen offset:1024 sc1
v_mfma_f32_16x16x32_bf16 v[100:103], v[196:199], a[128:131], v[100:103]
ds_read_b64_tr_b16 v[180:181], v209 offset:59392
ds_read_b64_tr_b16 v[182:183], v209 offset:63488
)"
R"(
v_mul_f32 v250, v31, v31
v_mfma_f32_16x16x32_bf16 v[116:119], v[196:199], a[132:135], v[116:119]
v_fmaak_f32 v250, v250, v223, 0xc0135761
v_mul_f32 v250, v250, v31
v_mfma_f32_16x16x32_bf16 v[104:107], v[200:203], a[128:131], v[104:107]
ds_read_b64_tr_b16 v[184:185], v210 offset:59392
ds_read_b64_tr_b16 v[186:187], v210 offset:63488
v_mfma_f32_16x16x32_bf16 v[120:123], v[200:203], a[132:135], v[120:123]
v_exp_f32 v250, v250
v_mfma_f32_16x16x32_bf16 v[108:111], v[204:207], a[128:131], v[108:111]
ds_read_b64_tr_b16 v[188:189], v211 offset:59392
ds_read_b64_tr_b16 v[190:191], v211 offset:63488
v_mfma_f32_16x16x32_bf16 v[124:127], v[204:207], a[132:135], v[124:127]
s_waitcnt vmcnt(33) lgkmcnt(8)
s_barrier
v_mfma_f32_16x16x32_bf16 v[64:67], v[160:163], a[136:139], v[64:67]
ds_read_b64_tr_b16 v[192:193], v212 offset:16384
ds_read_b64_tr_b16 v[194:195], v212 offset:20480
v_mfma_f32_16x16x32_bf16 v[80:83], v[160:163], a[140:143], v[80:83]
buffer_load_dwordx4 v224, s[48:51], s25 offen offset:0 lds
v_mfma_f32_16x16x32_bf16 v[68:71], v[164:167], a[136:139], v[68:71]
ds_read_b64_tr_b16 v[196:197], v213 offset:16384
ds_read_b64_tr_b16 v[198:199], v213 offset:20480
v_mul_f32 v31, v31, v63
v_mfma_f32_16x16x32_bf16 v[84:87], v[164:167], a[140:143], v[84:87]
buffer_load_dwordx4 a[224:227], v232, s[52:55], s100 offen offset:0 sc1
v_mfma_f32_16x16x32_bf16 v[72:75], v[168:171], a[136:139], v[72:75]
ds_read_b64_tr_b16 v[200:201], v214 offset:16384
ds_read_b64_tr_b16 v[202:203], v214 offset:20480
v_add_f32 v250, 1.0, v250
v_mfma_f32_16x16x32_bf16 v[88:91], v[168:171], a[140:143], v[88:91]
v_rcp_f32 v250, v250
v_mfma_f32_16x16x32_bf16 v[76:79], v[172:175], a[136:139], v[76:79]
ds_read_b64_tr_b16 v[204:205], v215 offset:16384
ds_read_b64_tr_b16 v[206:207], v215 offset:20480
v_mfma_f32_16x16x32_bf16 v[92:95], v[172:175], a[140:143], v[92:95]
v_add_u32 v224, 131072, v224
s_waitcnt lgkmcnt(8)
v_mfma_f32_16x16x32_bf16 v[96:99], v[176:179], a[136:139], v[96:99]
ds_read_b64_tr_b16 v[160:161], v212 offset:18432
ds_read_b64_tr_b16 v[162:163], v212 offset:22528
v_mul_f32 v31, v31, v250
v_mfma_f32_16x16x32_bf16 v[112:115], v[176:179], a[140:143], v[112:115]
buffer_load_dwordx4 v225, s[48:51], s25 offen offset:1024 lds
v_mfma_f32_16x16x32_bf16 v[100:103], v[180:183], a[136:139], v[100:103]
LC20_%=:
s_cbranch_execz LRETB_%=
ds_read_b64_tr_b16 v[164:165], v213 offset:18432
ds_read_b64_tr_b16 v[166:167], v213 offset:22528
v_cvt_pk_bf16_f32 v59, v30, v31
v_mfma_f32_16x16x32_bf16 v[116:119], v[180:183], a[140:143], v[116:119]
buffer_load_dwordx4 a[228:231], v233, s[52:55], s100 offen offset:0 sc1
v_mfma_f32_16x16x32_bf16 v[104:107], v[184:187], a[136:139], v[104:107]
ds_read_b64_tr_b16 v[168:169], v214 offset:18432
ds_read_b64_tr_b16 v[170:171], v214 offset:22528
v_min_u32 v56, -2, v56
v_mfma_f32_16x16x32_bf16 v[120:123], v[184:187], a[140:143], v[120:123]
buffer_load_dwordx4 v226, s[48:51], s25 offen offset:2048 lds
v_mfma_f32_16x16x32_bf16 v[108:111], v[188:191], a[136:139], v[108:111]
ds_read_b64_tr_b16 v[172:173], v215 offset:18432
ds_read_b64_tr_b16 v[174:175], v215 offset:22528
buffer_store_dwordx4 v[56:59], v221, s[52:55], s92 offen offset:1024 sc1
v_mfma_f32_16x16x32_bf16 v[124:127], v[188:191], a[140:143], v[124:127]
v_add_u32 v225, 131072, v225
v_mul_f32 v243, v128, v128
s_waitcnt lgkmcnt(8)
v_mfma_f32_16x16x32_bf16 v[64:67], v[192:195], a[144:147], v[64:67]
ds_read_b64_tr_b16 v[176:177], v212 offset:24576
ds_read_b64_tr_b16 v[178:179], v212 offset:28672
v_fmaak_f32 v243, v243, v223, 0xc0135761
v_mfma_f32_16x16x32_bf16 v[80:83], v[192:195], a[148:151], v[80:83]
buffer_load_dwordx4 a[232:235], v232, s[52:55], s100 offen offset:1024 sc1
v_mul_f32 v243, v243, v128
v_mfma_f32_16x16x32_bf16 v[68:71], v[196:199], a[144:147], v[68:71]
ds_read_b64_tr_b16 v[180:181], v213 offset:24576
ds_read_b64_tr_b16 v[182:183], v213 offset:28672
v_exp_f32 v243, v243
v_mfma_f32_16x16x32_bf16 v[84:87], v[196:199], a[148:151], v[84:87]
v_mul_f32 v128, v128, v144
v_add_f32 v243, 1.0, v243
v_mfma_f32_16x16x32_bf16 v[72:75], v[200:203], a[144:147], v[72:75]
ds_read_b64_tr_b16 v[184:185], v214 offset:24576
ds_read_b64_tr_b16 v[186:187], v214 offset:28672
v_mfma_f32_16x16x32_bf16 v[88:91], v[200:203], a[148:151], v[88:91]
buffer_load_dwordx4 v227, s[48:51], s25 offen offset:3072 lds
v_mfma_f32_16x16x32_bf16 v[76:79], v[204:207], a[144:147], v[76:79]
ds_read_b64_tr_b16 v[188:189], v215 offset:24576
ds_read_b64_tr_b16 v[190:191], v215 offset:28672
v_rcp_f32 v243, v243
v_mfma_f32_16x16x32_bf16 v[92:95], v[204:207], a[148:151], v[92:95]
s_add_u32 m0, s77, 49152
v_mul_f32 v128, v128, v243
s_waitcnt lgkmcnt(8)
v_mfma_f32_16x16x32_bf16 v[96:99], v[160:163], a[144:147], v[96:99]
ds_read_b64_tr_b16 v[192:193], v212 offset:26624
ds_read_b64_tr_b16 v[194:195], v212 offset:30720
v_mul_f32 v244, v129, v129
v_mfma_f32_16x16x32_bf16 v[112:115], v[160:163], a[148:151], v[112:115]
buffer_load_dwordx4 a[236:239], v233, s[52:55], s100 offen offset:1024 sc1
v_fmaak_f32 v244, v244, v223, 0xc0135761
v_mfma_f32_16x16x32_bf16 v[100:103], v[164:167], a[144:147], v[100:103]
ds_read_b64_tr_b16 v[196:197], v213 offset:26624
ds_read_b64_tr_b16 v[198:199], v213 offset:30720
v_mul_f32 v244, v244, v129
v_mfma_f32_16x16x32_bf16 v[116:119], v[164:167], a[148:151], v[116:119]
v_exp_f32 v244, v244
v_mfma_f32_16x16x32_bf16 v[104:107], v[168:171], a[144:147], v[104:107]
ds_read_b64_tr_b16 v[200:201], v214 offset:26624
ds_read_b64_tr_b16 v[202:203], v214 offset:30720
v_mul_f32 v129, v129, v145
v_mfma_f32_16x16x32_bf16 v[120:123], v[168:171], a[148:151], v[120:123]
v_add_f32 v244, 1.0, v244
v_mul_f32 v245, v130, v130
v_mfma_f32_16x16x32_bf16 v[108:111], v[172:175], a[144:147], v[108:111]
ds_read_b64_tr_b16 v[204:205], v215 offset:26624
ds_read_b64_tr_b16 v[206:207], v215 offset:30720
v_mfma_f32_16x16x32_bf16 v[124:127], v[172:175], a[148:151], v[124:127]
s_waitcnt vmcnt(33) lgkmcnt(8)
s_barrier
v_mfma_f32_16x16x32_bf16 v[64:67], v[176:179], a[152:155], v[64:67]
ds_read_b64_tr_b16 v[160:161], v208 offset:0
ds_read_b64_tr_b16 v[162:163], v208 offset:4096
v_rcp_f32 v244, v244
v_mfma_f32_16x16x32_bf16 v[80:83], v[176:179], a[156:159], v[80:83]
buffer_load_dwordx4 v224, s[48:51], s18 offen offset:0 lds
v_mul_f32 v129, v129, v244
v_mfma_f32_16x16x32_bf16 v[68:71], v[180:183], a[152:155], v[68:71]
ds_read_b64_tr_b16 v[164:165], v209 offset:0
ds_read_b64_tr_b16 v[166:167], v209 offset:4096
v_cvt_pk_bf16_f32 v144, v128, v129
v_mfma_f32_16x16x32_bf16 v[84:87], v[180:183], a[156:159], v[84:87]
buffer_load_dwordx4 a[240:243], v232, s[52:55], s101 offen offset:0 sc1
v_fmaak_f32 v245, v245, v223, 0xc0135761
v_mfma_f32_16x16x32_bf16 v[72:75], v[184:187], a[152:155], v[72:75]
ds_read_b64_tr_b16 v[168:169], v210 offset:0
ds_read_b64_tr_b16 v[170:171], v210 offset:4096
v_mul_f32 v245, v245, v130
v_mfma_f32_16x16x32_bf16 v[88:91], v[184:187], a[156:159], v[88:91]
v_add_u32 v227, 131072, v227
v_mul_f32 v130, v130, v146
v_mfma_f32_16x16x32_bf16 v[76:79], v[188:191], a[152:155], v[76:79]
ds_read_b64_tr_b16 v[172:173], v211 offset:0
ds_read_b64_tr_b16 v[174:175], v211 offset:4096
v_mfma_f32_16x16x32_bf16 v[92:95], v[188:191], a[156:159], v[92:95]
v_add_u32 v226, 131072, v226
v_exp_f32 v245, v245
s_waitcnt lgkmcnt(8)
v_mfma_f32_16x16x32_bf16 v[96:99], v[192:195], a[152:155], v[96:99]
ds_read_b64_tr_b16 v[176:177], v208 offset:2048
ds_read_b64_tr_b16 v[178:179], v208 offset:6144
v_add_f32 v245, 1.0, v245
v_mfma_f32_16x16x32_bf16 v[112:115], v[192:195], a[156:159], v[112:115]
buffer_load_dwordx4 v225, s[48:51], s18 offen offset:1024 lds
v_rcp_f32 v245, v245
v_mfma_f32_16x16x32_bf16 v[100:103], v[196:199], a[152:155], v[100:103]
ds_read_b64_tr_b16 v[180:181], v209 offset:2048
ds_read_b64_tr_b16 v[182:183], v209 offset:6144
v_mul_f32 v130, v130, v245
v_mfma_f32_16x16x32_bf16 v[116:119], v[196:199], a[156:159], v[116:119]
buffer_load_dwordx4 a[244:247], v233, s[52:55], s101 offen offset:0 sc1
v_mul_f32 v246, v131, v131
v_mfma_f32_16x16x32_bf16 v[104:107], v[200:203], a[152:155], v[104:107]
ds_read_b64_tr_b16 v[184:185], v210 offset:2048
ds_read_b64_tr_b16 v[186:187], v210 offset:6144
v_mfma_f32_16x16x32_bf16 v[120:123], v[200:203], a[156:159], v[120:123]
buffer_load_dwordx4 v226, s[48:51], s18 offen offset:2048 lds
v_fmaak_f32 v246, v246, v223, 0xc0135761
v_mfma_f32_16x16x32_bf16 v[108:111], v[204:207], a[152:155], v[108:111]
ds_read_b64_tr_b16 v[188:189], v211 offset:2048
ds_read_b64_tr_b16 v[190:191], v211 offset:6144
v_mul_f32 v246, v246, v131
v_mfma_f32_16x16x32_bf16 v[124:127], v[204:207], a[156:159], v[124:127]
v_mul_f32 v131, v131, v147
s_waitcnt lgkmcnt(8)
v_mfma_f32_16x16x32_bf16 v[64:67], v[160:163], a[160:163], v[64:67]
ds_read_b64_tr_b16 v[192:193], v208 offset:8192
ds_read_b64_tr_b16 v[194:195], v208 offset:12288
v_exp_f32 v246, v246
v_mfma_f32_16x16x32_bf16 v[80:83], v[160:163], a[164:167], v[80:83]
buffer_load_dwordx4 a[248:251], v232, s[52:55], s101 offen offset:1024 sc1
v_add_f32 v246, 1.0, v246
v_mfma_f32_16x16x32_bf16 v[68:71], v[164:167], a[160:163], v[68:71]
ds_read_b64_tr_b16 v[196:197], v209 offset:8192
ds_read_b64_tr_b16 v[198:199], v209 offset:12288
v_rcp_f32 v246, v246
v_mfma_f32_16x16x32_bf16 v[84:87], v[164:167], a[164:167], v[84:87]
v_mul_f32 v131, v131, v246
v_cvt_pk_bf16_f32 v145, v130, v131
v_mfma_f32_16x16x32_bf16 v[72:75], v[168:171], a[160:163], v[72:75]
ds_read_b64_tr_b16 v[200:201], v210 offset:8192
ds_read_b64_tr_b16 v[202:203], v210 offset:12288
v_mfma_f32_16x16x32_bf16 v[88:91], v[168:171], a[164:167], v[88:91]
buffer_load_dwordx4 v227, s[48:51], s18 offen offset:3072 lds
v_mfma_f32_16x16x32_bf16 v[76:79], v[172:175], a[160:163], v[76:79]
ds_read_b64_tr_b16 v[204:205], v211 offset:8192
ds_read_b64_tr_b16 v[206:207], v211 offset:12288
v_mul_f32 v247, v132, v132
v_mfma_f32_16x16x32_bf16 v[92:95], v[172:175], a[164:167], v[92:95]
s_add_u32 m0, s77, 81920
v_fmaak_f32 v247, v247, v223, 0xc0135761
s_waitcnt lgkmcnt(8)
v_mfma_f32_16x16x32_bf16 v[96:99], v[176:179], a[160:163], v[96:99]
ds_read_b64_tr_b16 v[160:161], v208 offset:10240
ds_read_b64_tr_b16 v[162:163], v208 offset:14336
v_mul_f32 v247, v247, v132
v_mfma_f32_16x16x32_bf16 v[112:115], v[176:179], a[164:167], v[112:115]
buffer_load_dwordx4 a[252:255], v233, s[52:55], s101 offen offset:1024 sc1
v_exp_f32 v247, v247
v_mfma_f32_16x16x32_bf16 v[100:103], v[180:183], a[160:163], v[100:103]
ds_read_b64_tr_b16 v[164:165], v209 offset:10240
ds_read_b64_tr_b16 v[166:167], v209 offset:14336
v_mul_f32 v132, v132, v148
v_mfma_f32_16x16x32_bf16 v[116:119], v[180:183], a[164:167], v[116:119]
v_add_f32 v247, 1.0, v247
v_mul_f32 v248, v133, v133
v_mfma_f32_16x16x32_bf16 v[104:107], v[184:187], a[160:163], v[104:107]
ds_read_b64_tr_b16 v[168:169], v210 offset:10240
ds_read_b64_tr_b16 v[170:171], v210 offset:14336
v_mfma_f32_16x16x32_bf16 v[120:123], v[184:187], a[164:167], v[120:123]
v_rcp_f32 v247, v247
v_mfma_f32_16x16x32_bf16 v[108:111], v[188:191], a[160:163], v[108:111]
ds_read_b64_tr_b16 v[172:173], v211 offset:10240
ds_read_b64_tr_b16 v[174:175], v211 offset:14336
v_mfma_f32_16x16x32_bf16 v[124:127], v[188:191], a[164:167], v[124:127]
v_mul_f32 v132, v132, v247
s_waitcnt vmcnt(33) lgkmcnt(8)
s_barrier
v_mfma_f32_16x16x32_bf16 v[64:67], v[192:195], a[168:171], v[64:67]
ds_read_b64_tr_b16 v[176:177], v212 offset:32768
ds_read_b64_tr_b16 v[178:179], v212 offset:36864
v_fmaak_f32 v248, v248, v223, 0xc0135761
v_mfma_f32_16x16x32_bf16 v[80:83], v[192:195], a[172:175], v[80:83]
buffer_load_dwordx4 v224, s[48:51], s19 offen offset:0 lds
v_mul_f32 v248, v248, v133
v_mfma_f32_16x16x32_bf16 v[68:71], v[196:199], a[168:171], v[68:71]
ds_read_b64_tr_b16 v[180:181], v213 offset:32768
ds_read_b64_tr_b16 v[182:183], v213 offset:36864
v_exp_f32 v248, v248
v_mfma_f32_16x16x32_bf16 v[84:87], v[196:199], a[172:175], v[84:87]
v_mul_f32 v133, v133, v149
v_add_f32 v248, 1.0, v248
v_mfma_f32_16x16x32_bf16 v[72:75], v[200:203], a[168:171], v[72:75]
ds_read_b64_tr_b16 v[184:185], v214 offset:32768
ds_read_b64_tr_b16 v[186:187], v214 offset:36864
v_mfma_f32_16x16x32_bf16 v[88:91], v[200:203], a[172:175], v[88:91]
v_rcp_f32 v248, v248
v_mfma_f32_16x16x32_bf16 v[76:79], v[204:207], a[168:171], v[76:79]
ds_read_b64_tr_b16 v[188:189], v215 offset:32768
ds_read_b64_tr_b16 v[190:191], v215 offset:36864
v_mfma_f32_16x16x32_bf16 v[92:95], v[204:207], a[172:175], v[92:95]
v_mul_f32 v133, v133, v248
s_waitcnt lgkmcnt(8)
v_mfma_f32_16x16x32_bf16 v[96:99], v[160:163], a[168:171], v[96:99]
ds_read_b64_tr_b16 v[192:193], v212 offset:34816
ds_read_b64_tr_b16 v[194:195], v212 offset:38912
v_cvt_pk_bf16_f32 v146, v132, v133
v_mfma_f32_16x16x32_bf16 v[112:115], v[160:163], a[172:175], v[112:115]
buffer_load_dwordx4 v225, s[48:51], s19 offen offset:1024 lds
v_mul_f32 v249, v134, v134
v_mfma_f32_16x16x32_bf16 v[100:103], v[164:167], a[168:171], v[100:103]
ds_read_b64_tr_b16 v[196:197], v213 offset:34816
ds_read_b64_tr_b16 v[198:199], v213 offset:38912
v_fmaak_f32 v249, v249, v223, 0xc0135761
v_mfma_f32_16x16x32_bf16 v[116:119], v[164:167], a[172:175], v[116:119]
v_mul_f32 v249, v249, v134
v_mul_f32 v134, v134, v150
v_mfma_f32_16x16x32_bf16 v[104:107], v[168:171], a[168:171], v[104:107]
ds_read_b64_tr_b16 v[200:201], v214 offset:34816
ds_read_b64_tr_b16 v[202:203], v214 offset:38912
v_mfma_f32_16x16x32_bf16 v[120:123], v[168:171], a[172:175], v[120:123]
buffer_load_dwordx4 v226, s[48:51], s19 offen offset:2048 lds
v_mfma_f32_16x16x32_bf16 v[108:111], v[172:175], a[168:171], v[108:111]
ds_read_b64_tr_b16 v[204:205], v215 offset:34816
ds_read_b64_tr_b16 v[206:207], v215 offset:38912
v_exp_f32 v249, v249
v_mfma_f32_16x16x32_bf16 v[124:127], v[172:175], a[172:175], v[124:127]
v_subrev_u32 v226, s30, v226
v_add_f32 v249, 1.0, v249
s_waitcnt lgkmcnt(8)
v_mfma_f32_16x16x32_bf16 v[64:67], v[176:179], a[176:179], v[64:67]
ds_read_b64_tr_b16 v[160:161], v212 offset:40960
ds_read_b64_tr_b16 v[162:163], v212 offset:45056
v_rcp_f32 v249, v249
v_mfma_f32_16x16x32_bf16 v[80:83], v[176:179], a[180:183], v[80:83]
v_mul_f32 v134, v134, v249
v_mul_f32 v250, v135, v135
v_mfma_f32_16x16x32_bf16 v[68:71], v[180:183], a[176:179], v[68:71]
ds_read_b64_tr_b16 v[164:165], v213 offset:40960
ds_read_b64_tr_b16 v[166:167], v213 offset:45056
v_mfma_f32_16x16x32_bf16 v[84:87], v[180:183], a[180:183], v[84:87]
v_fmaak_f32 v250, v250, v223, 0xc0135761
v_mul_f32 v250, v250, v135
v_mfma_f32_16x16x32_bf16 v[72:75], v[184:187], a[176:179], v[72:75]
ds_read_b64_tr_b16 v[168:169], v214 offset:40960
ds_read_b64_tr_b16 v[170:171], v214 offset:45056
v_mfma_f32_16x16x32_bf16 v[88:91], v[184:187], a[180:183], v[88:91]
buffer_load_dwordx4 v227, s[48:51], s19 offen offset:3072 lds
v_mfma_f32_16x16x32_bf16 v[76:79], v[188:191], a[176:179], v[76:79]
ds_read_b64_tr_b16 v[172:173], v215 offset:40960
ds_read_b64_tr_b16 v[174:175], v215 offset:45056
v_exp_f32 v250, v250
v_mfma_f32_16x16x32_bf16 v[92:95], v[188:191], a[180:183], v[92:95]
s_add_u32 m0, s77, 0
v_mul_f32 v135, v135, v151
s_waitcnt lgkmcnt(8)
v_mfma_f32_16x16x32_bf16 v[96:99], v[192:195], a[176:179], v[96:99]
ds_read_b64_tr_b16 v[176:177], v212 offset:43008
ds_read_b64_tr_b16 v[178:179], v212 offset:47104
v_add_f32 v250, 1.0, v250
v_mfma_f32_16x16x32_bf16 v[112:115], v[192:195], a[180:183], v[112:115]
v_rcp_f32 v250, v250
v_mfma_f32_16x16x32_bf16 v[100:103], v[196:199], a[176:179], v[100:103]
ds_read_b64_tr_b16 v[180:181], v213 offset:43008
ds_read_b64_tr_b16 v[182:183], v213 offset:47104
v_mul_f32 v135, v135, v250
v_mfma_f32_16x16x32_bf16 v[116:119], v[196:199], a[180:183], v[116:119]
v_cvt_pk_bf16_f32 v147, v134, v135
v_min_u32 v144, -2, v144
v_mfma_f32_16x16x32_bf16 v[104:107], v[200:203], a[176:179], v[104:107]
ds_read_b64_tr_b16 v[184:185], v214 offset:43008
ds_read_b64_tr_b16 v[186:187], v214 offset:47104
v_mfma_f32_16x16x32_bf16 v[120:123], v[200:203], a[180:183], v[120:123]
buffer_store_dwordx4 v[144:147], v220, s[52:55], s90 offen offset:0 sc1
v_mfma_f32_16x16x32_bf16 v[108:111], v[204:207], a[176:179], v[108:111]
ds_read_b64_tr_b16 v[188:189], v215 offset:43008
ds_read_b64_tr_b16 v[190:191], v215 offset:47104
v_mfma_f32_16x16x32_bf16 v[124:127], v[204:207], a[180:183], v[124:127]
v_mul_f32 v251, v136, v136
s_waitcnt vmcnt(30) lgkmcnt(8)
s_barrier
v_mfma_f32_16x16x32_bf16 v[64:67], v[160:163], a[184:187], v[64:67]
ds_read_b64_tr_b16 v[192:193], v208 offset:16384
ds_read_b64_tr_b16 v[194:195], v208 offset:20480
v_fmaak_f32 v251, v251, v223, 0xc0135761
v_mfma_f32_16x16x32_bf16 v[80:83], v[160:163], a[188:191], v[80:83]
buffer_load_dwordx4 v224, s[48:51], s20 offen offset:0 lds
v_mul_f32 v251, v251, v136
v_mfma_f32_16x16x32_bf16 v[68:71], v[164:167], a[184:187], v[68:71]
ds_read_b64_tr_b16 v[196:197], v209 offset:16384
ds_read_b64_tr_b16 v[198:199], v209 offset:20480
v_mfma_f32_16x16x32_bf16 v[84:87], v[164:167], a[188:191], v[84:87]
v_exp_f32 v251, v251
v_mfma_f32_16x16x32_bf16 v[72:75], v[168:171], a[184:187], v[72:75]
ds_read_b64_tr_b16 v[200:201], v210 offset:16384
ds_read_b64_tr_b16 v[202:203], v210 offset:20480
v_mul_f32 v136, v136, v152
v_mfma_f32_16x16x32_bf16 v[88:91], v[168:171], a[188:191], v[88:91]
v_add_f32 v251, 1.0, v251
v_mul_f32 v252, v137, v137
v_mfma_f32_16x16x32_bf16 v[76:79], v[172:175], a[184:187], v[76:79]
ds_read_b64_tr_b16 v[204:205], v211 offset:16384
ds_read_b64_tr_b16 v[206:207], v211 offset:20480
v_mfma_f32_16x16x32_bf16 v[92:95], v[172:175], a[188:191], v[92:95]
v_subrev_u32 v227, s30, v227
v_rcp_f32 v251, v251
s_waitcnt lgkmcnt(8)
v_mfma_f32_16x16x32_bf16 v[96:99], v[176:179], a[184:187], v[96:99]
ds_read_b64_tr_b16 v[160:161], v208 offset:18432
ds_read_b64_tr_b16 v[162:163], v208 offset:22528
v_mul_f32 v136, v136, v251
v_mfma_f32_16x16x32_bf16 v[112:115], v[176:179], a[188:191], v[112:115]
buffer_load_dwordx4 v225, s[48:51], s20 offen offset:1024 lds
v_fmaak_f32 v252, v252, v223, 0xc0135761
v_mfma_f32_16x16x32_bf16 v[100:103], v[180:183], a[184:187], v[100:103]
ds_read_b64_tr_b16 v[164:165], v209 offset:18432
ds_read_b64_tr_b16 v[166:167], v209 offset:22528
v_mfma_f32_16x16x32_bf16 v[116:119], v[180:183], a[188:191], v[116:119]
v_mul_f32 v252, v252, v137
v_mul_f32 v137, v137, v153
v_mfma_f32_16x16x32_bf16 v[104:107], v[184:187], a[184:187], v[104:107]
ds_read_b64_tr_b16 v[168:169], v210 offset:18432
ds_read_b64_tr_b16 v[170:171], v210 offset:22528
v_mfma_f32_16x16x32_bf16 v[120:123], v[184:187], a[188:191], v[120:123]
buffer_load_dwordx4 v226, s[48:51], s21 offen offset:2048 lds
v_exp_f32 v252, v252
v_mfma_f32_16x16x32_bf16 v[108:111], v[188:191], a[184:187], v[108:111]
ds_read_b64_tr_b16 v[172:173], v211 offset:18432
ds_read_b64_tr_b16 v[174:175], v211 offset:22528
v_add_f32 v252, 1.0, v252
v_mfma_f32_16x16x32_bf16 v[124:127], v[188:191], a[188:191], v[124:127]
v_add_u32 v232, 2048, v232
v_rcp_f32 v252, v252
s_waitcnt lgkmcnt(8)
v_mfma_f32_16x16x32_bf16 v[64:67], v[192:195], a[192:195], v[64:67]
ds_read_b64_tr_b16 v[176:177], v208 offset:24576
ds_read_b64_tr_b16 v[178:179], v208 offset:28672
v_mul_f32 v137, v137, v252
v_mfma_f32_16x16x32_bf16 v[80:83], v[192:195], a[196:199], v[80:83]
v_cvt_pk_bf16_f32 v152, v136, v137
v_mul_f32 v253, v138, v138
v_mfma_f32_16x16x32_bf16 v[68:71], v[196:199], a[192:195], v[68:71]
ds_read_b64_tr_b16 v[180:181], v209 offset:24576
ds_read_b64_tr_b16 v[182:183], v209 offset:28672
v_mfma_f32_16x16x32_bf16 v[84:87], v[196:199], a[196:199], v[84:87]
v_fmaak_f32 v253, v253, v223, 0xc0135761
v_mul_f32 v253, v253, v138
v_mfma_f32_16x16x32_bf16 v[72:75], v[200:203], a[192:195], v[72:75]
ds_read_b64_tr_b16 v[184:185], v210 offset:24576
ds_read_b64_tr_b16 v[186:187], v210 offset:28672
v_mfma_f32_16x16x32_bf16 v[88:91], v[200:203], a[196:199], v[88:91]
)"
R"(
buffer_load_dwordx4 v227, s[48:51], s21 offen offset:3072 lds
v_mfma_f32_16x16x32_bf16 v[76:79], v[204:207], a[192:195], v[76:79]
ds_read_b64_tr_b16 v[188:189], v211 offset:24576
ds_read_b64_tr_b16 v[190:191], v211 offset:28672
v_exp_f32 v253, v253
v_mfma_f32_16x16x32_bf16 v[92:95], v[204:207], a[196:199], v[92:95]
v_add_u32 v233, 2048, v233
v_mul_f32 v138, v138, v154
s_waitcnt lgkmcnt(8)
v_mfma_f32_16x16x32_bf16 v[96:99], v[160:163], a[192:195], v[96:99]
ds_read_b64_tr_b16 v[192:193], v208 offset:26624
ds_read_b64_tr_b16 v[194:195], v208 offset:30720
v_add_f32 v253, 1.0, v253
v_mfma_f32_16x16x32_bf16 v[112:115], v[160:163], a[196:199], v[112:115]
buffer_load_dwordx4 a[0:3], v232, s[52:55], s94 offen offset:0 sc1
v_mfma_f32_16x16x32_bf16 v[100:103], v[164:167], a[192:195], v[100:103]
ds_read_b64_tr_b16 v[196:197], v209 offset:26624
ds_read_b64_tr_b16 v[198:199], v209 offset:30720
v_rcp_f32 v253, v253
v_mfma_f32_16x16x32_bf16 v[116:119], v[164:167], a[196:199], v[116:119]
buffer_load_dwordx4 a[4:7], v233, s[52:55], s94 offen offset:0 sc1
v_mul_f32 v138, v138, v253
v_mfma_f32_16x16x32_bf16 v[104:107], v[168:171], a[192:195], v[104:107]
ds_read_b64_tr_b16 v[200:201], v210 offset:26624
ds_read_b64_tr_b16 v[202:203], v210 offset:30720
v_mul_f32 v254, v139, v139
v_mfma_f32_16x16x32_bf16 v[120:123], v[168:171], a[196:199], v[120:123]
v_fmaak_f32 v254, v254, v223, 0xc0135761
v_mul_f32 v254, v254, v139
v_mfma_f32_16x16x32_bf16 v[108:111], v[172:175], a[192:195], v[108:111]
ds_read_b64_tr_b16 v[204:205], v211 offset:26624
ds_read_b64_tr_b16 v[206:207], v211 offset:30720
v_mfma_f32_16x16x32_bf16 v[124:127], v[172:175], a[196:199], v[124:127]
v_exp_f32 v254, v254
s_waitcnt vmcnt(28) lgkmcnt(8)
s_barrier
v_mfma_f32_16x16x32_bf16 v[64:67], v[176:179], a[200:203], v[64:67]
ds_read_b64_tr_b16 v[160:161], v212 offset:49152
ds_read_b64_tr_b16 v[162:163], v212 offset:53248
v_mul_f32 v139, v139, v155
v_mfma_f32_16x16x32_bf16 v[80:83], v[176:179], a[204:207], v[80:83]
buffer_load_dwordx4 a[8:11], v232, s[52:55], s94 offen offset:1024 sc1
v_mfma_f32_16x16x32_bf16 v[68:71], v[180:183], a[200:203], v[68:71]
ds_read_b64_tr_b16 v[164:165], v213 offset:49152
ds_read_b64_tr_b16 v[166:167], v213 offset:53248
v_add_f32 v254, 1.0, v254
v_mfma_f32_16x16x32_bf16 v[84:87], v[180:183], a[204:207], v[84:87]
buffer_load_dwordx4 a[12:15], v233, s[52:55], s94 offen offset:1024 sc1
v_rcp_f32 v254, v254
v_mfma_f32_16x16x32_bf16 v[72:75], v[184:187], a[200:203], v[72:75]
ds_read_b64_tr_b16 v[168:169], v214 offset:49152
ds_read_b64_tr_b16 v[170:171], v214 offset:53248
v_mul_f32 v139, v139, v254
v_mfma_f32_16x16x32_bf16 v[88:91], v[184:187], a[204:207], v[88:91]
v_cvt_pk_bf16_f32 v153, v138, v139
v_mul_f32 v243, v140, v140
v_mfma_f32_16x16x32_bf16 v[76:79], v[188:191], a[200:203], v[76:79]
ds_read_b64_tr_b16 v[172:173], v215 offset:49152
ds_read_b64_tr_b16 v[174:175], v215 offset:53248
v_mfma_f32_16x16x32_bf16 v[92:95], v[188:191], a[204:207], v[92:95]
v_fmaak_f32 v243, v243, v223, 0xc0135761
s_waitcnt lgkmcnt(8)
v_mfma_f32_16x16x32_bf16 v[96:99], v[192:195], a[200:203], v[96:99]
ds_read_b64_tr_b16 v[176:177], v212 offset:51200
ds_read_b64_tr_b16 v[178:179], v212 offset:55296
v_mul_f32 v243, v243, v140
v_mfma_f32_16x16x32_bf16 v[112:115], v[192:195], a[204:207], v[112:115]
buffer_load_dwordx4 a[16:19], v232, s[52:55], s95 offen offset:0 sc1
v_mfma_f32_16x16x32_bf16 v[100:103], v[196:199], a[200:203], v[100:103]
ds_read_b64_tr_b16 v[180:181], v213 offset:51200
ds_read_b64_tr_b16 v[182:183], v213 offset:55296
v_exp_f32 v243, v243
v_mfma_f32_16x16x32_bf16 v[116:119], v[196:199], a[204:207], v[116:119]
v_mul_f32 v140, v140, v156
v_add_f32 v243, 1.0, v243
v_mfma_f32_16x16x32_bf16 v[104:107], v[200:203], a[200:203], v[104:107]
ds_read_b64_tr_b16 v[184:185], v214 offset:51200
ds_read_b64_tr_b16 v[186:187], v214 offset:55296
v_mfma_f32_16x16x32_bf16 v[120:123], v[200:203], a[204:207], v[120:123]
buffer_load_dwordx4 a[20:23], v233, s[52:55], s95 offen offset:0 sc1
v_rcp_f32 v243, v243
v_mfma_f32_16x16x32_bf16 v[108:111], v[204:207], a[200:203], v[108:111]
ds_read_b64_tr_b16 v[188:189], v215 offset:51200
ds_read_b64_tr_b16 v[190:191], v215 offset:55296
v_mul_f32 v140, v140, v243
v_mfma_f32_16x16x32_bf16 v[124:127], v[204:207], a[204:207], v[124:127]
v_mul_f32 v244, v141, v141
s_waitcnt lgkmcnt(8)
v_mfma_f32_16x16x32_bf16 v[64:67], v[160:163], a[208:211], v[64:67]
ds_read_b64_tr_b16 v[192:193], v212 offset:57344
ds_read_b64_tr_b16 v[194:195], v212 offset:61440
v_fmaak_f32 v244, v244, v223, 0xc0135761
v_mfma_f32_16x16x32_bf16 v[80:83], v[160:163], a[212:215], v[80:83]
v_mul_f32 v244, v244, v141
v_mul_f32 v141, v141, v157
v_mfma_f32_16x16x32_bf16 v[68:71], v[164:167], a[208:211], v[68:71]
ds_read_b64_tr_b16 v[196:197], v213 offset:57344
ds_read_b64_tr_b16 v[198:199], v213 offset:61440
v_mfma_f32_16x16x32_bf16 v[84:87], v[164:167], a[212:215], v[84:87]
buffer_load_dwordx4 a[24:27], v232, s[52:55], s95 offen offset:1024 sc1
v_mfma_f32_16x16x32_bf16 v[72:75], v[168:171], a[208:211], v[72:75]
ds_read_b64_tr_b16 v[200:201], v214 offset:57344
ds_read_b64_tr_b16 v[202:203], v214 offset:61440
v_exp_f32 v244, v244
v_mfma_f32_16x16x32_bf16 v[88:91], v[168:171], a[212:215], v[88:91]
v_add_f32 v244, 1.0, v244
v_mul_f32 v245, v142, v142
v_mfma_f32_16x16x32_bf16 v[76:79], v[172:175], a[208:211], v[76:79]
ds_read_b64_tr_b16 v[204:205], v215 offset:57344
ds_read_b64_tr_b16 v[206:207], v215 offset:61440
v_mfma_f32_16x16x32_bf16 v[92:95], v[172:175], a[212:215], v[92:95]
s_add_u32 m0, s77, 98304
v_rcp_f32 v244, v244
s_waitcnt lgkmcnt(8)
v_mfma_f32_16x16x32_bf16 v[96:99], v[176:179], a[208:211], v[96:99]
ds_read_b64_tr_b16 v[160:161], v212 offset:59392
ds_read_b64_tr_b16 v[162:163], v212 offset:63488
v_mfma_f32_16x16x32_bf16 v[112:115], v[176:179], a[212:215], v[112:115]
buffer_load_dwordx4 a[28:31], v233, s[52:55], s95 offen offset:1024 sc1
v_mul_f32 v141, v141, v244
v_mfma_f32_16x16x32_bf16 v[100:103], v[180:183], a[208:211], v[100:103]
ds_read_b64_tr_b16 v[164:165], v213 offset:59392
ds_read_b64_tr_b16 v[166:167], v213 offset:63488
v_cvt_pk_bf16_f32 v154, v140, v141
v_mfma_f32_16x16x32_bf16 v[116:119], v[180:183], a[212:215], v[116:119]
v_fmaak_f32 v245, v245, v223, 0xc0135761
v_mul_f32 v245, v245, v142
v_mfma_f32_16x16x32_bf16 v[104:107], v[184:187], a[208:211], v[104:107]
ds_read_b64_tr_b16 v[168:169], v214 offset:59392
ds_read_b64_tr_b16 v[170:171], v214 offset:63488
v_mfma_f32_16x16x32_bf16 v[120:123], v[184:187], a[212:215], v[120:123]
buffer_load_dwordx4 a[32:35], v232, s[52:55], s96 offen offset:0 sc1
v_exp_f32 v245, v245
v_mfma_f32_16x16x32_bf16 v[108:111], v[188:191], a[208:211], v[108:111]
ds_read_b64_tr_b16 v[172:173], v215 offset:59392
ds_read_b64_tr_b16 v[174:175], v215 offset:63488
v_mul_f32 v142, v142, v158
v_mfma_f32_16x16x32_bf16 v[124:127], v[188:191], a[212:215], v[124:127]
v_add_f32 v245, 1.0, v245
s_waitcnt vmcnt(26) lgkmcnt(8)
s_barrier
v_mfma_f32_16x16x32_bf16 v[64:67], v[192:195], a[216:219], v[64:67]
ds_read_b64_tr_b16 v[176:177], v208 offset:32768
ds_read_b64_tr_b16 v[178:179], v208 offset:36864
v_mfma_f32_16x16x32_bf16 v[80:83], v[192:195], a[220:223], v[80:83]
buffer_load_dwordx4 v224, s[48:51], s22 offen offset:0 lds
v_rcp_f32 v245, v245
v_mfma_f32_16x16x32_bf16 v[68:71], v[196:199], a[216:219], v[68:71]
ds_read_b64_tr_b16 v[180:181], v209 offset:32768
ds_read_b64_tr_b16 v[182:183], v209 offset:36864
v_mul_f32 v142, v142, v245
v_mfma_f32_16x16x32_bf16 v[84:87], v[196:199], a[220:223], v[84:87]
buffer_load_dwordx4 a[36:39], v233, s[52:55], s96 offen offset:0 sc1
v_mul_f32 v246, v143, v143
v_mfma_f32_16x16x32_bf16 v[72:75], v[200:203], a[216:219], v[72:75]
ds_read_b64_tr_b16 v[184:185], v210 offset:32768
ds_read_b64_tr_b16 v[186:187], v210 offset:36864
v_fmaak_f32 v246, v246, v223, 0xc0135761
v_mfma_f32_16x16x32_bf16 v[88:91], v[200:203], a[220:223], v[88:91]
v_mul_f32 v246, v246, v143
v_mul_f32 v143, v143, v159
v_mfma_f32_16x16x32_bf16 v[76:79], v[204:207], a[216:219], v[76:79]
ds_read_b64_tr_b16 v[188:189], v211 offset:32768
ds_read_b64_tr_b16 v[190:191], v211 offset:36864
v_mfma_f32_16x16x32_bf16 v[92:95], v[204:207], a[220:223], v[92:95]
v_min_u32 v152, -2, v152
s_waitcnt lgkmcnt(8)
v_mfma_f32_16x16x32_bf16 v[96:99], v[160:163], a[216:219], v[96:99]
ds_read_b64_tr_b16 v[192:193], v208 offset:34816
ds_read_b64_tr_b16 v[194:195], v208 offset:38912
v_mfma_f32_16x16x32_bf16 v[112:115], v[160:163], a[220:223], v[112:115]
buffer_load_dwordx4 v225, s[48:51], s22 offen offset:1024 lds
v_exp_f32 v246, v246
v_mfma_f32_16x16x32_bf16 v[100:103], v[164:167], a[216:219], v[100:103]
ds_read_b64_tr_b16 v[196:197], v209 offset:34816
ds_read_b64_tr_b16 v[198:199], v209 offset:38912
v_add_f32 v246, 1.0, v246
v_mfma_f32_16x16x32_bf16 v[116:119], v[164:167], a[220:223], v[116:119]
buffer_load_dwordx4 a[40:43], v232, s[52:55], s96 offen offset:1024 sc1
v_rcp_f32 v246, v246
v_mfma_f32_16x16x32_bf16 v[104:107], v[168:171], a[216:219], v[104:107]
ds_read_b64_tr_b16 v[200:201], v210 offset:34816
ds_read_b64_tr_b16 v[202:203], v210 offset:38912
v_mul_f32 v143, v143, v246
v_mfma_f32_16x16x32_bf16 v[120:123], v[168:171], a[220:223], v[120:123]
buffer_load_dwordx4 v226, s[48:51], s23 offen offset:2048 lds
v_cvt_pk_bf16_f32 v155, v142, v143
v_mfma_f32_16x16x32_bf16 v[108:111], v[172:175], a[216:219], v[108:111]
ds_read_b64_tr_b16 v[204:205], v211 offset:34816
ds_read_b64_tr_b16 v[206:207], v211 offset:38912
buffer_store_dwordx4 v[152:155], v221, s[52:55], s90 offen offset:0 sc1
v_mfma_f32_16x16x32_bf16 v[124:127], v[172:175], a[220:223], v[124:127]
s_waitcnt lgkmcnt(8)
v_mfma_f32_16x16x32_bf16 v[64:67], v[176:179], a[224:227], v[64:67]
ds_read_b64_tr_b16 v[160:161], v208 offset:40960
ds_read_b64_tr_b16 v[162:163], v208 offset:45056
v_mfma_f32_16x16x32_bf16 v[80:83], v[176:179], a[228:231], v[80:83]
buffer_load_dwordx4 a[44:47], v233, s[52:55], s96 offen offset:1024 sc1
v_mfma_f32_16x16x32_bf16 v[68:71], v[180:183], a[224:227], v[68:71]
ds_read_b64_tr_b16 v[164:165], v209 offset:40960
ds_read_b64_tr_b16 v[166:167], v209 offset:45056
v_mfma_f32_16x16x32_bf16 v[84:87], v[180:183], a[228:231], v[84:87]
buffer_load_dwordx4 a[48:51], v232, s[52:55], s97 offen offset:0 sc1
v_mfma_f32_16x16x32_bf16 v[72:75], v[184:187], a[224:227], v[72:75]
ds_read_b64_tr_b16 v[168:169], v210 offset:40960
ds_read_b64_tr_b16 v[170:171], v210 offset:45056
v_mfma_f32_16x16x32_bf16 v[88:91], v[184:187], a[228:231], v[88:91]
buffer_load_dwordx4 v227, s[48:51], s23 offen offset:3072 lds
v_mfma_f32_16x16x32_bf16 v[76:79], v[188:191], a[224:227], v[76:79]
ds_read_b64_tr_b16 v[172:173], v211 offset:40960
ds_read_b64_tr_b16 v[174:175], v211 offset:45056
v_mfma_f32_16x16x32_bf16 v[92:95], v[188:191], a[228:231], v[92:95]
s_add_u32 s85, s77, 131072
s_waitcnt lgkmcnt(8)
v_mfma_f32_16x16x32_bf16 v[96:99], v[192:195], a[224:227], v[96:99]
ds_read_b64_tr_b16 v[176:177], v208 offset:43008
ds_read_b64_tr_b16 v[178:179], v208 offset:47104
v_mfma_f32_16x16x32_bf16 v[112:115], v[192:195], a[228:231], v[112:115]
buffer_load_dwordx4 a[52:55], v233, s[52:55], s97 offen offset:0 sc1
v_mfma_f32_16x16x32_bf16 v[100:103], v[196:199], a[224:227], v[100:103]
ds_read_b64_tr_b16 v[180:181], v209 offset:43008
ds_read_b64_tr_b16 v[182:183], v209 offset:47104
v_mfma_f32_16x16x32_bf16 v[116:119], v[196:199], a[228:231], v[116:119]
buffer_load_dwordx4 a[56:59], v232, s[52:55], s97 offen offset:1024 sc1
v_mfma_f32_16x16x32_bf16 v[104:107], v[200:203], a[224:227], v[104:107]
ds_read_b64_tr_b16 v[184:185], v210 offset:43008
ds_read_b64_tr_b16 v[186:187], v210 offset:47104
v_mfma_f32_16x16x32_bf16 v[120:123], v[200:203], a[228:231], v[120:123]
v_add_u32 v239, s85, v230
v_lshrrev_b32 v240, 4, v231
v_mfma_f32_16x16x32_bf16 v[108:111], v[204:207], a[224:227], v[108:111]
ds_read_b64_tr_b16 v[188:189], v211 offset:43008
LC21_%=:
s_cbranch_execz LRETB_%=
ds_read_b64_tr_b16 v[190:191], v211 offset:47104
v_mfma_f32_16x16x32_bf16 v[124:127], v[204:207], a[228:231], v[124:127]
s_waitcnt vmcnt(29) lgkmcnt(8)
s_barrier
v_mfma_f32_16x16x32_bf16 v[64:67], v[160:163], a[232:235], v[64:67]
ds_read_b64_tr_b16 v[192:193], v212 offset:0
ds_read_b64_tr_b16 v[194:195], v212 offset:4096
v_mfma_f32_16x16x32_bf16 v[80:83], v[160:163], a[236:239], v[80:83]
buffer_load_dwordx4 a[60:63], v233, s[52:55], s97 offen offset:1024 sc1
v_mfma_f32_16x16x32_bf16 v[68:71], v[164:167], a[232:235], v[68:71]
ds_read_b64_tr_b16 v[196:197], v213 offset:0
ds_read_b64_tr_b16 v[198:199], v213 offset:4096
v_mfma_f32_16x16x32_bf16 v[84:87], v[164:167], a[236:239], v[84:87]
ds_read_b128 v[32:35], v239 offset:0
ds_read_b128 v[36:39], v239 offset:1024
v_mfma_f32_16x16x32_bf16 v[72:75], v[168:171], a[232:235], v[72:75]
ds_read_b64_tr_b16 v[200:201], v214 offset:0
ds_read_b64_tr_b16 v[202:203], v214 offset:4096
v_mfma_f32_16x16x32_bf16 v[88:91], v[168:171], a[236:239], v[88:91]
buffer_load_dwordx4 a[64:67], v232, s[52:55], s98 offen offset:0 sc1
v_mfma_f32_16x16x32_bf16 v[76:79], v[172:175], a[232:235], v[76:79]
ds_read_b64_tr_b16 v[204:205], v215 offset:0
ds_read_b64_tr_b16 v[206:207], v215 offset:4096
v_mfma_f32_16x16x32_bf16 v[92:95], v[172:175], a[236:239], v[92:95]
ds_read_b128 v[40:43], v239 offset:2048
s_waitcnt lgkmcnt(11)
v_mfma_f32_16x16x32_bf16 v[96:99], v[176:179], a[232:235], v[96:99]
ds_read_b64_tr_b16 v[160:161], v212 offset:2048
ds_read_b64_tr_b16 v[162:163], v212 offset:6144
v_mfma_f32_16x16x32_bf16 v[112:115], v[176:179], a[236:239], v[112:115]
buffer_load_dwordx4 a[68:71], v233, s[52:55], s98 offen offset:0 sc1
v_mfma_f32_16x16x32_bf16 v[100:103], v[180:183], a[232:235], v[100:103]
ds_read_b64_tr_b16 v[164:165], v213 offset:2048
ds_read_b64_tr_b16 v[166:167], v213 offset:6144
v_mfma_f32_16x16x32_bf16 v[116:119], v[180:183], a[236:239], v[116:119]
ds_read_b128 v[44:47], v239 offset:3072
v_lshlrev_b32 v240, 4, v240
v_mfma_f32_16x16x32_bf16 v[104:107], v[184:187], a[232:235], v[104:107]
ds_read_b64_tr_b16 v[168:169], v214 offset:2048
ds_read_b64_tr_b16 v[170:171], v214 offset:6144
v_mfma_f32_16x16x32_bf16 v[120:123], v[184:187], a[236:239], v[120:123]
v_add_u32 v240, 151296, v240
s_lshl_b32 s85, s73, 3
v_mfma_f32_16x16x32_bf16 v[108:111], v[188:191], a[232:235], v[108:111]
ds_read_b64_tr_b16 v[172:173], v215 offset:2048
ds_read_b64_tr_b16 v[174:175], v215 offset:6144
v_mfma_f32_16x16x32_bf16 v[124:127], v[188:191], a[236:239], v[124:127]
ds_read_b128 v[48:51], v240 offset:0
s_waitcnt lgkmcnt(11)
v_mfma_f32_16x16x32_bf16 v[64:67], v[192:195], a[240:243], v[64:67]
ds_read_b64_tr_b16 v[176:177], v212 offset:8192
ds_read_b64_tr_b16 v[178:179], v212 offset:12288
v_mfma_f32_16x16x32_bf16 v[80:83], v[192:195], a[244:247], v[80:83]
buffer_load_dwordx4 a[72:75], v232, s[52:55], s98 offen offset:1024 sc1
v_mfma_f32_16x16x32_bf16 v[68:71], v[196:199], a[240:243], v[68:71]
ds_read_b64_tr_b16 v[180:181], v213 offset:8192
ds_read_b64_tr_b16 v[182:183], v213 offset:12288
v_mfma_f32_16x16x32_bf16 v[84:87], v[196:199], a[244:247], v[84:87]
buffer_load_dwordx4 a[76:79], v233, s[52:55], s98 offen offset:1024 sc1
v_mfma_f32_16x16x32_bf16 v[72:75], v[200:203], a[240:243], v[72:75]
ds_read_b64_tr_b16 v[184:185], v214 offset:8192
ds_read_b64_tr_b16 v[186:187], v214 offset:12288
v_mfma_f32_16x16x32_bf16 v[88:91], v[200:203], a[244:247], v[88:91]
ds_read_b128 v[52:55], v240 offset:64
ds_read_b128 v[56:59], v240 offset:128
v_mfma_f32_16x16x32_bf16 v[76:79], v[204:207], a[240:243], v[76:79]
ds_read_b64_tr_b16 v[188:189], v215 offset:8192
ds_read_b64_tr_b16 v[190:191], v215 offset:12288
v_mfma_f32_16x16x32_bf16 v[92:95], v[204:207], a[244:247], v[92:95]
s_add_u32 m0, s77, 16384
s_waitcnt lgkmcnt(11)
v_mfma_f32_16x16x32_bf16 v[96:99], v[160:163], a[240:243], v[96:99]
ds_read_b64_tr_b16 v[192:193], v212 offset:10240
ds_read_b64_tr_b16 v[194:195], v212 offset:14336
v_mfma_f32_16x16x32_bf16 v[112:115], v[160:163], a[244:247], v[112:115]
buffer_load_dwordx4 a[80:83], v232, s[52:55], s99 offen offset:0 sc1
v_mfma_f32_16x16x32_bf16 v[100:103], v[164:167], a[240:243], v[100:103]
ds_read_b64_tr_b16 v[196:197], v213 offset:10240
ds_read_b64_tr_b16 v[198:199], v213 offset:14336
v_mfma_f32_16x16x32_bf16 v[116:119], v[164:167], a[244:247], v[116:119]
ds_read_b128 v[60:63], v240 offset:192
s_add_u32 s85, s85, s72
v_mfma_f32_16x16x32_bf16 v[104:107], v[168:171], a[240:243], v[104:107]
ds_read_b64_tr_b16 v[200:201], v214 offset:10240
ds_read_b64_tr_b16 v[202:203], v214 offset:14336
v_mfma_f32_16x16x32_bf16 v[120:123], v[168:171], a[244:247], v[120:123]
buffer_load_dwordx4 a[84:87], v233, s[52:55], s99 offen offset:0 sc1
v_mfma_f32_16x16x32_bf16 v[108:111], v[172:175], a[240:243], v[108:111]
ds_read_b64_tr_b16 v[204:205], v215 offset:10240
ds_read_b64_tr_b16 v[206:207], v215 offset:14336
v_mfma_f32_16x16x32_bf16 v[124:127], v[172:175], a[244:247], v[124:127]
s_waitcnt vmcnt(23) lgkmcnt(9)
s_barrier
v_mfma_f32_16x16x32_bf16 v[64:67], v[176:179], a[248:251], v[64:67]
ds_read_b64_tr_b16 v[160:161], v208 offset:49152
ds_read_b64_tr_b16 v[162:163], v208 offset:53248
v_mfma_f32_16x16x32_bf16 v[80:83], v[176:179], a[252:255], v[80:83]
v_accvgpr_read_b32 v248, a0
v_accvgpr_read_b32 v249, a4
v_mfma_f32_16x16x32_bf16 v[68:71], v[180:183], a[248:251], v[68:71]
ds_read_b64_tr_b16 v[164:165], v209 offset:49152
ds_read_b64_tr_b16 v[166:167], v209 offset:53248
v_mfma_f32_16x16x32_bf16 v[84:87], v[180:183], a[252:255], v[84:87]
v_accvgpr_read_b32 v250, a8
v_max3_u32 v248, v248, v249, v250
v_mfma_f32_16x16x32_bf16 v[72:75], v[184:187], a[248:251], v[72:75]
ds_read_b64_tr_b16 v[168:169], v210 offset:49152
ds_read_b64_tr_b16 v[170:171], v210 offset:53248
v_mfma_f32_16x16x32_bf16 v[88:91], v[184:187], a[252:255], v[88:91]
v_accvgpr_read_b32 v251, a12
v_max_u32 v248, v248, v251
v_mfma_f32_16x16x32_bf16 v[76:79], v[188:191], a[248:251], v[76:79]
ds_read_b64_tr_b16 v[172:173], v211 offset:49152
ds_read_b64_tr_b16 v[174:175], v211 offset:53248
v_mfma_f32_16x16x32_bf16 v[92:95], v[188:191], a[252:255], v[92:95]
buffer_load_dwordx4 a[88:91], v232, s[52:55], s99 offen offset:1024 sc1
s_waitcnt lgkmcnt(8)
v_mfma_f32_16x16x32_bf16 v[96:99], v[192:195], a[248:251], v[96:99]
ds_read_b64_tr_b16 v[176:177], v208 offset:51200
ds_read_b64_tr_b16 v[178:179], v208 offset:55296
v_mfma_f32_16x16x32_bf16 v[112:115], v[192:195], a[252:255], v[112:115]
v_cmp_eq_u32 vcc, -1, v248
s_nop 0
s_cbranch_vccnz LSt4k16_%=
LRt4k16_%=:
v_mfma_f32_16x16x32_bf16 v[100:103], v[196:199], a[248:251], v[100:103]
ds_read_b64_tr_b16 v[180:181], v209 offset:51200
ds_read_b64_tr_b16 v[182:183], v209 offset:55296
v_mfma_f32_16x16x32_bf16 v[116:119], v[196:199], a[252:255], v[116:119]
buffer_load_dwordx4 a[92:95], v233, s[52:55], s99 offen offset:1024 sc1
v_mfma_f32_16x16x32_bf16 v[104:107], v[200:203], a[248:251], v[104:107]
ds_read_b64_tr_b16 v[184:185], v210 offset:51200
ds_read_b64_tr_b16 v[186:187], v210 offset:55296
v_mfma_f32_16x16x32_bf16 v[120:123], v[200:203], a[252:255], v[120:123]
buffer_load_dwordx4 v224, s[48:51], s24 offen offset:0 lds
v_mfma_f32_16x16x32_bf16 v[108:111], v[204:207], a[248:251], v[108:111]
ds_read_b64_tr_b16 v[188:189], v211 offset:51200
ds_read_b64_tr_b16 v[190:191], v211 offset:55296
v_mfma_f32_16x16x32_bf16 v[124:127], v[204:207], a[252:255], v[124:127]
s_lshl_b32 s85, s85, 7
s_waitcnt lgkmcnt(8)
v_mfma_f32_16x16x32_bf16 v[64:67], v[160:163], a[0:3], v[64:67]
ds_read_b64_tr_b16 v[192:193], v208 offset:57344
ds_read_b64_tr_b16 v[194:195], v208 offset:61440
v_mfma_f32_16x16x32_bf16 v[80:83], v[160:163], a[4:7], v[80:83]
buffer_load_dwordx4 a[96:99], v232, s[52:55], s100 offen offset:0 sc1
v_mfma_f32_16x16x32_bf16 v[68:71], v[164:167], a[0:3], v[68:71]
ds_read_b64_tr_b16 v[196:197], v209 offset:57344
ds_read_b64_tr_b16 v[198:199], v209 offset:61440
v_mfma_f32_16x16x32_bf16 v[84:87], v[164:167], a[4:7], v[84:87]
buffer_load_dwordx4 v225, s[48:51], s24 offen offset:1024 lds
v_mfma_f32_16x16x32_bf16 v[72:75], v[168:171], a[0:3], v[72:75]
ds_read_b64_tr_b16 v[200:201], v210 offset:57344
ds_read_b64_tr_b16 v[202:203], v210 offset:61440
v_mfma_f32_16x16x32_bf16 v[88:91], v[168:171], a[4:7], v[88:91]
buffer_load_dwordx4 a[100:103], v233, s[52:55], s100 offen offset:0 sc1
v_mfma_f32_16x16x32_bf16 v[76:79], v[172:175], a[0:3], v[76:79]
ds_read_b64_tr_b16 v[204:205], v211 offset:57344
ds_read_b64_tr_b16 v[206:207], v211 offset:61440
v_mfma_f32_16x16x32_bf16 v[92:95], v[172:175], a[4:7], v[92:95]
s_add_u32 s85, s85, s76
s_waitcnt lgkmcnt(8)
v_mfma_f32_16x16x32_bf16 v[96:99], v[176:179], a[0:3], v[96:99]
ds_read_b64_tr_b16 v[160:161], v208 offset:59392
ds_read_b64_tr_b16 v[162:163], v208 offset:63488
v_mfma_f32_16x16x32_bf16 v[112:115], v[176:179], a[4:7], v[112:115]
)"
R"(
buffer_load_dwordx4 v226, s[48:51], s25 offen offset:2048 lds
v_mfma_f32_16x16x32_bf16 v[100:103], v[180:183], a[0:3], v[100:103]
ds_read_b64_tr_b16 v[164:165], v209 offset:59392
ds_read_b64_tr_b16 v[166:167], v209 offset:63488
v_mfma_f32_16x16x32_bf16 v[116:119], v[180:183], a[4:7], v[116:119]
buffer_load_dwordx4 a[104:107], v232, s[52:55], s100 offen offset:1024 sc1
v_mfma_f32_16x16x32_bf16 v[104:107], v[184:187], a[0:3], v[104:107]
ds_read_b64_tr_b16 v[168:169], v210 offset:59392
ds_read_b64_tr_b16 v[170:171], v210 offset:63488
buffer_load_dwordx4 a[108:111], v233, s[52:55], s100 offen offset:1024 sc1
v_mfma_f32_16x16x32_bf16 v[120:123], v[184:187], a[4:7], v[120:123]
buffer_load_dwordx4 v227, s[48:51], s25 offen offset:3072 lds
v_mfma_f32_16x16x32_bf16 v[108:111], v[188:191], a[0:3], v[108:111]
ds_read_b64_tr_b16 v[172:173], v211 offset:59392
ds_read_b64_tr_b16 v[174:175], v211 offset:63488
v_mfma_f32_16x16x32_bf16 v[124:127], v[188:191], a[4:7], v[124:127]
s_waitcnt vmcnt(29) lgkmcnt(8)
s_barrier
v_mfma_f32_16x16x32_bf16 v[64:67], v[192:195], a[8:11], v[64:67]
ds_read_b64_tr_b16 v[176:177], v212 offset:16384
ds_read_b64_tr_b16 v[178:179], v212 offset:20480
v_mfma_f32_16x16x32_bf16 v[80:83], v[192:195], a[12:15], v[80:83]
v_accvgpr_read_b32 v248, a16
v_accvgpr_read_b32 v249, a20
v_mfma_f32_16x16x32_bf16 v[68:71], v[196:199], a[8:11], v[68:71]
ds_read_b64_tr_b16 v[180:181], v213 offset:16384
ds_read_b64_tr_b16 v[182:183], v213 offset:20480
v_mfma_f32_16x16x32_bf16 v[84:87], v[196:199], a[12:15], v[84:87]
v_accvgpr_read_b32 v250, a24
v_max3_u32 v248, v248, v249, v250
v_mfma_f32_16x16x32_bf16 v[72:75], v[200:203], a[8:11], v[72:75]
ds_read_b64_tr_b16 v[184:185], v214 offset:16384
ds_read_b64_tr_b16 v[186:187], v214 offset:20480
v_mfma_f32_16x16x32_bf16 v[88:91], v[200:203], a[12:15], v[88:91]
v_accvgpr_read_b32 v251, a28
v_max_u32 v248, v248, v251
v_mfma_f32_16x16x32_bf16 v[76:79], v[204:207], a[8:11], v[76:79]
ds_read_b64_tr_b16 v[188:189], v215 offset:16384
ds_read_b64_tr_b16 v[190:191], v215 offset:20480
v_mfma_f32_16x16x32_bf16 v[92:95], v[204:207], a[12:15], v[92:95]
buffer_load_dwordx4 a[112:115], v232, s[52:55], s101 offen offset:0 sc1
s_waitcnt lgkmcnt(8)
v_mfma_f32_16x16x32_bf16 v[96:99], v[160:163], a[8:11], v[96:99]
ds_read_b64_tr_b16 v[192:193], v212 offset:18432
ds_read_b64_tr_b16 v[194:195], v212 offset:22528
v_mfma_f32_16x16x32_bf16 v[112:115], v[160:163], a[12:15], v[112:115]
v_cmp_eq_u32 vcc, -1, v248
s_nop 0
s_cbranch_vccnz LSt4k17_%=
LRt4k17_%=:
v_mfma_f32_16x16x32_bf16 v[100:103], v[164:167], a[8:11], v[100:103]
ds_read_b64_tr_b16 v[196:197], v213 offset:18432
ds_read_b64_tr_b16 v[198:199], v213 offset:22528
v_mfma_f32_16x16x32_bf16 v[116:119], v[164:167], a[12:15], v[116:119]
buffer_load_dwordx4 a[116:119], v233, s[52:55], s101 offen offset:0 sc1
v_mfma_f32_16x16x32_bf16 v[104:107], v[168:171], a[8:11], v[104:107]
ds_read_b64_tr_b16 v[200:201], v214 offset:18432
ds_read_b64_tr_b16 v[202:203], v214 offset:22528
s_add_u32 m0, s77, 114688
v_mfma_f32_16x16x32_bf16 v[120:123], v[168:171], a[12:15], v[120:123]
buffer_load_dwordx4 v224, s[48:51], s26 offen offset:0 lds
v_mfma_f32_16x16x32_bf16 v[108:111], v[172:175], a[8:11], v[108:111]
ds_read_b64_tr_b16 v[204:205], v215 offset:18432
ds_read_b64_tr_b16 v[206:207], v215 offset:22528
v_mfma_f32_16x16x32_bf16 v[124:127], v[172:175], a[12:15], v[124:127]
s_lshl_b32 s85, s85, 1
s_waitcnt lgkmcnt(8)
v_mfma_f32_16x16x32_bf16 v[64:67], v[176:179], a[16:19], v[64:67]
ds_read_b64_tr_b16 v[160:161], v212 offset:24576
ds_read_b64_tr_b16 v[162:163], v212 offset:28672
v_mfma_f32_16x16x32_bf16 v[80:83], v[176:179], a[20:23], v[80:83]
buffer_load_dwordx4 a[120:123], v232, s[52:55], s101 offen offset:1024 sc1
v_mfma_f32_16x16x32_bf16 v[68:71], v[180:183], a[16:19], v[68:71]
ds_read_b64_tr_b16 v[164:165], v213 offset:24576
ds_read_b64_tr_b16 v[166:167], v213 offset:28672
v_mfma_f32_16x16x32_bf16 v[84:87], v[180:183], a[20:23], v[84:87]
buffer_load_dwordx4 v225, s[48:51], s26 offen offset:1024 lds
v_mfma_f32_16x16x32_bf16 v[72:75], v[184:187], a[16:19], v[72:75]
ds_read_b64_tr_b16 v[168:169], v214 offset:24576
ds_read_b64_tr_b16 v[170:171], v214 offset:28672
v_mfma_f32_16x16x32_bf16 v[88:91], v[184:187], a[20:23], v[88:91]
buffer_load_dwordx4 a[124:127], v233, s[52:55], s101 offen offset:1024 sc1
v_mfma_f32_16x16x32_bf16 v[76:79], v[188:191], a[16:19], v[76:79]
ds_read_b64_tr_b16 v[172:173], v215 offset:24576
ds_read_b64_tr_b16 v[174:175], v215 offset:28672
v_mfma_f32_16x16x32_bf16 v[92:95], v[188:191], a[20:23], v[92:95]
v_add_u32 v232, s31, v232
s_waitcnt lgkmcnt(8)
v_mfma_f32_16x16x32_bf16 v[96:99], v[192:195], a[16:19], v[96:99]
ds_read_b64_tr_b16 v[176:177], v212 offset:26624
ds_read_b64_tr_b16 v[178:179], v212 offset:30720
v_mfma_f32_16x16x32_bf16 v[112:115], v[192:195], a[20:23], v[112:115]
buffer_load_dwordx4 v226, s[48:51], s27 offen offset:2048 lds
v_mfma_f32_16x16x32_bf16 v[100:103], v[196:199], a[16:19], v[100:103]
ds_read_b64_tr_b16 v[180:181], v213 offset:26624
ds_read_b64_tr_b16 v[182:183], v213 offset:30720
v_add_u32 v233, s31, v233
v_mfma_f32_16x16x32_bf16 v[116:119], v[196:199], a[20:23], v[116:119]
buffer_load_dwordx4 a[128:131], v232, s[52:55], s94 offen offset:0 sc1
v_mfma_f32_16x16x32_bf16 v[104:107], v[200:203], a[16:19], v[104:107]
ds_read_b64_tr_b16 v[184:185], v214 offset:26624
ds_read_b64_tr_b16 v[186:187], v214 offset:30720
buffer_load_dwordx4 a[132:135], v233, s[52:55], s94 offen offset:0 sc1
v_mfma_f32_16x16x32_bf16 v[120:123], v[200:203], a[20:23], v[120:123]
buffer_load_dwordx4 v227, s[48:51], s27 offen offset:3072 lds
buffer_load_dwordx4 a[136:139], v232, s[52:55], s94 offen offset:1024 sc1
v_mfma_f32_16x16x32_bf16 v[108:111], v[204:207], a[16:19], v[108:111]
ds_read_b64_tr_b16 v[188:189], v215 offset:26624
ds_read_b64_tr_b16 v[190:191], v215 offset:30720
v_mfma_f32_16x16x32_bf16 v[124:127], v[204:207], a[20:23], v[124:127]
s_waitcnt vmcnt(27) lgkmcnt(8)
s_barrier
v_mfma_f32_16x16x32_bf16 v[64:67], v[160:163], a[24:27], v[64:67]
ds_read_b64_tr_b16 v[192:193], v208 offset:0
ds_read_b64_tr_b16 v[194:195], v208 offset:4096
buffer_load_dwordx4 a[140:143], v233, s[52:55], s94 offen offset:1024 sc1
v_mfma_f32_16x16x32_bf16 v[80:83], v[160:163], a[28:31], v[80:83]
v_accvgpr_read_b32 v248, a32
v_accvgpr_read_b32 v249, a36
v_mfma_f32_16x16x32_bf16 v[68:71], v[164:167], a[24:27], v[68:71]
ds_read_b64_tr_b16 v[196:197], v209 offset:0
ds_read_b64_tr_b16 v[198:199], v209 offset:4096
v_mfma_f32_16x16x32_bf16 v[84:87], v[164:167], a[28:31], v[84:87]
v_accvgpr_read_b32 v250, a40
v_max3_u32 v248, v248, v249, v250
v_mfma_f32_16x16x32_bf16 v[72:75], v[168:171], a[24:27], v[72:75]
ds_read_b64_tr_b16 v[200:201], v210 offset:0
ds_read_b64_tr_b16 v[202:203], v210 offset:4096
buffer_load_dwordx4 a[144:147], v232, s[52:55], s96 offen offset:0 sc1
v_mfma_f32_16x16x32_bf16 v[88:91], v[168:171], a[28:31], v[88:91]
v_accvgpr_read_b32 v251, a44
v_accvgpr_read_b32 v252, a48
v_mfma_f32_16x16x32_bf16 v[76:79], v[172:175], a[24:27], v[76:79]
ds_read_b64_tr_b16 v[204:205], v211 offset:0
ds_read_b64_tr_b16 v[206:207], v211 offset:4096
v_mfma_f32_16x16x32_bf16 v[92:95], v[172:175], a[28:31], v[92:95]
v_max3_u32 v248, v248, v251, v252
s_waitcnt lgkmcnt(8)
v_mfma_f32_16x16x32_bf16 v[96:99], v[176:179], a[24:27], v[96:99]
ds_read_b64_tr_b16 v[160:161], v208 offset:8192
ds_read_b64_tr_b16 v[162:163], v208 offset:12288
v_accvgpr_read_b32 v249, a52
v_mfma_f32_16x16x32_bf16 v[112:115], v[176:179], a[28:31], v[112:115]
v_accvgpr_read_b32 v250, a56
v_max3_u32 v248, v248, v249, v250
v_mfma_f32_16x16x32_bf16 v[100:103], v[180:183], a[24:27], v[100:103]
ds_read_b64_tr_b16 v[164:165], v209 offset:8192
ds_read_b64_tr_b16 v[166:167], v209 offset:12288
v_mfma_f32_16x16x32_bf16 v[116:119], v[180:183], a[28:31], v[116:119]
v_accvgpr_read_b32 v251, a60
v_max_u32 v248, v248, v251
v_mfma_f32_16x16x32_bf16 v[104:107], v[184:187], a[24:27], v[104:107]
ds_read_b64_tr_b16 v[168:169], v210 offset:8192
ds_read_b64_tr_b16 v[170:171], v210 offset:12288
v_mfma_f32_16x16x32_bf16 v[120:123], v[184:187], a[28:31], v[120:123]
v_cmp_eq_u32 vcc, -1, v248
s_nop 0
s_cbranch_vccnz LSt4k18_%=
LRt4k18_%=:
v_mfma_f32_16x16x32_bf16 v[108:111], v[188:191], a[24:27], v[108:111]
ds_read_b64_tr_b16 v[172:173], v211 offset:8192
ds_read_b64_tr_b16 v[174:175], v211 offset:12288
v_mfma_f32_16x16x32_bf16 v[124:127], v[188:191], a[28:31], v[124:127]
buffer_load_dwordx4 a[148:151], v233, s[52:55], s96 offen offset:0 sc1
s_waitcnt lgkmcnt(8)
v_mfma_f32_16x16x32_bf16 v[64:67], v[192:195], a[32:35], v[64:67]
ds_read_b64_tr_b16 v[176:177], v208 offset:2048
ds_read_b64_tr_b16 v[178:179], v208 offset:6144
v_mfma_f32_16x16x32_bf16 v[80:83], v[192:195], a[36:39], v[80:83]
buffer_load_dwordx4 a[152:155], v232, s[52:55], s96 offen offset:1024 sc1
v_mfma_f32_16x16x32_bf16 v[68:71], v[196:199], a[32:35], v[68:71]
ds_read_b64_tr_b16 v[180:181], v209 offset:2048
ds_read_b64_tr_b16 v[182:183], v209 offset:6144
v_mfma_f32_16x16x32_bf16 v[84:87], v[196:199], a[36:39], v[84:87]
buffer_load_dwordx4 a[156:159], v233, s[52:55], s96 offen offset:1024 sc1
v_mfma_f32_16x16x32_bf16 v[72:75], v[200:203], a[32:35], v[72:75]
ds_read_b64_tr_b16 v[184:185], v210 offset:2048
ds_read_b64_tr_b16 v[186:187], v210 offset:6144
v_mfma_f32_16x16x32_bf16 v[88:91], v[200:203], a[36:39], v[88:91]
buffer_load_dwordx4 a[160:163], v232, s[52:55], s98 offen offset:0 sc1
v_mfma_f32_16x16x32_bf16 v[76:79], v[204:207], a[32:35], v[76:79]
ds_read_b64_tr_b16 v[188:189], v211 offset:2048
ds_read_b64_tr_b16 v[190:191], v211 offset:6144
v_mfma_f32_16x16x32_bf16 v[92:95], v[204:207], a[36:39], v[92:95]
s_add_u32 m0, s77, 32768
s_waitcnt lgkmcnt(8)
v_mfma_f32_16x16x32_bf16 v[64:67], v[160:163], a[40:43], v[64:67]
ds_read_b64_tr_b16 v[192:193], v208 offset:10240
ds_read_b64_tr_b16 v[194:195], v208 offset:14336
v_mfma_f32_16x16x32_bf16 v[80:83], v[160:163], a[44:47], v[80:83]
buffer_load_dwordx4 a[164:167], v233, s[52:55], s98 offen offset:0 sc1
v_mfma_f32_16x16x32_bf16 v[68:71], v[164:167], a[40:43], v[68:71]
ds_read_b64_tr_b16 v[196:197], v209 offset:10240
ds_read_b64_tr_b16 v[198:199], v209 offset:14336
v_mfma_f32_16x16x32_bf16 v[84:87], v[164:167], a[44:47], v[84:87]
s_add_u32 s85, s85, s75
s_lshl_b32 s85, s85, 7
v_mfma_f32_16x16x32_bf16 v[72:75], v[168:171], a[40:43], v[72:75]
ds_read_b64_tr_b16 v[200:201], v210 offset:10240
ds_read_b64_tr_b16 v[202:203], v210 offset:14336
v_mfma_f32_16x16x32_bf16 v[88:91], v[168:171], a[44:47], v[88:91]
buffer_load_dwordx4 a[168:171], v232, s[52:55], s98 offen offset:1024 sc1
v_mfma_f32_16x16x32_bf16 v[76:79], v[172:175], a[40:43], v[76:79]
ds_read_b64_tr_b16 v[204:205], v211 offset:10240
ds_read_b64_tr_b16 v[206:207], v211 offset:14336
v_mfma_f32_16x16x32_bf16 v[92:95], v[172:175], a[44:47], v[92:95]
s_waitcnt vmcnt(27) lgkmcnt(8)
s_barrier
v_mfma_f32_16x16x32_bf16 v[64:67], v[176:179], a[48:51], v[64:67]
ds_read_b64_tr_b16 v[160:161], v212 offset:32768
ds_read_b64_tr_b16 v[162:163], v212 offset:36864
buffer_load_dwordx4 a[172:175], v233, s[52:55], s98 offen offset:1024 sc1
v_mfma_f32_16x16x32_bf16 v[80:83], v[176:179], a[52:55], v[80:83]
v_accvgpr_read_b32 v248, a64
v_accvgpr_read_b32 v249, a68
v_mfma_f32_16x16x32_bf16 v[68:71], v[180:183], a[48:51], v[68:71]
ds_read_b64_tr_b16 v[164:165], v213 offset:32768
ds_read_b64_tr_b16 v[166:167], v213 offset:36864
v_mfma_f32_16x16x32_bf16 v[84:87], v[180:183], a[52:55], v[84:87]
v_accvgpr_read_b32 v250, a72
v_max3_u32 v248, v248, v249, v250
v_mfma_f32_16x16x32_bf16 v[72:75], v[184:187], a[48:51], v[72:75]
ds_read_b64_tr_b16 v[168:169], v214 offset:32768
ds_read_b64_tr_b16 v[170:171], v214 offset:36864
buffer_load_dwordx4 a[176:179], v232, s[52:55], s100 offen offset:0 sc1
v_mfma_f32_16x16x32_bf16 v[88:91], v[184:187], a[52:55], v[88:91]
v_accvgpr_read_b32 v251, a76
v_accvgpr_read_b32 v252, a80
v_mfma_f32_16x16x32_bf16 v[76:79], v[188:191], a[48:51], v[76:79]
ds_read_b64_tr_b16 v[172:173], v215 offset:32768
ds_read_b64_tr_b16 v[174:175], v215 offset:36864
v_mfma_f32_16x16x32_bf16 v[92:95], v[188:191], a[52:55], v[92:95]
v_max3_u32 v248, v248, v251, v252
s_waitcnt lgkmcnt(8)
v_mfma_f32_16x16x32_bf16 v[64:67], v[192:195], a[56:59], v[64:67]
ds_read_b64_tr_b16 v[176:177], v212 offset:40960
ds_read_b64_tr_b16 v[178:179], v212 offset:45056
v_accvgpr_read_b32 v249, a84
v_mfma_f32_16x16x32_bf16 v[80:83], v[192:195], a[60:63], v[80:83]
v_accvgpr_read_b32 v250, a88
v_max3_u32 v248, v248, v249, v250
v_mfma_f32_16x16x32_bf16 v[68:71], v[196:199], a[56:59], v[68:71]
ds_read_b64_tr_b16 v[180:181], v213 offset:40960
ds_read_b64_tr_b16 v[182:183], v213 offset:45056
v_mfma_f32_16x16x32_bf16 v[84:87], v[196:199], a[60:63], v[84:87]
v_accvgpr_read_b32 v251, a92
v_max_u32 v248, v248, v251
v_mfma_f32_16x16x32_bf16 v[72:75], v[200:203], a[56:59], v[72:75]
ds_read_b64_tr_b16 v[184:185], v214 offset:40960
ds_read_b64_tr_b16 v[186:187], v214 offset:45056
v_mfma_f32_16x16x32_bf16 v[88:91], v[200:203], a[60:63], v[88:91]
v_cmp_eq_u32 vcc, -1, v248
s_nop 0
s_cbranch_vccnz LSt4k20_%=
LRt4k20_%=:
v_mfma_f32_16x16x32_bf16 v[76:79], v[204:207], a[56:59], v[76:79]
ds_read_b64_tr_b16 v[188:189], v215 offset:40960
ds_read_b64_tr_b16 v[190:191], v215 offset:45056
v_mfma_f32_16x16x32_bf16 v[92:95], v[204:207], a[60:63], v[92:95]
buffer_load_dwordx4 a[180:183], v233, s[52:55], s100 offen offset:0 sc1
s_waitcnt lgkmcnt(8)
v_mfma_f32_16x16x32_bf16 v[64:67], v[160:163], a[64:67], v[64:67]
ds_read_b64_tr_b16 v[192:193], v212 offset:34816
ds_read_b64_tr_b16 v[194:195], v212 offset:38912
v_mfma_f32_16x16x32_bf16 v[80:83], v[160:163], a[68:71], v[80:83]
buffer_load_dwordx4 v224, s[48:51], s28 offen offset:0 lds
v_mfma_f32_16x16x32_bf16 v[68:71], v[164:167], a[64:67], v[68:71]
ds_read_b64_tr_b16 v[196:197], v213 offset:34816
ds_read_b64_tr_b16 v[198:199], v213 offset:38912
v_mfma_f32_16x16x32_bf16 v[84:87], v[164:167], a[68:71], v[84:87]
buffer_load_dwordx4 a[184:187], v232, s[52:55], s100 offen offset:1024 sc1
v_mfma_f32_16x16x32_bf16 v[72:75], v[168:171], a[64:67], v[72:75]
ds_read_b64_tr_b16 v[200:201], v214 offset:34816
ds_read_b64_tr_b16 v[202:203], v214 offset:38912
v_mfma_f32_16x16x32_bf16 v[88:91], v[168:171], a[68:71], v[88:91]
buffer_load_dwordx4 v225, s[48:51], s28 offen offset:1024 lds
v_mfma_f32_16x16x32_bf16 v[76:79], v[172:175], a[64:67], v[76:79]
ds_read_b64_tr_b16 v[204:205], v215 offset:34816
ds_read_b64_tr_b16 v[206:207], v215 offset:38912
v_mfma_f32_16x16x32_bf16 v[92:95], v[172:175], a[68:71], v[92:95]
v_add_u32 v224, s30, v224
s_waitcnt lgkmcnt(8)
v_mfma_f32_16x16x32_bf16 v[64:67], v[176:179], a[72:75], v[64:67]
ds_read_b64_tr_b16 v[160:161], v212 offset:43008
ds_read_b64_tr_b16 v[162:163], v212 offset:47104
v_mfma_f32_16x16x32_bf16 v[80:83], v[176:179], a[76:79], v[80:83]
buffer_load_dwordx4 v226, s[48:51], s29 offen offset:2048 lds
v_mfma_f32_16x16x32_bf16 v[68:71], v[180:183], a[72:75], v[68:71]
ds_read_b64_tr_b16 v[164:165], v213 offset:43008
ds_read_b64_tr_b16 v[166:167], v213 offset:47104
v_mfma_f32_16x16x32_bf16 v[84:87], v[180:183], a[76:79], v[84:87]
buffer_load_dwordx4 a[188:191], v233, s[52:55], s100 offen offset:1024 sc1
v_mfma_f32_16x16x32_bf16 v[72:75], v[184:187], a[72:75], v[72:75]
ds_read_b64_tr_b16 v[168:169], v214 offset:43008
ds_read_b64_tr_b16 v[170:171], v214 offset:47104
v_mfma_f32_16x16x32_bf16 v[88:91], v[184:187], a[76:79], v[88:91]
buffer_load_dwordx4 v227, s[48:51], s29 offen offset:3072 lds
v_mfma_f32_16x16x32_bf16 v[76:79], v[188:191], a[72:75], v[76:79]
ds_read_b64_tr_b16 v[172:173], v215 offset:43008
ds_read_b64_tr_b16 v[174:175], v215 offset:47104
v_mfma_f32_16x16x32_bf16 v[92:95], v[188:191], a[76:79], v[92:95]
s_waitcnt vmcnt(22) lgkmcnt(8)
s_barrier
v_mfma_f32_16x16x32_bf16 v[64:67], v[192:195], a[80:83], v[64:67]
ds_read_b64_tr_b16 v[176:177], v208 offset:16384
ds_read_b64_tr_b16 v[178:179], v208 offset:20480
v_add_u32 v225, s30, v225
s_add_u32 m0, s77, 65536
v_mfma_f32_16x16x32_bf16 v[80:83], v[192:195], a[84:87], v[80:83]
v_accvgpr_read_b32 v248, a96
v_accvgpr_read_b32 v249, a100
buffer_load_dwordx4 v224, s[48:51], s20 offen offset:0 lds
v_mfma_f32_16x16x32_bf16 v[68:71], v[196:199], a[80:83], v[68:71]
ds_read_b64_tr_b16 v[180:181], v209 offset:16384
ds_read_b64_tr_b16 v[182:183], v209 offset:20480
v_mfma_f32_16x16x32_bf16 v[84:87], v[196:199], a[84:87], v[84:87]
v_accvgpr_read_b32 v250, a104
v_max3_u32 v248, v248, v249, v250
v_mfma_f32_16x16x32_bf16 v[72:75], v[200:203], a[80:83], v[72:75]
ds_read_b64_tr_b16 v[184:185], v210 offset:16384
ds_read_b64_tr_b16 v[186:187], v210 offset:20480
v_mfma_f32_16x16x32_bf16 v[88:91], v[200:203], a[84:87], v[88:91]
v_accvgpr_read_b32 v251, a108
v_accvgpr_read_b32 v252, a112
buffer_load_dwordx4 v225, s[48:51], s20 offen offset:1024 lds
v_mfma_f32_16x16x32_bf16 v[76:79], v[204:207], a[80:83], v[76:79]
ds_read_b64_tr_b16 v[188:189], v211 offset:16384
ds_read_b64_tr_b16 v[190:191], v211 offset:20480
v_mfma_f32_16x16x32_bf16 v[92:95], v[204:207], a[84:87], v[92:95]
v_max3_u32 v248, v248, v251, v252
s_waitcnt lgkmcnt(8)
v_mfma_f32_16x16x32_bf16 v[64:67], v[160:163], a[88:91], v[64:67]
ds_read_b64_tr_b16 v[192:193], v208 offset:24576
ds_read_b64_tr_b16 v[194:195], v208 offset:28672
v_accvgpr_read_b32 v249, a116
v_add_u32 v226, s30, v226
v_add_u32 v227, s30, v227
v_mfma_f32_16x16x32_bf16 v[80:83], v[160:163], a[92:95], v[80:83]
v_accvgpr_read_b32 v250, a120
v_max3_u32 v248, v248, v249, v250
buffer_load_dwordx4 v226, s[48:51], s21 offen offset:2048 lds
v_mfma_f32_16x16x32_bf16 v[68:71], v[164:167], a[88:91], v[68:71]
ds_read_b64_tr_b16 v[196:197], v209 offset:24576
ds_read_b64_tr_b16 v[198:199], v209 offset:28672
v_mfma_f32_16x16x32_bf16 v[84:87], v[164:167], a[92:95], v[84:87]
v_accvgpr_read_b32 v251, a124
v_max_u32 v248, v248, v251
v_mfma_f32_16x16x32_bf16 v[72:75], v[168:171], a[88:91], v[72:75]
ds_read_b64_tr_b16 v[200:201], v210 offset:24576
ds_read_b64_tr_b16 v[202:203], v210 offset:28672
v_mfma_f32_16x16x32_bf16 v[88:91], v[168:171], a[92:95], v[88:91]
v_cmp_eq_u32 vcc, -1, v248
s_nop 0
s_cbranch_vccnz LSt4k22_%=
LRt4k22_%=:
buffer_load_dwordx4 v227, s[48:51], s21 offen offset:3072 lds
v_mfma_f32_16x16x32_bf16 v[76:79], v[172:175], a[88:91], v[76:79]
ds_read_b64_tr_b16 v[204:205], v211 offset:24576
ds_read_b64_tr_b16 v[206:207], v211 offset:28672
v_mfma_f32_16x16x32_bf16 v[92:95], v[172:175], a[92:95], v[92:95]
s_add_u32 m0, s77, 49152
s_waitcnt lgkmcnt(8)
v_mfma_f32_16x16x32_bf16 v[64:67], v[176:179], a[96:99], v[64:67]
ds_read_b64_tr_b16 v[160:161], v208 offset:18432
ds_read_b64_tr_b16 v[162:163], v208 offset:22528
v_mfma_f32_16x16x32_bf16 v[80:83], v[176:179], a[100:103], v[80:83]
buffer_load_dwordx4 v224, s[48:51], s22 offen offset:0 lds
v_mfma_f32_16x16x32_bf16 v[68:71], v[180:183], a[96:99], v[68:71]
ds_read_b64_tr_b16 v[164:165], v209 offset:18432
ds_read_b64_tr_b16 v[166:167], v209 offset:22528
v_mfma_f32_16x16x32_bf16 v[84:87], v[180:183], a[100:103], v[84:87]
v_and_b32 v241, 15, v231
v_lshl_add_u32 v241, v241, 11, s85
v_mfma_f32_16x16x32_bf16 v[72:75], v[184:187], a[96:99], v[72:75]
ds_read_b64_tr_b16 v[168:169], v210 offset:18432
ds_read_b64_tr_b16 v[170:171], v210 offset:22528
v_mfma_f32_16x16x32_bf16 v[88:91], v[184:187], a[100:103], v[88:91]
buffer_load_dwordx4 v225, s[48:51], s22 offen offset:1024 lds
v_mfma_f32_16x16x32_bf16 v[76:79], v[188:191], a[96:99], v[76:79]
ds_read_b64_tr_b16 v[172:173], v211 offset:18432
ds_read_b64_tr_b16 v[174:175], v211 offset:22528
v_mfma_f32_16x16x32_bf16 v[92:95], v[188:191], a[100:103], v[92:95]
LC22_%=:
s_cbranch_execz LRETB_%=
v_and_b32 v240, 16, v231
s_waitcnt lgkmcnt(8)
v_mfma_f32_16x16x32_bf16 v[64:67], v[192:195], a[104:107], v[64:67]
ds_read_b64_tr_b16 v[176:177], v208 offset:26624
ds_read_b64_tr_b16 v[178:179], v208 offset:30720
v_mfma_f32_16x16x32_bf16 v[80:83], v[192:195], a[108:111], v[80:83]
buffer_load_dwordx4 v226, s[48:51], s23 offen offset:2048 lds
v_mfma_f32_16x16x32_bf16 v[68:71], v[196:199], a[104:107], v[68:71]
ds_read_b64_tr_b16 v[180:181], v209 offset:26624
ds_read_b64_tr_b16 v[182:183], v209 offset:30720
v_mfma_f32_16x16x32_bf16 v[84:87], v[196:199], a[108:111], v[84:87]
v_lshl_add_u32 v241, v240, 1, v241
v_lshrrev_b32 v240, 5, v231
v_mfma_f32_16x16x32_bf16 v[72:75], v[200:203], a[104:107], v[72:75]
ds_read_b64_tr_b16 v[184:185], v210 offset:26624
ds_read_b64_tr_b16 v[186:187], v210 offset:30720
)"
R"(
v_mfma_f32_16x16x32_bf16 v[88:91], v[200:203], a[108:111], v[88:91]
buffer_load_dwordx4 v227, s[48:51], s23 offen offset:3072 lds
v_mfma_f32_16x16x32_bf16 v[76:79], v[204:207], a[104:107], v[76:79]
ds_read_b64_tr_b16 v[188:189], v211 offset:26624
ds_read_b64_tr_b16 v[190:191], v211 offset:30720
v_mfma_f32_16x16x32_bf16 v[92:95], v[204:207], a[108:111], v[92:95]
s_waitcnt vmcnt(20) lgkmcnt(8)
s_barrier
v_mfma_f32_16x16x32_bf16 v[64:67], v[160:163], a[112:115], v[64:67]
ds_read_b64_tr_b16 v[192:193], v212 offset:49152
ds_read_b64_tr_b16 v[194:195], v212 offset:53248
s_add_u32 m0, s77, 81920
v_mfma_f32_16x16x32_bf16 v[80:83], v[160:163], a[116:119], v[80:83]
v_accvgpr_read_b32 v248, a128
v_accvgpr_read_b32 v249, a132
buffer_load_dwordx4 v224, s[48:51], s24 offen offset:0 lds
v_mfma_f32_16x16x32_bf16 v[68:71], v[164:167], a[112:115], v[68:71]
ds_read_b64_tr_b16 v[196:197], v213 offset:49152
ds_read_b64_tr_b16 v[198:199], v213 offset:53248
v_mfma_f32_16x16x32_bf16 v[84:87], v[164:167], a[116:119], v[84:87]
v_accvgpr_read_b32 v250, a136
v_max3_u32 v248, v248, v249, v250
v_mfma_f32_16x16x32_bf16 v[72:75], v[168:171], a[112:115], v[72:75]
ds_read_b64_tr_b16 v[200:201], v214 offset:49152
ds_read_b64_tr_b16 v[202:203], v214 offset:53248
v_mfma_f32_16x16x32_bf16 v[88:91], v[168:171], a[116:119], v[88:91]
v_accvgpr_read_b32 v251, a140
v_accvgpr_read_b32 v252, a144
buffer_load_dwordx4 v225, s[48:51], s24 offen offset:1024 lds
v_mfma_f32_16x16x32_bf16 v[76:79], v[172:175], a[112:115], v[76:79]
ds_read_b64_tr_b16 v[204:205], v215 offset:49152
ds_read_b64_tr_b16 v[206:207], v215 offset:53248
v_mfma_f32_16x16x32_bf16 v[92:95], v[172:175], a[116:119], v[92:95]
v_max3_u32 v248, v248, v251, v252
s_waitcnt lgkmcnt(8)
v_mfma_f32_16x16x32_bf16 v[64:67], v[176:179], a[120:123], v[64:67]
ds_read_b64_tr_b16 v[160:161], v212 offset:57344
ds_read_b64_tr_b16 v[162:163], v212 offset:61440
v_accvgpr_read_b32 v249, a148
v_mfma_f32_16x16x32_bf16 v[80:83], v[176:179], a[124:127], v[80:83]
v_accvgpr_read_b32 v250, a152
v_max3_u32 v248, v248, v249, v250
buffer_load_dwordx4 v226, s[48:51], s25 offen offset:2048 lds
v_mfma_f32_16x16x32_bf16 v[68:71], v[180:183], a[120:123], v[68:71]
ds_read_b64_tr_b16 v[164:165], v213 offset:57344
ds_read_b64_tr_b16 v[166:167], v213 offset:61440
v_mfma_f32_16x16x32_bf16 v[84:87], v[180:183], a[124:127], v[84:87]
v_accvgpr_read_b32 v251, a156
v_max_u32 v248, v248, v251
v_mfma_f32_16x16x32_bf16 v[72:75], v[184:187], a[120:123], v[72:75]
ds_read_b64_tr_b16 v[168:169], v214 offset:57344
ds_read_b64_tr_b16 v[170:171], v214 offset:61440
v_mfma_f32_16x16x32_bf16 v[88:91], v[184:187], a[124:127], v[88:91]
v_cmp_eq_u32 vcc, -1, v248
s_nop 0
s_cbranch_vccnz LSt4k24_%=
LRt4k24_%=:
buffer_load_dwordx4 v227, s[48:51], s25 offen offset:3072 lds
v_mfma_f32_16x16x32_bf16 v[76:79], v[188:191], a[120:123], v[76:79]
ds_read_b64_tr_b16 v[172:173], v215 offset:57344
ds_read_b64_tr_b16 v[174:175], v215 offset:61440
v_mfma_f32_16x16x32_bf16 v[92:95], v[188:191], a[124:127], v[92:95]
s_add_u32 m0, s77, 0
s_waitcnt lgkmcnt(8)
v_mfma_f32_16x16x32_bf16 v[64:67], v[192:195], a[128:131], v[64:67]
ds_read_b64_tr_b16 v[176:177], v212 offset:51200
ds_read_b64_tr_b16 v[178:179], v212 offset:55296
v_mfma_f32_16x16x32_bf16 v[80:83], v[192:195], a[132:135], v[80:83]
buffer_load_dwordx4 v224, s[48:51], s26 offen offset:0 lds
v_mfma_f32_16x16x32_bf16 v[68:71], v[196:199], a[128:131], v[68:71]
ds_read_b64_tr_b16 v[180:181], v213 offset:51200
ds_read_b64_tr_b16 v[182:183], v213 offset:55296
v_mfma_f32_16x16x32_bf16 v[84:87], v[196:199], a[132:135], v[84:87]
v_lshl_add_u32 v241, v240, 4, v241
v_add_u32 v242, 131072, v241
v_mfma_f32_16x16x32_bf16 v[72:75], v[200:203], a[128:131], v[72:75]
ds_read_b64_tr_b16 v[184:185], v214 offset:51200
ds_read_b64_tr_b16 v[186:187], v214 offset:55296
v_mfma_f32_16x16x32_bf16 v[88:91], v[200:203], a[132:135], v[88:91]
buffer_load_dwordx4 v225, s[48:51], s26 offen offset:1024 lds
v_mfma_f32_16x16x32_bf16 v[76:79], v[204:207], a[128:131], v[76:79]
ds_read_b64_tr_b16 v[188:189], v215 offset:51200
ds_read_b64_tr_b16 v[190:191], v215 offset:55296
v_mfma_f32_16x16x32_bf16 v[92:95], v[204:207], a[132:135], v[92:95]
v_lshlrev_b32 v0, 16, v32
s_waitcnt lgkmcnt(8)
v_mfma_f32_16x16x32_bf16 v[64:67], v[160:163], a[136:139], v[64:67]
ds_read_b64_tr_b16 v[192:193], v212 offset:59392
ds_read_b64_tr_b16 v[194:195], v212 offset:63488
v_mfma_f32_16x16x32_bf16 v[80:83], v[160:163], a[140:143], v[80:83]
buffer_load_dwordx4 v226, s[48:51], s27 offen offset:2048 lds
v_mfma_f32_16x16x32_bf16 v[68:71], v[164:167], a[136:139], v[68:71]
ds_read_b64_tr_b16 v[196:197], v213 offset:59392
ds_read_b64_tr_b16 v[198:199], v213 offset:63488
v_mfma_f32_16x16x32_bf16 v[84:87], v[164:167], a[140:143], v[84:87]
v_and_b32 v1, s93, v32
v_lshlrev_b32 v2, 16, v33
v_mfma_f32_16x16x32_bf16 v[72:75], v[168:171], a[136:139], v[72:75]
ds_read_b64_tr_b16 v[200:201], v214 offset:59392
ds_read_b64_tr_b16 v[202:203], v214 offset:63488
v_mfma_f32_16x16x32_bf16 v[88:91], v[168:171], a[140:143], v[88:91]
buffer_load_dwordx4 v227, s[48:51], s27 offen offset:3072 lds
v_mfma_f32_16x16x32_bf16 v[76:79], v[172:175], a[136:139], v[76:79]
ds_read_b64_tr_b16 v[204:205], v215 offset:59392
ds_read_b64_tr_b16 v[206:207], v215 offset:63488
v_mfma_f32_16x16x32_bf16 v[92:95], v[172:175], a[140:143], v[92:95]
s_waitcnt vmcnt(16) lgkmcnt(8)
s_barrier
v_mfma_f32_16x16x32_bf16 v[64:67], v[176:179], a[144:147], v[64:67]
ds_read_b64_tr_b16 v[160:161], v208 offset:32768
ds_read_b64_tr_b16 v[162:163], v208 offset:36864
v_mfma_f32_16x16x32_bf16 v[80:83], v[176:179], a[148:151], v[80:83]
v_accvgpr_read_b32 v248, a160
v_accvgpr_read_b32 v249, a164
v_mfma_f32_16x16x32_bf16 v[68:71], v[180:183], a[144:147], v[68:71]
ds_read_b64_tr_b16 v[164:165], v209 offset:32768
ds_read_b64_tr_b16 v[166:167], v209 offset:36864
v_mfma_f32_16x16x32_bf16 v[84:87], v[180:183], a[148:151], v[84:87]
v_accvgpr_read_b32 v250, a168
v_max3_u32 v248, v248, v249, v250
v_mfma_f32_16x16x32_bf16 v[72:75], v[184:187], a[144:147], v[72:75]
ds_read_b64_tr_b16 v[168:169], v210 offset:32768
ds_read_b64_tr_b16 v[170:171], v210 offset:36864
v_mfma_f32_16x16x32_bf16 v[88:91], v[184:187], a[148:151], v[88:91]
v_accvgpr_read_b32 v251, a172
v_accvgpr_read_b32 v252, a176
v_mfma_f32_16x16x32_bf16 v[76:79], v[188:191], a[144:147], v[76:79]
ds_read_b64_tr_b16 v[172:173], v211 offset:32768
ds_read_b64_tr_b16 v[174:175], v211 offset:36864
v_mfma_f32_16x16x32_bf16 v[92:95], v[188:191], a[148:151], v[92:95]
v_max3_u32 v248, v248, v251, v252
s_waitcnt lgkmcnt(8)
v_mfma_f32_16x16x32_bf16 v[64:67], v[192:195], a[152:155], v[64:67]
ds_read_b64_tr_b16 v[176:177], v208 offset:40960
ds_read_b64_tr_b16 v[178:179], v208 offset:45056
v_accvgpr_read_b32 v249, a180
v_mfma_f32_16x16x32_bf16 v[80:83], v[192:195], a[156:159], v[80:83]
v_accvgpr_read_b32 v250, a184
v_max3_u32 v248, v248, v249, v250
v_mfma_f32_16x16x32_bf16 v[68:71], v[196:199], a[152:155], v[68:71]
ds_read_b64_tr_b16 v[180:181], v209 offset:40960
ds_read_b64_tr_b16 v[182:183], v209 offset:45056
v_mfma_f32_16x16x32_bf16 v[84:87], v[196:199], a[156:159], v[84:87]
v_accvgpr_read_b32 v251, a188
v_max_u32 v248, v248, v251
v_mfma_f32_16x16x32_bf16 v[72:75], v[200:203], a[152:155], v[72:75]
ds_read_b64_tr_b16 v[184:185], v210 offset:40960
ds_read_b64_tr_b16 v[186:187], v210 offset:45056
v_mfma_f32_16x16x32_bf16 v[88:91], v[200:203], a[156:159], v[88:91]
v_cmp_eq_u32 vcc, -1, v248
s_nop 0
s_cbranch_vccnz LSt4k26_%=
LRt4k26_%=:
v_mfma_f32_16x16x32_bf16 v[76:79], v[204:207], a[152:155], v[76:79]
ds_read_b64_tr_b16 v[188:189], v211 offset:40960
ds_read_b64_tr_b16 v[190:191], v211 offset:45056
v_mfma_f32_16x16x32_bf16 v[92:95], v[204:207], a[156:159], v[92:95]
s_add_u32 m0, s77, 98304
s_waitcnt lgkmcnt(8)
v_mfma_f32_16x16x32_bf16 v[64:67], v[160:163], a[160:163], v[64:67]
ds_read_b64_tr_b16 v[192:193], v208 offset:34816
ds_read_b64_tr_b16 v[194:195], v208 offset:38912
v_mfma_f32_16x16x32_bf16 v[80:83], v[160:163], a[164:167], v[80:83]
buffer_load_dwordx4 v224, s[48:51], s28 offen offset:0 lds
v_mfma_f32_16x16x32_bf16 v[68:71], v[164:167], a[160:163], v[68:71]
ds_read_b64_tr_b16 v[196:197], v209 offset:34816
ds_read_b64_tr_b16 v[198:199], v209 offset:38912
v_mfma_f32_16x16x32_bf16 v[84:87], v[164:167], a[164:167], v[84:87]
v_and_b32 v3, s93, v33
v_lshlrev_b32 v4, 16, v34
v_mfma_f32_16x16x32_bf16 v[72:75], v[168:171], a[160:163], v[72:75]
ds_read_b64_tr_b16 v[200:201], v210 offset:34816
ds_read_b64_tr_b16 v[202:203], v210 offset:38912
v_mfma_f32_16x16x32_bf16 v[88:91], v[168:171], a[164:167], v[88:91]
buffer_load_dwordx4 v225, s[48:51], s28 offen offset:1024 lds
v_mfma_f32_16x16x32_bf16 v[76:79], v[172:175], a[160:163], v[76:79]
ds_read_b64_tr_b16 v[204:205], v211 offset:34816
ds_read_b64_tr_b16 v[206:207], v211 offset:38912
v_mfma_f32_16x16x32_bf16 v[92:95], v[172:175], a[164:167], v[92:95]
v_and_b32 v5, s93, v34
s_waitcnt lgkmcnt(8)
v_mfma_f32_16x16x32_bf16 v[64:67], v[176:179], a[168:171], v[64:67]
ds_read_b64_tr_b16 v[160:161], v208 offset:43008
ds_read_b64_tr_b16 v[162:163], v208 offset:47104
v_mfma_f32_16x16x32_bf16 v[80:83], v[176:179], a[172:175], v[80:83]
buffer_load_dwordx4 v226, s[48:51], s29 offen offset:2048 lds
v_mfma_f32_16x16x32_bf16 v[68:71], v[180:183], a[168:171], v[68:71]
ds_read_b64_tr_b16 v[164:165], v209 offset:43008
ds_read_b64_tr_b16 v[166:167], v209 offset:47104
v_mfma_f32_16x16x32_bf16 v[84:87], v[180:183], a[172:175], v[84:87]
v_lshlrev_b32 v6, 16, v35
v_and_b32 v7, s93, v35
v_mfma_f32_16x16x32_bf16 v[72:75], v[184:187], a[168:171], v[72:75]
ds_read_b64_tr_b16 v[168:169], v210 offset:43008
ds_read_b64_tr_b16 v[170:171], v210 offset:47104
v_mfma_f32_16x16x32_bf16 v[88:91], v[184:187], a[172:175], v[88:91]
buffer_load_dwordx4 v227, s[48:51], s29 offen offset:3072 lds
v_mfma_f32_16x16x32_bf16 v[76:79], v[188:191], a[168:171], v[76:79]
ds_read_b64_tr_b16 v[172:173], v211 offset:43008
ds_read_b64_tr_b16 v[174:175], v211 offset:47104
v_mfma_f32_16x16x32_bf16 v[92:95], v[188:191], a[172:175], v[92:95]
s_waitcnt vmcnt(16) lgkmcnt(8)
s_barrier
v_mfma_f32_16x16x32_bf16 v[64:67], v[192:195], a[176:179], v[64:67]
ds_read_b64_tr_b16 v[176:177], v212 offset:0
ds_read_b64_tr_b16 v[178:179], v212 offset:4096
v_mfma_f32_16x16x32_bf16 v[80:83], v[192:195], a[180:183], v[80:83]
v_lshlrev_b32 v8, 16, v36
v_and_b32 v9, s93, v36
v_mfma_f32_16x16x32_bf16 v[68:71], v[196:199], a[176:179], v[68:71]
ds_read_b64_tr_b16 v[180:181], v213 offset:0
ds_read_b64_tr_b16 v[182:183], v213 offset:4096
v_mfma_f32_16x16x32_bf16 v[84:87], v[196:199], a[180:183], v[84:87]
v_lshlrev_b32 v10, 16, v37
v_and_b32 v11, s93, v37
v_mfma_f32_16x16x32_bf16 v[72:75], v[200:203], a[176:179], v[72:75]
ds_read_b64_tr_b16 v[184:185], v214 offset:0
ds_read_b64_tr_b16 v[186:187], v214 offset:4096
v_mfma_f32_16x16x32_bf16 v[88:91], v[200:203], a[180:183], v[88:91]
v_lshlrev_b32 v12, 16, v38
v_and_b32 v13, s93, v38
v_mfma_f32_16x16x32_bf16 v[76:79], v[204:207], a[176:179], v[76:79]
ds_read_b64_tr_b16 v[188:189], v215 offset:0
ds_read_b64_tr_b16 v[190:191], v215 offset:4096
v_mfma_f32_16x16x32_bf16 v[92:95], v[204:207], a[180:183], v[92:95]
v_lshlrev_b32 v14, 16, v39
s_waitcnt lgkmcnt(8)
v_mfma_f32_16x16x32_bf16 v[64:67], v[160:163], a[184:187], v[64:67]
ds_read_b64_tr_b16 v[192:193], v212 offset:8192
ds_read_b64_tr_b16 v[194:195], v212 offset:12288
v_mfma_f32_16x16x32_bf16 v[80:83], v[160:163], a[188:191], v[80:83]
v_and_b32 v15, s93, v39
v_lshlrev_b32 v16, 16, v40
v_mfma_f32_16x16x32_bf16 v[68:71], v[164:167], a[184:187], v[68:71]
ds_read_b64_tr_b16 v[196:197], v213 offset:8192
ds_read_b64_tr_b16 v[198:199], v213 offset:12288
v_mfma_f32_16x16x32_bf16 v[84:87], v[164:167], a[188:191], v[84:87]
v_and_b32 v17, s93, v40
v_lshlrev_b32 v18, 16, v41
v_mfma_f32_16x16x32_bf16 v[72:75], v[168:171], a[184:187], v[72:75]
ds_read_b64_tr_b16 v[200:201], v214 offset:8192
ds_read_b64_tr_b16 v[202:203], v214 offset:12288
v_mfma_f32_16x16x32_bf16 v[88:91], v[168:171], a[188:191], v[88:91]
v_and_b32 v19, s93, v41
v_lshlrev_b32 v20, 16, v42
v_mfma_f32_16x16x32_bf16 v[76:79], v[172:175], a[184:187], v[76:79]
ds_read_b64_tr_b16 v[204:205], v215 offset:8192
ds_read_b64_tr_b16 v[206:207], v215 offset:12288
v_mfma_f32_16x16x32_bf16 v[92:95], v[172:175], a[188:191], v[92:95]
v_and_b32 v21, s93, v42
s_waitcnt lgkmcnt(8)
v_mfma_f32_16x16x32_bf16 v[96:99], v[176:179], a[32:35], v[96:99]
ds_read_b64_tr_b16 v[160:161], v212 offset:2048
ds_read_b64_tr_b16 v[162:163], v212 offset:6144
v_mfma_f32_16x16x32_bf16 v[112:115], v[176:179], a[36:39], v[112:115]
v_lshlrev_b32 v22, 16, v43
v_and_b32 v23, s93, v43
v_mfma_f32_16x16x32_bf16 v[100:103], v[180:183], a[32:35], v[100:103]
ds_read_b64_tr_b16 v[164:165], v213 offset:2048
ds_read_b64_tr_b16 v[166:167], v213 offset:6144
v_mfma_f32_16x16x32_bf16 v[116:119], v[180:183], a[36:39], v[116:119]
v_cvt_pk_bf16_f32 v128, v64, v65
v_cvt_pk_bf16_f32 v129, v66, v67
v_mfma_f32_16x16x32_bf16 v[104:107], v[184:187], a[32:35], v[104:107]
ds_read_b64_tr_b16 v[168:169], v214 offset:2048
ds_read_b64_tr_b16 v[170:171], v214 offset:6144
v_mfma_f32_16x16x32_bf16 v[120:123], v[184:187], a[36:39], v[120:123]
v_cvt_pk_bf16_f32 v130, v68, v69
v_cvt_pk_bf16_f32 v131, v70, v71
v_mfma_f32_16x16x32_bf16 v[108:111], v[188:191], a[32:35], v[108:111]
ds_read_b64_tr_b16 v[172:173], v215 offset:2048
ds_read_b64_tr_b16 v[174:175], v215 offset:6144
v_mfma_f32_16x16x32_bf16 v[124:127], v[188:191], a[36:39], v[124:127]
v_and_b32 v128, -2, v128
s_waitcnt lgkmcnt(8)
v_mfma_f32_16x16x32_bf16 v[96:99], v[192:195], a[40:43], v[96:99]
ds_read_b64_tr_b16 v[176:177], v212 offset:10240
ds_read_b64_tr_b16 v[178:179], v212 offset:14336
v_mfma_f32_16x16x32_bf16 v[112:115], v[192:195], a[44:47], v[112:115]
buffer_store_dwordx4 v[128:131], v230, s[52:55], s7 offen offset:0 sc1
v_mfma_f32_16x16x32_bf16 v[100:103], v[196:199], a[40:43], v[100:103]
ds_read_b64_tr_b16 v[180:181], v213 offset:10240
ds_read_b64_tr_b16 v[182:183], v213 offset:14336
v_mfma_f32_16x16x32_bf16 v[116:119], v[196:199], a[44:47], v[116:119]
v_cvt_pk_bf16_f32 v132, v72, v73
v_cvt_pk_bf16_f32 v133, v74, v75
v_mfma_f32_16x16x32_bf16 v[104:107], v[200:203], a[40:43], v[104:107]
ds_read_b64_tr_b16 v[184:185], v214 offset:10240
ds_read_b64_tr_b16 v[186:187], v214 offset:14336
v_mfma_f32_16x16x32_bf16 v[120:123], v[200:203], a[44:47], v[120:123]
v_cvt_pk_bf16_f32 v134, v76, v77
v_cvt_pk_bf16_f32 v135, v78, v79
v_mfma_f32_16x16x32_bf16 v[108:111], v[204:207], a[40:43], v[108:111]
ds_read_b64_tr_b16 v[188:189], v215 offset:10240
ds_read_b64_tr_b16 v[190:191], v215 offset:14336
v_mfma_f32_16x16x32_bf16 v[124:127], v[204:207], a[44:47], v[124:127]
s_waitcnt vmcnt(13) lgkmcnt(8)
s_barrier
v_mfma_f32_16x16x32_bf16 v[96:99], v[160:163], a[48:51], v[96:99]
ds_read_b64_tr_b16 v[192:193], v208 offset:49152
ds_read_b64_tr_b16 v[194:195], v208 offset:53248
v_mfma_f32_16x16x32_bf16 v[112:115], v[160:163], a[52:55], v[112:115]
v_and_b32 v132, -2, v132
v_cvt_pk_bf16_f32 v136, v80, v81
v_mfma_f32_16x16x32_bf16 v[100:103], v[164:167], a[48:51], v[100:103]
ds_read_b64_tr_b16 v[196:197], v209 offset:49152
ds_read_b64_tr_b16 v[198:199], v209 offset:53248
v_mfma_f32_16x16x32_bf16 v[116:119], v[164:167], a[52:55], v[116:119]
buffer_store_dwordx4 v[132:135], v230, s[52:55], s7 offen offset:1024 sc1
v_mfma_f32_16x16x32_bf16 v[104:107], v[168:171], a[48:51], v[104:107]
ds_read_b64_tr_b16 v[200:201], v210 offset:49152
ds_read_b64_tr_b16 v[202:203], v210 offset:53248
v_mfma_f32_16x16x32_bf16 v[120:123], v[168:171], a[52:55], v[120:123]
v_cvt_pk_bf16_f32 v137, v82, v83
v_cvt_pk_bf16_f32 v138, v84, v85
v_mfma_f32_16x16x32_bf16 v[108:111], v[172:175], a[48:51], v[108:111]
ds_read_b64_tr_b16 v[204:205], v211 offset:49152
ds_read_b64_tr_b16 v[206:207], v211 offset:53248
v_mfma_f32_16x16x32_bf16 v[124:127], v[172:175], a[52:55], v[124:127]
v_cvt_pk_bf16_f32 v139, v86, v87
s_waitcnt lgkmcnt(8)
v_mfma_f32_16x16x32_bf16 v[96:99], v[176:179], a[56:59], v[96:99]
ds_read_b64_tr_b16 v[160:161], v208 offset:57344
ds_read_b64_tr_b16 v[162:163], v208 offset:61440
v_mfma_f32_16x16x32_bf16 v[112:115], v[176:179], a[60:63], v[112:115]
v_and_b32 v136, -2, v136
v_cvt_pk_bf16_f32 v140, v88, v89
v_mfma_f32_16x16x32_bf16 v[100:103], v[180:183], a[56:59], v[100:103]
ds_read_b64_tr_b16 v[164:165], v209 offset:57344
ds_read_b64_tr_b16 v[166:167], v209 offset:61440
v_mfma_f32_16x16x32_bf16 v[116:119], v[180:183], a[60:63], v[116:119]
buffer_store_dwordx4 v[136:139], v230, s[52:55], s7 offen offset:2048 sc1
v_mfma_f32_16x16x32_bf16 v[104:107], v[184:187], a[56:59], v[104:107]
ds_read_b64_tr_b16 v[168:169], v210 offset:57344
ds_read_b64_tr_b16 v[170:171], v210 offset:61440
v_mfma_f32_16x16x32_bf16 v[120:123], v[184:187], a[60:63], v[120:123]
v_cvt_pk_bf16_f32 v141, v90, v91
v_cvt_pk_bf16_f32 v142, v92, v93
v_mfma_f32_16x16x32_bf16 v[108:111], v[188:191], a[56:59], v[108:111]
ds_read_b64_tr_b16 v[172:173], v211 offset:57344
ds_read_b64_tr_b16 v[174:175], v211 offset:61440
v_mfma_f32_16x16x32_bf16 v[124:127], v[188:191], a[60:63], v[124:127]
v_cvt_pk_bf16_f32 v143, v94, v95
s_waitcnt lgkmcnt(8)
v_mfma_f32_16x16x32_bf16 v[96:99], v[192:195], a[64:67], v[96:99]
ds_read_b64_tr_b16 v[176:177], v208 offset:51200
ds_read_b64_tr_b16 v[178:179], v208 offset:55296
v_mfma_f32_16x16x32_bf16 v[112:115], v[192:195], a[68:71], v[112:115]
v_and_b32 v140, -2, v140
v_lshlrev_b32 v24, 16, v44
v_mfma_f32_16x16x32_bf16 v[100:103], v[196:199], a[64:67], v[100:103]
ds_read_b64_tr_b16 v[180:181], v209 offset:51200
ds_read_b64_tr_b16 v[182:183], v209 offset:55296
v_mfma_f32_16x16x32_bf16 v[116:119], v[196:199], a[68:71], v[116:119]
buffer_store_dwordx4 v[140:143], v230, s[52:55], s7 offen offset:3072 sc1
v_mfma_f32_16x16x32_bf16 v[104:107], v[200:203], a[64:67], v[104:107]
ds_read_b64_tr_b16 v[184:185], v210 offset:51200
ds_read_b64_tr_b16 v[186:187], v210 offset:55296
v_mfma_f32_16x16x32_bf16 v[120:123], v[200:203], a[68:71], v[120:123]
v_and_b32 v25, s93, v44
v_lshlrev_b32 v26, 16, v45
v_mfma_f32_16x16x32_bf16 v[108:111], v[204:207], a[64:67], v[108:111]
ds_read_b64_tr_b16 v[188:189], v211 offset:51200
ds_read_b64_tr_b16 v[190:191], v211 offset:55296
v_mfma_f32_16x16x32_bf16 v[124:127], v[204:207], a[68:71], v[124:127]
v_and_b32 v27, s93, v45
s_waitcnt lgkmcnt(8)
v_mfma_f32_16x16x32_bf16 v[96:99], v[160:163], a[72:75], v[96:99]
ds_read_b64_tr_b16 v[192:193], v208 offset:59392
ds_read_b64_tr_b16 v[194:195], v208 offset:63488
v_mfma_f32_16x16x32_bf16 v[112:115], v[160:163], a[76:79], v[112:115]
v_lshlrev_b32 v28, 16, v46
v_and_b32 v29, s93, v46
v_mfma_f32_16x16x32_bf16 v[100:103], v[164:167], a[72:75], v[100:103]
ds_read_b64_tr_b16 v[196:197], v209 offset:59392
ds_read_b64_tr_b16 v[198:199], v209 offset:63488
v_mfma_f32_16x16x32_bf16 v[116:119], v[164:167], a[76:79], v[116:119]
v_lshlrev_b32 v30, 16, v47
v_and_b32 v31, s93, v47
v_mfma_f32_16x16x32_bf16 v[104:107], v[168:171], a[72:75], v[104:107]
ds_read_b64_tr_b16 v[200:201], v210 offset:59392
ds_read_b64_tr_b16 v[202:203], v210 offset:63488
v_mfma_f32_16x16x32_bf16 v[120:123], v[168:171], a[76:79], v[120:123]
s_xor_b32 s88, s7, 16384
v_mfma_f32_16x16x32_bf16 v[108:111], v[172:175], a[72:75], v[108:111]
ds_read_b64_tr_b16 v[204:205], v211 offset:59392
ds_read_b64_tr_b16 v[206:207], v211 offset:63488
v_mfma_f32_16x16x32_bf16 v[124:127], v[172:175], a[76:79], v[124:127]
s_waitcnt vmcnt(12) lgkmcnt(8)
s_barrier
v_mfma_f32_16x16x32_bf16 v[96:99], v[176:179], a[80:83], v[96:99]
ds_read_b64_tr_b16 v[160:161], v212 offset:16384
ds_read_b64_tr_b16 v[162:163], v212 offset:20480
v_mfma_f32_16x16x32_bf16 v[112:115], v[176:179], a[84:87], v[112:115]
v_mfma_f32_16x16x32_bf16 v[100:103], v[180:183], a[80:83], v[100:103]
ds_read_b64_tr_b16 v[164:165], v213 offset:16384
ds_read_b64_tr_b16 v[166:167], v213 offset:20480
v_mfma_f32_16x16x32_bf16 v[116:119], v[180:183], a[84:87], v[116:119]
v_mfma_f32_16x16x32_bf16 v[104:107], v[184:187], a[80:83], v[104:107]
ds_read_b64_tr_b16 v[168:169], v214 offset:16384
)"
R"(
ds_read_b64_tr_b16 v[170:171], v214 offset:20480
v_mfma_f32_16x16x32_bf16 v[120:123], v[184:187], a[84:87], v[120:123]
v_mfma_f32_16x16x32_bf16 v[108:111], v[188:191], a[80:83], v[108:111]
ds_read_b64_tr_b16 v[172:173], v215 offset:16384
ds_read_b64_tr_b16 v[174:175], v215 offset:20480
v_mfma_f32_16x16x32_bf16 v[124:127], v[188:191], a[84:87], v[124:127]
s_waitcnt lgkmcnt(8)
v_mfma_f32_16x16x32_bf16 v[96:99], v[192:195], a[88:91], v[96:99]
ds_read_b64_tr_b16 v[176:177], v212 offset:24576
ds_read_b64_tr_b16 v[178:179], v212 offset:28672
v_mfma_f32_16x16x32_bf16 v[112:115], v[192:195], a[92:95], v[112:115]
v_mfma_f32_16x16x32_bf16 v[100:103], v[196:199], a[88:91], v[100:103]
ds_read_b64_tr_b16 v[180:181], v213 offset:24576
ds_read_b64_tr_b16 v[182:183], v213 offset:28672
v_mfma_f32_16x16x32_bf16 v[116:119], v[196:199], a[92:95], v[116:119]
v_mfma_f32_16x16x32_bf16 v[104:107], v[200:203], a[88:91], v[104:107]
ds_read_b64_tr_b16 v[184:185], v214 offset:24576
ds_read_b64_tr_b16 v[186:187], v214 offset:28672
v_mfma_f32_16x16x32_bf16 v[120:123], v[200:203], a[92:95], v[120:123]
v_mfma_f32_16x16x32_bf16 v[108:111], v[204:207], a[88:91], v[108:111]
ds_read_b64_tr_b16 v[188:189], v215 offset:24576
ds_read_b64_tr_b16 v[190:191], v215 offset:28672
v_mfma_f32_16x16x32_bf16 v[124:127], v[204:207], a[92:95], v[124:127]
s_waitcnt lgkmcnt(8)
v_mfma_f32_16x16x32_bf16 v[96:99], v[160:163], a[96:99], v[96:99]
ds_read_b64_tr_b16 v[192:193], v212 offset:18432
ds_read_b64_tr_b16 v[194:195], v212 offset:22528
v_mfma_f32_16x16x32_bf16 v[112:115], v[160:163], a[100:103], v[112:115]
buffer_load_dwordx4 v[144:147], v230, s[52:55], s88 offen offset:0 sc1
v_mfma_f32_16x16x32_bf16 v[100:103], v[164:167], a[96:99], v[100:103]
ds_read_b64_tr_b16 v[196:197], v213 offset:18432
ds_read_b64_tr_b16 v[198:199], v213 offset:22528
v_mfma_f32_16x16x32_bf16 v[116:119], v[164:167], a[100:103], v[116:119]
buffer_load_dwordx4 v[148:151], v230, s[52:55], s88 offen offset:1024 sc1
v_mfma_f32_16x16x32_bf16 v[104:107], v[168:171], a[96:99], v[104:107]
ds_read_b64_tr_b16 v[200:201], v214 offset:18432
ds_read_b64_tr_b16 v[202:203], v214 offset:22528
v_mfma_f32_16x16x32_bf16 v[120:123], v[168:171], a[100:103], v[120:123]
buffer_load_dwordx4 v[152:155], v230, s[52:55], s88 offen offset:2048 sc1
v_mfma_f32_16x16x32_bf16 v[108:111], v[172:175], a[96:99], v[108:111]
ds_read_b64_tr_b16 v[204:205], v215 offset:18432
ds_read_b64_tr_b16 v[206:207], v215 offset:22528
v_mfma_f32_16x16x32_bf16 v[124:127], v[172:175], a[100:103], v[124:127]
s_waitcnt lgkmcnt(8)
v_mfma_f32_16x16x32_bf16 v[96:99], v[176:179], a[104:107], v[96:99]
ds_read_b64_tr_b16 v[160:161], v212 offset:26624
ds_read_b64_tr_b16 v[162:163], v212 offset:30720
v_mfma_f32_16x16x32_bf16 v[112:115], v[176:179], a[108:111], v[112:115]
buffer_load_dwordx4 v[156:159], v230, s[52:55], s88 offen offset:3072 sc1
v_mfma_f32_16x16x32_bf16 v[100:103], v[180:183], a[104:107], v[100:103]
ds_read_b64_tr_b16 v[164:165], v213 offset:26624
ds_read_b64_tr_b16 v[166:167], v213 offset:30720
v_mfma_f32_16x16x32_bf16 v[116:119], v[180:183], a[108:111], v[116:119]
v_mfma_f32_16x16x32_bf16 v[104:107], v[184:187], a[104:107], v[104:107]
ds_read_b64_tr_b16 v[168:169], v214 offset:26624
ds_read_b64_tr_b16 v[170:171], v214 offset:30720
v_mfma_f32_16x16x32_bf16 v[120:123], v[184:187], a[108:111], v[120:123]
v_mfma_f32_16x16x32_bf16 v[108:111], v[188:191], a[104:107], v[108:111]
ds_read_b64_tr_b16 v[172:173], v215 offset:26624
ds_read_b64_tr_b16 v[174:175], v215 offset:30720
v_mfma_f32_16x16x32_bf16 v[124:127], v[188:191], a[108:111], v[124:127]
s_waitcnt vmcnt(12) lgkmcnt(8)
s_barrier
v_mfma_f32_16x16x32_bf16 v[96:99], v[192:195], a[112:115], v[96:99]
ds_read_b64_tr_b16 v[176:177], v208 offset:0
ds_read_b64_tr_b16 v[178:179], v208 offset:4096
v_mfma_f32_16x16x32_bf16 v[112:115], v[192:195], a[116:119], v[112:115]
v_mfma_f32_16x16x32_bf16 v[100:103], v[196:199], a[112:115], v[100:103]
ds_read_b64_tr_b16 v[180:181], v209 offset:0
ds_read_b64_tr_b16 v[182:183], v209 offset:4096
v_mfma_f32_16x16x32_bf16 v[116:119], v[196:199], a[116:119], v[116:119]
v_mfma_f32_16x16x32_bf16 v[104:107], v[200:203], a[112:115], v[104:107]
ds_read_b64_tr_b16 v[184:185], v208 offset:8192
ds_read_b64_tr_b16 v[186:187], v208 offset:12288
v_mfma_f32_16x16x32_bf16 v[120:123], v[200:203], a[116:119], v[120:123]
v_mfma_f32_16x16x32_bf16 v[108:111], v[204:207], a[112:115], v[108:111]
ds_read_b64_tr_b16 v[188:189], v209 offset:8192
ds_read_b64_tr_b16 v[190:191], v209 offset:12288
v_mfma_f32_16x16x32_bf16 v[124:127], v[204:207], a[116:119], v[124:127]
s_waitcnt lgkmcnt(8)
v_mfma_f32_16x16x32_bf16 v[96:99], v[160:163], a[120:123], v[96:99]
ds_read_b64_tr_b16 v[192:193], v208 offset:2048
ds_read_b64_tr_b16 v[194:195], v208 offset:6144
v_mfma_f32_16x16x32_bf16 v[112:115], v[160:163], a[124:127], v[112:115]
v_mfma_f32_16x16x32_bf16 v[100:103], v[164:167], a[120:123], v[100:103]
ds_read_b64_tr_b16 v[196:197], v209 offset:2048
ds_read_b64_tr_b16 v[198:199], v209 offset:6144
v_mfma_f32_16x16x32_bf16 v[116:119], v[164:167], a[124:127], v[116:119]
v_mfma_f32_16x16x32_bf16 v[104:107], v[168:171], a[120:123], v[104:107]
ds_read_b64_tr_b16 v[200:201], v208 offset:10240
ds_read_b64_tr_b16 v[202:203], v208 offset:14336
v_mfma_f32_16x16x32_bf16 v[120:123], v[168:171], a[124:127], v[120:123]
v_mfma_f32_16x16x32_bf16 v[108:111], v[172:175], a[120:123], v[108:111]
ds_read_b64_tr_b16 v[204:205], v209 offset:10240
ds_read_b64_tr_b16 v[206:207], v209 offset:14336
v_mfma_f32_16x16x32_bf16 v[124:127], v[172:175], a[124:127], v[124:127]
s_waitcnt vmcnt(8) lgkmcnt(8)
s_barrier
v_mfma_f32_16x16x32_bf16 v[96:99], v[176:179], a[128:131], v[96:99]
ds_read_b64_tr_b16 v[160:161], v212 offset:32768
ds_read_b64_tr_b16 v[162:163], v212 offset:36864
v_mfma_f32_16x16x32_bf16 v[112:115], v[176:179], a[132:135], v[112:115]
v_mfma_f32_16x16x32_bf16 v[100:103], v[180:183], a[128:131], v[100:103]
ds_read_b64_tr_b16 v[164:165], v213 offset:32768
ds_read_b64_tr_b16 v[166:167], v213 offset:36864
v_mfma_f32_16x16x32_bf16 v[116:119], v[180:183], a[132:135], v[116:119]
v_mfma_f32_16x16x32_bf16 v[96:99], v[184:187], a[136:139], v[96:99]
ds_read_b64_tr_b16 v[168:169], v212 offset:40960
ds_read_b64_tr_b16 v[170:171], v212 offset:45056
s_waitcnt vmcnt(1)
v_or3_b32 v236, v144, v148, v152
v_mfma_f32_16x16x32_bf16 v[112:115], v[184:187], a[140:143], v[112:115]
s_waitcnt vmcnt(0)
v_bitop3_b32 v236, v236, v156, 1 bitop3:0xa8
v_mfma_f32_16x16x32_bf16 v[100:103], v[188:191], a[136:139], v[100:103]
ds_read_b64_tr_b16 v[172:173], v213 offset:40960
ds_read_b64_tr_b16 v[174:175], v213 offset:45056
v_mfma_f32_16x16x32_bf16 v[116:119], v[188:191], a[140:143], v[116:119]
s_waitcnt lgkmcnt(8)
v_mfma_f32_16x16x32_bf16 v[96:99], v[192:195], a[144:147], v[96:99]
ds_read_b64_tr_b16 v[176:177], v212 offset:34816
ds_read_b64_tr_b16 v[178:179], v212 offset:38912
v_mfma_f32_16x16x32_bf16 v[112:115], v[192:195], a[148:151], v[112:115]
v_cmp_ne_u32 vcc, 0, v236
s_nop 0
s_cbranch_vccz LRr4_%=
LSr4_%=:
s_sub_u32 s84, s84, 1
s_cbranch_scc1 LZr4_%=
buffer_load_dwordx4 v[144:147], v230, s[52:55], s88 offen offset:0 sc1
buffer_load_dwordx4 v[148:151], v230, s[52:55], s88 offen offset:1024 sc1
buffer_load_dwordx4 v[152:155], v230, s[52:55], s88 offen offset:2048 sc1
buffer_load_dwordx4 v[156:159], v230, s[52:55], s88 offen offset:3072 sc1
s_waitcnt vmcnt(0)
v_or3_b32 v236, v144, v148, v152
v_bitop3_b32 v236, v236, v156, 1 bitop3:0xa8
v_cmp_ne_u32 vcc, 0, v236
s_nop 0
s_cbranch_vccnz LSr4_%=
LZr4_%=:
s_max_i32 s84, s84, 0
LRr4_%=:
v_mfma_f32_16x16x32_bf16 v[100:103], v[196:199], a[144:147], v[100:103]
ds_read_b64_tr_b16 v[180:181], v213 offset:34816
ds_read_b64_tr_b16 v[182:183], v213 offset:38912
v_and_b32 v128, s93, v144
v_mfma_f32_16x16x32_bf16 v[116:119], v[196:199], a[148:151], v[116:119]
LC23_%=:
s_cbranch_execz LRETB_%=
v_lshlrev_b32 v144, 16, v144
v_fmac_f32 v0, v144, v48
v_mfma_f32_16x16x32_bf16 v[96:99], v[200:203], a[152:155], v[96:99]
ds_read_b64_tr_b16 v[184:185], v212 offset:43008
ds_read_b64_tr_b16 v[186:187], v212 offset:47104
v_mfma_f32_16x16x32_bf16 v[112:115], v[200:203], a[156:159], v[112:115]
v_fmac_f32 v1, v128, v49
v_and_b32 v129, s93, v145
v_mfma_f32_16x16x32_bf16 v[100:103], v[204:207], a[152:155], v[100:103]
ds_read_b64_tr_b16 v[188:189], v213 offset:43008
ds_read_b64_tr_b16 v[190:191], v213 offset:47104
v_lshlrev_b32 v145, 16, v145
v_mfma_f32_16x16x32_bf16 v[116:119], v[204:207], a[156:159], v[116:119]
v_fmac_f32 v2, v145, v50
s_waitcnt lgkmcnt(8)
v_mfma_f32_16x16x32_bf16 v[96:99], v[160:163], a[160:163], v[96:99]
ds_read_b64_tr_b16 v[192:193], v210 offset:0
ds_read_b64_tr_b16 v[194:195], v210 offset:4096
v_fmac_f32 v3, v129, v51
v_mfma_f32_16x16x32_bf16 v[112:115], v[160:163], a[164:167], v[112:115]
v_and_b32 v130, s93, v146
v_lshlrev_b32 v146, 16, v146
v_mfma_f32_16x16x32_bf16 v[100:103], v[164:167], a[160:163], v[100:103]
ds_read_b64_tr_b16 v[196:197], v211 offset:0
ds_read_b64_tr_b16 v[198:199], v211 offset:4096
v_mfma_f32_16x16x32_bf16 v[116:119], v[164:167], a[164:167], v[116:119]
v_fmac_f32 v4, v146, v52
v_fmac_f32 v5, v130, v53
v_mfma_f32_16x16x32_bf16 v[96:99], v[168:171], a[168:171], v[96:99]
ds_read_b64_tr_b16 v[200:201], v210 offset:8192
ds_read_b64_tr_b16 v[202:203], v210 offset:12288
v_and_b32 v131, s93, v147
v_mfma_f32_16x16x32_bf16 v[112:115], v[168:171], a[172:175], v[112:115]
v_lshlrev_b32 v147, 16, v147
v_fmac_f32 v6, v147, v54
v_mfma_f32_16x16x32_bf16 v[100:103], v[172:175], a[168:171], v[100:103]
ds_read_b64_tr_b16 v[204:205], v211 offset:8192
ds_read_b64_tr_b16 v[206:207], v211 offset:12288
v_mfma_f32_16x16x32_bf16 v[116:119], v[172:175], a[172:175], v[116:119]
v_fmac_f32 v7, v131, v55
s_waitcnt lgkmcnt(8)
v_mfma_f32_16x16x32_bf16 v[96:99], v[176:179], a[176:179], v[96:99]
ds_read_b64_tr_b16 v[160:161], v210 offset:2048
ds_read_b64_tr_b16 v[162:163], v210 offset:6144
v_and_b32 v136, s93, v152
v_mfma_f32_16x16x32_bf16 v[112:115], v[176:179], a[180:183], v[112:115]
v_lshlrev_b32 v152, 16, v152
v_fmac_f32 v16, v152, v48
v_mfma_f32_16x16x32_bf16 v[100:103], v[180:183], a[176:179], v[100:103]
ds_read_b64_tr_b16 v[164:165], v211 offset:2048
ds_read_b64_tr_b16 v[166:167], v211 offset:6144
v_fmac_f32 v17, v136, v49
v_mfma_f32_16x16x32_bf16 v[116:119], v[180:183], a[180:183], v[116:119]
v_and_b32 v137, s93, v153
v_lshlrev_b32 v153, 16, v153
v_mfma_f32_16x16x32_bf16 v[96:99], v[184:187], a[184:187], v[96:99]
ds_read_b64_tr_b16 v[168:169], v210 offset:10240
ds_read_b64_tr_b16 v[170:171], v210 offset:14336
v_mfma_f32_16x16x32_bf16 v[112:115], v[184:187], a[188:191], v[112:115]
v_fmac_f32 v18, v153, v50
v_fmac_f32 v19, v137, v51
v_mfma_f32_16x16x32_bf16 v[100:103], v[188:191], a[184:187], v[100:103]
ds_read_b64_tr_b16 v[172:173], v211 offset:10240
ds_read_b64_tr_b16 v[174:175], v211 offset:14336
v_and_b32 v138, s93, v154
v_mfma_f32_16x16x32_bf16 v[116:119], v[188:191], a[188:191], v[116:119]
v_lshlrev_b32 v154, 16, v154
s_waitcnt lgkmcnt(8)
v_mfma_f32_16x16x32_bf16 v[104:107], v[192:195], a[128:131], v[104:107]
ds_read_b64_tr_b16 v[176:177], v214 offset:32768
ds_read_b64_tr_b16 v[178:179], v214 offset:36864
v_fmac_f32 v20, v154, v52
v_mfma_f32_16x16x32_bf16 v[120:123], v[192:195], a[132:135], v[120:123]
v_fmac_f32 v21, v138, v53
v_and_b32 v139, s93, v155
v_mfma_f32_16x16x32_bf16 v[108:111], v[196:199], a[128:131], v[108:111]
ds_read_b64_tr_b16 v[180:181], v215 offset:32768
ds_read_b64_tr_b16 v[182:183], v215 offset:36864
v_mfma_f32_16x16x32_bf16 v[124:127], v[196:199], a[132:135], v[124:127]
v_lshlrev_b32 v155, 16, v155
v_fmac_f32 v22, v155, v54
v_mfma_f32_16x16x32_bf16 v[104:107], v[200:203], a[136:139], v[104:107]
ds_read_b64_tr_b16 v[184:185], v214 offset:40960
ds_read_b64_tr_b16 v[186:187], v214 offset:45056
v_fmac_f32 v23, v139, v55
v_mfma_f32_16x16x32_bf16 v[120:123], v[200:203], a[140:143], v[120:123]
v_mfma_f32_16x16x32_bf16 v[108:111], v[204:207], a[136:139], v[108:111]
ds_read_b64_tr_b16 v[188:189], v215 offset:40960
ds_read_b64_tr_b16 v[190:191], v215 offset:45056
v_mfma_f32_16x16x32_bf16 v[124:127], v[204:207], a[140:143], v[124:127]
s_waitcnt lgkmcnt(8)
v_mfma_f32_16x16x32_bf16 v[104:107], v[160:163], a[144:147], v[104:107]
ds_read_b64_tr_b16 v[192:193], v214 offset:34816
ds_read_b64_tr_b16 v[194:195], v214 offset:38912
v_fmac_f32 v0, v96, v48
v_fmac_f32 v1, v97, v49
v_fmac_f32 v2, v98, v50
v_mfma_f32_16x16x32_bf16 v[120:123], v[160:163], a[148:151], v[120:123]
v_fmac_f32 v3, v99, v51
v_fmac_f32 v4, v100, v52
v_fmac_f32 v5, v101, v53
v_mfma_f32_16x16x32_bf16 v[108:111], v[164:167], a[144:147], v[108:111]
ds_read_b64_tr_b16 v[196:197], v215 offset:34816
ds_read_b64_tr_b16 v[198:199], v215 offset:38912
v_fmac_f32 v6, v102, v54
v_fmac_f32 v7, v103, v55
v_mfma_f32_16x16x32_bf16 v[124:127], v[164:167], a[148:151], v[124:127]
v_cvt_pk_bf16_f32 v0, v0, v1
v_cvt_pk_bf16_f32 v1, v2, v3
v_cvt_pk_bf16_f32 v2, v4, v5
v_mfma_f32_16x16x32_bf16 v[104:107], v[168:171], a[152:155], v[104:107]
ds_read_b64_tr_b16 v[200:201], v214 offset:43008
ds_read_b64_tr_b16 v[202:203], v214 offset:47104
v_cvt_pk_bf16_f32 v3, v6, v7
v_fmac_f32 v16, v112, v48
v_mfma_f32_16x16x32_bf16 v[120:123], v[168:171], a[156:159], v[120:123]
v_permlane16_swap_b32 v0, v2
v_fmac_f32 v17, v113, v49
v_fmac_f32 v18, v114, v50
v_mfma_f32_16x16x32_bf16 v[108:111], v[172:175], a[152:155], v[108:111]
ds_read_b64_tr_b16 v[204:205], v215 offset:43008
ds_read_b64_tr_b16 v[206:207], v215 offset:47104
v_permlane16_swap_b32 v1, v3
buffer_store_dwordx4 v[0:3], v241, s[60:63], 0 offen offset:0 sc1
v_fmac_f32 v19, v115, v51
v_mfma_f32_16x16x32_bf16 v[124:127], v[172:175], a[156:159], v[124:127]
v_fmac_f32 v20, v116, v52
v_fmac_f32 v21, v117, v53
s_waitcnt lgkmcnt(8)
v_mfma_f32_16x16x32_bf16 v[104:107], v[176:179], a[160:163], v[104:107]
v_fmac_f32 v22, v118, v54
v_fmac_f32 v23, v119, v55
v_cvt_pk_bf16_f32 v16, v16, v17
v_mfma_f32_16x16x32_bf16 v[120:123], v[176:179], a[164:167], v[120:123]
v_cvt_pk_bf16_f32 v17, v18, v19
v_cvt_pk_bf16_f32 v18, v20, v21
v_mfma_f32_16x16x32_bf16 v[108:111], v[180:183], a[160:163], v[108:111]
v_cvt_pk_bf16_f32 v19, v22, v23
v_and_b32 v132, s93, v148
v_lshlrev_b32 v148, 16, v148
v_mfma_f32_16x16x32_bf16 v[124:127], v[180:183], a[164:167], v[124:127]
v_permlane16_swap_b32 v16, v18
v_fmac_f32 v8, v148, v56
v_mfma_f32_16x16x32_bf16 v[104:107], v[184:187], a[168:171], v[104:107]
v_permlane16_swap_b32 v17, v19
v_fmac_f32 v9, v132, v57
buffer_store_dwordx4 v[16:19], v242, s[60:63], 0 offen offset:0 sc1
v_mfma_f32_16x16x32_bf16 v[120:123], v[184:187], a[172:175], v[120:123]
v_and_b32 v133, s93, v149
v_lshlrev_b32 v149, 16, v149
v_fmac_f32 v10, v149, v58
v_mfma_f32_16x16x32_bf16 v[108:111], v[188:191], a[168:171], v[108:111]
v_fmac_f32 v11, v133, v59
v_and_b32 v134, s93, v150
v_mfma_f32_16x16x32_bf16 v[124:127], v[188:191], a[172:175], v[124:127]
v_lshlrev_b32 v150, 16, v150
v_fmac_f32 v12, v150, v60
v_fmac_f32 v13, v134, v61
s_waitcnt lgkmcnt(0)
v_mfma_f32_16x16x32_bf16 v[104:107], v[192:195], a[176:179], v[104:107]
v_and_b32 v135, s93, v151
v_lshlrev_b32 v151, 16, v151
v_mfma_f32_16x16x32_bf16 v[120:123], v[192:195], a[180:183], v[120:123]
v_fmac_f32 v14, v151, v62
v_fmac_f32 v15, v135, v63
v_and_b32 v140, s93, v156
v_mfma_f32_16x16x32_bf16 v[108:111], v[196:199], a[176:179], v[108:111]
v_lshlrev_b32 v156, 16, v156
v_fmac_f32 v24, v156, v56
v_fmac_f32 v25, v140, v57
v_mfma_f32_16x16x32_bf16 v[124:127], v[196:199], a[180:183], v[124:127]
v_and_b32 v141, s93, v157
v_lshlrev_b32 v157, 16, v157
v_mfma_f32_16x16x32_bf16 v[104:107], v[200:203], a[184:187], v[104:107]
v_fmac_f32 v26, v157, v58
v_fmac_f32 v27, v141, v59
v_and_b32 v142, s93, v158
v_mfma_f32_16x16x32_bf16 v[120:123], v[200:203], a[188:191], v[120:123]
v_lshlrev_b32 v158, 16, v158
v_fmac_f32 v28, v158, v60
v_mfma_f32_16x16x32_bf16 v[108:111], v[204:207], a[184:187], v[108:111]
v_fmac_f32 v29, v142, v61
v_and_b32 v143, s93, v159
v_lshlrev_b32 v159, 16, v159
v_mfma_f32_16x16x32_bf16 v[124:127], v[204:207], a[188:191], v[124:127]
v_fmac_f32 v30, v159, v62
v_fmac_f32 v31, v143, v63
s_nop 5
v_pk_fma_f32 v[8:9], v[104:105], v[56:57], v[8:9]
v_pk_fma_f32 v[10:11], v[106:107], v[58:59], v[10:11]
v_pk_fma_f32 v[12:13], v[108:109], v[60:61], v[12:13]
v_pk_fma_f32 v[14:15], v[110:111], v[62:63], v[14:15]
v_cvt_pk_bf16_f32 v8, v8, v9
v_cvt_pk_bf16_f32 v9, v10, v11
v_cvt_pk_bf16_f32 v10, v12, v13
v_cvt_pk_bf16_f32 v11, v14, v15
v_pk_fma_f32 v[24:25], v[120:121], v[56:57], v[24:25]
v_pk_fma_f32 v[26:27], v[122:123], v[58:59], v[26:27]
v_pk_fma_f32 v[28:29], v[124:125], v[60:61], v[28:29]
v_pk_fma_f32 v[30:31], v[126:127], v[62:63], v[30:31]
v_cvt_pk_bf16_f32 v24, v24, v25
v_cvt_pk_bf16_f32 v25, v26, v27
v_cvt_pk_bf16_f32 v26, v28, v29
v_cvt_pk_bf16_f32 v27, v30, v31
v_permlane16_swap_b32 v8, v10
v_permlane16_swap_b32 v9, v11
buffer_store_dwordx4 v[8:11], v241, s[60:63], 0 offen offset:64 sc1
v_permlane16_swap_b32 v24, v26
v_permlane16_swap_b32 v25, v27
buffer_store_dwordx4 v[24:27], v242, s[60:63], 0 offen offset:64 sc1
s_endpgm
LSt2k0_%=:
s_sub_u32 s84, s84, 1
s_cbranch_scc1 LZt2k0_%=
s_xor_b32 s1, s83, 0
buffer_load_dwordx4 a[0:3], v228, s[52:55], s1 offen offset:0 sc1
buffer_load_dwordx4 a[4:7], v229, s[52:55], s1 offen offset:0 sc1
buffer_load_dwordx4 a[8:11], v228, s[52:55], s1 offen offset:1024 sc1
buffer_load_dwordx4 a[12:15], v229, s[52:55], s1 offen offset:1024 sc1
s_xor_b32 s1, s83, 4096
buffer_load_dwordx4 a[16:19], v228, s[52:55], s1 offen offset:0 sc1
buffer_load_dwordx4 a[20:23], v229, s[52:55], s1 offen offset:0 sc1
buffer_load_dwordx4 a[24:27], v228, s[52:55], s1 offen offset:1024 sc1
buffer_load_dwordx4 a[28:31], v229, s[52:55], s1 offen offset:1024 sc1
s_xor_b32 s1, s83, 8192
buffer_load_dwordx4 a[32:35], v228, s[52:55], s1 offen offset:0 sc1
buffer_load_dwordx4 a[36:39], v229, s[52:55], s1 offen offset:0 sc1
buffer_load_dwordx4 a[40:43], v228, s[52:55], s1 offen offset:1024 sc1
buffer_load_dwordx4 a[44:47], v229, s[52:55], s1 offen offset:1024 sc1
s_xor_b32 s1, s83, 12288
buffer_load_dwordx4 a[48:51], v228, s[52:55], s1 offen offset:0 sc1
buffer_load_dwordx4 a[52:55], v229, s[52:55], s1 offen offset:0 sc1
buffer_load_dwordx4 a[56:59], v228, s[52:55], s1 offen offset:1024 sc1
buffer_load_dwordx4 a[60:63], v229, s[52:55], s1 offen offset:1024 sc1
s_xor_b32 s1, s83, 16384
buffer_load_dwordx4 a[64:67], v228, s[52:55], s1 offen offset:0 sc1
buffer_load_dwordx4 a[68:71], v229, s[52:55], s1 offen offset:0 sc1
buffer_load_dwordx4 a[72:75], v228, s[52:55], s1 offen offset:1024 sc1
buffer_load_dwordx4 a[76:79], v229, s[52:55], s1 offen offset:1024 sc1
s_waitcnt vmcnt(0)
v_accvgpr_read_b32 v248, a0
v_accvgpr_read_b32 v249, a4
v_accvgpr_read_b32 v250, a8
v_or3_b32 v248, v248, v249, v250
v_accvgpr_read_b32 v251, a12
v_bitop3_b32 v248, v248, v251, 1 bitop3:0xa8
v_cmp_ne_u32 vcc, 0, v248
s_nop 0
s_cbranch_vccnz LSt2k0_%=
LZt2k0_%=:
s_max_i32 s84, s84, 0
)"
R"(
s_branch LRt2k0_%=
LSt2k1_%=:
s_sub_u32 s84, s84, 1
s_cbranch_scc1 LZt2k1_%=
s_xor_b32 s1, s83, 4096
buffer_load_dwordx4 a[16:19], v228, s[52:55], s1 offen offset:0 sc1
buffer_load_dwordx4 a[20:23], v229, s[52:55], s1 offen offset:0 sc1
buffer_load_dwordx4 a[24:27], v228, s[52:55], s1 offen offset:1024 sc1
buffer_load_dwordx4 a[28:31], v229, s[52:55], s1 offen offset:1024 sc1
s_xor_b32 s1, s83, 8192
buffer_load_dwordx4 a[32:35], v228, s[52:55], s1 offen offset:0 sc1
buffer_load_dwordx4 a[36:39], v229, s[52:55], s1 offen offset:0 sc1
buffer_load_dwordx4 a[40:43], v228, s[52:55], s1 offen offset:1024 sc1
buffer_load_dwordx4 a[44:47], v229, s[52:55], s1 offen offset:1024 sc1
s_xor_b32 s1, s83, 12288
buffer_load_dwordx4 a[48:51], v228, s[52:55], s1 offen offset:0 sc1
buffer_load_dwordx4 a[52:55], v229, s[52:55], s1 offen offset:0 sc1
buffer_load_dwordx4 a[56:59], v228, s[52:55], s1 offen offset:1024 sc1
buffer_load_dwordx4 a[60:63], v229, s[52:55], s1 offen offset:1024 sc1
s_xor_b32 s1, s83, 16384
buffer_load_dwordx4 a[64:67], v228, s[52:55], s1 offen offset:0 sc1
buffer_load_dwordx4 a[68:71], v229, s[52:55], s1 offen offset:0 sc1
buffer_load_dwordx4 a[72:75], v228, s[52:55], s1 offen offset:1024 sc1
buffer_load_dwordx4 a[76:79], v229, s[52:55], s1 offen offset:1024 sc1
s_xor_b32 s1, s83, 20480
buffer_load_dwordx4 a[80:83], v228, s[52:55], s1 offen offset:0 sc1
buffer_load_dwordx4 a[84:87], v229, s[52:55], s1 offen offset:0 sc1
buffer_load_dwordx4 a[88:91], v228, s[52:55], s1 offen offset:1024 sc1
buffer_load_dwordx4 a[92:95], v229, s[52:55], s1 offen offset:1024 sc1
s_waitcnt vmcnt(0)
v_accvgpr_read_b32 v248, a16
v_accvgpr_read_b32 v249, a20
v_accvgpr_read_b32 v250, a24
v_or3_b32 v248, v248, v249, v250
v_accvgpr_read_b32 v251, a28
v_bitop3_b32 v248, v248, v251, 1 bitop3:0xa8
v_cmp_ne_u32 vcc, 0, v248
s_nop 0
s_cbranch_vccnz LSt2k1_%=
LZt2k1_%=:
s_max_i32 s84, s84, 0
s_branch LRt2k1_%=
LSt2k2_%=:
s_sub_u32 s84, s84, 1
s_cbranch_scc1 LZt2k2_%=
s_xor_b32 s1, s83, 8192
buffer_load_dwordx4 a[32:35], v228, s[52:55], s1 offen offset:0 sc1
buffer_load_dwordx4 a[36:39], v229, s[52:55], s1 offen offset:0 sc1
buffer_load_dwordx4 a[40:43], v228, s[52:55], s1 offen offset:1024 sc1
buffer_load_dwordx4 a[44:47], v229, s[52:55], s1 offen offset:1024 sc1
s_xor_b32 s1, s83, 12288
buffer_load_dwordx4 a[48:51], v228, s[52:55], s1 offen offset:0 sc1
buffer_load_dwordx4 a[52:55], v229, s[52:55], s1 offen offset:0 sc1
buffer_load_dwordx4 a[56:59], v228, s[52:55], s1 offen offset:1024 sc1
buffer_load_dwordx4 a[60:63], v229, s[52:55], s1 offen offset:1024 sc1
s_xor_b32 s1, s83, 16384
buffer_load_dwordx4 a[64:67], v228, s[52:55], s1 offen offset:0 sc1
buffer_load_dwordx4 a[68:71], v229, s[52:55], s1 offen offset:0 sc1
buffer_load_dwordx4 a[72:75], v228, s[52:55], s1 offen offset:1024 sc1
buffer_load_dwordx4 a[76:79], v229, s[52:55], s1 offen offset:1024 sc1
s_xor_b32 s1, s83, 20480
buffer_load_dwordx4 a[80:83], v228, s[52:55], s1 offen offset:0 sc1
buffer_load_dwordx4 a[84:87], v229, s[52:55], s1 offen offset:0 sc1
buffer_load_dwordx4 a[88:91], v228, s[52:55], s1 offen offset:1024 sc1
buffer_load_dwordx4 a[92:95], v229, s[52:55], s1 offen offset:1024 sc1
s_xor_b32 s1, s83, 24576
buffer_load_dwordx4 a[96:99], v228, s[52:55], s1 offen offset:0 sc1
s_waitcnt vmcnt(0)
v_accvgpr_read_b32 v248, a32
v_accvgpr_read_b32 v249, a36
v_accvgpr_read_b32 v250, a40
v_or3_b32 v248, v248, v249, v250
v_accvgpr_read_b32 v251, a44
v_bitop3_b32 v248, v248, v251, 1 bitop3:0xa8
v_cmp_ne_u32 vcc, 0, v248
s_nop 0
s_cbranch_vccnz LSt2k2_%=
LZt2k2_%=:
s_max_i32 s84, s84, 0
s_branch LRt2k2_%=
LSt2k3_%=:
s_sub_u32 s84, s84, 1
s_cbranch_scc1 LZt2k3_%=
s_xor_b32 s1, s83, 12288
buffer_load_dwordx4 a[48:51], v228, s[52:55], s1 offen offset:0 sc1
buffer_load_dwordx4 a[52:55], v229, s[52:55], s1 offen offset:0 sc1
buffer_load_dwordx4 a[56:59], v228, s[52:55], s1 offen offset:1024 sc1
buffer_load_dwordx4 a[60:63], v229, s[52:55], s1 offen offset:1024 sc1
s_xor_b32 s1, s83, 16384
buffer_load_dwordx4 a[64:67], v228, s[52:55], s1 offen offset:0 sc1
buffer_load_dwordx4 a[68:71], v229, s[52:55], s1 offen offset:0 sc1
buffer_load_dwordx4 a[72:75], v228, s[52:55], s1 offen offset:1024 sc1
buffer_load_dwordx4 a[76:79], v229, s[52:55], s1 offen offset:1024 sc1
s_xor_b32 s1, s83, 20480
buffer_load_dwordx4 a[80:83], v228, s[52:55], s1 offen offset:0 sc1
buffer_load_dwordx4 a[84:87], v229, s[52:55], s1 offen offset:0 sc1
buffer_load_dwordx4 a[88:91], v228, s[52:55], s1 offen offset:1024 sc1
buffer_load_dwordx4 a[92:95], v229, s[52:55], s1 offen offset:1024 sc1
s_xor_b32 s1, s83, 24576
buffer_load_dwordx4 a[96:99], v228, s[52:55], s1 offen offset:0 sc1
buffer_load_dwordx4 a[100:103], v229, s[52:55], s1 offen offset:0 sc1
buffer_load_dwordx4 a[104:107], v228, s[52:55], s1 offen offset:1024 sc1
buffer_load_dwordx4 a[108:111], v229, s[52:55], s1 offen offset:1024 sc1
s_waitcnt vmcnt(0)
v_accvgpr_read_b32 v248, a48
v_accvgpr_read_b32 v249, a52
v_accvgpr_read_b32 v250, a56
v_or3_b32 v248, v248, v249, v250
v_accvgpr_read_b32 v251, a60
v_bitop3_b32 v248, v248, v251, 1 bitop3:0xa8
v_cmp_ne_u32 vcc, 0, v248
s_nop 0
s_cbranch_vccnz LSt2k3_%=
LZt2k3_%=:
s_max_i32 s84, s84, 0
s_branch LRt2k3_%=
LSt2k4_%=:
s_sub_u32 s84, s84, 1
s_cbranch_scc1 LZt2k4_%=
s_xor_b32 s1, s83, 16384
buffer_load_dwordx4 a[64:67], v228, s[52:55], s1 offen offset:0 sc1
buffer_load_dwordx4 a[68:71], v229, s[52:55], s1 offen offset:0 sc1
buffer_load_dwordx4 a[72:75], v228, s[52:55], s1 offen offset:1024 sc1
buffer_load_dwordx4 a[76:79], v229, s[52:55], s1 offen offset:1024 sc1
s_xor_b32 s1, s83, 20480
buffer_load_dwordx4 a[80:83], v228, s[52:55], s1 offen offset:0 sc1
buffer_load_dwordx4 a[84:87], v229, s[52:55], s1 offen offset:0 sc1
buffer_load_dwordx4 a[88:91], v228, s[52:55], s1 offen offset:1024 sc1
buffer_load_dwordx4 a[92:95], v229, s[52:55], s1 offen offset:1024 sc1
s_xor_b32 s1, s83, 24576
buffer_load_dwordx4 a[96:99], v228, s[52:55], s1 offen offset:0 sc1
buffer_load_dwordx4 a[100:103], v229, s[52:55], s1 offen offset:0 sc1
buffer_load_dwordx4 a[104:107], v228, s[52:55], s1 offen offset:1024 sc1
buffer_load_dwordx4 a[108:111], v229, s[52:55], s1 offen offset:1024 sc1
s_xor_b32 s1, s83, 28672
buffer_load_dwordx4 a[112:115], v228, s[52:55], s1 offen offset:0 sc1
buffer_load_dwordx4 a[116:119], v229, s[52:55], s1 offen offset:0 sc1
buffer_load_dwordx4 a[120:123], v228, s[52:55], s1 offen offset:1024 sc1
buffer_load_dwordx4 a[124:127], v229, s[52:55], s1 offen offset:1024 sc1
s_waitcnt vmcnt(0)
v_accvgpr_read_b32 v248, a64
v_accvgpr_read_b32 v249, a68
v_accvgpr_read_b32 v250, a72
v_or3_b32 v248, v248, v249, v250
v_accvgpr_read_b32 v251, a76
v_bitop3_b32 v248, v248, v251, 1 bitop3:0xa8
v_cmp_ne_u32 vcc, 0, v248
s_nop 0
s_cbranch_vccnz LSt2k4_%=
LZt2k4_%=:
s_max_i32 s84, s84, 0
s_branch LRt2k4_%=
LSt2k5_%=:
s_sub_u32 s84, s84, 1
s_cbranch_scc1 LZt2k5_%=
s_xor_b32 s1, s83, 20480
buffer_load_dwordx4 a[80:83], v228, s[52:55], s1 offen offset:0 sc1
buffer_load_dwordx4 a[84:87], v229, s[52:55], s1 offen offset:0 sc1
buffer_load_dwordx4 a[88:91], v228, s[52:55], s1 offen offset:1024 sc1
buffer_load_dwordx4 a[92:95], v229, s[52:55], s1 offen offset:1024 sc1
s_xor_b32 s1, s83, 24576
buffer_load_dwordx4 a[96:99], v228, s[52:55], s1 offen offset:0 sc1
buffer_load_dwordx4 a[100:103], v229, s[52:55], s1 offen offset:0 sc1
buffer_load_dwordx4 a[104:107], v228, s[52:55], s1 offen offset:1024 sc1
buffer_load_dwordx4 a[108:111], v229, s[52:55], s1 offen offset:1024 sc1
s_xor_b32 s1, s83, 28672
buffer_load_dwordx4 a[112:115], v228, s[52:55], s1 offen offset:0 sc1
buffer_load_dwordx4 a[116:119], v229, s[52:55], s1 offen offset:0 sc1
buffer_load_dwordx4 a[120:123], v228, s[52:55], s1 offen offset:1024 sc1
buffer_load_dwordx4 a[124:127], v229, s[52:55], s1 offen offset:1024 sc1
s_xor_b32 s1, s83, 2048
buffer_load_dwordx4 a[128:131], v228, s[52:55], s1 offen offset:0 sc1
buffer_load_dwordx4 a[132:135], v229, s[52:55], s1 offen offset:0 sc1
buffer_load_dwordx4 a[136:139], v228, s[52:55], s1 offen offset:1024 sc1
buffer_load_dwordx4 a[140:143], v229, s[52:55], s1 offen offset:1024 sc1
s_waitcnt vmcnt(0)
v_accvgpr_read_b32 v248, a80
v_accvgpr_read_b32 v249, a84
v_accvgpr_read_b32 v250, a88
v_or3_b32 v248, v248, v249, v250
v_accvgpr_read_b32 v251, a92
v_bitop3_b32 v248, v248, v251, 1 bitop3:0xa8
v_cmp_ne_u32 vcc, 0, v248
s_nop 0
s_cbranch_vccnz LSt2k5_%=
LZt2k5_%=:
s_max_i32 s84, s84, 0
s_branch LRt2k5_%=
LSt2k6_%=:
s_sub_u32 s84, s84, 1
s_cbranch_scc1 LZt2k6_%=
s_xor_b32 s1, s83, 24576
buffer_load_dwordx4 a[96:99], v228, s[52:55], s1 offen offset:0 sc1
buffer_load_dwordx4 a[100:103], v229, s[52:55], s1 offen offset:0 sc1
buffer_load_dwordx4 a[104:107], v228, s[52:55], s1 offen offset:1024 sc1
buffer_load_dwordx4 a[108:111], v229, s[52:55], s1 offen offset:1024 sc1
s_xor_b32 s1, s83, 28672
buffer_load_dwordx4 a[112:115], v228, s[52:55], s1 offen offset:0 sc1
buffer_load_dwordx4 a[116:119], v229, s[52:55], s1 offen offset:0 sc1
buffer_load_dwordx4 a[120:123], v228, s[52:55], s1 offen offset:1024 sc1
buffer_load_dwordx4 a[124:127], v229, s[52:55], s1 offen offset:1024 sc1
s_xor_b32 s1, s83, 2048
buffer_load_dwordx4 a[128:131], v228, s[52:55], s1 offen offset:0 sc1
buffer_load_dwordx4 a[132:135], v229, s[52:55], s1 offen offset:0 sc1
buffer_load_dwordx4 a[136:139], v228, s[52:55], s1 offen offset:1024 sc1
buffer_load_dwordx4 a[140:143], v229, s[52:55], s1 offen offset:1024 sc1
s_xor_b32 s1, s83, 6144
buffer_load_dwordx4 a[144:147], v228, s[52:55], s1 offen offset:0 sc1
buffer_load_dwordx4 a[148:151], v229, s[52:55], s1 offen offset:0 sc1
buffer_load_dwordx4 a[152:155], v228, s[52:55], s1 offen offset:1024 sc1
buffer_load_dwordx4 a[156:159], v229, s[52:55], s1 offen offset:1024 sc1
s_waitcnt vmcnt(0)
v_accvgpr_read_b32 v248, a96
v_accvgpr_read_b32 v249, a100
v_accvgpr_read_b32 v250, a104
v_or3_b32 v248, v248, v249, v250
v_accvgpr_read_b32 v251, a108
v_bitop3_b32 v248, v248, v251, 1 bitop3:0xa8
v_cmp_ne_u32 vcc, 0, v248
s_nop 0
s_cbranch_vccnz LSt2k6_%=
LZt2k6_%=:
s_max_i32 s84, s84, 0
s_branch LRt2k6_%=
LSt2k7_%=:
s_sub_u32 s84, s84, 1
s_cbranch_scc1 LZt2k7_%=
s_xor_b32 s1, s83, 28672
buffer_load_dwordx4 a[112:115], v228, s[52:55], s1 offen offset:0 sc1
buffer_load_dwordx4 a[116:119], v229, s[52:55], s1 offen offset:0 sc1
buffer_load_dwordx4 a[120:123], v228, s[52:55], s1 offen offset:1024 sc1
buffer_load_dwordx4 a[124:127], v229, s[52:55], s1 offen offset:1024 sc1
s_xor_b32 s1, s83, 2048
buffer_load_dwordx4 a[128:131], v228, s[52:55], s1 offen offset:0 sc1
buffer_load_dwordx4 a[132:135], v229, s[52:55], s1 offen offset:0 sc1
buffer_load_dwordx4 a[136:139], v228, s[52:55], s1 offen offset:1024 sc1
buffer_load_dwordx4 a[140:143], v229, s[52:55], s1 offen offset:1024 sc1
s_xor_b32 s1, s83, 6144
buffer_load_dwordx4 a[144:147], v228, s[52:55], s1 offen offset:0 sc1
buffer_load_dwordx4 a[148:151], v229, s[52:55], s1 offen offset:0 sc1
buffer_load_dwordx4 a[152:155], v228, s[52:55], s1 offen offset:1024 sc1
buffer_load_dwordx4 a[156:159], v229, s[52:55], s1 offen offset:1024 sc1
s_xor_b32 s1, s83, 10240
buffer_load_dwordx4 a[160:163], v228, s[52:55], s1 offen offset:0 sc1
buffer_load_dwordx4 a[164:167], v229, s[52:55], s1 offen offset:0 sc1
buffer_load_dwordx4 a[168:171], v228, s[52:55], s1 offen offset:1024 sc1
buffer_load_dwordx4 a[172:175], v229, s[52:55], s1 offen offset:1024 sc1
s_xor_b32 s1, s83, 14336
buffer_load_dwordx4 a[176:179], v228, s[52:55], s1 offen offset:0 sc1
buffer_load_dwordx4 a[180:183], v229, s[52:55], s1 offen offset:0 sc1
buffer_load_dwordx4 a[184:187], v228, s[52:55], s1 offen offset:1024 sc1
s_waitcnt vmcnt(0)
v_accvgpr_read_b32 v248, a112
v_accvgpr_read_b32 v249, a116
v_accvgpr_read_b32 v250, a120
v_or3_b32 v248, v248, v249, v250
v_accvgpr_read_b32 v251, a124
v_bitop3_b32 v248, v248, v251, 1 bitop3:0xa8
v_cmp_ne_u32 vcc, 0, v248
s_nop 0
s_cbranch_vccnz LSt2k7_%=
LZt2k7_%=:
s_max_i32 s84, s84, 0
s_branch LRt2k7_%=
LSt2k8_%=:
s_sub_u32 s84, s84, 1
s_cbranch_scc1 LZt2k8_%=
s_xor_b32 s1, s83, 2048
buffer_load_dwordx4 a[128:131], v228, s[52:55], s1 offen offset:0 sc1
buffer_load_dwordx4 a[132:135], v229, s[52:55], s1 offen offset:0 sc1
buffer_load_dwordx4 a[136:139], v228, s[52:55], s1 offen offset:1024 sc1
buffer_load_dwordx4 a[140:143], v229, s[52:55], s1 offen offset:1024 sc1
s_xor_b32 s1, s83, 6144
buffer_load_dwordx4 a[144:147], v228, s[52:55], s1 offen offset:0 sc1
buffer_load_dwordx4 a[148:151], v229, s[52:55], s1 offen offset:0 sc1
buffer_load_dwordx4 a[152:155], v228, s[52:55], s1 offen offset:1024 sc1
buffer_load_dwordx4 a[156:159], v229, s[52:55], s1 offen offset:1024 sc1
s_xor_b32 s1, s83, 10240
buffer_load_dwordx4 a[160:163], v228, s[52:55], s1 offen offset:0 sc1
buffer_load_dwordx4 a[164:167], v229, s[52:55], s1 offen offset:0 sc1
buffer_load_dwordx4 a[168:171], v228, s[52:55], s1 offen offset:1024 sc1
buffer_load_dwordx4 a[172:175], v229, s[52:55], s1 offen offset:1024 sc1
s_xor_b32 s1, s83, 14336
buffer_load_dwordx4 a[176:179], v228, s[52:55], s1 offen offset:0 sc1
buffer_load_dwordx4 a[180:183], v229, s[52:55], s1 offen offset:0 sc1
buffer_load_dwordx4 a[184:187], v228, s[52:55], s1 offen offset:1024 sc1
buffer_load_dwordx4 a[188:191], v229, s[52:55], s1 offen offset:1024 sc1
s_xor_b32 s1, s83, 18432
buffer_load_dwordx4 a[192:195], v228, s[52:55], s1 offen offset:0 sc1
buffer_load_dwordx4 a[196:199], v229, s[52:55], s1 offen offset:0 sc1
buffer_load_dwordx4 a[200:203], v228, s[52:55], s1 offen offset:1024 sc1
buffer_load_dwordx4 a[204:207], v229, s[52:55], s1 offen offset:1024 sc1
s_xor_b32 s1, s83, 22528
buffer_load_dwordx4 a[208:211], v228, s[52:55], s1 offen offset:0 sc1
s_waitcnt vmcnt(0)
v_accvgpr_read_b32 v248, a128
v_accvgpr_read_b32 v249, a132
v_accvgpr_read_b32 v250, a136
v_or3_b32 v248, v248, v249, v250
v_accvgpr_read_b32 v251, a140
v_accvgpr_read_b32 v252, a144
v_or3_b32 v248, v248, v251, v252
v_accvgpr_read_b32 v249, a148
v_accvgpr_read_b32 v250, a152
v_or3_b32 v248, v248, v249, v250
v_accvgpr_read_b32 v251, a156
v_bitop3_b32 v248, v248, v251, 1 bitop3:0xa8
v_cmp_ne_u32 vcc, 0, v248
s_nop 0
s_cbranch_vccnz LSt2k8_%=
LZt2k8_%=:
s_max_i32 s84, s84, 0
s_branch LRt2k8_%=
LSt2k10_%=:
s_sub_u32 s84, s84, 1
s_cbranch_scc1 LZt2k10_%=
s_xor_b32 s1, s83, 10240
buffer_load_dwordx4 a[160:163], v228, s[52:55], s1 offen offset:0 sc1
buffer_load_dwordx4 a[164:167], v229, s[52:55], s1 offen offset:0 sc1
buffer_load_dwordx4 a[168:171], v228, s[52:55], s1 offen offset:1024 sc1
buffer_load_dwordx4 a[172:175], v229, s[52:55], s1 offen offset:1024 sc1
s_xor_b32 s1, s83, 14336
buffer_load_dwordx4 a[176:179], v228, s[52:55], s1 offen offset:0 sc1
buffer_load_dwordx4 a[180:183], v229, s[52:55], s1 offen offset:0 sc1
buffer_load_dwordx4 a[184:187], v228, s[52:55], s1 offen offset:1024 sc1
buffer_load_dwordx4 a[188:191], v229, s[52:55], s1 offen offset:1024 sc1
s_xor_b32 s1, s83, 18432
buffer_load_dwordx4 a[192:195], v228, s[52:55], s1 offen offset:0 sc1
buffer_load_dwordx4 a[196:199], v229, s[52:55], s1 offen offset:0 sc1
buffer_load_dwordx4 a[200:203], v228, s[52:55], s1 offen offset:1024 sc1
buffer_load_dwordx4 a[204:207], v229, s[52:55], s1 offen offset:1024 sc1
s_xor_b32 s1, s83, 22528
buffer_load_dwordx4 a[208:211], v228, s[52:55], s1 offen offset:0 sc1
buffer_load_dwordx4 a[212:215], v229, s[52:55], s1 offen offset:0 sc1
buffer_load_dwordx4 a[216:219], v228, s[52:55], s1 offen offset:1024 sc1
buffer_load_dwordx4 a[220:223], v229, s[52:55], s1 offen offset:1024 sc1
s_xor_b32 s1, s83, 26624
buffer_load_dwordx4 a[224:227], v228, s[52:55], s1 offen offset:0 sc1
buffer_load_dwordx4 a[228:231], v229, s[52:55], s1 offen offset:0 sc1
buffer_load_dwordx4 a[232:235], v228, s[52:55], s1 offen offset:1024 sc1
s_waitcnt vmcnt(0)
v_accvgpr_read_b32 v248, a160
v_accvgpr_read_b32 v249, a164
v_accvgpr_read_b32 v250, a168
v_or3_b32 v248, v248, v249, v250
v_accvgpr_read_b32 v251, a172
v_accvgpr_read_b32 v252, a176
v_or3_b32 v248, v248, v251, v252
v_accvgpr_read_b32 v249, a180
v_accvgpr_read_b32 v250, a184
v_or3_b32 v248, v248, v249, v250
v_accvgpr_read_b32 v251, a188
v_bitop3_b32 v248, v248, v251, 1 bitop3:0xa8
v_cmp_ne_u32 vcc, 0, v248
s_nop 0
s_cbranch_vccnz LSt2k10_%=
LZt2k10_%=:
s_max_i32 s84, s84, 0
s_branch LRt2k10_%=
LSt2k12_%=:
s_sub_u32 s84, s84, 1
s_cbranch_scc1 LZt2k12_%=
s_xor_b32 s1, s83, 18432
buffer_load_dwordx4 a[192:195], v228, s[52:55], s1 offen offset:0 sc1
buffer_load_dwordx4 a[196:199], v229, s[52:55], s1 offen offset:0 sc1
buffer_load_dwordx4 a[200:203], v228, s[52:55], s1 offen offset:1024 sc1
buffer_load_dwordx4 a[204:207], v229, s[52:55], s1 offen offset:1024 sc1
s_xor_b32 s1, s83, 22528
buffer_load_dwordx4 a[208:211], v228, s[52:55], s1 offen offset:0 sc1
buffer_load_dwordx4 a[212:215], v229, s[52:55], s1 offen offset:0 sc1
buffer_load_dwordx4 a[216:219], v228, s[52:55], s1 offen offset:1024 sc1
buffer_load_dwordx4 a[220:223], v229, s[52:55], s1 offen offset:1024 sc1
s_xor_b32 s1, s83, 26624
buffer_load_dwordx4 a[224:227], v228, s[52:55], s1 offen offset:0 sc1
buffer_load_dwordx4 a[228:231], v229, s[52:55], s1 offen offset:0 sc1
buffer_load_dwordx4 a[232:235], v228, s[52:55], s1 offen offset:1024 sc1
buffer_load_dwordx4 a[236:239], v229, s[52:55], s1 offen offset:1024 sc1
s_xor_b32 s1, s83, 30720
buffer_load_dwordx4 a[240:243], v228, s[52:55], s1 offen offset:0 sc1
buffer_load_dwordx4 a[244:247], v229, s[52:55], s1 offen offset:0 sc1
buffer_load_dwordx4 a[248:251], v228, s[52:55], s1 offen offset:1024 sc1
buffer_load_dwordx4 a[252:255], v229, s[52:55], s1 offen offset:1024 sc1
s_waitcnt vmcnt(0)
v_accvgpr_read_b32 v248, a192
v_accvgpr_read_b32 v249, a196
v_accvgpr_read_b32 v250, a200
v_or3_b32 v248, v248, v249, v250
v_accvgpr_read_b32 v251, a204
v_accvgpr_read_b32 v252, a208
v_or3_b32 v248, v248, v251, v252
v_accvgpr_read_b32 v249, a212
v_accvgpr_read_b32 v250, a216
v_or3_b32 v248, v248, v249, v250
v_accvgpr_read_b32 v251, a220
v_bitop3_b32 v248, v248, v251, 1 bitop3:0xa8
v_cmp_ne_u32 vcc, 0, v248
s_nop 0
s_cbranch_vccnz LSt2k12_%=
LZt2k12_%=:
s_max_i32 s84, s84, 0
)"
R"(
s_branch LRt2k12_%=
LSt2k14_%=:
s_sub_u32 s84, s84, 1
s_cbranch_scc1 LZt2k14_%=
s_xor_b32 s1, s83, 26624
buffer_load_dwordx4 a[224:227], v228, s[52:55], s1 offen offset:0 sc1
buffer_load_dwordx4 a[228:231], v229, s[52:55], s1 offen offset:0 sc1
buffer_load_dwordx4 a[232:235], v228, s[52:55], s1 offen offset:1024 sc1
buffer_load_dwordx4 a[236:239], v229, s[52:55], s1 offen offset:1024 sc1
s_xor_b32 s1, s83, 30720
buffer_load_dwordx4 a[240:243], v228, s[52:55], s1 offen offset:0 sc1
buffer_load_dwordx4 a[244:247], v229, s[52:55], s1 offen offset:0 sc1
buffer_load_dwordx4 a[248:251], v228, s[52:55], s1 offen offset:1024 sc1
buffer_load_dwordx4 a[252:255], v229, s[52:55], s1 offen offset:1024 sc1
s_waitcnt vmcnt(0)
v_accvgpr_read_b32 v248, a224
v_accvgpr_read_b32 v249, a228
v_accvgpr_read_b32 v250, a232
v_or3_b32 v248, v248, v249, v250
v_accvgpr_read_b32 v251, a236
v_accvgpr_read_b32 v252, a240
v_or3_b32 v248, v248, v251, v252
v_accvgpr_read_b32 v249, a244
v_accvgpr_read_b32 v250, a248
v_or3_b32 v248, v248, v249, v250
v_accvgpr_read_b32 v251, a252
v_bitop3_b32 v248, v248, v251, 1 bitop3:0xa8
v_cmp_ne_u32 vcc, 0, v248
s_nop 0
s_cbranch_vccnz LSt2k14_%=
LZt2k14_%=:
s_max_i32 s84, s84, 0
s_branch LRt2k14_%=
LSt3k0_%=:
s_sub_u32 s84, s84, 1
s_cbranch_scc1 LZt3k0_%=
s_xor_b32 s1, s83, 0
buffer_load_dwordx4 a[0:3], v228, s[52:55], s1 offen offset:0 sc1
buffer_load_dwordx4 a[4:7], v229, s[52:55], s1 offen offset:0 sc1
buffer_load_dwordx4 a[8:11], v228, s[52:55], s1 offen offset:1024 sc1
buffer_load_dwordx4 a[12:15], v229, s[52:55], s1 offen offset:1024 sc1
s_xor_b32 s1, s83, 2048
buffer_load_dwordx4 a[16:19], v228, s[52:55], s1 offen offset:0 sc1
buffer_load_dwordx4 a[20:23], v229, s[52:55], s1 offen offset:0 sc1
buffer_load_dwordx4 a[24:27], v228, s[52:55], s1 offen offset:1024 sc1
buffer_load_dwordx4 a[28:31], v229, s[52:55], s1 offen offset:1024 sc1
s_xor_b32 s1, s83, 4096
buffer_load_dwordx4 a[32:35], v228, s[52:55], s1 offen offset:0 sc1
buffer_load_dwordx4 a[36:39], v229, s[52:55], s1 offen offset:0 sc1
buffer_load_dwordx4 a[40:43], v228, s[52:55], s1 offen offset:1024 sc1
buffer_load_dwordx4 a[44:47], v229, s[52:55], s1 offen offset:1024 sc1
s_waitcnt vmcnt(0)
v_accvgpr_read_b32 v248, a0
v_accvgpr_read_b32 v249, a4
v_accvgpr_read_b32 v250, a8
v_bitop3_b32 v248, v248, v249, v250 bitop3:0x80
v_accvgpr_read_b32 v251, a12
v_bitop3_b32 v248, v248, v251, 1 bitop3:0x80
v_cmp_ne_u32 vcc, 1, v248
s_nop 0
s_cbranch_vccnz LSt3k0_%=
LZt3k0_%=:
s_max_i32 s84, s84, 0
s_branch LRt3k0_%=
LSt3k1_%=:
s_sub_u32 s84, s84, 1
s_cbranch_scc1 LZt3k1_%=
s_xor_b32 s1, s83, 2048
buffer_load_dwordx4 a[16:19], v228, s[52:55], s1 offen offset:0 sc1
buffer_load_dwordx4 a[20:23], v229, s[52:55], s1 offen offset:0 sc1
buffer_load_dwordx4 a[24:27], v228, s[52:55], s1 offen offset:1024 sc1
buffer_load_dwordx4 a[28:31], v229, s[52:55], s1 offen offset:1024 sc1
s_xor_b32 s1, s83, 4096
buffer_load_dwordx4 a[32:35], v228, s[52:55], s1 offen offset:0 sc1
buffer_load_dwordx4 a[36:39], v229, s[52:55], s1 offen offset:0 sc1
buffer_load_dwordx4 a[40:43], v228, s[52:55], s1 offen offset:1024 sc1
buffer_load_dwordx4 a[44:47], v229, s[52:55], s1 offen offset:1024 sc1
s_xor_b32 s1, s83, 6144
buffer_load_dwordx4 a[48:51], v228, s[52:55], s1 offen offset:0 sc1
buffer_load_dwordx4 a[52:55], v229, s[52:55], s1 offen offset:0 sc1
buffer_load_dwordx4 a[56:59], v228, s[52:55], s1 offen offset:1024 sc1
s_waitcnt vmcnt(0)
v_accvgpr_read_b32 v248, a16
v_accvgpr_read_b32 v249, a20
v_accvgpr_read_b32 v250, a24
v_bitop3_b32 v248, v248, v249, v250 bitop3:0x80
v_accvgpr_read_b32 v251, a28
v_bitop3_b32 v248, v248, v251, 1 bitop3:0x80
v_cmp_ne_u32 vcc, 1, v248
s_nop 0
s_cbranch_vccnz LSt3k1_%=
LZt3k1_%=:
s_max_i32 s84, s84, 0
s_branch LRt3k1_%=
LSt3k2_%=:
s_sub_u32 s84, s84, 1
s_cbranch_scc1 LZt3k2_%=
s_xor_b32 s1, s83, 4096
buffer_load_dwordx4 a[32:35], v228, s[52:55], s1 offen offset:0 sc1
buffer_load_dwordx4 a[36:39], v229, s[52:55], s1 offen offset:0 sc1
buffer_load_dwordx4 a[40:43], v228, s[52:55], s1 offen offset:1024 sc1
buffer_load_dwordx4 a[44:47], v229, s[52:55], s1 offen offset:1024 sc1
s_xor_b32 s1, s83, 6144
buffer_load_dwordx4 a[48:51], v228, s[52:55], s1 offen offset:0 sc1
buffer_load_dwordx4 a[52:55], v229, s[52:55], s1 offen offset:0 sc1
buffer_load_dwordx4 a[56:59], v228, s[52:55], s1 offen offset:1024 sc1
buffer_load_dwordx4 a[60:63], v229, s[52:55], s1 offen offset:1024 sc1
s_xor_b32 s1, s83, 8192
buffer_load_dwordx4 a[64:67], v228, s[52:55], s1 offen offset:0 sc1
buffer_load_dwordx4 a[68:71], v229, s[52:55], s1 offen offset:0 sc1
buffer_load_dwordx4 a[72:75], v228, s[52:55], s1 offen offset:1024 sc1
s_waitcnt vmcnt(0)
v_accvgpr_read_b32 v248, a32
v_accvgpr_read_b32 v249, a36
v_accvgpr_read_b32 v250, a40
v_bitop3_b32 v248, v248, v249, v250 bitop3:0x80
v_accvgpr_read_b32 v251, a44
v_bitop3_b32 v248, v248, v251, 1 bitop3:0x80
v_cmp_ne_u32 vcc, 1, v248
s_nop 0
s_cbranch_vccnz LSt3k2_%=
LZt3k2_%=:
s_max_i32 s84, s84, 0
s_branch LRt3k2_%=
LSt3k3_%=:
s_sub_u32 s84, s84, 1
s_cbranch_scc1 LZt3k3_%=
s_xor_b32 s1, s83, 6144
buffer_load_dwordx4 a[48:51], v228, s[52:55], s1 offen offset:0 sc1
buffer_load_dwordx4 a[52:55], v229, s[52:55], s1 offen offset:0 sc1
buffer_load_dwordx4 a[56:59], v228, s[52:55], s1 offen offset:1024 sc1
buffer_load_dwordx4 a[60:63], v229, s[52:55], s1 offen offset:1024 sc1
s_xor_b32 s1, s83, 8192
buffer_load_dwordx4 a[64:67], v228, s[52:55], s1 offen offset:0 sc1
buffer_load_dwordx4 a[68:71], v229, s[52:55], s1 offen offset:0 sc1
buffer_load_dwordx4 a[72:75], v228, s[52:55], s1 offen offset:1024 sc1
buffer_load_dwordx4 a[76:79], v229, s[52:55], s1 offen offset:1024 sc1
s_xor_b32 s1, s83, 10240
buffer_load_dwordx4 a[80:83], v228, s[52:55], s1 offen offset:0 sc1
buffer_load_dwordx4 a[84:87], v229, s[52:55], s1 offen offset:0 sc1
buffer_load_dwordx4 a[88:91], v228, s[52:55], s1 offen offset:1024 sc1
buffer_load_dwordx4 a[92:95], v229, s[52:55], s1 offen offset:1024 sc1
s_waitcnt vmcnt(0)
v_accvgpr_read_b32 v248, a48
v_accvgpr_read_b32 v249, a52
v_accvgpr_read_b32 v250, a56
v_bitop3_b32 v248, v248, v249, v250 bitop3:0x80
v_accvgpr_read_b32 v251, a60
v_bitop3_b32 v248, v248, v251, 1 bitop3:0x80
v_cmp_ne_u32 vcc, 1, v248
s_nop 0
s_cbranch_vccnz LSt3k3_%=
LZt3k3_%=:
s_max_i32 s84, s84, 0
s_branch LRt3k3_%=
LSt3k4_%=:
s_sub_u32 s84, s84, 1
s_cbranch_scc1 LZt3k4_%=
s_xor_b32 s1, s83, 8192
buffer_load_dwordx4 a[64:67], v228, s[52:55], s1 offen offset:0 sc1
buffer_load_dwordx4 a[68:71], v229, s[52:55], s1 offen offset:0 sc1
buffer_load_dwordx4 a[72:75], v228, s[52:55], s1 offen offset:1024 sc1
buffer_load_dwordx4 a[76:79], v229, s[52:55], s1 offen offset:1024 sc1
s_xor_b32 s1, s83, 10240
buffer_load_dwordx4 a[80:83], v228, s[52:55], s1 offen offset:0 sc1
buffer_load_dwordx4 a[84:87], v229, s[52:55], s1 offen offset:0 sc1
buffer_load_dwordx4 a[88:91], v228, s[52:55], s1 offen offset:1024 sc1
buffer_load_dwordx4 a[92:95], v229, s[52:55], s1 offen offset:1024 sc1
s_xor_b32 s1, s83, 12288
buffer_load_dwordx4 a[96:99], v228, s[52:55], s1 offen offset:0 sc1
buffer_load_dwordx4 a[100:103], v229, s[52:55], s1 offen offset:0 sc1
buffer_load_dwordx4 a[104:107], v228, s[52:55], s1 offen offset:1024 sc1
buffer_load_dwordx4 a[108:111], v229, s[52:55], s1 offen offset:1024 sc1
s_waitcnt vmcnt(0)
v_accvgpr_read_b32 v248, a64
v_accvgpr_read_b32 v249, a68
v_accvgpr_read_b32 v250, a72
v_bitop3_b32 v248, v248, v249, v250 bitop3:0x80
v_accvgpr_read_b32 v251, a76
v_bitop3_b32 v248, v248, v251, 1 bitop3:0x80
v_cmp_ne_u32 vcc, 1, v248
s_nop 0
s_cbranch_vccnz LSt3k4_%=
LZt3k4_%=:
s_max_i32 s84, s84, 0
s_branch LRt3k4_%=
LSt3k5_%=:
s_sub_u32 s84, s84, 1
s_cbranch_scc1 LZt3k5_%=
s_xor_b32 s1, s83, 10240
buffer_load_dwordx4 a[80:83], v228, s[52:55], s1 offen offset:0 sc1
buffer_load_dwordx4 a[84:87], v229, s[52:55], s1 offen offset:0 sc1
buffer_load_dwordx4 a[88:91], v228, s[52:55], s1 offen offset:1024 sc1
buffer_load_dwordx4 a[92:95], v229, s[52:55], s1 offen offset:1024 sc1
s_xor_b32 s1, s83, 12288
buffer_load_dwordx4 a[96:99], v228, s[52:55], s1 offen offset:0 sc1
buffer_load_dwordx4 a[100:103], v229, s[52:55], s1 offen offset:0 sc1
buffer_load_dwordx4 a[104:107], v228, s[52:55], s1 offen offset:1024 sc1
buffer_load_dwordx4 a[108:111], v229, s[52:55], s1 offen offset:1024 sc1
s_xor_b32 s1, s83, 14336
buffer_load_dwordx4 a[112:115], v228, s[52:55], s1 offen offset:0 sc1
buffer_load_dwordx4 a[116:119], v229, s[52:55], s1 offen offset:0 sc1
buffer_load_dwordx4 a[120:123], v228, s[52:55], s1 offen offset:1024 sc1
buffer_load_dwordx4 a[124:127], v229, s[52:55], s1 offen offset:1024 sc1
s_waitcnt vmcnt(0)
v_accvgpr_read_b32 v248, a80
v_accvgpr_read_b32 v249, a84
v_accvgpr_read_b32 v250, a88
v_bitop3_b32 v248, v248, v249, v250 bitop3:0x80
v_accvgpr_read_b32 v251, a92
v_bitop3_b32 v248, v248, v251, 1 bitop3:0x80
v_cmp_ne_u32 vcc, 1, v248
s_nop 0
s_cbranch_vccnz LSt3k5_%=
LZt3k5_%=:
s_max_i32 s84, s84, 0
s_branch LRt3k5_%=
LSt3k6_%=:
s_sub_u32 s84, s84, 1
s_cbranch_scc1 LZt3k6_%=
s_xor_b32 s1, s83, 12288
buffer_load_dwordx4 a[96:99], v228, s[52:55], s1 offen offset:0 sc1
buffer_load_dwordx4 a[100:103], v229, s[52:55], s1 offen offset:0 sc1
buffer_load_dwordx4 a[104:107], v228, s[52:55], s1 offen offset:1024 sc1
buffer_load_dwordx4 a[108:111], v229, s[52:55], s1 offen offset:1024 sc1
s_xor_b32 s1, s83, 14336
buffer_load_dwordx4 a[112:115], v228, s[52:55], s1 offen offset:0 sc1
buffer_load_dwordx4 a[116:119], v229, s[52:55], s1 offen offset:0 sc1
buffer_load_dwordx4 a[120:123], v228, s[52:55], s1 offen offset:1024 sc1
buffer_load_dwordx4 a[124:127], v229, s[52:55], s1 offen offset:1024 sc1
s_xor_b32 s1, s83, 16384
buffer_load_dwordx4 a[128:131], v228, s[52:55], s1 offen offset:0 sc1
buffer_load_dwordx4 a[132:135], v229, s[52:55], s1 offen offset:0 sc1
buffer_load_dwordx4 a[136:139], v228, s[52:55], s1 offen offset:1024 sc1
buffer_load_dwordx4 a[140:143], v229, s[52:55], s1 offen offset:1024 sc1
s_waitcnt vmcnt(0)
v_accvgpr_read_b32 v248, a96
v_accvgpr_read_b32 v249, a100
v_accvgpr_read_b32 v250, a104
v_bitop3_b32 v248, v248, v249, v250 bitop3:0x80
v_accvgpr_read_b32 v251, a108
v_bitop3_b32 v248, v248, v251, 1 bitop3:0x80
v_cmp_ne_u32 vcc, 1, v248
s_nop 0
s_cbranch_vccnz LSt3k6_%=
LZt3k6_%=:
s_max_i32 s84, s84, 0
s_branch LRt3k6_%=
LSt3k7_%=:
s_sub_u32 s84, s84, 1
s_cbranch_scc1 LZt3k7_%=
s_xor_b32 s1, s83, 14336
buffer_load_dwordx4 a[112:115], v228, s[52:55], s1 offen offset:0 sc1
buffer_load_dwordx4 a[116:119], v229, s[52:55], s1 offen offset:0 sc1
buffer_load_dwordx4 a[120:123], v228, s[52:55], s1 offen offset:1024 sc1
buffer_load_dwordx4 a[124:127], v229, s[52:55], s1 offen offset:1024 sc1
s_xor_b32 s1, s83, 16384
buffer_load_dwordx4 a[128:131], v228, s[52:55], s1 offen offset:0 sc1
buffer_load_dwordx4 a[132:135], v229, s[52:55], s1 offen offset:0 sc1
buffer_load_dwordx4 a[136:139], v228, s[52:55], s1 offen offset:1024 sc1
buffer_load_dwordx4 a[140:143], v229, s[52:55], s1 offen offset:1024 sc1
s_xor_b32 s1, s83, 18432
buffer_load_dwordx4 a[144:147], v228, s[52:55], s1 offen offset:0 sc1
buffer_load_dwordx4 a[148:151], v229, s[52:55], s1 offen offset:0 sc1
buffer_load_dwordx4 a[152:155], v228, s[52:55], s1 offen offset:1024 sc1
buffer_load_dwordx4 a[156:159], v229, s[52:55], s1 offen offset:1024 sc1
s_xor_b32 s1, s83, 20480
buffer_load_dwordx4 a[160:163], v228, s[52:55], s1 offen offset:0 sc1
s_waitcnt vmcnt(0)
v_accvgpr_read_b32 v248, a112
v_accvgpr_read_b32 v249, a116
v_accvgpr_read_b32 v250, a120
v_bitop3_b32 v248, v248, v249, v250 bitop3:0x80
v_accvgpr_read_b32 v251, a124
v_bitop3_b32 v248, v248, v251, 1 bitop3:0x80
v_cmp_ne_u32 vcc, 1, v248
s_nop 0
s_cbranch_vccnz LSt3k7_%=
LZt3k7_%=:
s_max_i32 s84, s84, 0
s_branch LRt3k7_%=
LSt3k8_%=:
s_sub_u32 s84, s84, 1
s_cbranch_scc1 LZt3k8_%=
s_xor_b32 s1, s83, 16384
buffer_load_dwordx4 a[128:131], v228, s[52:55], s1 offen offset:0 sc1
buffer_load_dwordx4 a[132:135], v229, s[52:55], s1 offen offset:0 sc1
buffer_load_dwordx4 a[136:139], v228, s[52:55], s1 offen offset:1024 sc1
buffer_load_dwordx4 a[140:143], v229, s[52:55], s1 offen offset:1024 sc1
s_xor_b32 s1, s83, 18432
buffer_load_dwordx4 a[144:147], v228, s[52:55], s1 offen offset:0 sc1
buffer_load_dwordx4 a[148:151], v229, s[52:55], s1 offen offset:0 sc1
buffer_load_dwordx4 a[152:155], v228, s[52:55], s1 offen offset:1024 sc1
buffer_load_dwordx4 a[156:159], v229, s[52:55], s1 offen offset:1024 sc1
s_xor_b32 s1, s83, 20480
buffer_load_dwordx4 a[160:163], v228, s[52:55], s1 offen offset:0 sc1
buffer_load_dwordx4 a[164:167], v229, s[52:55], s1 offen offset:0 sc1
buffer_load_dwordx4 a[168:171], v228, s[52:55], s1 offen offset:1024 sc1
buffer_load_dwordx4 a[172:175], v229, s[52:55], s1 offen offset:1024 sc1
s_xor_b32 s1, s83, 22528
buffer_load_dwordx4 a[176:179], v228, s[52:55], s1 offen offset:0 sc1
s_waitcnt vmcnt(0)
v_accvgpr_read_b32 v248, a128
v_accvgpr_read_b32 v249, a132
v_accvgpr_read_b32 v250, a136
v_bitop3_b32 v248, v248, v249, v250 bitop3:0x80
v_accvgpr_read_b32 v251, a140
v_bitop3_b32 v248, v248, v251, 1 bitop3:0x80
v_cmp_ne_u32 vcc, 1, v248
s_nop 0
s_cbranch_vccnz LSt3k8_%=
LZt3k8_%=:
s_max_i32 s84, s84, 0
s_branch LRt3k8_%=
LSt3k9_%=:
s_sub_u32 s84, s84, 1
s_cbranch_scc1 LZt3k9_%=
s_xor_b32 s1, s83, 18432
buffer_load_dwordx4 a[144:147], v228, s[52:55], s1 offen offset:0 sc1
buffer_load_dwordx4 a[148:151], v229, s[52:55], s1 offen offset:0 sc1
buffer_load_dwordx4 a[152:155], v228, s[52:55], s1 offen offset:1024 sc1
buffer_load_dwordx4 a[156:159], v229, s[52:55], s1 offen offset:1024 sc1
s_xor_b32 s1, s83, 20480
buffer_load_dwordx4 a[160:163], v228, s[52:55], s1 offen offset:0 sc1
buffer_load_dwordx4 a[164:167], v229, s[52:55], s1 offen offset:0 sc1
buffer_load_dwordx4 a[168:171], v228, s[52:55], s1 offen offset:1024 sc1
buffer_load_dwordx4 a[172:175], v229, s[52:55], s1 offen offset:1024 sc1
s_xor_b32 s1, s83, 22528
buffer_load_dwordx4 a[176:179], v228, s[52:55], s1 offen offset:0 sc1
buffer_load_dwordx4 a[180:183], v229, s[52:55], s1 offen offset:0 sc1
buffer_load_dwordx4 a[184:187], v228, s[52:55], s1 offen offset:1024 sc1
buffer_load_dwordx4 a[188:191], v229, s[52:55], s1 offen offset:1024 sc1
s_xor_b32 s1, s83, 24576
buffer_load_dwordx4 a[192:195], v228, s[52:55], s1 offen offset:0 sc1
s_waitcnt vmcnt(0)
v_accvgpr_read_b32 v248, a144
v_accvgpr_read_b32 v249, a148
v_accvgpr_read_b32 v250, a152
v_bitop3_b32 v248, v248, v249, v250 bitop3:0x80
v_accvgpr_read_b32 v251, a156
v_bitop3_b32 v248, v248, v251, 1 bitop3:0x80
v_cmp_ne_u32 vcc, 1, v248
s_nop 0
s_cbranch_vccnz LSt3k9_%=
LZt3k9_%=:
s_max_i32 s84, s84, 0
s_branch LRt3k9_%=
LSt3k10_%=:
s_sub_u32 s84, s84, 1
s_cbranch_scc1 LZt3k10_%=
s_xor_b32 s1, s83, 20480
buffer_load_dwordx4 a[160:163], v228, s[52:55], s1 offen offset:0 sc1
buffer_load_dwordx4 a[164:167], v229, s[52:55], s1 offen offset:0 sc1
buffer_load_dwordx4 a[168:171], v228, s[52:55], s1 offen offset:1024 sc1
buffer_load_dwordx4 a[172:175], v229, s[52:55], s1 offen offset:1024 sc1
s_xor_b32 s1, s83, 22528
buffer_load_dwordx4 a[176:179], v228, s[52:55], s1 offen offset:0 sc1
buffer_load_dwordx4 a[180:183], v229, s[52:55], s1 offen offset:0 sc1
buffer_load_dwordx4 a[184:187], v228, s[52:55], s1 offen offset:1024 sc1
buffer_load_dwordx4 a[188:191], v229, s[52:55], s1 offen offset:1024 sc1
s_xor_b32 s1, s83, 24576
buffer_load_dwordx4 a[192:195], v228, s[52:55], s1 offen offset:0 sc1
buffer_load_dwordx4 a[196:199], v229, s[52:55], s1 offen offset:0 sc1
buffer_load_dwordx4 a[200:203], v228, s[52:55], s1 offen offset:1024 sc1
buffer_load_dwordx4 a[204:207], v229, s[52:55], s1 offen offset:1024 sc1
s_xor_b32 s1, s83, 26624
buffer_load_dwordx4 a[208:211], v228, s[52:55], s1 offen offset:0 sc1
s_waitcnt vmcnt(0)
v_accvgpr_read_b32 v248, a160
v_accvgpr_read_b32 v249, a164
v_accvgpr_read_b32 v250, a168
v_bitop3_b32 v248, v248, v249, v250 bitop3:0x80
v_accvgpr_read_b32 v251, a172
v_bitop3_b32 v248, v248, v251, 1 bitop3:0x80
v_cmp_ne_u32 vcc, 1, v248
s_nop 0
s_cbranch_vccnz LSt3k10_%=
LZt3k10_%=:
s_max_i32 s84, s84, 0
s_branch LRt3k10_%=
LSt3k11_%=:
s_sub_u32 s84, s84, 1
s_cbranch_scc1 LZt3k11_%=
s_xor_b32 s1, s83, 22528
buffer_load_dwordx4 a[176:179], v228, s[52:55], s1 offen offset:0 sc1
buffer_load_dwordx4 a[180:183], v229, s[52:55], s1 offen offset:0 sc1
buffer_load_dwordx4 a[184:187], v228, s[52:55], s1 offen offset:1024 sc1
buffer_load_dwordx4 a[188:191], v229, s[52:55], s1 offen offset:1024 sc1
s_xor_b32 s1, s83, 24576
buffer_load_dwordx4 a[192:195], v228, s[52:55], s1 offen offset:0 sc1
buffer_load_dwordx4 a[196:199], v229, s[52:55], s1 offen offset:0 sc1
buffer_load_dwordx4 a[200:203], v228, s[52:55], s1 offen offset:1024 sc1
buffer_load_dwordx4 a[204:207], v229, s[52:55], s1 offen offset:1024 sc1
s_xor_b32 s1, s83, 26624
buffer_load_dwordx4 a[208:211], v228, s[52:55], s1 offen offset:0 sc1
buffer_load_dwordx4 a[212:215], v229, s[52:55], s1 offen offset:0 sc1
buffer_load_dwordx4 a[216:219], v228, s[52:55], s1 offen offset:1024 sc1
buffer_load_dwordx4 a[220:223], v229, s[52:55], s1 offen offset:1024 sc1
s_xor_b32 s1, s83, 28672
buffer_load_dwordx4 a[224:227], v228, s[52:55], s1 offen offset:0 sc1
)"
R"(
buffer_load_dwordx4 a[228:231], v229, s[52:55], s1 offen offset:0 sc1
s_waitcnt vmcnt(0)
v_accvgpr_read_b32 v248, a176
v_accvgpr_read_b32 v249, a180
v_accvgpr_read_b32 v250, a184
v_bitop3_b32 v248, v248, v249, v250 bitop3:0x80
v_accvgpr_read_b32 v251, a188
v_bitop3_b32 v248, v248, v251, 1 bitop3:0x80
v_cmp_ne_u32 vcc, 1, v248
s_nop 0
s_cbranch_vccnz LSt3k11_%=
LZt3k11_%=:
s_max_i32 s84, s84, 0
s_branch LRt3k11_%=
LSt3k12_%=:
s_sub_u32 s84, s84, 1
s_cbranch_scc1 LZt3k12_%=
s_xor_b32 s1, s83, 24576
buffer_load_dwordx4 a[192:195], v228, s[52:55], s1 offen offset:0 sc1
buffer_load_dwordx4 a[196:199], v229, s[52:55], s1 offen offset:0 sc1
buffer_load_dwordx4 a[200:203], v228, s[52:55], s1 offen offset:1024 sc1
buffer_load_dwordx4 a[204:207], v229, s[52:55], s1 offen offset:1024 sc1
s_xor_b32 s1, s83, 26624
buffer_load_dwordx4 a[208:211], v228, s[52:55], s1 offen offset:0 sc1
buffer_load_dwordx4 a[212:215], v229, s[52:55], s1 offen offset:0 sc1
buffer_load_dwordx4 a[216:219], v228, s[52:55], s1 offen offset:1024 sc1
buffer_load_dwordx4 a[220:223], v229, s[52:55], s1 offen offset:1024 sc1
s_xor_b32 s1, s83, 28672
buffer_load_dwordx4 a[224:227], v228, s[52:55], s1 offen offset:0 sc1
buffer_load_dwordx4 a[228:231], v229, s[52:55], s1 offen offset:0 sc1
buffer_load_dwordx4 a[232:235], v228, s[52:55], s1 offen offset:1024 sc1
buffer_load_dwordx4 a[236:239], v229, s[52:55], s1 offen offset:1024 sc1
s_xor_b32 s1, s83, 30720
buffer_load_dwordx4 a[240:243], v228, s[52:55], s1 offen offset:0 sc1
buffer_load_dwordx4 a[244:247], v229, s[52:55], s1 offen offset:0 sc1
s_waitcnt vmcnt(0)
v_accvgpr_read_b32 v248, a192
v_accvgpr_read_b32 v249, a196
v_accvgpr_read_b32 v250, a200
v_bitop3_b32 v248, v248, v249, v250 bitop3:0x80
v_accvgpr_read_b32 v251, a204
v_bitop3_b32 v248, v248, v251, 1 bitop3:0x80
v_cmp_ne_u32 vcc, 1, v248
s_nop 0
s_cbranch_vccnz LSt3k12_%=
LZt3k12_%=:
s_max_i32 s84, s84, 0
s_branch LRt3k12_%=
LSt3k13_%=:
s_sub_u32 s84, s84, 1
s_cbranch_scc1 LZt3k13_%=
s_xor_b32 s1, s83, 26624
buffer_load_dwordx4 a[208:211], v228, s[52:55], s1 offen offset:0 sc1
buffer_load_dwordx4 a[212:215], v229, s[52:55], s1 offen offset:0 sc1
buffer_load_dwordx4 a[216:219], v228, s[52:55], s1 offen offset:1024 sc1
buffer_load_dwordx4 a[220:223], v229, s[52:55], s1 offen offset:1024 sc1
s_xor_b32 s1, s83, 28672
buffer_load_dwordx4 a[224:227], v228, s[52:55], s1 offen offset:0 sc1
buffer_load_dwordx4 a[228:231], v229, s[52:55], s1 offen offset:0 sc1
buffer_load_dwordx4 a[232:235], v228, s[52:55], s1 offen offset:1024 sc1
buffer_load_dwordx4 a[236:239], v229, s[52:55], s1 offen offset:1024 sc1
s_xor_b32 s1, s83, 30720
buffer_load_dwordx4 a[240:243], v228, s[52:55], s1 offen offset:0 sc1
buffer_load_dwordx4 a[244:247], v229, s[52:55], s1 offen offset:0 sc1
buffer_load_dwordx4 a[248:251], v228, s[52:55], s1 offen offset:1024 sc1
buffer_load_dwordx4 a[252:255], v229, s[52:55], s1 offen offset:1024 sc1
s_waitcnt vmcnt(0)
v_accvgpr_read_b32 v248, a208
v_accvgpr_read_b32 v249, a212
v_accvgpr_read_b32 v250, a216
v_bitop3_b32 v248, v248, v249, v250 bitop3:0x80
v_accvgpr_read_b32 v251, a220
v_bitop3_b32 v248, v248, v251, 1 bitop3:0x80
v_cmp_ne_u32 vcc, 1, v248
s_nop 0
s_cbranch_vccnz LSt3k13_%=
LZt3k13_%=:
s_max_i32 s84, s84, 0
s_branch LRt3k13_%=
LSt3k14_%=:
s_sub_u32 s84, s84, 1
s_cbranch_scc1 LZt3k14_%=
s_xor_b32 s1, s83, 28672
buffer_load_dwordx4 a[224:227], v228, s[52:55], s1 offen offset:0 sc1
buffer_load_dwordx4 a[228:231], v229, s[52:55], s1 offen offset:0 sc1
buffer_load_dwordx4 a[232:235], v228, s[52:55], s1 offen offset:1024 sc1
buffer_load_dwordx4 a[236:239], v229, s[52:55], s1 offen offset:1024 sc1
s_xor_b32 s1, s83, 30720
buffer_load_dwordx4 a[240:243], v228, s[52:55], s1 offen offset:0 sc1
buffer_load_dwordx4 a[244:247], v229, s[52:55], s1 offen offset:0 sc1
buffer_load_dwordx4 a[248:251], v228, s[52:55], s1 offen offset:1024 sc1
buffer_load_dwordx4 a[252:255], v229, s[52:55], s1 offen offset:1024 sc1
s_waitcnt vmcnt(0)
v_accvgpr_read_b32 v248, a224
v_accvgpr_read_b32 v249, a228
v_accvgpr_read_b32 v250, a232
v_bitop3_b32 v248, v248, v249, v250 bitop3:0x80
v_accvgpr_read_b32 v251, a236
v_bitop3_b32 v248, v248, v251, 1 bitop3:0x80
v_cmp_ne_u32 vcc, 1, v248
s_nop 0
s_cbranch_vccnz LSt3k14_%=
LZt3k14_%=:
s_max_i32 s84, s84, 0
s_branch LRt3k14_%=
LSt3k15_%=:
s_sub_u32 s84, s84, 1
s_cbranch_scc1 LZt3k15_%=
s_xor_b32 s1, s83, 30720
buffer_load_dwordx4 a[240:243], v228, s[52:55], s1 offen offset:0 sc1
buffer_load_dwordx4 a[244:247], v229, s[52:55], s1 offen offset:0 sc1
buffer_load_dwordx4 a[248:251], v228, s[52:55], s1 offen offset:1024 sc1
buffer_load_dwordx4 a[252:255], v229, s[52:55], s1 offen offset:1024 sc1
s_waitcnt vmcnt(0)
v_accvgpr_read_b32 v248, a240
v_accvgpr_read_b32 v249, a244
v_accvgpr_read_b32 v250, a248
v_bitop3_b32 v248, v248, v249, v250 bitop3:0x80
v_accvgpr_read_b32 v251, a252
v_bitop3_b32 v248, v248, v251, 1 bitop3:0x80
v_cmp_ne_u32 vcc, 1, v248
s_nop 0
s_cbranch_vccnz LSt3k15_%=
LZt3k15_%=:
s_max_i32 s84, s84, 0
s_branch LRt3k15_%=
LSt4k16_%=:
s_sub_u32 s84, s84, 1
s_cbranch_scc1 LZt4k16_%=
buffer_load_dwordx4 a[0:3], v232, s[52:55], s94 offen offset:0 sc1
buffer_load_dwordx4 a[4:7], v233, s[52:55], s94 offen offset:0 sc1
buffer_load_dwordx4 a[8:11], v232, s[52:55], s94 offen offset:1024 sc1
buffer_load_dwordx4 a[12:15], v233, s[52:55], s94 offen offset:1024 sc1
buffer_load_dwordx4 a[16:19], v232, s[52:55], s95 offen offset:0 sc1
buffer_load_dwordx4 a[20:23], v233, s[52:55], s95 offen offset:0 sc1
buffer_load_dwordx4 a[24:27], v232, s[52:55], s95 offen offset:1024 sc1
buffer_load_dwordx4 a[28:31], v233, s[52:55], s95 offen offset:1024 sc1
buffer_load_dwordx4 a[32:35], v232, s[52:55], s96 offen offset:0 sc1
buffer_load_dwordx4 a[36:39], v233, s[52:55], s96 offen offset:0 sc1
buffer_load_dwordx4 a[40:43], v232, s[52:55], s96 offen offset:1024 sc1
buffer_load_dwordx4 a[44:47], v233, s[52:55], s96 offen offset:1024 sc1
buffer_load_dwordx4 a[48:51], v232, s[52:55], s97 offen offset:0 sc1
buffer_load_dwordx4 a[52:55], v233, s[52:55], s97 offen offset:0 sc1
buffer_load_dwordx4 a[56:59], v232, s[52:55], s97 offen offset:1024 sc1
buffer_load_dwordx4 a[60:63], v233, s[52:55], s97 offen offset:1024 sc1
buffer_load_dwordx4 a[64:67], v232, s[52:55], s98 offen offset:0 sc1
buffer_load_dwordx4 a[68:71], v233, s[52:55], s98 offen offset:0 sc1
buffer_load_dwordx4 a[72:75], v232, s[52:55], s98 offen offset:1024 sc1
buffer_load_dwordx4 a[76:79], v233, s[52:55], s98 offen offset:1024 sc1
buffer_load_dwordx4 a[80:83], v232, s[52:55], s99 offen offset:0 sc1
buffer_load_dwordx4 a[84:87], v233, s[52:55], s99 offen offset:0 sc1
buffer_load_dwordx4 a[88:91], v232, s[52:55], s99 offen offset:1024 sc1
s_waitcnt vmcnt(0)
v_accvgpr_read_b32 v248, a0
v_accvgpr_read_b32 v249, a4
v_accvgpr_read_b32 v250, a8
v_max3_u32 v248, v248, v249, v250
v_accvgpr_read_b32 v251, a12
v_max_u32 v248, v248, v251
v_cmp_eq_u32 vcc, -1, v248
s_nop 0
s_cbranch_vccnz LSt4k16_%=
LZt4k16_%=:
s_max_i32 s84, s84, 0
s_branch LRt4k16_%=
LSt4k17_%=:
s_sub_u32 s84, s84, 1
s_cbranch_scc1 LZt4k17_%=
buffer_load_dwordx4 a[16:19], v232, s[52:55], s95 offen offset:0 sc1
buffer_load_dwordx4 a[20:23], v233, s[52:55], s95 offen offset:0 sc1
buffer_load_dwordx4 a[24:27], v232, s[52:55], s95 offen offset:1024 sc1
buffer_load_dwordx4 a[28:31], v233, s[52:55], s95 offen offset:1024 sc1
buffer_load_dwordx4 a[32:35], v232, s[52:55], s96 offen offset:0 sc1
buffer_load_dwordx4 a[36:39], v233, s[52:55], s96 offen offset:0 sc1
buffer_load_dwordx4 a[40:43], v232, s[52:55], s96 offen offset:1024 sc1
buffer_load_dwordx4 a[44:47], v233, s[52:55], s96 offen offset:1024 sc1
buffer_load_dwordx4 a[48:51], v232, s[52:55], s97 offen offset:0 sc1
buffer_load_dwordx4 a[52:55], v233, s[52:55], s97 offen offset:0 sc1
buffer_load_dwordx4 a[56:59], v232, s[52:55], s97 offen offset:1024 sc1
buffer_load_dwordx4 a[60:63], v233, s[52:55], s97 offen offset:1024 sc1
buffer_load_dwordx4 a[64:67], v232, s[52:55], s98 offen offset:0 sc1
buffer_load_dwordx4 a[68:71], v233, s[52:55], s98 offen offset:0 sc1
buffer_load_dwordx4 a[72:75], v232, s[52:55], s98 offen offset:1024 sc1
buffer_load_dwordx4 a[76:79], v233, s[52:55], s98 offen offset:1024 sc1
buffer_load_dwordx4 a[80:83], v232, s[52:55], s99 offen offset:0 sc1
buffer_load_dwordx4 a[84:87], v233, s[52:55], s99 offen offset:0 sc1
buffer_load_dwordx4 a[88:91], v232, s[52:55], s99 offen offset:1024 sc1
buffer_load_dwordx4 a[92:95], v233, s[52:55], s99 offen offset:1024 sc1
buffer_load_dwordx4 a[96:99], v232, s[52:55], s100 offen offset:0 sc1
buffer_load_dwordx4 a[100:103], v233, s[52:55], s100 offen offset:0 sc1
buffer_load_dwordx4 a[104:107], v232, s[52:55], s100 offen offset:1024 sc1
buffer_load_dwordx4 a[108:111], v233, s[52:55], s100 offen offset:1024 sc1
buffer_load_dwordx4 a[112:115], v232, s[52:55], s101 offen offset:0 sc1
s_waitcnt vmcnt(0)
v_accvgpr_read_b32 v248, a16
v_accvgpr_read_b32 v249, a20
v_accvgpr_read_b32 v250, a24
v_max3_u32 v248, v248, v249, v250
v_accvgpr_read_b32 v251, a28
v_max_u32 v248, v248, v251
v_cmp_eq_u32 vcc, -1, v248
s_nop 0
s_cbranch_vccnz LSt4k17_%=
LZt4k17_%=:
s_max_i32 s84, s84, 0
s_branch LRt4k17_%=
LSt4k18_%=:
s_sub_u32 s84, s84, 1
s_cbranch_scc1 LZt4k18_%=
v_subrev_u32 v248, s31, v232
buffer_load_dwordx4 a[32:35], v248, s[52:55], s96 offen offset:0 sc1
v_subrev_u32 v248, s31, v233
buffer_load_dwordx4 a[36:39], v248, s[52:55], s96 offen offset:0 sc1
v_subrev_u32 v248, s31, v232
buffer_load_dwordx4 a[40:43], v248, s[52:55], s96 offen offset:1024 sc1
v_subrev_u32 v248, s31, v233
buffer_load_dwordx4 a[44:47], v248, s[52:55], s96 offen offset:1024 sc1
v_subrev_u32 v248, s31, v232
buffer_load_dwordx4 a[48:51], v248, s[52:55], s97 offen offset:0 sc1
v_subrev_u32 v248, s31, v233
buffer_load_dwordx4 a[52:55], v248, s[52:55], s97 offen offset:0 sc1
v_subrev_u32 v248, s31, v232
buffer_load_dwordx4 a[56:59], v248, s[52:55], s97 offen offset:1024 sc1
v_subrev_u32 v248, s31, v233
buffer_load_dwordx4 a[60:63], v248, s[52:55], s97 offen offset:1024 sc1
v_subrev_u32 v248, s31, v232
buffer_load_dwordx4 a[64:67], v248, s[52:55], s98 offen offset:0 sc1
v_subrev_u32 v248, s31, v233
buffer_load_dwordx4 a[68:71], v248, s[52:55], s98 offen offset:0 sc1
v_subrev_u32 v248, s31, v232
buffer_load_dwordx4 a[72:75], v248, s[52:55], s98 offen offset:1024 sc1
v_subrev_u32 v248, s31, v233
buffer_load_dwordx4 a[76:79], v248, s[52:55], s98 offen offset:1024 sc1
v_subrev_u32 v248, s31, v232
buffer_load_dwordx4 a[80:83], v248, s[52:55], s99 offen offset:0 sc1
v_subrev_u32 v248, s31, v233
buffer_load_dwordx4 a[84:87], v248, s[52:55], s99 offen offset:0 sc1
v_subrev_u32 v248, s31, v232
buffer_load_dwordx4 a[88:91], v248, s[52:55], s99 offen offset:1024 sc1
v_subrev_u32 v248, s31, v233
buffer_load_dwordx4 a[92:95], v248, s[52:55], s99 offen offset:1024 sc1
v_subrev_u32 v248, s31, v232
buffer_load_dwordx4 a[96:99], v248, s[52:55], s100 offen offset:0 sc1
v_subrev_u32 v248, s31, v233
buffer_load_dwordx4 a[100:103], v248, s[52:55], s100 offen offset:0 sc1
v_subrev_u32 v248, s31, v232
buffer_load_dwordx4 a[104:107], v248, s[52:55], s100 offen offset:1024 sc1
v_subrev_u32 v248, s31, v233
buffer_load_dwordx4 a[108:111], v248, s[52:55], s100 offen offset:1024 sc1
v_subrev_u32 v248, s31, v232
buffer_load_dwordx4 a[112:115], v248, s[52:55], s101 offen offset:0 sc1
v_subrev_u32 v248, s31, v233
buffer_load_dwordx4 a[116:119], v248, s[52:55], s101 offen offset:0 sc1
v_subrev_u32 v248, s31, v232
buffer_load_dwordx4 a[120:123], v248, s[52:55], s101 offen offset:1024 sc1
v_subrev_u32 v248, s31, v233
buffer_load_dwordx4 a[124:127], v248, s[52:55], s101 offen offset:1024 sc1
buffer_load_dwordx4 a[128:131], v232, s[52:55], s94 offen offset:0 sc1
buffer_load_dwordx4 a[132:135], v233, s[52:55], s94 offen offset:0 sc1
buffer_load_dwordx4 a[136:139], v232, s[52:55], s94 offen offset:1024 sc1
buffer_load_dwordx4 a[140:143], v233, s[52:55], s94 offen offset:1024 sc1
buffer_load_dwordx4 a[144:147], v232, s[52:55], s96 offen offset:0 sc1
s_waitcnt vmcnt(0)
v_accvgpr_read_b32 v248, a32
v_accvgpr_read_b32 v249, a36
v_accvgpr_read_b32 v250, a40
v_max3_u32 v248, v248, v249, v250
v_accvgpr_read_b32 v251, a44
v_accvgpr_read_b32 v252, a48
v_max3_u32 v248, v248, v251, v252
v_accvgpr_read_b32 v249, a52
v_accvgpr_read_b32 v250, a56
v_max3_u32 v248, v248, v249, v250
v_accvgpr_read_b32 v251, a60
v_max_u32 v248, v248, v251
v_cmp_eq_u32 vcc, -1, v248
s_nop 0
s_cbranch_vccnz LSt4k18_%=
LZt4k18_%=:
s_max_i32 s84, s84, 0
s_branch LRt4k18_%=
LSt4k20_%=:
s_sub_u32 s84, s84, 1
s_cbranch_scc1 LZt4k20_%=
v_subrev_u32 v248, s31, v232
buffer_load_dwordx4 a[64:67], v248, s[52:55], s98 offen offset:0 sc1
v_subrev_u32 v248, s31, v233
buffer_load_dwordx4 a[68:71], v248, s[52:55], s98 offen offset:0 sc1
v_subrev_u32 v248, s31, v232
buffer_load_dwordx4 a[72:75], v248, s[52:55], s98 offen offset:1024 sc1
v_subrev_u32 v248, s31, v233
buffer_load_dwordx4 a[76:79], v248, s[52:55], s98 offen offset:1024 sc1
v_subrev_u32 v248, s31, v232
buffer_load_dwordx4 a[80:83], v248, s[52:55], s99 offen offset:0 sc1
v_subrev_u32 v248, s31, v233
buffer_load_dwordx4 a[84:87], v248, s[52:55], s99 offen offset:0 sc1
v_subrev_u32 v248, s31, v232
buffer_load_dwordx4 a[88:91], v248, s[52:55], s99 offen offset:1024 sc1
v_subrev_u32 v248, s31, v233
buffer_load_dwordx4 a[92:95], v248, s[52:55], s99 offen offset:1024 sc1
v_subrev_u32 v248, s31, v232
buffer_load_dwordx4 a[96:99], v248, s[52:55], s100 offen offset:0 sc1
v_subrev_u32 v248, s31, v233
buffer_load_dwordx4 a[100:103], v248, s[52:55], s100 offen offset:0 sc1
v_subrev_u32 v248, s31, v232
buffer_load_dwordx4 a[104:107], v248, s[52:55], s100 offen offset:1024 sc1
v_subrev_u32 v248, s31, v233
buffer_load_dwordx4 a[108:111], v248, s[52:55], s100 offen offset:1024 sc1
v_subrev_u32 v248, s31, v232
buffer_load_dwordx4 a[112:115], v248, s[52:55], s101 offen offset:0 sc1
v_subrev_u32 v248, s31, v233
buffer_load_dwordx4 a[116:119], v248, s[52:55], s101 offen offset:0 sc1
v_subrev_u32 v248, s31, v232
buffer_load_dwordx4 a[120:123], v248, s[52:55], s101 offen offset:1024 sc1
v_subrev_u32 v248, s31, v233
buffer_load_dwordx4 a[124:127], v248, s[52:55], s101 offen offset:1024 sc1
buffer_load_dwordx4 a[128:131], v232, s[52:55], s94 offen offset:0 sc1
buffer_load_dwordx4 a[132:135], v233, s[52:55], s94 offen offset:0 sc1
buffer_load_dwordx4 a[136:139], v232, s[52:55], s94 offen offset:1024 sc1
buffer_load_dwordx4 a[140:143], v233, s[52:55], s94 offen offset:1024 sc1
buffer_load_dwordx4 a[144:147], v232, s[52:55], s96 offen offset:0 sc1
buffer_load_dwordx4 a[148:151], v233, s[52:55], s96 offen offset:0 sc1
buffer_load_dwordx4 a[152:155], v232, s[52:55], s96 offen offset:1024 sc1
buffer_load_dwordx4 a[156:159], v233, s[52:55], s96 offen offset:1024 sc1
buffer_load_dwordx4 a[160:163], v232, s[52:55], s98 offen offset:0 sc1
buffer_load_dwordx4 a[164:167], v233, s[52:55], s98 offen offset:0 sc1
buffer_load_dwordx4 a[168:171], v232, s[52:55], s98 offen offset:1024 sc1
buffer_load_dwordx4 a[172:175], v233, s[52:55], s98 offen offset:1024 sc1
buffer_load_dwordx4 a[176:179], v232, s[52:55], s100 offen offset:0 sc1
s_waitcnt vmcnt(0)
v_accvgpr_read_b32 v248, a64
v_accvgpr_read_b32 v249, a68
v_accvgpr_read_b32 v250, a72
v_max3_u32 v248, v248, v249, v250
v_accvgpr_read_b32 v251, a76
v_accvgpr_read_b32 v252, a80
v_max3_u32 v248, v248, v251, v252
v_accvgpr_read_b32 v249, a84
v_accvgpr_read_b32 v250, a88
v_max3_u32 v248, v248, v249, v250
v_accvgpr_read_b32 v251, a92
v_max_u32 v248, v248, v251
v_cmp_eq_u32 vcc, -1, v248
s_nop 0
s_cbranch_vccnz LSt4k20_%=
LZt4k20_%=:
s_max_i32 s84, s84, 0
s_branch LRt4k20_%=
LSt4k22_%=:
s_sub_u32 s84, s84, 1
s_cbranch_scc1 LZt4k22_%=
v_subrev_u32 v248, s31, v232
buffer_load_dwordx4 a[96:99], v248, s[52:55], s100 offen offset:0 sc1
v_subrev_u32 v248, s31, v233
buffer_load_dwordx4 a[100:103], v248, s[52:55], s100 offen offset:0 sc1
v_subrev_u32 v248, s31, v232
buffer_load_dwordx4 a[104:107], v248, s[52:55], s100 offen offset:1024 sc1
v_subrev_u32 v248, s31, v233
buffer_load_dwordx4 a[108:111], v248, s[52:55], s100 offen offset:1024 sc1
v_subrev_u32 v248, s31, v232
buffer_load_dwordx4 a[112:115], v248, s[52:55], s101 offen offset:0 sc1
v_subrev_u32 v248, s31, v233
buffer_load_dwordx4 a[116:119], v248, s[52:55], s101 offen offset:0 sc1
v_subrev_u32 v248, s31, v232
buffer_load_dwordx4 a[120:123], v248, s[52:55], s101 offen offset:1024 sc1
v_subrev_u32 v248, s31, v233
buffer_load_dwordx4 a[124:127], v248, s[52:55], s101 offen offset:1024 sc1
buffer_load_dwordx4 a[128:131], v232, s[52:55], s94 offen offset:0 sc1
buffer_load_dwordx4 a[132:135], v233, s[52:55], s94 offen offset:0 sc1
buffer_load_dwordx4 a[136:139], v232, s[52:55], s94 offen offset:1024 sc1
buffer_load_dwordx4 a[140:143], v233, s[52:55], s94 offen offset:1024 sc1
buffer_load_dwordx4 a[144:147], v232, s[52:55], s96 offen offset:0 sc1
buffer_load_dwordx4 a[148:151], v233, s[52:55], s96 offen offset:0 sc1
buffer_load_dwordx4 a[152:155], v232, s[52:55], s96 offen offset:1024 sc1
buffer_load_dwordx4 a[156:159], v233, s[52:55], s96 offen offset:1024 sc1
buffer_load_dwordx4 a[160:163], v232, s[52:55], s98 offen offset:0 sc1
buffer_load_dwordx4 a[164:167], v233, s[52:55], s98 offen offset:0 sc1
buffer_load_dwordx4 a[168:171], v232, s[52:55], s98 offen offset:1024 sc1
buffer_load_dwordx4 a[172:175], v233, s[52:55], s98 offen offset:1024 sc1
buffer_load_dwordx4 a[176:179], v232, s[52:55], s100 offen offset:0 sc1
buffer_load_dwordx4 a[180:183], v233, s[52:55], s100 offen offset:0 sc1
buffer_load_dwordx4 a[184:187], v232, s[52:55], s100 offen offset:1024 sc1
buffer_load_dwordx4 a[188:191], v233, s[52:55], s100 offen offset:1024 sc1
s_waitcnt vmcnt(0)
v_accvgpr_read_b32 v248, a96
v_accvgpr_read_b32 v249, a100
v_accvgpr_read_b32 v250, a104
v_max3_u32 v248, v248, v249, v250
v_accvgpr_read_b32 v251, a108
v_accvgpr_read_b32 v252, a112
v_max3_u32 v248, v248, v251, v252
v_accvgpr_read_b32 v249, a116
v_accvgpr_read_b32 v250, a120
v_max3_u32 v248, v248, v249, v250
v_accvgpr_read_b32 v251, a124
v_max_u32 v248, v248, v251
v_cmp_eq_u32 vcc, -1, v248
s_nop 0
s_cbranch_vccnz LSt4k22_%=
LZt4k22_%=:
)"
R"(
s_max_i32 s84, s84, 0
s_branch LRt4k22_%=
LSt4k24_%=:
s_sub_u32 s84, s84, 1
s_cbranch_scc1 LZt4k24_%=
buffer_load_dwordx4 a[128:131], v232, s[52:55], s94 offen offset:0 sc1
buffer_load_dwordx4 a[132:135], v233, s[52:55], s94 offen offset:0 sc1
buffer_load_dwordx4 a[136:139], v232, s[52:55], s94 offen offset:1024 sc1
buffer_load_dwordx4 a[140:143], v233, s[52:55], s94 offen offset:1024 sc1
buffer_load_dwordx4 a[144:147], v232, s[52:55], s96 offen offset:0 sc1
buffer_load_dwordx4 a[148:151], v233, s[52:55], s96 offen offset:0 sc1
buffer_load_dwordx4 a[152:155], v232, s[52:55], s96 offen offset:1024 sc1
buffer_load_dwordx4 a[156:159], v233, s[52:55], s96 offen offset:1024 sc1
buffer_load_dwordx4 a[160:163], v232, s[52:55], s98 offen offset:0 sc1
buffer_load_dwordx4 a[164:167], v233, s[52:55], s98 offen offset:0 sc1
buffer_load_dwordx4 a[168:171], v232, s[52:55], s98 offen offset:1024 sc1
buffer_load_dwordx4 a[172:175], v233, s[52:55], s98 offen offset:1024 sc1
buffer_load_dwordx4 a[176:179], v232, s[52:55], s100 offen offset:0 sc1
buffer_load_dwordx4 a[180:183], v233, s[52:55], s100 offen offset:0 sc1
buffer_load_dwordx4 a[184:187], v232, s[52:55], s100 offen offset:1024 sc1
buffer_load_dwordx4 a[188:191], v233, s[52:55], s100 offen offset:1024 sc1
s_waitcnt vmcnt(0)
v_accvgpr_read_b32 v248, a128
v_accvgpr_read_b32 v249, a132
v_accvgpr_read_b32 v250, a136
v_max3_u32 v248, v248, v249, v250
v_accvgpr_read_b32 v251, a140
v_accvgpr_read_b32 v252, a144
v_max3_u32 v248, v248, v251, v252
v_accvgpr_read_b32 v249, a148
v_accvgpr_read_b32 v250, a152
v_max3_u32 v248, v248, v249, v250
v_accvgpr_read_b32 v251, a156
v_max_u32 v248, v248, v251
v_cmp_eq_u32 vcc, -1, v248
s_nop 0
s_cbranch_vccnz LSt4k24_%=
LZt4k24_%=:
s_max_i32 s84, s84, 0
s_branch LRt4k24_%=
LSt4k26_%=:
s_sub_u32 s84, s84, 1
s_cbranch_scc1 LZt4k26_%=
buffer_load_dwordx4 a[160:163], v232, s[52:55], s98 offen offset:0 sc1
buffer_load_dwordx4 a[164:167], v233, s[52:55], s98 offen offset:0 sc1
buffer_load_dwordx4 a[168:171], v232, s[52:55], s98 offen offset:1024 sc1
buffer_load_dwordx4 a[172:175], v233, s[52:55], s98 offen offset:1024 sc1
buffer_load_dwordx4 a[176:179], v232, s[52:55], s100 offen offset:0 sc1
buffer_load_dwordx4 a[180:183], v233, s[52:55], s100 offen offset:0 sc1
buffer_load_dwordx4 a[184:187], v232, s[52:55], s100 offen offset:1024 sc1
buffer_load_dwordx4 a[188:191], v233, s[52:55], s100 offen offset:1024 sc1
s_waitcnt vmcnt(0)
v_accvgpr_read_b32 v248, a160
v_accvgpr_read_b32 v249, a164
v_accvgpr_read_b32 v250, a168
v_max3_u32 v248, v248, v249, v250
v_accvgpr_read_b32 v251, a172
v_accvgpr_read_b32 v252, a176
v_max3_u32 v248, v248, v251, v252
v_accvgpr_read_b32 v249, a180
v_accvgpr_read_b32 v250, a184
v_max3_u32 v248, v248, v249, v250
v_accvgpr_read_b32 v251, a188
v_max_u32 v248, v248, v251
v_cmp_eq_u32 vcc, -1, v248
s_nop 0
s_cbranch_vccnz LSt4k26_%=
LZt4k26_%=:
s_max_i32 s84, s84, 0
s_branch LRt4k26_%=
LE_%=:
)"
      :
      : "{s[4:5]}"(__builtin_amdgcn_kernarg_segment_ptr()), "{s6}"(blockIdx.x), "{v0}"(threadIdx.x)
      : "v1", "v2", "v3", "v4", "v5", "v6", "v7", "v8", "v9", "v10", "v11", "v12", "v13", "v14", "v15", "v16", "v17", "v18", "v19", "v20", "v21", "v22", "v23", "v24", "v25", "v26", "v27", "v28", "v29", "v30", "v31", "v32", "v33", "v34", "v35", "v36", "v37", "v38", "v39", "v40", "v41", "v42", "v43", "v44", "v45", "v46", "v47", "v48", "v49", "v50", "v51", "v52", "v53", "v54", "v55", "v56", "v57", "v58", "v59", "v60", "v61", "v62", "v63", "v64", "v65", "v66", "v67", "v68", "v69", "v70", "v71", "v72", "v73", "v74", "v75", "v76", "v77", "v78", "v79", "v80", "v81", "v82", "v83", "v84", "v85", "v86", "v87", "v88", "v89", "v90", "v91", "v92", "v93", "v94", "v95", "v96", "v97", "v98", "v99", "v100", "v101", "v102", "v103", "v104", "v105", "v106", "v107", "v108", "v109", "v110", "v111", "v112", "v113", "v114", "v115", "v116", "v117", "v118", "v119", "v120", "v121", "v122", "v123", "v124", "v125", "v126", "v127", "v128", "v129", "v130", "v131", "v132", "v133", "v134", "v135", "v136", "v137", "v138", "v139", "v140", "v141", "v142", "v143", "v144", "v145", "v146", "v147", "v148", "v149", "v150", "v151", "v152", "v153", "v154", "v155", "v156", "v157", "v158", "v159", "v160", "v161", "v162", "v163", "v164", "v165", "v166", "v167", "v168", "v169", "v170", "v171", "v172", "v173", "v174", "v175", "v176", "v177", "v178", "v179", "v180", "v181", "v182", "v183", "v184", "v185", "v186", "v187", "v188", "v189", "v190", "v191", "v192", "v193", "v194", "v195", "v196", "v197", "v198", "v199", "v200", "v201", "v202", "v203", "v204", "v205", "v206", "v207", "v208", "v209", "v210", "v211", "v212", "v213", "v214", "v215", "v216", "v217", "v218", "v219", "v220", "v221", "v222", "v223", "v224", "v225", "v226", "v227", "v228", "v229", "v230", "v231", "v232", "v233", "v234", "v235", "v236", "v237", "v238", "v239", "v240", "v241", "v242", "v243", "v244", "v245", "v246", "v247", "v248", "v249", "v250", "v251", "v252", "v253", "v254", "v255", "a0", "a1", "a2", "a3", "a4", "a5", "a6", "a7", "a8", "a9", "a10", "a11", "a12", "a13", "a14", "a15", "a16", "a17", "a18", "a19", "a20", "a21", "a22", "a23", "a24", "a25", "a26", "a27", "a28", "a29", "a30", "a31", "a32", "a33", "a34", "a35", "a36", "a37", "a38", "a39", "a40", "a41", "a42", "a43", "a44", "a45", "a46", "a47", "a48", "a49", "a50", "a51", "a52", "a53", "a54", "a55", "a56", "a57", "a58", "a59", "a60", "a61", "a62", "a63", "a64", "a65", "a66", "a67", "a68", "a69", "a70", "a71", "a72", "a73", "a74", "a75", "a76", "a77", "a78", "a79", "a80", "a81", "a82", "a83", "a84", "a85", "a86", "a87", "a88", "a89", "a90", "a91", "a92", "a93", "a94", "a95", "a96", "a97", "a98", "a99", "a100", "a101", "a102", "a103", "a104", "a105", "a106", "a107", "a108", "a109", "a110", "a111", "a112", "a113", "a114", "a115", "a116", "a117", "a118", "a119", "a120", "a121", "a122", "a123", "a124", "a125", "a126", "a127", "a128", "a129", "a130", "a131", "a132", "a133", "a134", "a135", "a136", "a137", "a138", "a139", "a140", "a141", "a142", "a143", "a144", "a145", "a146", "a147", "a148", "a149", "a150", "a151", "a152", "a153", "a154", "a155", "a156", "a157", "a158", "a159", "a160", "a161", "a162", "a163", "a164", "a165", "a166", "a167", "a168", "a169", "a170", "a171", "a172", "a173", "a174", "a175", "a176", "a177", "a178", "a179", "a180", "a181", "a182", "a183", "a184", "a185", "a186", "a187", "a188", "a189", "a190", "a191", "a192", "a193", "a194", "a195", "a196", "a197", "a198", "a199", "a200", "a201", "a202", "a203", "a204", "a205", "a206", "a207", "a208", "a209", "a210", "a211", "a212", "a213", "a214", "a215", "a216", "a217", "a218", "a219", "a220", "a221", "a222", "a223", "a224", "a225", "a226", "a227", "a228", "a229", "a230", "a231", "a232", "a233", "a234", "a235", "a236", "a237", "a238", "a239", "a240", "a241", "a242", "a243", "a244", "a245", "a246", "a247", "a248", "a249", "a250", "a251", "a252", "a253", "a254", "a255", "s0", "s1", "s2", "s3", "s7", "s8", "s9", "s10", "s11", "s12", "s13", "s14", "s15", "s16", "s17", "s18", "s19", "s20", "s21", "s22", "s23", "s24", "s25", "s26", "s27", "s28", "s29", "s30", "s31", "s32", "s33", "s34", "s35", "s36", "s37", "s38", "s39", "s40", "s41", "s42", "s43", "s44", "s45", "s46", "s47", "s48", "s49", "s50", "s51", "s52", "s53", "s54", "s55", "s56", "s57", "s58", "s59", "s60", "s61", "s62", "s63", "s64", "s65", "s66", "s67", "s68", "s69", "s70", "s71", "s72", "s73", "s74", "s75", "s76", "s77", "s78", "s79", "s80", "s81", "s82", "s83", "s84", "s85", "s86", "s87", "s88", "s89", "s90", "s91", "s92", "s93", "s94", "s95", "s96", "s97", "s98", "s99", "s100", "s101", "vcc", "m0", "scc", "memory");
}

extern "C" void kernel_launch(void* const* d_in, const int* in_sizes, int n_in, void* d_out, int out_size, void* d_ws, size_t ws_size,
                              hipStream_t stream) {
  (void)hipFuncSetAttribute((const void*)fused_kernel, hipFuncAttributeMaxDynamicSharedMemorySize, 163840);
  fused_kernel<<<256, 256, 163840, stream>>>(d_in[0], d_in[1], d_in[2], d_in[3], d_in[4], d_in[6], d_in[7], d_in[8], d_in[9], d_in[10], d_out, d_ws);
}
```
